```python
import torch

def _softmax(x, axis=-1):
    return torch.softmax(x, dim=axis)

def _gelu(x):
    return torch.nn.functional.gelu(x, approximate='tanh')

def _silu(x):
    return torch.nn.functional.silu(x)

def _sigmoid(x):
    return torch.sigmoid(x)

def _layernorm(x, axis=-1, eps=1e-05):
    m = x.mean(dim=axis, keepdim=True)
    v = x.var(dim=axis, keepdim=True, unbiased=False)
    return (x - m) / torch.sqrt(v + eps)

def _scatter_add(tgt, idx, src, axis=0):
    return tgt.clone().index_add_(axis, idx, src)

def _segment_sum(src, seg, n):
    out = torch.zeros((n,) + src.shape[1:], dtype=src.dtype)
    return out.index_add_(0, seg, src)

def _pad(x, pw):
    flat = tuple((v for pair in reversed(pw) for v in pair))
    return torch.nn.functional.pad(x, flat)
kernel_name = 'gated_mix__sliding_local_dilated__memory_aug_recurrent_mem_tr__prompt'

def setup_inputs():

    _w = lambda a: (lambda t: t.to(torch.bfloat16) if t.is_floating_point() else t)(torch.as_tensor(a))

    def _A_setup():
        B, S, D = (1, 2048, 576)
        return {'x': _w((lambda *s: torch.randn(*s, dtype=torch.bfloat16) * 0.05)(B, S, D)), 'wqkv': _w((lambda *s: torch.randn(*s, dtype=torch.bfloat16) * 0.8)(D, 3 * D)), 'wo': _w((lambda *s: torch.randn(*s, dtype=torch.bfloat16) * 0.05)(D, D))}
    _A_in = _A_setup()

    def _B_setup():
        B, S, Dm = (1, 2048, 576)
        H, Dh = (32, 128)
        M, seg = (8, 32)
        seg = min(seg, S)
        S = S // seg * seg
        return {'x': _w((lambda *s: torch.randn(*s, dtype=torch.bfloat16) * 0.05)(B, S, Dm)), 'mem0': _w((lambda *s: torch.randn(*s, dtype=torch.bfloat16) * 1.6)(B, M, Dm)), 'Wq': _w((lambda *s: torch.randn(*s, dtype=torch.bfloat16) * 0.8)(Dm, H, Dh) * Dm ** (-0.5)), 'Wk': _w((lambda *s: torch.randn(*s, dtype=torch.bfloat16) * 0.8)(Dm, H, Dh) * Dm ** (-0.5)), 'Wv': _w((lambda *s: torch.randn(*s, dtype=torch.bfloat16) * 1.6)(Dm, H, Dh) * Dm ** (-0.5)), 'Wo': _w((lambda *s: torch.randn(*s, dtype=torch.bfloat16) * 1.6)(H, Dh, Dm) * (H * Dh) ** (-0.5)), 'seg_len': seg}
    _B_in = _B_setup()
    return {'A_x': _A_in['x'], 'A_wqkv': _A_in['wqkv'], 'A_wo': _A_in['wo'], 'B_x': _B_in['x'], 'B_mem0': _B_in['mem0'], 'B_Wq': _B_in['Wq'], 'B_Wk': _B_in['Wk'], 'B_Wv': _B_in['Wv'], 'B_Wo': _B_in['Wo'], 'B_seg_len': _B_in['seg_len'], 'gate_w': _w((lambda *s: torch.randn(*s, dtype=torch.bfloat16) * 0.05)(576) * 16.0)}

def reference(A_x, A_wqkv, A_wo, B_x, B_mem0, B_Wq, B_Wk, B_Wv, B_Wo, B_seg_len, gate_w):

    def _A_ref(x, wqkv, wo):
        B, S, D = x.shape
        H = 32
        Dh = D // H
        W, dil = (8, 2)
        qkv = (lambda x, *d: x.permute(*d))((x @ wqkv).reshape(B, S, 3, H, Dh), 2, 0, 3, 1, 4)
        q, k, v = (qkv[0], qkv[1], qkv[2])
        scale = Dh ** (-0.5)
        pos = torch.arange(S)
        off = torch.arange(W) * dil
        idx = pos[:, None] - off[None, :]
        mask = idx >= 0
        idx_c = torch.where(mask, idx, 0).reshape(-1)
        kg = k[:, :, idx_c, :].reshape(B, H, S, W, Dh)
        vg = v[:, :, idx_c, :].reshape(B, H, S, W, Dh)
        logits = torch.einsum('bhsd,bhswd->bhsw', q, kg) * scale
        logits = torch.where(mask[None, None, :, :], logits, -1000000000.0)
        attn = _softmax(logits, axis=-1)
        o = torch.einsum('bhsw,bhswd->bhsd', attn, vg)
        return (lambda x, *d: x.permute(*d))(o, 0, 2, 1, 3).reshape(B, S, D) @ wo

    def _B_ref(x, mem0, Wq, Wk, Wv, Wo, seg_len):
        B, S, Dm = x.shape
        H, Dh = (Wq.shape[1], Wq.shape[2])
        M = mem0.shape[1]
        scale = Dh ** (-0.5)
        n_seg = S // seg_len
        L = M + seg_len
        idx = torch.arange(L)
        mask = (idx[:, None] < M) | (idx[:, None] >= idx[None, :])
        mem = mem0
        outs = []
        for s in range(n_seg):
            xs = x[:, s * seg_len:(s + 1) * seg_len, :].clone()
            h = torch.concatenate([mem, xs], axis=1)
            q = torch.einsum('btd,dhe->bhte', h, Wq)
            k = torch.einsum('btd,dhe->bhte', h, Wk)
            v = torch.einsum('btd,dhe->bhte', h, Wv)
            scores = torch.einsum('bhqe,bhke->bhqk', q, k) * scale
            scores = torch.where(mask[None, None], scores, -1000000000.0)
            p = _softmax(scores, axis=-1)
            o = torch.einsum('bhqk,bhke->bhqe', p, v)
            o = torch.einsum('bhte,hed->btd', o, Wo)
            mem = o[:, :M, :].clone()
            outs.append(o[:, M:, :])
        return torch.concatenate(outs, axis=1)
    x = A_x
    g = _sigmoid((x * gate_w).sum(-1, keepdims=True))
    a = _A_ref(x, A_wqkv, A_wo)
    b = _B_ref(x, B_mem0, B_Wq, B_Wk, B_Wv, B_Wo, B_seg_len)
    return g * a + (1 - g) * b
```

```cpp
#define EFIRST
#define CSLP 1
#define RTOL 2
#define QDL 0
#define LTOL 16
#define GRID 48
#define XT 6
#define NOAT
#define AWD 8
#define XPD 8
#define NTM 15
#define FIXL
#define K2 68
#define T1D 100
#define DE0 100
#define DELTA0 172
#include <hip/hip_runtime.h>
#include <stdint.h>
#include <type_traits>

typedef unsigned short u16;
typedef __attribute__((ext_vector_type(2))) __bf16 bf16x2;
typedef __attribute__((ext_vector_type(4))) __bf16 bf16x4;
typedef __attribute__((ext_vector_type(8))) __bf16 bf16x8;
typedef __attribute__((ext_vector_type(16))) __bf16 bf16x16;
typedef __attribute__((ext_vector_type(2))) float f32x2;
typedef __attribute__((ext_vector_type(4))) float f32x4;
typedef __attribute__((ext_vector_type(2))) unsigned u32x2;
typedef __attribute__((ext_vector_type(4))) unsigned u32x4;
typedef __attribute__((ext_vector_type(8))) unsigned u32x8;
typedef __attribute__((ext_vector_type(4))) short s16x4;
typedef __attribute__((address_space(3))) s16x4 lds_s16x4;
typedef __attribute__((address_space(3))) u32x4 lds_u32x4;
typedef __attribute__((address_space(3))) f32x4 lds_f32x4;
typedef __attribute__((address_space(3))) unsigned lds_u32;
typedef __attribute__((address_space(3))) u32x2 lds_u32x2;
typedef __attribute__((address_space(3))) char lds_char;

#define S_LEN 2048
#define DM 576
#define NSEG 64
#define HD 4096

#define SLOT_REAL  131072
#define NEAR_PM    (8192 + 1024)
#define NEAR_AL    (8192 + 5120)
#define NEAR_E     16384
#define OFF_FLAGS  0x100000
#define FL_FLIP    (OFF_FLAGS + 0)
#define FL_WP      (OFF_FLAGS + 16384)
#define FL_AN      (OFF_FLAGS + 18432)
#define FL_MRDY    (OFF_FLAGS + 2048)
#define FL_PROG    (OFF_FLAGS + 4096)
#define FL_UPROG   (OFF_FLAGS + 8192)
#define FL_KMPROG  (OFF_FLAGS + 12288)
#define FL_YPROG   (OFF_FLAGS + 20480)
#define FL_PXPROG  (OFF_FLAGS + 24576)
#define FL_APROG   (OFF_FLAGS + 40960)
#define FL_PXLAST  (OFF_FLAGS + 32768)
#define FL_ACK     (OFF_FLAGS + 28672)
#define FLAG_BYTES 0xC000
#define FL_GO      (OFF_FLAGS + FLAG_BYTES)
#define NACK       945
#define REAL_CAL   0x140000
#define OFF_YR     0x200000
#define YSLOT_REAL 0x100000
#define CPY1       16384
#define CPY2       0x80000
#define OFF_KF   (4u << 20)
#define OFF_QF   (OFF_KF + (16u << 20))
#define OFF_KMF  (OFF_QF + (16u << 20))
#define OFF_PXF  (OFF_KMF + (8u << 20))
#define OFF_ZF   (OFF_PXF + (6u << 20))
#define ZS       (36u * 40960u)
#define OFF_OA   (OFF_ZF + (91u << 20))
#define OFF_GATE (OFF_OA + (3u << 20))
#define OFF_END  (OFF_GATE + (1u << 20))

#define RMASK 63
#define SPINCAP (1u << 19)
#define SC1 16

#define BAR() do { asm volatile("s_waitcnt lgkmcnt(0)" ::: "memory"); __builtin_amdgcn_s_barrier(); asm volatile("" ::: "memory"); } while (0)
#define VMW(n) asm volatile("s_waitcnt vmcnt(" #n ")" ::: "memory")
#define CBAR() asm volatile("" ::: "memory")
#define PADA
#define PADN "s_nop 0\n\t"
#define PADM
#define PADZ
#define PAD()
#define PADS(id)
#define PADE()
#define WALK(id)

#define NOSTEP 0
#define OFF_STAMP (200u << 20)
#define TS(v)
#define RT(i)
#define SV(i, v)
#define ACC(k, d)
#define STEPRT(j, f)
#define EVASM
#define EV(v)
#define EVK(v)
#define EVPUT(j, v)
#define EVPUTI(j, v)
#define EVDECL
#define EVDUMP()
#define CKDECL
#define CKS()
#define CKDUMP()
typedef unsigned long long u64;
struct Args {
  const u16* x; const u16* mem0; const u16* Wq; const u16* Wk; const u16* Wv; const u16* Wo;
  const u16* wqkv; const u16* wo; const u16* gw; u16* out; char* wb;
};

__device__ __forceinline__ float bf2f(u16 v) { return __uint_as_float(((unsigned)v) << 16); }
__device__ __forceinline__ unsigned pk2(float a, float b) {
  f32x2 v = {a, b};
  bf16x2 r = __builtin_convertvector(v, bf16x2);
  return __builtin_bit_cast(unsigned, r);
}
__device__ __forceinline__ int nearoff(int o) { return ((o >> 13) << 15) + (o & 8191); }
__device__ __forceinline__ unsigned own_cpy(int cls, int x) { return ((cls >> (2 * x)) & 2) ? CPY1 : 0u; }
__device__ __forceinline__ unsigned ywin(int cls, int m) {
  const int x = m / 9, row = (x >> 1) * 9 + m - 9 * x;
  const int se = (cls >> (4 * (x >> 1) + 1)) & 1, so = (cls >> (4 * (x >> 1) + 3)) & 1, sd = se == so ? x & 1 : ((x & 1) ? so : se);
  return (unsigned)(OFF_YR + row * 32768 + ((cls >> 8) & 1) * 8192 + ((sd ^ (cls >> 1) ^ (cls >> 9) ^ (row >> 4)) & 1) * 16384);
}
__device__ __forceinline__ u32x2 trd(lds_char* p) {
  s16x4 a = __builtin_amdgcn_ds_read_tr16_b64_v4i16((lds_s16x4*)p);
  return __builtin_bit_cast(u32x2, a);
}
__device__ __forceinline__ f32x4 mfma(u32x4 a, u32x4 b, f32x4 c) {
  return __builtin_amdgcn_mfma_f32_16x16x32_bf16(__builtin_bit_cast(bf16x8, a), __builtin_bit_cast(bf16x8, b), c, 0, 0, 0);
}
__device__ __forceinline__ f32x4 mfv0(u32x4 a, u32x4 b) {
  f32x4 d; asm volatile("s_nop 1\n\tv_mfma_f32_16x16x32_bf16 %0, %1, %2, 0" : "=&v"(d) : "v"(a), "v"(b)); return d;
}
__device__ __forceinline__ f32x4 mfvc(u32x4 a, u32x4 b, f32x4 c) {
  f32x4 d; asm volatile("s_nop 1\n\tv_mfma_f32_16x16x32_bf16 %0, %1, %2, %3" : "=&v"(d) : "v"(a), "v"(b), "v"(c)); return d;
}
__device__ __forceinline__ void mfva(f32x4& d, u32x4 a, u32x4 b) {
  asm volatile("s_nop 1\n\tv_mfma_f32_16x16x32_bf16 %0, %1, %2, %0" : "+v"(d) : "v"(a), "v"(b));
}
__device__ __forceinline__ void mf_wait(f32x4& d) { asm volatile("s_nop 7" : "+v"(d)); }
__device__ __forceinline__ void mf_wait2(f32x4& d, f32x4& e) { asm volatile("s_nop 7" : "+v"(d), "+v"(e)); }
__device__ __forceinline__ f32x4 smf(u32x4 a, u32x8 b, f32x4 c, int idx) {
  return __builtin_amdgcn_smfmac_f32_16x16x64_bf16(__builtin_bit_cast(bf16x8, a), __builtin_bit_cast(bf16x16, b), c, idx, 0, 0);
}
__device__ __forceinline__ float xmax(float x) {
  auto s = __builtin_amdgcn_permlane16_swap(__float_as_uint(x), __float_as_uint(x), false, false);
  x = fmaxf(__uint_as_float(s[0]), __uint_as_float(s[1]));
  auto t = __builtin_amdgcn_permlane32_swap(__float_as_uint(x), __float_as_uint(x), false, false);
  return fmaxf(__uint_as_float(t[0]), __uint_as_float(t[1]));
}
__device__ __forceinline__ float xsum(float x) {
  auto s = __builtin_amdgcn_permlane16_swap(__float_as_uint(x), __float_as_uint(x), false, false);
  x = __uint_as_float(s[0]) + __uint_as_float(s[1]);
  auto t = __builtin_amdgcn_permlane32_swap(__float_as_uint(x), __float_as_uint(x), false, false);
  return __uint_as_float(t[0]) + __uint_as_float(t[1]);
}
__device__ __forceinline__ f32x4 halfadd(f32x4 a, f32x4 b) {
  f32x4 o;
#pragma unroll
  for (int r = 0; r < 4; ++r) {
    auto s = __builtin_amdgcn_permlane32_swap(__float_as_uint(a[r]), __float_as_uint(b[r]), false, false);
    o[r] = __uint_as_float(s[0]) + __uint_as_float(s[1]);
  }
  return o;
}
template <class T> __device__ __forceinline__ void pin_a(T& v) { asm volatile("" : "+a"(v)); }
typedef __amdgpu_buffer_rsrc_t rsrc_t;
__device__ __forceinline__ u32x4 ld128(rsrc_t R, unsigned vo, unsigned so) { return __builtin_amdgcn_raw_buffer_load_b128(R, vo, so, SC1); }
__device__ __forceinline__ unsigned ld32(rsrc_t R, unsigned vo, unsigned so) { return __builtin_amdgcn_raw_buffer_load_b32(R, vo, so, SC1); }
__device__ __forceinline__ void st128(u32x4 v, rsrc_t R, unsigned vo, unsigned so) { __builtin_amdgcn_raw_buffer_store_b128(v, R, vo, so, SC1); }
__device__ __forceinline__ void st32(unsigned v, rsrc_t R, unsigned vo, unsigned so) { __builtin_amdgcn_raw_buffer_store_b32(v, R, vo, so, SC1); }
#define OOB 0x7ff00000
__device__ __forceinline__ unsigned lda(rsrc_t R, unsigned vo) { return ld32(R, vo, 0); }

struct Go { unsigned vo, v0; };
__device__ __forceinline__ Go go_look(rsrc_t R, int b) {
  Go g; g.vo = FL_GO + (b & 15) * 128; g.v0 = ld32(R, g.vo, 0); return g;
}
__device__ __forceinline__ bool go_wait(rsrc_t R, const Go& g, int lane) {
  for (unsigned spins = 0; spins < SPINCAP; ++spins) {
    CBAR();
    if (__all(ld32(R, g.vo, 0) != g.v0)) {
      __builtin_amdgcn_raw_ptr_buffer_atomic_add_i32(1, R, lane == 0 ? g.vo - FL_GO + FL_ACK : OOB, 0, SC1);
      return true;
    }
    __builtin_amdgcn_s_sleep(6);
  }
  return false;
}
__device__ __forceinline__ void go_lead(rsrc_t R, int tid, int wave, int lane) {
#pragma unroll
  for (int k = 0; k < FLAG_BYTES / 4096; ++k) st128((u32x4){~0u, ~0u, ~0u, ~0u}, R, OFF_FLAGS + k * 4096 + tid * 16, 0);
  VMW(0);
  BAR();
  if (wave != 0) return;
  for (unsigned k = 0; k < SPINCAP; ++k) {
    st32(k, R, lane < 16 ? FL_GO + lane * 128 : OOB, 0);
    int n = lane < 16 ? (int)ld32(R, FL_ACK + lane * 128, 0) + 1 : 0;
#pragma unroll
    for (int d = 1; d < 16; d <<= 1) n += __shfl_xor(n, d);
    if (__builtin_amdgcn_readfirstlane(n) >= NACK) break;
    __builtin_amdgcn_s_sleep(4);
  }
}
__device__ __forceinline__ bool wait_flag(rsrc_t R, unsigned vo, int need) {
  for (unsigned spins = 0; spins < SPINCAP; ++spins) {
    CBAR();
    int v = (int)ld32(R, vo, 0);
    if (__all(v >= need)) return true;
    __builtin_amdgcn_s_sleep(8);
  }
  return false;
}

template <int SLP, int AT = 0>
__device__ __forceinline__ bool wait_flag_c(rsrc_t R, unsigned vo, int need, int& cache) {
  if (__all(cache >= need)) return true;
  for (unsigned spins = 0; spins < SPINCAP; ++spins) {
    CBAR();
    cache = (int)(AT ? lda(R, vo) : ld32(R, vo, 0));
    if (__all(cache >= need)) return true;
    __builtin_amdgcn_s_sleep(SLP);
  }
  return false;
}
__device__ __forceinline__ unsigned umax3(unsigned a, unsigned b, unsigned c) { return max(max(a, b), c); }

template <int NTB> __device__ __forceinline__ u32x4 gld(const u16* p) {
  if (NTB) return __builtin_nontemporal_load((const u32x4*)p);
  return *(const u32x4*)p;
}
#define XSTR2(x) #x
#define XSTR(x) XSTR2(x)
#define LSLP 3
#define ESLP 0
#define MSLP 0
#define PXS 6
#define TSLP 2
#define XSLP 2
#define KSLP 10
#define OSLP 24
#define OSL2 4
#define UE 1
#define YL
#define CPF 3
#define PARK 58
#define OFAST 60
#define CPS 8
#define CSL 2
#define TSL 6
#define UGO 5
#define UPO 1
#define CALN 4
#define QDEP 4
#define PDEP 3
#define WDEP 3
#define VSLP 2
#define QLN 24
#define VLN 8
#define PLN 32
#define LMASK(n) ((n) >= 64 ? ~0ull : ((1ull << ((n) & 63)) - 1))
#define UP3 0
#define ORDT 0x73652410u
#define USLP 8
#define UT 0
#define UTD 1
#define EMF 0
#define EMV 0
#define MWPOS 1
#define DSLP "s_sleep 1\n\t"
typedef __attribute__((ext_vector_type(4))) int i32x4;
__device__ __forceinline__ u64 ballot(bool c) { return __builtin_amdgcn_ballot_w64(c); }
#define UNLIKELY(c) __builtin_expect(!!(c), 0)
#define DELTA 164
#define D1 98
#define DVQ 4
#define LEV0 1
#define LEV3 1
#define LEVMAX 7
#define CL0 8
#define HEAT 3
#define COOL0 4
#define PSLP 8
#define VK0 64
#define AWE 0
#define ASLP 0
#define LEVF 99
#define RX 0
#define VSL 6
#define D0 14
#define WLAST 41
#define UDL 8
#define MGAP 0
#define SMIN 8
#define DE 92
#define K1 4
#define PAD0 4
#define PADD 4
#define PADMAX 16
#define OAUX 0
__device__ __forceinline__ unsigned rt_now() {
  u64 t; asm volatile("s_memrealtime %0\n\ts_waitcnt lgkmcnt(0)" : "=s"(t) :: "memory"); return (unsigned)t;
}
#define S1 26
#define DEA 96
#define KA_ 70
#define S2 30
#define DEC 90
#define KC_ 66
#define S0 3
#define PACE0 ((DE0 << 8) | DELTA0)
#define PACEA ((DEA << 8) | (DEA + KA_))
#define PACEB ((DE << 8) | (DE + K2))
#define PACEC ((DEC << 8) | (DEC + KC_))
__device__ __forceinline__ void next_time(unsigned& t, int s) {
  t += (s < S0 ? PACE0 : (s < S1 ? PACEA : (s < S2 ? PACEB : PACEC))) & 255u;
  t = __builtin_amdgcn_readfirstlane(t);
  asm volatile("" : "+s"(t));
}
#define SNOP 1
#define SPIN(T, a, b, c) a ":\n\ts_memrealtime vcc\n\ts_waitcnt lgkmcnt(0)\n\ts_sub_u32 %[r], %[" #T "], vcc_lo\n\ts_sub_u32 %[k], %[r], 4\n\t"   \
               "s_cmp_lt_u32 %[k], 2000\n\ts_cbranch_scc1 " a "b\n\t"                                                                       \
               "s_sub_u32 %[k], %[r], 1\n\ts_cmp_lt_u32 %[k], 3\n\ts_cbranch_scc0 " c "f\n\t"                                               \
               b ":\n\ts_nop " XSTR(SNOP) "\n\ts_sub_u32 %[k], %[k], 1\n\ts_cbranch_scc0 " b "b\n\t" c ":\n\t"
__device__ __forceinline__ int spin_until(unsigned T) {
  int r, k;
  asm volatile(SPIN(T, "1", "2", "3") : [r] "=&s"(r), [k] "=&s"(k) : [T] "s"(T) : "memory", "vcc", "scc");
  return r;
}
#define KA "v_mov_b32 v240, v240\n\ts_nop 2\n\t"
#define KAN KA KA
#define EVA(n)
#define EVC(k)
#define BUD(l) "s_sub_u32 %[bud], %[bud], 1\n\ts_cbranch_scc0 " l "\n\ts_mov_b32 %[bud], 0\n\t"
__device__ __forceinline__ i32x4 mkdesc(char* wb) {
  const u64 p = (u64)wb;
  return (i32x4){(int)(unsigned)p, (int)(unsigned)(p >> 32), (int)OFF_END, 0x00020000};
}
template <class T> __device__ __forceinline__ void pin_v(T& v) { asm volatile("" : "+v"(v)); }
#define SB() __builtin_amdgcn_sched_barrier(0)
__device__ __forceinline__ unsigned pkv(float a, float b) {
  unsigned d; asm volatile("v_cvt_pk_bf16_f32 %0, %1, %2" : "=v"(d) : "v"(a), "v"(b)); return d;
}
__device__ __forceinline__ f32x4 mfB0(u32x4 a, u32x4 b) {
  f32x4 d; asm volatile("v_mfma_f32_16x16x32_bf16 %0, %1, %2, 0" : "=&v"(d) : "v"(a), "a"(b)); return d;
}
__device__ __forceinline__ void mfB(f32x4& d, u32x4 a, u32x4 b) {
  asm volatile("v_mfma_f32_16x16x32_bf16 %0, %1, %2, %0" : "+v"(d) : "v"(a), "a"(b));
}

template <int NTB>
__device__ __forceinline__ void qk_gload(u32x4 (&st)[8], const Args& a, int h, int c, int tid) {
#pragma unroll
  for (int k = 0; k < 8; ++k) {
    int u = tid + 256 * k; int row = u >> 5, cu = u & 31;
    const u16* W = (cu < 16) ? a.Wq : a.Wk;
    st[k] = gld<NTB>(W + (size_t)(64 * c + row) * HD + h * 128 + (cu & 15) * 8);
  }
}
__device__ __forceinline__ void qk_swrite(const u32x4 (&st)[8], lds_char* buf, int tid) {
#pragma unroll
  for (int k = 0; k < 8; ++k) { int u = tid + 256 * k; int row = u >> 5, cu = u & 31; *(lds_u32x4*)(buf + row * 544 + cu * 16) = st[k]; }
}
#define STG_BYTES 38912
#define X0_OFF (2 * STG_BYTES)
#define X1_OFF (X0_OFF + 37888)

__device__ __forceinline__ int cls_look(rsrc_t R, int lane) { return (int)ld32(R, FL_FLIP + (lane & 31) * 4, 0); }
__device__ __forceinline__ int rowsum(int v) {
  v += __builtin_amdgcn_update_dpp(v, v, 0x128, 0xf, 0xf, false);
  v += __builtin_amdgcn_update_dpp(v, v, 0x124, 0xf, 0xf, false);
  v += __builtin_amdgcn_update_dpp(v, v, 0x122, 0xf, 0xf, false);
  return v + __builtin_amdgcn_update_dpp(v, v, 0x121, 0xf, 0xf, false);
}
__device__ __forceinline__ int get_cls(rsrc_t R, int lane, int v = -1) {
  for (unsigned spins = 0; spins < SPINCAP; ++spins) {
    if (__all(v >= 0)) {
      const int n = rowsum((lane & 1) ? -v : v) > 0;
      const int d = rowsum((lane & 1) != n ? 0 : (((lane >> 1) ^ (lane >> 2)) & 1 ? -v : v)) > 0;
      const int c = n | (d << 1);
      return (__builtin_amdgcn_readlane(c, 0) * 0x55 ^ 0x88) | (__builtin_amdgcn_readlane(c, 16) << 8);
    }
    if (spins) __builtin_amdgcn_s_sleep(CPS);
    CBAR();
    v = cls_look(R, lane);
  }
  return -1;
}
__device__ __forceinline__ void calibrate(rsrc_t R, const Go& go, int r, int k, int lane) {
  unsigned best = 0xFFFFFFu;
  for (int it = 0; it < CALN; ++it) {
    unsigned long long t0, t1;
    asm volatile("s_waitcnt vmcnt(0)\n\ts_memtime %0\n\ts_waitcnt lgkmcnt(0)" : "=s"(t0) :: "memory");
    st32((unsigned)it, R, (lane == 0) ? (unsigned)((r >> 2) * OFF_YR + REAL_CAL + (r & 3) * 256 + k * 8192 + (it & 1) * 4096) : OOB, 0);
    asm volatile("s_waitcnt vmcnt(0)\n\ts_memtime %0\n\ts_waitcnt lgkmcnt(0)" : "=s"(t1) :: "memory");
    best = min(best, (unsigned)(t1 - t0));
  }
  if (go_wait(R, go, lane)) st32(best, R, lane == 0 ? (unsigned)(FL_FLIP + (r * 4 + k) * 4) : OOB, 0);
}
__device__ __forceinline__ unsigned get_t1(rsrc_t R, unsigned vo, unsigned& bud) {
  unsigned v = 0;
#pragma nounroll
  for (; bud != 0; --bud) {
    CBAR();
    v = __builtin_amdgcn_readfirstlane(ld32(R, vo, 0));
    if (v != ~0u) break;
    __builtin_amdgcn_s_sleep(2);
  }
  return v;
}

#define F8_OFF 18432
#define QSTG 34816
#define ISOT()
#define ISOQ(n)
__device__ __forceinline__ float vmax(float a, float b) { float d; asm("v_max_f32 %0, %1, %2" : "=v"(d) : "v"(a), "v"(b)); return d; }
__device__ __forceinline__ float vmax3(float a, float b, float c) { float d; asm("v_max3_f32 %0, %1, %2, %3" : "=v"(d) : "v"(a), "v"(b), "v"(c)); return d; }
__device__ __forceinline__ float max32(float x) {
  auto t = __builtin_amdgcn_permlane32_swap(__float_as_uint(x), __float_as_uint(x), false, false);
  return vmax(__uint_as_float(t[0]), __uint_as_float(t[1]));
}
__device__ __forceinline__ float max16(float x) {
  auto t = __builtin_amdgcn_permlane16_swap(__float_as_uint(x), __float_as_uint(x), false, false);
  return vmax(__uint_as_float(t[0]), __uint_as_float(t[1]));
}
__device__ __forceinline__ float sum32(float x) {
  auto t = __builtin_amdgcn_permlane32_swap(__float_as_uint(x), __float_as_uint(x), false, false);
  return __uint_as_float(t[0]) + __uint_as_float(t[1]);
}
template <int r> __device__ __forceinline__ void swp32(f32x4& a, f32x4& b) {
  auto t = __builtin_amdgcn_permlane32_swap(__float_as_uint(a[r]), __float_as_uint(b[r]), false, false);
  a[r] = __uint_as_float(t[0]); b[r] = __uint_as_float(t[1]);
}
__device__ __forceinline__ float vadd(float a, float b) { float d; asm("v_add_f32 %0, %1, %2" : "=v"(d) : "v"(a), "v"(b)); return d; }
__device__ __forceinline__ float max8(float x) {
  float d; asm("s_nop 1\n\tv_max_f32_dpp %0, %1, %1 row_ror:8 row_mask:0xf bank_mask:0xf" : "=&v"(d) : "v"(x)); return d;
}
__device__ __forceinline__ float sum8(float x) {
  float d; asm("s_nop 1\n\tv_add_f32_dpp %0, %1, %1 row_ror:8 row_mask:0xf bank_mask:0xf" : "=&v"(d) : "v"(x)); return d;
}
__device__ __forceinline__ float sum16(float x) {
  auto t = __builtin_amdgcn_permlane16_swap(__float_as_uint(x), __float_as_uint(x), false, false);
  return __uint_as_float(t[0]) + __uint_as_float(t[1]);
}
__device__ __forceinline__ void st64(u32x2 v, rsrc_t R, unsigned vo, unsigned so) { __builtin_amdgcn_raw_buffer_store_b64(v, R, vo, so, SC1); }
template <int N>
__device__ __forceinline__ void mem_issue2(i32x4 D, unsigned m0, unsigned m1, unsigned m2, unsigned so, unsigned l0, unsigned l1, unsigned l2, u64& ew) {
  asm volatile("s_mov_b32 m0, %[l0]\n\ts_barrier\n\t" EVASM
               "buffer_load_dwordx4 %[m0], %[D], %[so] offen sc1 lds\n\t"
               "s_mov_b32 m0, %[l1]\n\ts_nop 0\n\t"
               "buffer_load_dwordx4 %[m1], %[D], %[so] offen sc1 lds\n\t"
               ".if %[n] == 3\n\ts_mov_b32 m0, %[l2]\n\ts_nop 0\n\t"
               "buffer_load_dwordx4 %[m2], %[D], %[so] offen sc1 lds\n\t.endif"
               : [ew] "=&s"(ew)
               : [m0] "v"(m0), [m1] "v"(m1), [m2] "v"(m2), [D] "s"(D), [so] "s"(so), [l0] "s"(l0), [l1] "s"(l1), [l2] "s"(l2), [n] "n"(N)
               : "memory");
}
template <int ID>
__device__ __forceinline__ void try_top(unsigned& nt, unsigned& ms, unsigned& tm, u64& keep) {
  asm volatile(".Ltry%5:\n\ts_waitcnt lgkmcnt(0)\n\ts_nop 4\n\ts_xor_b32 %1, %1, %4\n\ts_add_u32 %2, %2, " XSTR(GRID) "+" XSTR(XT) "\n\t"
               "s_add_u32 %0, %0, 1\n\ts_cmp_lt_u32 %0, 3\n\ts_cselect_b64 %3, -1, 0"
               : "+s"(nt), "+s"(ms), "+s"(tm), "=&s"(keep) : "n"(CPY2 + CPY1), "n"(ID) : "memory", "scc");
}
template <int ID>
__device__ __forceinline__ void try_end(u64 bad, u64 keep, unsigned ia, unsigned nt, unsigned ms, unsigned tm, unsigned& rcnt,
                                        unsigned m0, unsigned m1, unsigned m2, unsigned l0, unsigned l1, unsigned l2, i32x4 D) {
  asm volatile("s_and_b64 %[keep], %[keep], %[bad]\n\ts_cbranch_scc1 .Ltry%[id]\n\ts_cmp_lg_u64 %[bad], 0\n\ts_cbranch_scc1 1f\n"
               "2:\n\t.subsection 1\n"
               "1:\n\tv_mov_b32 v240, 0\n\t"
               ".irp o, 0, 1024, 2048, 3072, 4096, 5120, 6144, 7168, " XSTR(F8_OFF) "\n\tds_read_b128 v[236:239], %[ia] offset:\\o\n\ts_waitcnt lgkmcnt(0)\n\t"
               "v_max3_u32 v240, v240, v236, v237\n\tv_max3_u32 v240, v240, v238, v239\n\t.endr\n\t"
               "v_cmp_eq_u32 vcc, -1, v240\n\ts_cbranch_vccz 2b\n\ts_add_u32 %[rc], %[rc], 1\n\ts_cmp_lt_u32 %[rc], %[cap]\n\t"
               "s_cbranch_scc1 .Ltry%[id]\n\ts_branch 2b\n\t.subsection 0"
               : [keep] "+s"(keep), [rc] "+s"(rcnt)
               : [bad] "s"(bad), "s"(nt), "s"(ms), "s"(tm), [id] "n"(ID), [ia] "v"(ia), [cap] "n"(SPINCAP / 2), "v"(m0), "v"(m1), "v"(m2),
                 "s"(l0), "s"(l1), "s"(l2), "s"(D)
               : "memory", "scc", "vcc", "v236", "v237", "v238", "v239", "v240");
}
__device__ __forceinline__ int spin_late(unsigned& t, unsigned d) {
  int r, k; unsigned T;
  asm volatile("s_add_u32 %[T], %[t], %[d]\n\t" SPIN(T, "1", "2", "3") : [r] "=&s"(r), [k] "=&s"(k), [T] "=&s"(T), [t] "+s"(t) : [d] "n"(d) : "memory", "vcc", "scc");
  return r;
}
__device__ __forceinline__ void move_on(unsigned& t, int r) {
  asm volatile("1:\n\ts_cmp_lt_i32 %1, -" XSTR(LTOL) "\n\ts_cbranch_scc0 2f\n\ts_add_u32 %0, %0, " XSTR(GRID) "+" XSTR(XT) "\n\ts_add_u32 %1, %1, " XSTR(GRID) "+" XSTR(XT) "\n\ts_branch 1b\n2:"
               : "+s"(t), "+s"(r) :: "scc");
}
__device__ __forceinline__ void gate_wait(rsrc_t R, unsigned vo, int need, int& cache, unsigned& bud) {
#pragma nounroll
  for (; bud != 0; --bud) {
    if (__all(cache >= need)) break;
    CBAR();
    cache = (int)ld32(R, vo, 0);
    __builtin_amdgcn_s_sleep(4);
  }
}
__device__ __forceinline__ unsigned um3v(unsigned a, unsigned b, unsigned c) {
  unsigned d; asm volatile("v_max3_u32 %0, %1, %2, %3" : "=v"(d) : "v"(a), "v"(b), "v"(c)); return d;
}
template <int OFF>
__device__ __forceinline__ void k_put(f32x4 k, unsigned ad) {
  unsigned t0, t1;
  asm volatile("v_cvt_pk_bf16_f32 %0, %2, %3\n\tv_cvt_pk_bf16_f32 %1, %4, %5\n\tds_write2_b32 %6, %0, %1 offset0:%7 offset1:%8"
               : "=&v"(t0), "=&v"(t1) : "v"(k[0]), "v"(k[1]), "v"(k[2]), "v"(k[3]), "v"(ad), "n"(OFF), "n"(OFF + 2) : "memory");
}
template <int OFF>
__device__ __forceinline__ void q_put(f32x4 k, unsigned ad) {
  unsigned t0, t1;
  asm volatile("v_cvt_pk_bf16_f32 %0, %2, %3\n\tv_cvt_pk_bf16_f32 %1, %4, %5\n\tds_write2_b32 %6, %0, %1 offset0:%7 offset1:%8"
               : "=&v"(t0), "=&v"(t1) : "v"(k[0]), "v"(k[1]), "v"(k[2]), "v"(k[3]), "v"(ad), "n"(OFF), "n"(OFF + 1) : "memory");
}
__device__ __forceinline__ f32x4 mf0(u32x4 a, u32x4 b) {
  f32x4 d; asm volatile("v_mfma_f32_16x16x32_bf16 %0, %1, %2, 0" : "=&v"(d) : "v"(a), "v"(b)); return d;
}
__device__ __forceinline__ void mfa(f32x4& d, u32x4 a, u32x4 b) {
  asm volatile("v_mfma_f32_16x16x32_bf16 %0, %1, %2, %0" : "+v"(d) : "v"(a), "v"(b));
}
__device__ __forceinline__ f32x4 mfA0(u32x4 a, u32x4 b) {
  f32x4 d; asm volatile("v_mfma_f32_16x16x32_bf16 %0, %1, %2, 0" : "=&v"(d) : "a"(a), "v"(b)); return d;
}
__device__ __forceinline__ void mfA(f32x4& d, u32x4 a, u32x4 b) {
  asm volatile("v_mfma_f32_16x16x32_bf16 %0, %1, %2, %0" : "+v"(d) : "a"(a), "v"(b));
}
#define DPPLO " row_ror:8 row_mask:0xf bank_mask:0x3\n\t"
#define DPPHI " row_ror:8 row_mask:0xf bank_mask:0xc\n\t"
#define DPPAL " row_ror:8 row_mask:0xf bank_mask:0xf\n\t"
#define SCK "0x3e0293ee"
#define SCN "0xbe0293ee"
__device__ __forceinline__ void px_mask(f32x4& T0, f32x4& T1, int qt, int lane) {
  const float ninf = -__builtin_inff();
#pragma unroll
  for (int r = 0; r < 4; ++r) {
    const bool diag = (4 * (lane >> 4) + r) <= (lane & 15);
    T0[r] = (qt == 1 || diag) ? T0[r] : ninf;
    T1[r] = (qt == 1 && diag) ? T1[r] : ninf;
  }
}
__device__ __forceinline__ void px_seg(f32x4& T0, f32x4& T1, float& mx, float& sx) {
  const float sc = 0.08838834764831845f * 1.4426950408889634f;
  mx = xmax(fmaxf(fmaxf(fmaxf(T0[0], T0[1]), fmaxf(T0[2], T0[3])), fmaxf(fmaxf(T1[0], T1[1]), fmaxf(T1[2], T1[3]))));
  const float ms = -mx * sc;
  sx = 0.f;
#pragma unroll
  for (int r = 0; r < 4; ++r) {
    T0[r] = __builtin_amdgcn_exp2f(fmaf(T0[r], sc, ms)); T1[r] = __builtin_amdgcn_exp2f(fmaf(T1[r], sc, ms));
    sx += T0[r] + T1[r];
  }
}
__device__ __forceinline__ void px_fin(f32x4 G, f32x4 T0, f32x4 T1, float mx, float sx, u32x4& px, unsigned& pw) {
  const float sc = 0.08838834764831845f * 1.4426950408889634f;
  float g0 = G[0] + G[2], g1 = G[1] + G[3];
  const float ms = -xmax(fmaxf(fmaxf(g0, g1), mx)) * sc;
  g0 = __builtin_amdgcn_exp2f(fmaf(g0, sc, ms)); g1 = __builtin_amdgcn_exp2f(fmaf(g1, sc, ms));
  const float f = __builtin_amdgcn_exp2f(fmaf(mx, sc, ms));
  asm volatile("" : "+v"(g0), "+v"(g1)); PAD();
  const float inv = __builtin_amdgcn_rcpf(xsum(fmaf(f, sx, g0 + g1))), fi = f * inv;
  px = (u32x4){pk2(T0[0] * fi, T0[1] * fi), pk2(T0[2] * fi, T0[3] * fi), pk2(T1[0] * fi, T1[1] * fi), pk2(T1[2] * fi, T1[3] * fi)};
  pw = pk2(g0 * inv, g1 * inv);
}
#define WPOS 4
#define QPOS 1
#define LPOS 1
#define QIM_OFF 8192
#define KIM_OFF 12288
#define FLG_OFF 17408
#define F8W 3
#define KX_OFF 36864
__device__ __forceinline__ void ds_flag(unsigned ad, unsigned v) { asm volatile("ds_write_b32 %0, %1" :: "v"(ad), "v"(v) : "memory"); }
#define SMX(d, j, w) "v_smfmac_f32_16x16x64_bf16 v[" d "], %[a" #j "], %[" #w "], %[ix]\n\t"
#define CVX(d, x, y) "v_cvt_pk_bf16_f32 v" #d ", v" #x ", v" #y "\n\t"
template <int T, int AG>
__device__ __forceinline__ void smo(f32x4& c, u32x4 a, u32x8 b, int ix) {
  if (T == 0 && AG) asm volatile(SMX("140:143", 0, w) : "+{v[140:143]}"(c) : [a0] "v"(a), [w] "a"(b), [ix] "v"(ix));
  if (T == 0 && !AG) asm volatile(SMX("140:143", 0, w) : "+{v[140:143]}"(c) : [a0] "v"(a), [w] "v"(b), [ix] "v"(ix));
  if (T == 1 && AG) asm volatile(SMX("144:147", 0, w) : "+{v[144:147]}"(c) : [a0] "v"(a), [w] "a"(b), [ix] "v"(ix));
  if (T == 1 && !AG) asm volatile(SMX("144:147", 0, w) : "+{v[144:147]}"(c) : [a0] "v"(a), [w] "v"(b), [ix] "v"(ix));
}
__device__ __forceinline__ void acc_zero(f32x4& q0, f32x4& q1, f32x4& k0) {
  asm volatile("v_mov_b64 v[140:141], 0\n\tv_mov_b64 v[142:143], 0\n\tv_mov_b64 v[144:145], 0\n\tv_mov_b64 v[146:147], 0\n\t"
               "v_mov_b64 v[148:149], 0\n\tv_mov_b64 v[150:151], 0\n\ts_nop 1"
               : "={v[140:143]}"(q0), "={v[144:147]}"(q1), "={v[148:151]}"(k0));
}
__device__ __forceinline__ void acc_zero2(f32x4& k1, f32x4& k2) {
  asm volatile("v_mov_b64 v[152:153], 0\n\tv_mov_b64 v[154:155], 0\n\tv_mov_b64 v[156:157], 0\n\tv_mov_b64 v[158:159], 0"
               : "={v[152:155]}"(k1), "={v[156:159]}"(k2));
}
__device__ __forceinline__ u64 q_mid(const u32x4 (&af)[9], const u32x8 (&wq)[9], int ix, f32x4 q0, f32x4& q1, u32x2& t) {
  u64 bad;
  asm volatile(SMX("144:147", 8, w8) SMX("144:147", 1, w1) "v_fma_f32 v234, v140, v141, v142\n\t" SMX("144:147", 2, w2)
               "v_cmp_u_f32 %[bad], v234, v143\n\t" SMX("144:147", 3, w3) CVX(231, 142, 143) SMX("144:147", 5, w5) CVX(230, 140, 141)
               : "+{v[144:147]}"(q1), "={v[230:231]}"(t), [bad] "=s"(bad)
               : "{v[140:143]}"(q0), [a8] "v"(af[8]), [a1] "v"(af[1]), [a2] "v"(af[2]), [a3] "v"(af[3]), [a5] "v"(af[5]),
                 [w8] "v"(wq[8]), [w1] "a"(wq[1]), [w2] "a"(wq[2]), [w3] "a"(wq[3]), [w5] "a"(wq[5]), [ix] "v"(ix)
               : "v234");
  return bad;
}
__device__ __forceinline__ void q_end(const u32x4 (&af)[9], const u32x8 (&wq)[9], const u32x8 (&wk)[9], int ix, unsigned qw, f32x4 q1, u32x2 t, f32x4& k0) {
  asm volatile(SMX("144:147", 6, w6) SMX("144:147", 7, w7)
               SMX("148:151", 8, u8) SMX("148:151", 0, u0) CVX(232, 144, 145) CVX(233, 146, 147)
               SMX("148:151", 4, u4) "ds_write2_b64 %[qw], v[230:231], v[232:233] offset0:0 offset1:64\n\t"
               SMX("148:151", 1, u1)
               : "+{v[144:147]}"(q1), "+{v[148:151]}"(k0)
               : "{v[230:231]}"(t), [a6] "v"(af[6]), [a7] "v"(af[7]), [a8] "v"(af[8]), [a0] "v"(af[0]), [a4] "v"(af[4]), [a1] "v"(af[1]),
                 [w6] "a"(wq[6]), [w7] "a"(wq[7]), [u8] "v"(wk[8]), [u0] "a"(wk[0]), [u4] "a"(wk[4]), [u1] "a"(wk[1]),
                 [ix] "v"(ix), [qw] "v"(qw)
               : "memory", "v232", "v233");
}
#define WSX(n) ".if %[wp] == " #n "\n\tbuffer_store_dword %[wv], %[wa], %[D], 0 offen sc1\n\t.endif\n\t"
#define KPX(a) "ds_write2_b32 %[" #a "], v230, v231 offset0:0 offset1:2\n\t"
template <int WP>
__device__ __forceinline__ void k_mid(const u32x4 (&af)[9], u32x4 ac, const u32x8 (&wk)[9], const u32x8 (&wp)[3], int ix, f32x4& k0, f32x4& k2, u32x2& t,
                                      i32x4 D, unsigned wa, unsigned wv) {
  asm volatile(SMX("148:151", 5, u5) PADA WSX(0) SMX("148:151", 6, u6) WSX(1) SMX("148:151", 7, u7) WSX(2)
               SMX("156:159", 0, p0) WSX(3) SMX("156:159", 4, p1) CVX(230, 148, 149) SMX("156:159", 9, p2) CVX(231, 150, 151)
               : "+{v[148:151]}"(k0), "+{v[156:159]}"(k2), "={v[230:231]}"(t)
               : [a5] "v"(af[5]), [a6] "v"(af[6]), [a7] "v"(af[7]), [a0] "v"(af[0]), [a4] "v"(af[4]), [a9] "v"(ac),
                 [u5] "a"(wk[5]), [u6] "a"(wk[6]), [u7] "a"(wk[7]), [p0] "v"(wp[0]), [p1] "v"(wp[1]), [p2] "v"(wp[2]),
                 [ix] "v"(ix), [D] "s"(D), [wa] "v"(wa), [wv] "v"(wv), [wp] "n"(WP)
               : "memory");
}
template <int WP>
__device__ __forceinline__ void k_end(const u32x4 (&af)[9], const u32x8 (&wk)[9], int ix, f32x4& k1, f32x4& k2, u32x2& t, unsigned ka, unsigned kp, unsigned kb,
                                      unsigned fw, unsigned fv, i32x4 D, unsigned wa, unsigned wv) {
  asm volatile(SMX("152:155", 8, u8) KPX(ka) SMX("152:155", 0, u0) CVX(230, 156, 157) SMX("152:155", 4, u4) CVX(231, 158, 159)
               SMX("152:155", 1, u1) KPX(kp) SMX("152:155", 2, u2) PADA WSX(4) SMX("152:155", 3, u3) WSX(5) SMX("152:155", 5, u5) WSX(6)
               SMX("152:155", 6, u6) WSX(7) SMX("152:155", 7, u7) "s_nop 7\n\t" CVX(230, 152, 153) CVX(231, 154, 155) KPX(kb)
               "ds_write_b32 %[fw], %[fv]\n\t" WSX(8)
               : "+{v[152:155]}"(k1), "+{v[156:159]}"(k2), "+{v[230:231]}"(t)
               : [a0] "v"(af[0]), [a1] "v"(af[1]), [a2] "v"(af[2]), [a3] "v"(af[3]), [a4] "v"(af[4]), [a5] "v"(af[5]), [a6] "v"(af[6]),
                 [a7] "v"(af[7]), [a8] "v"(af[8]), [u0] "a"(wk[0]), [u1] "a"(wk[1]), [u2] "a"(wk[2]), [u3] "a"(wk[3]), [u4] "a"(wk[4]),
                 [u5] "a"(wk[5]), [u6] "a"(wk[6]), [u7] "a"(wk[7]), [u8] "v"(wk[8]), [ix] "v"(ix), [ka] "v"(ka), [kp] "v"(kp), [kb] "v"(kb),
                 [fw] "v"(fw), [fv] "v"(fv), [D] "s"(D), [wa] "v"(wa), [wv] "v"(wv), [wp] "n"(WP)
               : "memory");
}
__device__ __forceinline__ unsigned add3bf(unsigned a, unsigned b, unsigned c) {
  return pk2(__uint_as_float(a << 16) + __uint_as_float(b << 16) + __uint_as_float(c << 16),
             __uint_as_float(a & 0xffff0000u) + __uint_as_float(b & 0xffff0000u) + __uint_as_float(c & 0xffff0000u));
}
#define IS(n)
#define TRD(r, o) "ds_read_b64_tr_b16 v[" r "], %[qa] offset:" o "\n\t"
#define FRD "ds_read_b32 v224, %[fa]\n\t"
#define q0 "160:163"
#define q1 "164:167"
#define q2 "168:171"
#define q3 "172:175"
#define q4 "176:179"
#define MF0(x, y) "v_mfma_f32_16x16x32_bf16 v[208:211], v[" x "], v[" y "], 0\n\t"
#define MFA(x, y) "v_mfma_f32_16x16x32_bf16 v[208:211], v[" x "], v[" y "], v[208:211]\n\t"
#define LGK(n) "s_waitcnt lgkmcnt(" #n ")\n\t"
#define FBR "s_cbranch_vccnz 20f\n\t"
#define FBR2 FBR
#define CN(l, k)
#define EST "buffer_store_dwordx2 v[222:223], %[pxo], %[D], %[ps] offen sc1\n\tbuffer_store_dwordx2 v[222:223], %[pxo], %[D], %[pt] offen sc1\n\t"
#define MT0(d, x, y) "v_mfma_f32_16x16x32_bf16 v[" d "], %[" #x "], v[" y "], 0\n\t"
#define MTA(d, x, y) "v_mfma_f32_16x16x32_bf16 v[" d "], %[" #x "], v[" y "], v[" d "]\n\t"
#define QR0 TRD("160:161", "0") TRD("162:163", "512")
#define QR1 TRD("164:165", "1024") TRD("166:167", "1536")
#define QR2 TRD("168:169", "2048") TRD("170:171", "2560")
#define QR3 TRD("172:173", "3072") TRD("174:175", "3584")
#define KL(n) ".if " XSTR(LPOS) " == " #n "\n\t" TRD("188:189", "6144") TRD("190:191", "6656") TRD("192:193", "7168") TRD("194:195", "7680") ".endif\n\t"
#define QLATE 1
#define QS0
#define QS1 QR2
#define QS2 QR1
#define QS3 QR0
template <int RD>
__device__ __forceinline__ void k_two(const u32x4 (&af)[9], const u32x8 (&wk)[9], int ix, f32x4& k0, unsigned qa, u32x4& QC) {
  if (RD)
    asm volatile(SMX("148:151", 2, u2) QR3 SMX("148:151", 3, u3) QS0
                 : "+{v[148:151]}"(k0), "={v[172:175]}"(QC)
                 : [a2] "v"(af[2]), [a3] "v"(af[3]), [u2] "a"(wk[2]), [u3] "a"(wk[3]), [ix] "v"(ix), [qa] "v"(qa) : "memory");
  else
    asm volatile(SMX("148:151", 2, u2) SMX("148:151", 3, u3)
                 : "+{v[148:151]}"(k0) : [a2] "v"(af[2]), [a3] "v"(af[3]), [u2] "a"(wk[2]), [u3] "a"(wk[3]), [ix] "v"(ix));
}
template <int RD>
__device__ __forceinline__ void k_three(const u32x4 (&af)[9], const u32x8 (&wk)[9], int ix, f32x4& k0, unsigned qa, u32x8& QA, u32x4& QB) {
  if (RD)
    asm volatile(SMX("148:151", 5, u5) QS1 SMX("148:151", 6, u6) QS2 SMX("148:151", 7, u7) QS3
                 : "+{v[148:151]}"(k0), "={v[160:167]}"(QA), "={v[168:171]}"(QB)
                 : [a5] "v"(af[5]), [a6] "v"(af[6]), [a7] "v"(af[7]), [u5] "a"(wk[5]), [u6] "a"(wk[6]), [u7] "a"(wk[7]), [ix] "v"(ix), [qa] "v"(qa)
                 : "memory");
  else
    asm volatile(SMX("148:151", 5, u5) SMX("148:151", 6, u6) SMX("148:151", 7, u7) "s_nop 7\n\t" CVX(230, 148, 149) CVX(231, 150, 151) KPX(qa)
                 : "+{v[148:151]}"(k0)
                 : [a5] "v"(af[5]), [a6] "v"(af[6]), [a7] "v"(af[7]), [u5] "a"(wk[5]), [u6] "a"(wk[6]), [u7] "a"(wk[7]), [ix] "v"(ix), [qa] "v"(qa)
                 : "memory", "v230", "v231");
}
template <int PH>
__device__ __forceinline__ void el_phase(i32x4 D, const u32x4 (&kx)[8], u32x8& QA, u32x4& QB, u32x4& QC, f32x4 k, unsigned kw, unsigned qa,
                                         unsigned fa, int n1, unsigned pxo, unsigned lo, float c0, float c1, float c2,
                                         unsigned& bud, u64& e1, u64& e4, unsigned& nr, unsigned ps, unsigned pt, unsigned pu, unsigned pv, unsigned fw, unsigned fv) {
#define BODY(BR, BR2)                                                                                                   \
               IS(3) LGK(6) MT0("200:203", x3, q3) PADA MT0("204:207", x7, q3) CVX(230, 148, 149) LGK(4)                                \
               MTA("200:203", x2, q2) CVX(231, 150, 151) MTA("204:207", x6, q2) KPX(kw) LGK(3)                                     \
               MTA("200:203", x1, q1) LGK(1) MTA("200:203", x0, q0) "ds_write_b32 %[fw], %[fv]\n\t" TRD("180:181", "4096") TRD("182:183", "4608")                  \
               MTA("204:207", x5, q1) TRD("184:185", "5120") TRD("186:187", "5632") MTA("204:207", x4, q0) IS(4)                   \
               "v_add_f32_dpp v212, v200, v200" DPPLO "v_add_f32_dpp v213, v201, v201" DPPLO                                       \
               "v_add_f32_dpp v214, v202, v202" DPPLO "v_add_f32_dpp v215, v203, v203" DPPLO                                       \
               "v_cmp_u_f32 vcc, v200, v201\n\t" LGK(2) MF0("180:183", q0) PADA TRD("196:197", "8192") TRD("198:199", "8704")           \
               "v_add_f32_dpp v212, v204, v204" DPPHI "v_add_f32_dpp v213, v205, v205" DPPHI                                       \
               "v_add_f32_dpp v214, v206, v206" DPPHI "v_add_f32_dpp v215, v207, v207" DPPHI                                       \
               "v_max3_f32 v216, v212, v213, v214\n\tv_max_f32 v216, v216, v215\n\t"                                               \
               "v_mov_b64 v[176:177], v[174:175]\n\tv_mov_b64 v[178:179], v[174:175]\n\t"                                          \
               "v_max_f32_dpp v217, v216, v216" DPPAL "v_max_f32_dpp v216, v216, v216" DPPAL LGK(2) MFA("184:187", q1)             \
               "v_permlane16_swap_b32 v216, v217\n\tv_max_f32 v218, v216, v217\n\tv_max_f32 v217, v216, v217\n\t"                  \
               LGK(0) MFA("196:199", q4) PADA KL(0)                                                                                     \
               "v_permlane32_swap_b32 v218, v217\n\tv_max_f32 v218, v218, v217\n\t" IS(5) BR KL(1)                                 \
               "v_mul_f32 v216, " SCN ", v218\n\t"                                                                                 \
               "v_fmamk_f32 v212, v212, " SCK ", v216\n\tv_fmamk_f32 v213, v213, " SCK ", v216\n\t" KL(2)                          \
               "v_exp_f32 v212, v212\n\tv_exp_f32 v213, v213\n\t" KL(3)                                                            \
               "v_fmamk_f32 v214, v214, " SCK ", v216\n\tv_fmamk_f32 v215, v215, " SCK ", v216\n\t" KL(4)                          \
               "v_exp_f32 v214, v214\n\tv_exp_f32 v215, v215\n\t" KL(5)                                                            \
               "v_cvt_pk_bf16_f32 v222, v212, v213\n\tv_cvt_pk_bf16_f32 v223, v214, v215\n\t"                                      \
               ES0 IS(6)                                                                                   \
               LGK(2) MFA("188:191", q2) "v_add_f32 v212, v212, v213\n\t" LGK(0) MFA("192:195", q3) IS(7) ES1                      \
               "v_add_f32 v214, v214, v215\n\tv_mul_f32 v226, " SCK ", v218\n\tv_add_f32 v212, v212, v214\n\t" GN                  \
               "v_add_f32 v220, v208, v210\n\tv_add_f32 v221, v209, v211\n\t"                                                      \
               "v_add_f32_dpp v212, v212, v212" DPPAL                                                                              \
               "v_add_f32_dpp v220, v220, v220" DPPAL "v_add_f32_dpp v221, v221, v221" DPPAL                                       \
               "v_max3_f32 v216, v220, v221, v218\n\tv_max3_f32 v217, v220, v221, v218\n\t"                                        \
               "v_cmp_u_f32 vcc, v220, v221\n\tv_mul_f32 v220, " SCK ", v220\n\t"                                                  \
               "v_permlane16_swap_b32 v216, v217\n\tv_max_f32 v219, v216, v217\n\tv_max_f32 v217, v216, v217\n\t"                  \
               "v_mul_f32 v221, " SCK ", v221\n\t" BR2                                                                             \
               "v_permlane32_swap_b32 v219, v217\n\tv_max_f32 v216, v219, v217\n\t"                                                \
               "v_fmamk_f32 v226, v216, " SCN ", v226\n\tv_fmamk_f32 v220, v216, " SCN ", v220\n\tv_fmamk_f32 v221, v216, " SCN ", v221\n\t" \
               "v_exp_f32 v226, v226\n\tv_exp_f32 v220, v220\n\tv_exp_f32 v221, v221\n\t"                                          \
               "v_fma_f32 v217, v226, v212, v220\n\t"                                                                              \
               "v_add_f32 v219, v217, v221\n\tv_add_f32 v217, v217, v221\n\t"                                                      \
               "v_mul_f32 v225, v226, %[c1]\n\tv_mul_f32 v226, v226, %[c2]\n\t"                                                    \
               "v_permlane16_swap_b32 v219, v217\n\tv_add_f32 v216, v219, v217\n\tv_add_f32 v217, v219, v217\n\t"                  \
               "v_fma_f32 v220, v220, %[c0], v225\n\tv_fma_f32 v221, v221, %[c0], v226\n\t"                                        \
               "v_permlane32_swap_b32 v216, v217\n\tv_add_f32 v216, v216, v217\n\t"                                                \
               "v_rcp_f32 v216, v216\n\t" PADN                                                                               \
               "v_mul_f32 v220, v220, v216\n\tv_mul_f32 v221, v221, v216\n\t"                                                      \
               "v_cvt_pk_bf16_f32 v227, v220, v221\n\t" IS(8)                                                                      \
               "buffer_store_dword v227, %[lo], %[D], %[ps] offen sc1\n\t"                                                         \
               "buffer_store_dword v227, %[lo], %[D], %[pt] offen sc1\n\t" EVA(1) IS(9)                                            \
               "buffer_store_dwordx2 v[222:223], %[pxo], %[D], %[pu] offen sc1\n\t"                                                \
               "buffer_store_dword v227, %[lo], %[D], %[pu] offen sc1\n\t"                                                         \
               "buffer_store_dwordx2 v[222:223], %[pxo], %[D], %[pv] offen sc1\n\t"                                                \
               "buffer_store_dword v227, %[lo], %[D], %[pv] offen sc1\n\t"
#define ES0 EST EVA(4)
#define ES1
#define GN PADN "s_nop 3\n\t"
#define EL()                                                                                                              \
  asm volatile(BODY(FBR, FBR2) "s_branch 13f\n\t" CN(18, 100) CN(19, 10000)                                             \
               "20:\n\t" FRD LGK(0) "v_cmp_gt_i32 vcc, %[n1], v224\n\ts_cbranch_vccz 21f\n\t" BUD("20b")                           \
               "21:\n\t" QR3 QR2 QR1 QR0 "s_nop 0\n\t"                                 \
               BODY("", "s_nop 0\n\t")                                                                                  \
               "13:"                                                                                                               \
               : [bud] "+s"(bud), "+{v[160:167]}"(QA), "+{v[168:171]}"(QB), "+{v[172:175]}"(QC) EVO                                                     \
               : [x0] "v"(kx[0]), [x1] "v"(kx[1]), [x2] "v"(kx[2]), [x3] "v"(kx[3]), [x4] "v"(kx[4]), [x5] "v"(kx[5]), [x6] "v"(kx[6]), \
                 [x7] "v"(kx[7]), "{v[148:151]}"(k), [kw] "v"(kw), [n1] "s"(n1),                                                   \
                 [qa] "v"(qa), [fa] "v"(fa), [pxo] "v"(pxo), [c0] "v"(c0), [c1] "v"(c1), [c2] "v"(c2),                       \
                 [lo] "v"(lo), [D] "s"(D), [ps] "s"(ps), [pt] "s"(pt), [pu] "s"(pu), [pv] "s"(pv), [fw] "v"(fw), [fv] "v"(fv)                                          \
               : "memory", "vcc", "scc", "v176", "v177", "v178", "v179", "v180", "v181", "v182", "v183", "v184",                   \
                 "v185", "v186", "v187", "v188", "v189", "v190", "v191", "v192", "v193", "v194", "v195", "v196", "v197", "v198",   \
                 "v199", "v200", "v201", "v202", "v203", "v204", "v205", "v206", "v207", "v208", "v209", "v210", "v211", "v212",   \
                 "v213", "v214", "v215", "v216", "v217", "v218", "v219", "v220", "v221", "v222", "v223", "v224", "v225", "v226",   \
                 "v227", "v228", "v230", "v231")
#define EVO
  EL();
#undef EL
#undef BODY
#undef q0
#undef q1
#undef q2
#undef q3
#undef q4
}
__device__ __forceinline__ void role_qkc(const Args& a, rsrc_t R, const Go& go, lds_char* lds, int h, int tid, int hw, int lane) {
  const int wave = 3 - hw;
  const int l15 = lane & 15, g = lane >> 4;
  u32x8 wq[2][9], wk[2][9], wp[3];
  int cls = -1, kv0 = -1;
  const int wz = h >> 3, tq = (h >> 1) & 3, hp = h & 1, qi = l15 & 7;
  unsigned pxo = (unsigned)nearoff(NEAR_E + (h >> 1) * 1024 + ((2 * (l15 >> 3) + (g >> 1)) * 16 + 8 * (qi >> 2) + 4 * hp + (qi & 3)) * 16 + (g & 1) * 8);
  unsigned plo = l15 < 8 ? (unsigned)nearoff(NEAR_PM + wz * 1024 + ((2 * hp + (tq >> 1)) * 16 + 4 * (qi >> 1) + (g & 1) * 2 + (qi & 1)) * 16 + (tq & 1) * 8 + (g >> 1) * 4)
                         : (g == 0 ? (unsigned)nearoff(NEAR_AL + wz * 256 + ((tq >> 1) * 8 + qi) * 16 + (2 * hp + (tq & 1)) * 4) : OOB);
  {
    u32x4 sg[QDEP][8];
#pragma unroll
    for (int c = 0; c < QDEP; ++c) qk_gload<NTM & 1>(sg[c], a, h, c, tid);
#define WK3(b) do { WALK(b + 2); WALK(b + 1); WALK(b); } while (0)
    if (wave == 0) WK3(200); else if (wave == 1) WK3(208); else if (wave == 2) WK3(216); else WK3(224);
    const int q = l15 >> 2;
    const int lrow = 16 * (g >> 1) + 4 * (g & 1) + 8 * (q >> 1) + (q & 1);
    auto wrd = [&](lds_char* p) {
      u32x2 v0 = trd(p), v1 = trd(p + 2 * 544), v2 = trd(p + 32 * 544), v3 = trd(p + 34 * 544);
      return (u32x8){v0[0], v0[1], v1[0], v1[1], v2[0], v2[1], v3[0], v3[1]};
    };
#pragma unroll
    for (int ph = 0; ph < 3; ++ph) {
#pragma unroll
      for (int i = 0; i < 4; ++i) {
        const int c = ph * 4 + i;
        if (c < 9) {
          qk_swrite(sg[c % QDEP], lds + i * QSTG, tid);
          if (c + QDEP < 9) qk_gload<NTM & 1>(sg[c % QDEP], a, h, c + QDEP, tid);
          CBAR();
        }
      }
      RT(8 + ph);
      if (ph == 2 && go_wait(R, go, lane)) {
        kv0 = (int)ld32(R, FL_PROG + h * 128 + (lane & 3) * 4, 0);
        cls = get_cls(R, lane, cls_look(R, lane));
        pxo += (unsigned)(cls & 1) * 8192u; plo += (unsigned)(cls & 1) * 8192u;
        if (wave < 3) {
#pragma unroll
          for (int k = 0; k < 4; ++k) {
            const unsigned so = wave * SLOT_REAL + (k & 1) * CPY1 + (k >> 1) * CPY2;
            st64((u32x2){~0u, ~0u}, R, pxo, so); st32(~0u, R, plo, so);
          }
        }
        CBAR();
        RT(11);
      }
      BAR();
#pragma unroll
      for (int i = 0; i < 4; ++i) {
        const int j = ph * 4 + i;
        if (j < 9) {
          lds_char* buf = lds + (ph < 2 ? ((i + wave) & 3) : 0) * QSTG + lrow * 544 + (l15 & 3) * 8;
#pragma unroll
          for (int t = 0; t < 4; ++t) {
            u32x8& w = t < 2 ? wq[t & 1][j] : wk[t & 1][j];
            w = wrd(buf + (t < 2 ? wave * 64 + t * 32 : 256 + (t - 2) * 128 + wave * 32));
          }
          if (j == 0 || j == 4) wp[j >> 2] = wrd(buf + 256 + 224);
        }
      }
      SB();
#pragma unroll
      for (int k = 0; k < 16; ++k) {
        const int j = ph * 4 + (k >> 2);
        if (j < 9) { u32x8& w = (k & 2) ? wk[k & 1][j] : wq[k & 1][j]; if (j < 8) pin_a(w); else pin_v(w); }
      }
      if (ph < 2) pin_v(wp[ph]);
      if (ph == 0 || wave == ph) wp[2] = wrd(lds + (ph < 2 ? 3 : 0) * QSTG + lrow * 544 + (l15 & 3) * 8 + 256 + 224);
      pin_v(wp[2]);
      BAR();
    }
  }
  RT(1);
  if (cls < 0) return;
  RT(2);
  auto run = [&](auto ROLEC) {
  constexpr int ROLE = decltype(ROLEC)::value;
  lds_char* img = lds;
  const unsigned fo = (unsigned)(cls & 1) * 8192u;
  const int r0 = ROLE * 1024 + lane * 16, r1 = r0 + 4096, r2 = 8192 + lane * 16;
  const unsigned m0 = nearoff(r0) + fo, m1 = nearoff(r1) + fo, m2 = nearoff(r2) + fo;
  const unsigned lb = (unsigned)(uintptr_t)lds, lw0 = lb + ROLE * 1024;
  const i32x4 D = mkdesc(a.wb);
  lds_char* ard1 = img + ((ROLE + 1) & 3) * 1024 + lane * 16;
  lds_char* ard2 = img + ((ROLE + 2) & 3) * 1024 + lane * 16;
  lds_char* ard3 = img + ((ROLE + 3) & 3) * 1024 + lane * 16;
  const int gy = ((tq & 1) << 1) | (tq >> 1);
  float lc0 = l15 < 8 ? 1.f : 0.f, lc1 = (l15 & 9) == 8 ? 1.f : 0.f, lc2 = (l15 & 9) == 9 ? 1.f : 0.f;
  const unsigned pcl = lane < 32 ? (unsigned)nearoff(NEAR_E + (h >> 1) * 1024 + ((lane >> 2) * 8 + 4 * hp + (lane & 3)) * 16) + fo
                     : (lane < 40 ? (unsigned)nearoff(NEAR_PM + wz * 1024 + ((2 * hp + (tq >> 1)) * 16 + (tq & 1) * 8 + (lane & 7)) * 16) + fo
                                  : (lane < 42 ? (unsigned)nearoff(NEAR_AL + wz * 256 + ((tq >> 1) * 8 + (2 * hp + (tq & 1)) * 2 + (lane & 1)) * 16) + fo : OOB));
  const unsigned kfo = OFF_KF + (unsigned)h * 8192u + lane * 16;
  constexpr int QT = ROLE >> 1;
  const unsigned kmo = OFF_KMF + (unsigned)h * 4096u + lane * 16;
  const unsigned pxs = OFF_PXF + (unsigned)((QT * 40 + h) * 1024 + lane * 16);
  const unsigned pws = OFF_PXF + (unsigned)((QT * 40 + 32 + wz * 2 + hp) * 1024 + (gy * 16 + l15) * 16 + g * 4);
  const unsigned kpf = FL_PROG + h * 128 + (lane & 3) * 4;
  const unsigned kmf = (lane == 0) ? (unsigned)(FL_KMPROG + h * 128) : OOB;
  const unsigned wpo = lane == 0 ? (unsigned)(FL_WP + h * 32) : OOB;
  const int idx = (lane & 8) ? 0xEEEE : 0x4444;
  const unsigned qw = lb + QIM_OFF + (ROLE * 32 + l15) * 32 + g * 8;
  const unsigned kwa = lb + KIM_OFF + (ROLE * 16 + l15) * 32 + (g & 1) * 16 + (g >> 1) * 4, kwb = kwa + 2048;
  const unsigned kwp = kwa + (ROLE == 2 ? 5 : 8) * 512;
  lds_char* qrd = lds + QIM_OFF + (4 * g + (l15 >> 2)) * 32 + (l15 & 3) * 8;
  lds_char* krd = qrd + (KIM_OFF - QIM_OFF);
  unsigned fw = lb + FLG_OFF + ROLE * 4;
  pin_v(fw);
  lds_char* frd = lds + FLG_OFF + (lane & 3) * 4;
  const unsigned fr3 = lb + FLG_OFF + min(lane & 3, 2) * 4;
  const unsigned own = own_cpy(cls, h >> 3);
  unsigned ima = lb + lane * 16;
  pin_v(ima);
  ds_flag(fw, 0u);
  ds_flag(fw + 16, 0u);
  auto poison = [&]() {
#pragma unroll
    for (int f = 0; f < 9; ++f) *(lds_u32x4*)(lds + QIM_OFF + f * 1024 + lane * 16) = (u32x4){~0u, ~0u, ~0u, ~0u};
    asm volatile("s_waitcnt lgkmcnt(0)" ::: "memory");
  };
  if (ROLE == 0) poison();

  u32x4 kx[8];
  int fl = kv0;
  u32x4 af0[3];
#pragma unroll
  for (int f = 0; f < 3; ++f) {
    const int u = (f == 2 ? 8 : f * 4 + ROLE) * 64 + lane;
    af0[f] = *(const u32x4*)(a.mem0 + (u & 7) * DM + (u >> 3) * 8);
  }
  auto k_dma = [&](int sg, int half) {
#pragma unroll
    for (int f = 0; f < 4; ++f)
      asm volatile("s_mov_b32 m0, %2\n\ts_nop 0\n\tbuffer_load_dwordx4 %0, %1, %3 offen offset:%4 sc1 lds"
                   :: "v"(kfo), "s"(D), "s"(lb + KX_OFF + (sg & 1) * 8192 + half * 4096), "s"((unsigned)(sg & RMASK) * 262144u + half * 4096), "n"(f * 1024) : "memory");
  };
  u32x4 qq[4], F[4];
#pragma unroll
  for (int f = 0; f < 4; ++f) { qq[f] = af0[0]; F[f] = af0[0]; }
  f32x4 T0 = {0.f, 0.f, 0.f, 0.f}, T1 = T0;
  float tmx = 0.f, tsx = 0.f;
  auto q_ld = [&](u32x4 (&qx)[4], int sg) {
#pragma unroll
    for (int f = 0; f < 4; ++f)
      asm volatile("buffer_load_dwordx4 %0, %1, %2, %3 offen offset:%4 sc1"
                   : "+v"(qx[f]) : "v"(kfo), "s"(D), "s"((unsigned)sg * 262144u + (OFF_QF - OFF_KF) + QT * 4096), "n"(f * 1024) : "memory");
  };
  auto t_part = [&](u32x4 (&qx)[4], int par, auto NC) {
    asm volatile("s_waitcnt vmcnt(%4)" : "+v"(qx[0]), "+v"(qx[1]), "+v"(qx[2]), "+v"(qx[3]) : "n"(decltype(NC)::value) : "memory");
    T0 = (f32x4){0.f, 0.f, 0.f, 0.f}; T1 = T0;
#pragma unroll
    for (int ks = 0; ks < 4; ++ks) {
      T0 = mfma(*(lds_u32x4*)(lds + KX_OFF + par * 8192 + ks * 1024 + lane * 16), qx[ks], T0);
      if (QT) T1 = mfma(*(lds_u32x4*)(lds + KX_OFF + par * 8192 + 4096 + ks * 1024 + lane * 16), qx[ks], T1);
    }
    pin_v(T0); pin_v(T1); PAD();
    px_mask(T0, T1, QT, lane);
    pin_v(T0); pin_v(T1); PAD();
    px_seg(T0, T1, tmx, tsx);
    pin_v(T0); pin_v(T1); pin_v(tmx); pin_v(tsx); PAD();
  };
  if (ROLE == 3) {
    const bool ok = wait_flag_c<8>(R, kpf, 0, fl);
    k_dma(0, 0); k_dma(0, 1);
    if (!ok) return;
  }
  VMW(0);

  unsigned iso_acc = 0, bud = SPINCAP, tm = 0, rcnt = 0;
  unsigned cv = 0;
  const unsigned cwo = FL_AN + (h & 15) * 128;
  EVDECL; CKDECL;
  auto k_get = [&](u32x4 (&F)[4], int s) {
#pragma nounroll
    for (; bud != 0; --bud) {
      CBAR();
      if (__all((int)*(lds_u32*)frd > s)) break;
      __builtin_amdgcn_s_sleep(1);
    }
    CBAR();
#pragma unroll
    for (int f = 0; f < 4; ++f) { u32x2 lo = trd(krd + f * 1024), hi = trd(krd + f * 1024 + 512); F[f] = (u32x4){lo[0], lo[1], hi[0], hi[1]}; }
    const u32x2 p0 = trd(krd + 4096), p1 = trd(krd + 4608);
    F[3][2] = add3bf(F[3][2], p0[0], p1[0]); F[3][3] = add3bf(F[3][3], p0[1], p1[1]);
  };
  auto step = [&](auto PHC, int s) {
    constexpr int PH = decltype(PHC)::value & 1, FIRST = (decltype(PHC)::value >> 1) & 1, LAST = decltype(PHC)::value >> 2;
    constexpr int JC = ROLE == 0 ? 3 : (ROLE == 1 ? 6 : 8);
    u64 e0 = 0, e1 = 0, e3 = 0, e4 = 0; unsigned nr0 = 0;
    u32x4 af[9];
    f32x4 q0, q1, k0, k1, k2;
    PADS(200 + ROLE * 8 + decltype(PHC)::value);
    unsigned f3 = s + 1;
    pin_v(f3);
    const unsigned wv = f3, wsel = FIRST ? wpo : fw;
    if (ROLE < 3) acc_zero2(k1, k2);
#define MQ(t, j) do { smo<t, (j) < 8>(q##t, af[j], wq[t][j], idx); SB(); } while (0)
    constexpr int RD = ROLE == 3 && !LAST;
    u32x8 QA;
    u32x4 QB, QC;
    u32x2 qt;
    lds_char* f8 = lds + F8_OFF + lane * 16;
    bud = __builtin_amdgcn_readfirstlane(bud);
    asm volatile("" : "+s"(bud));
    unsigned nt = 0; int rq = 0;
    tm = __builtin_amdgcn_readfirstlane(tm); rcnt = __builtin_amdgcn_readfirstlane(rcnt);
    asm volatile("" : "+s"(tm), "+s"(rcnt));
    if (ROLE == 3) { pin_v(lc0); pin_v(lc1); pin_v(lc2); }
    unsigned p0 = (s & 7) * SLOT_REAL, p1 = p0 + CPY1, p2 = p0 ^ CPY2, p3 = p2 + CPY1;
    unsigned ms = (p2 + own) ^ ((s & 4) ? 0 : CPY1);
    if (ROLE == 3) asm volatile("" : "+s"(p0), "+s"(p1), "+s"(p2), "+s"(p3));
    auto kx_get = [&]() {
#pragma unroll
      for (int f = 0; f < 8; ++f) kx[f] = *(lds_u32x4*)(lds + KX_OFF + PH * 8192 + f * 1024 + lane * 16);
#pragma unroll
      for (int f = 0; f < 8; ++f) pin_v(kx[f]);
    };
    auto head = [&]() -> u64 {
    acc_zero(q0, q1, k0);
    if (!FIRST) {
      int r = 0;
      if (ROLE == 1) r = spin_late(tm, 0);
      rq = r;
      mem_issue2<ROLE == F8W ? 3 : 2>(D, m0, m1, m2, ms, lw0, lw0 + 4096, lb + F8_OFF, e3);
      if (ROLE == 1) { move_on(tm, r); SB(); }
      PAD();
      if (RD) kx_get();
      if (ROLE == 3 || nt != 0) VMW(0);
      else {
      __builtin_amdgcn_s_sleep(XSLP);
      if (!LAST && (ROLE == 1 || ROLE == 2)) k_dma(s + 1, ROLE - 1);
      if (ROLE == 0 && !LAST) {
#pragma unroll
        for (int ks = 0; ks < 4; ++ks) st128(F[ks], R, kmo + ks * 1024, (unsigned)(s - 1) * 131072u);
      }
      if (ROLE == 0 && !LAST) {
        st128((u32x4){~0u, ~0u, ~0u, ~0u}, R, pcl, (unsigned)((s + 2) & 3) * SLOT_REAL);
        st128((u32x4){~0u, ~0u, ~0u, ~0u}, R, pcl, (unsigned)((s + 2) & 3) * SLOT_REAL + CPY1);
        st128((u32x4){~0u, ~0u, ~0u, ~0u}, R, pcl, (unsigned)((s + 2) & 3) * SLOT_REAL + CPY2);
        st128((u32x4){~0u, ~0u, ~0u, ~0u}, R, pcl, (unsigned)((s + 2) & 3) * SLOT_REAL + CPY2 + CPY1);
      }
      PAD();
      CBAR();
      asm volatile("s_waitcnt vmcnt(%0)" :: "n"(ROLE == 0 ? (LAST ? 0 : 8) : (LAST ? 0 : 4)) : "memory");
      }
    } else {
      *(lds_u32x4*)(img + r0) = af0[0]; *(lds_u32x4*)(img + r1) = af0[1]; *(lds_u32x4*)f8 = af0[2];
      if (RD) kx_get();
      asm volatile("s_waitcnt lgkmcnt(0)" ::: "memory");
    }
    SB();
    af[0] = *(lds_u32x4*)(img + r0); SB(); af[4] = *(lds_u32x4*)(img + r1); SB();
    __builtin_amdgcn_s_barrier(); CBAR(); SB();
    if (s == 0) RT(3);
    if (s == 1) RT(4);
    if (s == 8) RT(5);
    if (s == 60) RT(6);
    af[8] = *(lds_u32x4*)f8; SB(); af[1] = *(lds_u32x4*)ard1; SB(); af[2] = *(lds_u32x4*)ard2; SB();
    ISOQ(0);
    MQ(0, 0); af[3] = *(lds_u32x4*)ard3; SB();
    MQ(1, 0); af[5] = *(lds_u32x4*)(ard1 + 4096); SB();
    MQ(0, 4); af[6] = *(lds_u32x4*)(ard2 + 4096); SB();
    MQ(1, 4); af[7] = *(lds_u32x4*)(ard3 + 4096); SB();
    MQ(0, 8);
    ISOQ(1);
    MQ(0, 1); MQ(0, 2); MQ(0, 3); MQ(0, 5); MQ(0, 6); MQ(0, 7);
    u64 bad = q_mid(af, wq[1], idx, q0, q1, qt); SB();
    return bad;
    };
    if (!FIRST) {
      u64 keep;
      tm -= GRID + XT; nt = ~0u;
      try_top<0 + ROLE * 8 + decltype(PHC)::value>(nt, ms, tm, keep); SB();
      const u64 bad = head();
      try_end<0 + ROLE * 8 + decltype(PHC)::value>(bad, keep, ima, nt, ms, tm, rcnt, m0, m1, m2, lw0, lw0 + 4096, lb + F8_OFF, D); SB();
    } else
    head();
    q_end(af, wq[1], wk[0], idx, qw, q1, qt, k0); SB();
    ISOQ(2);
    EV(e0);
    k_two<RD>(af, wk[0], idx, k0, (unsigned)(uintptr_t)qrd, QC); SB();
    if (ROLE < 3) {
      u32x2 t;
      ISOQ(3);
      k_mid<ROLE == 1 && FIRST ? WPOS : -1>(af, af[JC], wk[0], wp, idx, k0, k2, t, D, wsel, wv); SB();
      ISOQ(4);
      k_end<ROLE == 1 && FIRST ? WPOS : -1>(af, wk[1], idx, k1, k2, t, kwa, kwp, kwb, fw, f3, D, wsel, wv); SB();
      ISOQ(6);
      PAD();
      if (ROLE == 1 && !LAST) {
        nr0 = nt != 0;
        if (!FIRST) next_time(tm, s);
      }
      if (ROLE == 0 && !LAST) {
        __builtin_amdgcn_s_sleep(TSLP);
        k_get(F, s);
        ds_flag(lb + FLG_OFF + 20, f3);
        pin_v(F[0]); pin_v(F[1]); pin_v(F[2]); pin_v(F[3]);
        VMW(0);
        st32((unsigned)(s - 1), R, kmf, 0);
        if (!PH && !FIRST && UNLIKELY(s == NSEG - 2)) {
#pragma unroll
          for (int ks = 0; ks < 4; ++ks) st128(F[ks], R, kmo + ks * 1024, (unsigned)s * 131072u);
          VMW(0);
          st32((unsigned)s, R, kmf, 0);
        }
      }
      if (ROLE == 2 && !LAST) {
        if (FIRST) { gate_wait(R, kpf, 2, fl, bud); k_dma(1, 0); k_dma(1, 1); }
        gate_wait(R, kpf, min(2 * s + 4 + (s >= NSEG - 4), 2 * NSEG - 1), fl, bud);
        fl = (int)ld32(R, kpf, 0);
        VMW(0);
        pin_v(fl);
      }
      if ((ROLE == 0 || ROLE == 2) && !PH && !FIRST && UNLIKELY(s == NSEG - 2)) {
        k_dma(s + 1, 0);
        q_ld(qq, s + 1);
        t_part(qq, 1, std::integral_constant<int, 0>());
      }
      if ((ROLE == 0 || ROLE == 2) && LAST) {
        f32x4 G = {0.f, 0.f, 0.f, 0.f};
        u32x4 px; unsigned pw;
        k_get(F, s);
#pragma unroll
        for (int ks = 0; ks < 4; ++ks) G = mfma(F[ks], qq[ks], G);
        px_fin(G, T0, T1, tmx, tsx, px, pw);
        st128(px, R, pxs, (unsigned)s * 81920u); st32(pw, R, pws, (unsigned)s * 81920u);
        VMW(0);
        st32((unsigned)s, R, lane == 0 ? (unsigned)(FL_PXLAST + h * 256 + QT * 128) : OOB, 0);
      }
      CBAR();
      if (ROLE == 0) CKS();
    } else if (!LAST) {
      k_three<1>(af, wk[0], idx, k0, (unsigned)(uintptr_t)qrd, QA, QB); SB();
      el_phase<PH>(D, kx, QA, QB, QC, k0, kwa, (unsigned)(uintptr_t)qrd, fr3, s + 1, pxo, plo, lc0, lc1, lc2, bud, e1, e4, nr0, p0, p1, p2, p3, fw, f3);
      ds_flag(fw + 4, f3);
      EVPUT(2, e1); EVPUT(4, e4);
    } else {
      k_three<0>(af, wk[0], idx, k0, kwa, QA, QB);
      ds_flag(fw, f3);
    }
    EVPUT(0, e0);
    EVPUTI(1, nr0); if (!FIRST) EVPUT(3, e3);
    if (ROLE == 1 && !LAST) {
#pragma nounroll
      for (; bud != 0; --bud) {
        CBAR();
        if (__all((int)*(lds_u32*)(lds + FLG_OFF + 16 + (lane & 1) * 4) > s)) break;
        __builtin_amdgcn_s_sleep(1);
      }
      poison();
      VMW(0);
      if (FIRST) { tm = get_t1(R, cwo, bud); asm volatile("" : "+s"(tm)); }
    }
    if (PH && !LAST && UNLIKELY(s == WLAST)) WALK(200 + ROLE * 8 + 5);
    PADE();
#undef MQ
  };
  step(std::integral_constant<int, 2>(), 0);
#pragma nounroll
  for (int s = 1; s < NSEG - 1; s += 2) {
    step(std::integral_constant<int, 1>(), s);
    step(std::integral_constant<int, 0>(), s + 1);
  }
  step(std::integral_constant<int, 5>(), NSEG - 1);
  EVDUMP();
  if (ROLE == 0) CKDUMP();
  };
  if (hw == 0) run(std::integral_constant<int, 3>());
  else if (hw == 1) run(std::integral_constant<int, 2>());
  else if (hw == 2) run(std::integral_constant<int, 1>());
  else run(std::integral_constant<int, 0>());
  RT(7);
  SV(15, cls);
}

#define VIM_OFF 8192
#define YSLP 2
typedef __attribute__((ext_vector_type(16))) float f32x16;
#define NOP(n) do { asm volatile("s_nop " #n); SB(); } while (0)
#define YM(t, k) "v_smfmac_f32_16x16x64_bf16 v[148+4*" #t ":151+4*" #t "], v[184+4*" #k ":187+4*" #k "], %[w" #t #k "], %[ix]\n\t"
#define HA(d, b) "v_add_f32 v" #d ", v[" b "], v[" b "+2]\n\t"
#define HB(d, b) "v_add_f32 v" #d ", v[" b "+1], v[" b "+3]\n\t"
#define YA(t) HA(201, "148+4*" #t)
#define YB(t) HB(202, "148+4*" #t)
#define YC(t) "v_cvt_pk_bf16_f32 v[192+" #t "], v201, v202\n\t"
#define S32(a, b) "v_permlane32_swap_b32 v" #a ", v" #b "\n\t"
#define S16(a, b) "v_permlane16_swap_b32 v" #a ", v" #b "\n\t"
__device__ __forceinline__ void y_zero(f32x16& ya, f32x16& yb, f32x4& yc) {
  asm volatile(".rept 18\n\tv_mov_b64 v[148+2*\\+:149+2*\\+], 0\n\t.endr" : "={v[148:163]}"(ya), "={v[164:179]}"(yb), "={v[180:183]}"(yc));
}
__device__ __forceinline__ void v_zero(f32x4& v0, f32x4& v1) {
  asm volatile("v_mov_b64 v[140:141], 0\n\tv_mov_b64 v[142:143], 0\n\tv_mov_b64 v[144:145], 0\n\tv_mov_b64 v[146:147], 0\n\ts_nop 1"
               : "={v[140:143]}"(v0), "={v[144:147]}"(v1));
}
__device__ __forceinline__ u64 v_end(const u32x4 (&af)[9], const u32x8 (&w)[9], int ix, unsigned vw, f32x4 v0, f32x4& v1) {
  u64 bad;
  asm volatile(SMX("144:147", 8, w8) SMX("144:147", 1, w1) HA(201, "140") HB(202, "140") SMX("144:147", 2, w2) CVX(203, 201, 202)
               "ds_write_b32 %[vw], v203\n\t" SMX("144:147", 3, w3) "v_cmp_u_f32 %[bad], v201, v202\n\t" SMX("144:147", 5, w5)
               : "+{v[144:147]}"(v1), [bad] "=s"(bad)
               : "{v[140:143]}"(v0), [a8] "v"(af[8]), [a1] "v"(af[1]), [a2] "v"(af[2]), [a3] "v"(af[3]), [a5] "v"(af[5]),
                 [w8] "v"(w[8]), [w1] "a"(w[1]), [w2] "a"(w[2]), [w3] "a"(w[3]), [w5] "a"(w[5]), [ix] "v"(ix), [vw] "v"(vw)
               : "memory", "v201", "v202", "v203");
  return bad;
}
__device__ __forceinline__ void y_first(const u32x4 (&af)[9], const u32x8 (&w)[9], const u32x8 (&wo)[9][2], int ix, unsigned vw, unsigned r0, unsigned r1,
                                        f32x4 v1, f32x16& ya, f32x16& yb, u32x8& A) {
  asm volatile("s_waitcnt lgkmcnt(0)\n\ts_barrier\n\t"
               "ds_read_b64_tr_b16 v[184:185], %[r0]\n\tds_read_b64_tr_b16 v[186:187], %[r1]\n\t"
               SMX("144:147", 6, u6) SMX("144:147", 7, u7) PADN "s_nop 4\n\t" "s_waitcnt lgkmcnt(0)\n\t"
               YM(0, 0) HA(201, "144") HB(202, "144") YM(1, 0) CVX(203, 201, 202) "ds_write_b32 %[vw], v203 offset:1024\n\t"
               YM(2, 0) YM(3, 0) YM(4, 0) "s_waitcnt lgkmcnt(0)\n\ts_barrier\n\t"
               "ds_read_b64_tr_b16 v[188:189], %[r0] offset:1024\n\tds_read_b64_tr_b16 v[190:191], %[r1] offset:1024\n\t"
               YM(5, 0) PADA YM(6, 0) YM(7, 0)
               : "+{v[144:147]}"(v1), "+{v[148:163]}"(ya), "+{v[164:179]}"(yb), "={v[184:191]}"(A)
               : [a6] "v"(af[6]), [a7] "v"(af[7]), [u6] "a"(w[6]), [u7] "a"(w[7]), [w00] "a"(wo[0][0]), [w10] "a"(wo[1][0]), [w20] "a"(wo[2][0]),
                 [w30] "a"(wo[3][0]), [w40] "a"(wo[4][0]), [w50] "a"(wo[5][0]), [w60] "a"(wo[6][0]), [w70] "a"(wo[7][0]),
                 [ix] "v"(ix), [vw] "v"(vw), [r0] "v"(r0), [r1] "v"(r1)
               : "memory", "v201", "v202", "v203");
}
template <int ST>
__device__ __forceinline__ void y_second(const u32x8 (&wo)[9][2], int ix, f32x16 ya, f32x16 yb, f32x4 yc, u32x8 A, i32x4 D, const unsigned (&yr)[3],
                                         unsigned ys, u32x4& o0, u32x4& o1, unsigned& o2) {
  asm volatile("s_waitcnt lgkmcnt(0)\n\t"
               YM(0, 1) YM(1, 1) YM(2, 1) YA(0) YB(0) YM(3, 1) YC(0) YA(1) YM(8, 0) YB(1) YC(1) YM(4, 1) YA(2) YB(2) YM(5, 1) YC(2) YA(3)
               YM(6, 1) YB(3) YC(3) PADA YM(7, 1) S32(192, 194) YA(4) YM(8, 1) YB(4) S32(193, 195) YC(4) YA(5) YB(5)
               S16(192, 193) YC(5) S16(194, 195) YA(6) YB(6)
               ".if %[st]\n\tbuffer_store_dwordx4 v[192:195], %[y0], %[D], %[ys] offen sc1\n\t.endif\n\t"
               YC(6) YA(7) YB(7) YC(7) S32(196, 198) YA(8) S32(197, 199) YB(8) YC(8) S16(196, 197) S16(198, 199)
               ".if %[st]\n\tbuffer_store_dwordx4 v[196:199], %[y1], %[D], %[ys] offen sc1\n\t"
               "buffer_store_dword v200, %[y2], %[D], %[ys] offen sc1\n\t.endif"
               : "+{v[148:163]}"(ya), "+{v[164:179]}"(yb), "+{v[180:183]}"(yc), "={v[192:195]}"(o0), "={v[196:199]}"(o1), "={v200}"(o2)
               : "{v[184:191]}"(A), [w01] "a"(wo[0][1]), [w11] "a"(wo[1][1]), [w21] "a"(wo[2][1]), [w31] "a"(wo[3][1]), [w41] "a"(wo[4][1]),
                 [w51] "a"(wo[5][1]), [w61] "a"(wo[6][1]), [w71] "a"(wo[7][1]), [w80] "v"(wo[8][0]), [w81] "v"(wo[8][1]),
                 [ix] "v"(ix), [D] "s"(D), [y0] "v"(yr[0]), [y1] "v"(yr[1]), [y2] "v"(yr[2]), [ys] "s"(ys), [st] "n"(ST)
               : "memory", "v201", "v202");
}
__device__ __forceinline__ void role_vcu(const Args& a, rsrc_t R, const Go& go, lds_char* lds, int h, int tid, int wave, int lane) {
  const int l15 = lane & 15, g = lane >> 4;
  u32x8 wv[2][9], wo[9][2];
  int cv0 = -1;
  {
    u32x4 st[36], so[36];
#pragma unroll
    for (int k = 0; k < 36; ++k) so[k] = gld<NTM & 2>(a.Wo + (size_t)h * 128 * DM + (tid + 256 * k) * 8);
    if (wave == 0) WK3(232); else if (wave == 1) WK3(240); else if (wave == 2) WK3(248); else WK3(256);
    const int q = l15 >> 2;
    const int lrow = 16 * (g >> 1) + 4 * (g & 1) + 8 * (q >> 1) + (q & 1), lrow2 = 8 * (g >> 1) + 4 * ((g & 1) ^ (g >> 1)) + 32 * (q >> 1) + (q & 1);
    auto wrd = [&](lds_char* p, int rs, int up) {
      u32x2 v0 = trd(p), v1 = trd(p + 2 * rs), v2 = trd(p + up * rs), v3 = trd(p + (up + 2) * rs);
      return (u32x8){v0[0], v0[1], v1[0], v1[1], v2[0], v2[1], v3[0], v3[1]};
    };
#pragma unroll
    for (int ph = 0; ph < 4; ++ph) {
      if (ph < 2) {
#pragma unroll
        for (int k = 0; k < 18; ++k) { const int u = tid + 256 * k; *(lds_u32x4*)(lds + (u / 72) * 1184 + (u % 72) * 16) = so[ph * 18 + k]; }
#pragma unroll
        for (int k = ph * 18; k < ph * 18 + 18; ++k) { const int u = tid + 256 * k; st[k] = gld<NTM & 2>(a.Wv + (size_t)(u >> 4) * HD + h * 128 + (u & 15) * 8); }
      } else {
#pragma unroll
        for (int k = (ph - 2) * 16; k < (ph == 2 ? 16 : 36); ++k) { const int u = tid + 256 * k; *(lds_u32x4*)(lds + ((u >> 4) - (ph - 2) * 256) * 288 + (u & 15) * 16) = st[k]; }
      }
      if (ph == 3 && go_wait(R, go, lane)) { cv0 = cls_look(R, lane); CBAR(); }
      BAR();
      if (ph < 2) {
#pragma unroll
        for (int ct = 0; ct < 9; ++ct) wo[ct][ph] = wrd(lds + lrow2 * 1184 + (wave * 9 + ct) * 32 + (l15 & 3) * 8, 1184, 16);
        SB();
#pragma unroll
        for (int ct = 0; ct < 9; ++ct) { if (ct < 8) pin_a(wo[ct][ph]); else pin_v(wo[ct][ph]); }
      } else {
#pragma unroll
        for (int k = 0; k < (ph == 2 ? 8 : 10); ++k) {
          const int i = k >> 1, t = k & 1;
          wv[t][(ph - 2) * 4 + i] = wrd(lds + ((i < 4 ? (i + wave) & 3 : 4) * 64 + lrow) * 288 + (64 * t + 16 * wave) * 2 + (l15 & 3) * 8, 288, 32);
        }
        SB();
#pragma unroll
        for (int k = 0; k < (ph == 2 ? 8 : 10); ++k) { if (k < 8) pin_a(wv[k & 1][(ph - 2) * 4 + (k >> 1)]); else pin_v(wv[k & 1][8]); }
      }
      BAR();
    }
  }
  RT(1);
  const int cls = get_cls(R, lane, cv0);
  if (cls < 0) return;
  RT(2);
  auto run = [&](auto ROLEC) {
  constexpr int ROLE = decltype(ROLEC)::value;
  lds_char* img = lds;
  const unsigned fo = (unsigned)(cls & 1) * 8192u;
  const int r0 = ROLE * 1024 + lane * 16, r1 = r0 + 4096, r2 = 8192 + lane * 16;
  const unsigned m0 = nearoff(r0) + fo, m1 = nearoff(r1) + fo, m2 = nearoff(r2) + fo;
  const unsigned lb = (unsigned)(uintptr_t)lds, lw0 = lb + ROLE * 1024;
  const i32x4 D = mkdesc(a.wb);
  const int ar = g * 256 + ((l15 >> 1) & 1) * 128 + (2 * (l15 >> 2) + (l15 & 1)) * 16;
  lds_char* ard0 = img + ROLE * 1024 + ar;
  lds_char* ard1 = img + ((ROLE + 1) & 3) * 1024 + ar;
  lds_char* ard2 = img + ((ROLE + 2) & 3) * 1024 + ar;
  lds_char* ard3 = img + ((ROLE + 3) & 3) * 1024 + ar;
  lds_char* f8 = lds + F8_OFF + ar;
  unsigned vw = lb + VIM_OFF + ((ROLE & 1) * 16 + l15) * 32 + g * 8 + (ROLE >> 1) * 4;
  unsigned vr0 = lb + VIM_OFF + (8 * g + (l15 >> 2) + 4 * (g & 1)) * 32 + (l15 & 3) * 8;
  unsigned vr1 = lb + VIM_OFF + (8 * g + (l15 >> 2) + 4 * ((g & 1) ^ 1)) * 32 + (l15 & 3) * 8;
  pin_v(vw); pin_v(vr0); pin_v(vr1);
  const int wz = h >> 3, tq = (h >> 1) & 3, hp = h & 1;
  const int gh = ((tq & 1) << 1) | (tq >> 1);
  unsigned yr[3], yz[3];
#pragma unroll
  for (int q = 0; q < 3; ++q) {
    const int m = ROLE * 9 + q * 4 + (q < 2 ? g : 0);
    yr[q] = ywin(cls, m) + (unsigned)((wz * 2 + hp) * 1024 + tq * 256 + l15 * 16 + (q < 2 ? 0 : g * 4));
    yz[q] = OFF_ZF + (unsigned)(m * 40960 + (32 + wz * 2 + hp) * 1024 + (gh * 16 + l15) * 16 + (q < 2 ? 0 : g * 4));
  }
  const unsigned own = own_cpy(cls, h >> 3);
  unsigned ima = lb + lane * 16;
  pin_v(ima);
  const int idx = (lane & 2) ? 0xEEEE : 0x4444;
  u32x4 af0[3];
#pragma unroll
  for (int f = 0; f < 3; ++f) {
    const int u = (f == 2 ? 8 : f * 4 + ROLE) * 64 + lane;
    af0[f] = *(const u32x4*)(a.mem0 + (u & 7) * DM + (u >> 3) * 8);
  }
  unsigned tm = 0, rcnt = 0, cv = 0, iso_acc = 0, o2 = 0, bud = SPINCAP;
  u32x4 o0 = af0[0], o1 = af0[0];
  const unsigned cwo = FL_AN + ((h + 8) & 15) * 128;
  EVDECL;
  auto step = [&](auto PHC, int s) {
    constexpr int PH = decltype(PHC)::value & 1, FIRST = (decltype(PHC)::value >> 1) & 1, LAST = decltype(PHC)::value >> 2;
    u64 e0 = 0, e1 = 0, e3 = 0, e4 = 0, e5 = 0, e6 = 0;
    u32x4 af[9];
    f32x4 v0, v1, yc;
    f32x16 ya, yb;
    PADS(232 + ROLE * 8 + decltype(PHC)::value);
    y_zero(ya, yb, yc);
    PAD();
    unsigned nt = 0;
    tm = __builtin_amdgcn_readfirstlane(tm); rcnt = __builtin_amdgcn_readfirstlane(rcnt);
    asm volatile("" : "+s"(tm), "+s"(rcnt));
#define MV(t, j) do { smo<t, (j) < 8>(v##t, af[j], wv[t][j], idx); SB(); } while (0)
    unsigned ms = ((((s & 7) * SLOT_REAL) ^ CPY2) + own) ^ ((s & 4) ? 0 : CPY1);
    auto head = [&]() -> u64 {
      v_zero(v0, v1);
      if (!FIRST) {
        int r = 0;
        if (ROLE == 1) r = spin_late(tm, DVQ);
        mem_issue2<ROLE == F8W ? 3 : 2>(D, m0, m1, m2, ms, lw0, lw0 + 4096, lb + F8_OFF, e3);
        if (ROLE == 1) { move_on(tm, r); SB(); }
        PAD();
        if (LAST || nt != 0) VMW(0);
        else {
          __builtin_amdgcn_s_sleep(XSLP);
          st128(o0, R, yz[0], (unsigned)(s - 1) * ZS); st128(o1, R, yz[1], (unsigned)(s - 1) * ZS); st32(o2, R, yz[2], (unsigned)(s - 1) * ZS);
          CBAR();
          VMW(3);
        }
      } else {
        *(lds_u32x4*)(img + r0) = af0[0]; *(lds_u32x4*)(img + r1) = af0[1]; *(lds_u32x4*)(lds + F8_OFF + lane * 16) = af0[2];
        asm volatile("s_waitcnt lgkmcnt(0)" ::: "memory");
      }
      SB();
      EV(e6);
      af[0] = *(lds_u32x4*)ard0; SB(); af[4] = *(lds_u32x4*)(ard0 + 4096); SB();
      __builtin_amdgcn_s_barrier(); CBAR(); SB();
      af[8] = *(lds_u32x4*)f8; SB(); af[1] = *(lds_u32x4*)ard1; SB(); af[2] = *(lds_u32x4*)ard2; SB();
      MV(0, 0); af[3] = *(lds_u32x4*)ard3; SB();
      MV(1, 0); af[5] = *(lds_u32x4*)(ard1 + 4096); SB();
      MV(0, 4); af[6] = *(lds_u32x4*)(ard2 + 4096); SB();
      MV(1, 4); af[7] = *(lds_u32x4*)(ard3 + 4096); SB();
      ISOQ(10);
      MV(0, 8); PAD(); SB(); MV(0, 1); MV(0, 2); MV(0, 3); MV(0, 5); MV(0, 6); MV(0, 7);
      ISOQ(11);
      const u64 bad = v_end(af, wv[1], idx, vw, v0, v1); SB();
      return bad;
    };
    if (!FIRST) {
      u64 keep;
      tm -= GRID + XT; nt = ~0u;
      try_top<32 + ROLE * 8 + decltype(PHC)::value>(nt, ms, tm, keep); SB();
      const u64 bad = head();
      try_end<32 + ROLE * 8 + decltype(PHC)::value>(bad, keep, ima, nt, ms, tm, rcnt, m0, m1, m2, lw0, lw0 + 4096, lb + F8_OFF, D); SB();
    } else
    head();
    if (LAST) { st32((unsigned)(s - 1), R, lane == 0 ? (unsigned)(FL_YPROG + h * 16 + ROLE * 4) : OOB, 0); SB(); }
    u32x8 A;
    y_first(af, wv[1], wo, idx, vw, vr0, vr1, v1, ya, yb, A); SB();
    EV(e4); ISOQ(13);
    y_second<!LAST>(wo, idx, ya, yb, yc, A, D, yr, (unsigned)(s & 1) * YSLOT_REAL, o0, o1, o2); SB();
    EVK(e1); ISOQ(15);
    PAD();
#undef MV
    if (FIRST) {
      VMW(0); BAR();
      st32(0u, R, tid == 0 ? (unsigned)(FL_WP + (32 + h) * 32) : OOB, 0);
    }
    if (LAST || (!PH && !FIRST && UNLIKELY(s == NSEG - 2))) { st128(o0, R, yz[0], (unsigned)s * ZS); st128(o1, R, yz[1], (unsigned)s * ZS); st32(o2, R, yz[2], (unsigned)s * ZS); }
    CBAR();
    if (ROLE == 1 && !LAST) {
      if (FIRST) { tm = get_t1(R, cwo, bud); asm volatile("" : "+s"(tm)); }
      else
      next_time(tm, s);
    }
    if (PH && !LAST && UNLIKELY(s == WLAST)) WALK(232 + ROLE * 8 + 5);
    PADE();
    EVPUT(0, e0); EVPUT(6, e6); EVPUT(4, e4);
    EVPUTI(1, nt); EVPUT(2, e1); if (!FIRST) EVPUT(3, e3);
  };
  step(std::integral_constant<int, 2>(), 0);
#pragma nounroll
  for (int s = 1; s < NSEG - 1; s += 2) {
    step(std::integral_constant<int, 1>(), s);
    step(std::integral_constant<int, 0>(), s + 1);
  }
  step(std::integral_constant<int, 5>(), NSEG - 1);
  VMW(0);
  st32((unsigned)(NSEG - 1), R, lane == 0 ? (unsigned)(FL_YPROG + h * 16 + ROLE * 4) : OOB, 0);
  EVDUMP();
  };
  if (wave == 0) run(std::integral_constant<int, 0>());
  else if (wave == 1) run(std::integral_constant<int, 1>());
  else if (wave == 2) run(std::integral_constant<int, 2>());
  else run(std::integral_constant<int, 3>());
  RT(7);
}

#define UL(p, b, k, o) "buffer_load_dwordx4 " p "[" b "+4*" #k ":" b "+4*" #k "+3], %[u" #o ", %[D], %[su] offen offset:(" #k "&3)*1024 sc1\n\t"
#define UL4(p, b, k0, k1, k2, k3, o) UL(p, b, k0, o) UL(p, b, k1, o) UL(p, b, k2, o) UL(p, b, k3, o)
#define ULA(...) UL4("a", "160", __VA_ARGS__)
#define ULV(...) UL4("v", "-32", __VA_ARGS__)
#define ULD0 ULA(0, 1, 2, 3, 0]) ULA(4, 5, 6, 7, 1]) ULA(8, 9, 10, 11, 2])
#define ULD1 ULA(12, 13, 14, 15, 3]) ULA(16, 17, 18, 19, 4]) ULA(20, 21, 22, 23, 5]) ULV(24, 25, 26, 27, 6]) ULV(28, 29, 30, 31, 7])
#define UX1(i, p, b, T, d) i " a[8*" #T "+2*" #d "], " p "[" b "+8*" #T "+" #d "]\n\t" i " a[8*" #T "+2*" #d "+1], " p "[" b "+8*" #T "+4+" #d "]\n\t"
#define UX(i, p, b, T) UX1(i, p, b, T, 0) UX1(i, p, b, T, 1) UX1(i, p, b, T, 2) UX1(i, p, b, T, 3)
#define UW(n, T) "s_waitcnt vmcnt(" n "-2*" #T ")\n\t"
#define UMA(T) UX("v_accvgpr_mov_b32", "a", "160", T)
#define UMV(T) UX("v_accvgpr_write_b32", "v", "-32", T)
#define UM0(T) UW("38", T) UMA(T)
#define UQ(j, T) "7" #j ":\n\ts_getreg_b32 %[k], hwreg(HW_REG_IB_STS)\n\ts_and_b32 %[r], %[k], 15\n\ts_lshr_b32 %[k], %[k], 18\n\ts_and_b32 %[k], %[k], 0x30\n\t" \
                 "s_or_b32 %[k], %[k], %[r]\n\ts_cmp_le_u32 %[k], 39-2*" #T "\n\ts_cbranch_scc1 8" #j "f\n\ts_memrealtime vcc\n\ts_waitcnt lgkmcnt(0)\n\t"    \
                 "s_sub_u32 %[r], %[tt], vcc_lo\n\ts_cmp_gt_i32 %[r], " XSTR(UDL) "\n\ts_cbranch_scc1 7" #j "b\n\ts_mov_b32 %[sx], " #j "\n\t" MCA(10000) "s_branch 75f\n8" #j ":\n\t" UW("39", T)
#define DZERO ".rept 32\n\tv_mov_b64 v[192+2*\\+:193+2*\\+], 0\n\t.endr\n\tv_mov_b64 v[60:61], 0\n\tv_mov_b64 v[62:63], 0\n\t"
#define YLD(k, o) "buffer_load_dwordx4 a[128+4*" #k ":128+4*" #k "+3], %[y" #o ", %[D], %[sy] offen offset:(" #k "&3)*1024 sc1\n\t"
#define YPO(k, o) "buffer_store_dwordx4 %[one], %[y" #o ", %[D], %[sy] offen offset:(" #k "&3)*1024 sc1\n\t"
#define ELD(T, o) "buffer_load_dwordx4 v[128+4*" #T ":128+4*" #T "+3], %[e" #o ", %[D], %[sq] offen offset:(" #T "&3)*1024 sc1\n\t"
#define PLD(G) "buffer_load_dwordx4 v[96+4*" #G ":96+4*" #G "+3], %[pm], %[D], %[sq] offen offset:" #G "*1024 sc1\n\t"
#define ALD(G) "buffer_load_dwordx4 v[112+4*" #G ":112+4*" #G "+3], %[al], %[D], %[sq] offen offset:" #G "*256 sc1\n\t"
#define VW(X, n) "s_waitcnt vmcnt(1+" n ")\n\t"
#define EM(T) "v_smfmac_f32_16x16x64_bf16 v[192+4*" T ":192+4*" T "+3], v[128+4*" T ":128+4*" T "+3], a[8*" T ":8*" T "+7], %[ie]\n\t"
#define WC0(T) "v_cvt_pk_bf16_f32 v[64+2*" #T "], v[192+4*" #T "], v[192+4*" #T "+1]\n\t"
#define WC1(T) "v_cvt_pk_bf16_f32 v[65+2*" #T "], v[192+4*" #T "+2], v[192+4*" #T "+3]\n\t"
#define WCV(T) WC0(T) WC1(T)
#define EM2_0(T, C, o) VW("0", "30-" #T) EM(#T) WC0(C) EM("(" #T "+1)") WC1(C)

#define PMM(X, G) VW(X, "7-" #G) "v_smfmac_f32_16x16x64_bf16 v[60:63], v[96+4*" #G ":96+4*" #G "+3], a[128+8*" #G ":128+8*" #G "+7], %[ip]\n\t"
#define AMM(X, G) VW(X, "3-" #G) "v_smfmac_f32_16x16x64_bf16 v[60:63], v[112+4*" #G ":112+4*" #G "+3], v[64+8*" #G ":64+8*" #G "+7], %[ia]\n\t"
#define TRY(X, EM2, a, b, c, d)                                                                                                       \
      SPIN(tt, a, b, c) MEV(1) MCY(0)                                                                                                \
      "buffer_load_dword v59, %[es], %[D], %[sq] offen sc1\n\t" ELD(0, 0]) ELD(1, 0]) ELD(2, 0]) ELD(3, 0]) ELD(4, 1]) ELD(5, 1]) ELD(6, 1]) ELD(7, 1])                                        \
      ELD(8, 2]) ELD(9, 2]) ELD(10, 2]) ELD(11, 2]) ELD(12, 3]) ELD(13, 3]) ELD(14, 3]) ELD(15, 3])                                  \
      YLD(0, 0]) YLD(1, 0]) YLD(2, 0]) YLD(3, 0]) YLD(4, 1]) YLD(5, 1]) YLD(6, 1]) YLD(7, 1])                                        \
      PLD(0) PLD(1) PLD(2) PLD(3) ALD(0) ALD(1) ALD(2) ALD(3) MCY(1) "buffer_load_dword %[ub], %[uq], %[D], 0 offen sc1\n\ts_cmp_lg_u32 %[sx], 4\n\ts_cbranch_scc1 76f\n77:\n\t"                                                                 \
      VW("0", "32") "v_cmp_eq_u32 vcc, -1, v59\n\t" VW("0", "30") "s_cbranch_vccnz 61f\n62:\n\t" EM("0") MCY(2) EM("1")                                                                                           \
      EM2(2, 0, 0]) EM2(4, 1, 0]) EM2(6, 2, 0]) EM2(8, 3, 0]) EM2(10, 4, 1]) EM2(12, 5, 1]) EM2(14, 6, 1]) MCY(3)                           \
      "s_cmp_lt_i32 %[r], -" XSTR(LTOL) "\n\ts_cbranch_scc0 " d "f\n\t"                              \
      "63:\n\ts_add_u32 %[tq], %[tq], " XSTR(GRID) "\n\ts_add_u32 %[t0], %[t0], " XSTR(XT) "\n\ts_add_u32 %[tn], %[tn], " XSTR(GRID) "+" XSTR(XT) "\n\ts_add_u32 %[r], %[r], " XSTR(GRID) "\n\t"   \
      "s_cmp_lt_i32 %[r], -" XSTR(LTOL) "\n\ts_cbranch_scc1 63b\n\t" MCA(1000)                                                        \
      d ":\n\t" WCV(7) WCV(8) WCV(9) WCV(10) WCV(11) WCV(12) WCV(13) WCV(14) WCV(15)                                                 \
      PMM(X, 0) MCY(4) PMM(X, 1) PMM(X, 2) PMM(X, 3) AMM(X, 0) AMM(X, 1) AMM(X, 2) AMM(X, 3) MCY(5)                                  \
      PADN "s_nop 6\n\t"                                                                                                             \
      "v_pk_add_f32 v[60:61], v[60:61], v[62:63]\n\t"                                                                                \
      "v_cvt_pk_bf16_f32 v62, v60, v61\n\tv_cmp_u_f32 vcc, v60, v61\n\ts_nop 0\n\t"                                                  \
      "v_mov_b32_dpp v63, v62 quad_perm:[1,0,3,2] row_mask:0xf bank_mask:0xf\n\t"                                                    \
      "v_perm_b32 v61, v63, v62, %[sel]\n\ts_cbranch_vccnz 50f\n\t"
#define MEV(n)
#define MEVPUT
#define MCY(n)
#define MCYPUT
#define MCYO
#define MCN
#define MCA(k)
#define FME
#define FML
#define FMZ
#define VR8(p, b) p #b "0", p #b "1", p #b "2", p #b "3", p #b "4", p #b "5", p #b "6", p #b "7", p #b "8", p #b "9"
__device__ __forceinline__ void role_mix(const Args& a, rsrc_t R, const Go& go, int m, int wave, int lane) {
  if (wave >= 2) {
    unsigned t[4];
#pragma unroll
    for (int k = 0; k < 4; ++k) {
      const int u = ((m * 2 + wave - 2) * 4 + k) * 64 + lane;
      t[k] = ld32(R, u < 8192 ? (unsigned)u * 128u : (u < 12800 ? OFF_YR + (unsigned)(u - 8192) * 128u : (u < 17408 ? OFF_YR + YSLOT_REAL + (unsigned)(u - 12800) * 128u : OOB)), 0);
    }
    asm volatile("" :: "v"(t[0]), "v"(t[1]), "v"(t[2]), "v"(t[3]));
  }
  if (wave != 0 && (wave != 2 || m != 0)) return;
  const int l15 = lane & 15, g = lane >> 4;
  if (!go_wait(R, go, lane)) return;
  RT(3);
  if (wave == 2) {
    for (int k = 0; k < TSL; ++k) __builtin_amdgcn_s_sleep(32);
    wait_flag(R, FL_MRDY + min(lane, 35) * 4, 0);
    int c = -1;
    wait_flag_c<1>(R, FL_WP + lane * 32, 0, c);
    st32((rt_now() + T1D) & ~1u, R, lane < 16 ? (unsigned)(FL_AN + lane * 128) : OOB, 0);
    return;
  }
  for (int k = 0; k < CSL; ++k) __builtin_amdgcn_s_sleep(32);
  const int cls = get_cls(R, lane);
  if (cls < 0) return;
  RT(4);
  const unsigned fo = (unsigned)(cls & 1) * 8192u, y0 = ywin(cls, m) + lane * 16;
  const u32x4 one = {~0u, ~0u, ~0u, ~0u};
  const unsigned mc = (unsigned)nearoff(m * 256 + l15 * 16) + fo + (g & 1) * CPY1 + (g >> 1) * CPY2;
#pragma unroll
  for (int k = 0; k < 8; ++k) st128(one, R, y0 + k * 1024, YSLOT_REAL);
  st128(one, R, mc, SLOT_REAL);
  RT(1);
  unsigned uo[8], eo[4];
#pragma unroll
  for (int k = 0; k < 8; ++k) uo[k] = OFF_ZF + (unsigned)(m * 40960 + k * 4096 + lane * 16);
#pragma unroll
  for (int k = 0; k < 4; ++k) {
    eo[k] = (unsigned)nearoff(NEAR_E + k * 4096 + lane * 16) + fo;
  }
  const unsigned pm = (unsigned)nearoff(NEAR_PM + lane * 16) + fo;
  const int qi = 2 * (l15 >> 2) + (l15 & 1);
  const unsigned al = !(l15 & 2) && (g & 1) == (qi >> 2) ? (unsigned)nearoff(NEAR_AL + ((g >> 1) * 8 + qi) * 16) + fo : OOB;
  const unsigned mo = (unsigned)nearoff((2 * m + (l15 >> 3)) * 128 + (2 * g + (l15 & 1)) * 16 + ((l15 & 7) >> 1) * 4) + fo;
  const unsigned sm = lane < 36 ? (unsigned)nearoff(lane * 256) + fo : OOB, own = own_cpy(cls, m / 9);
  const unsigned an = FL_AN + (m & 15) * 128, wp = FL_WP + lane * 32;
  const unsigned ie = (lane & 4) ? 0xEEEE : 0x4444, ip = (lane & 2) ? 0xEEEE : 0x4444, ia = ie;
  const unsigned sel = (lane & 1) ? 0x03020706u : 0x05040100u;
  const i32x4 D = mkdesc(a.wb);
  const unsigned uf = FL_UPROG + (lane & 31) * 128 + (m / 9) * 4;
  for (unsigned spins = 0; spins < SPINCAP; ++spins) {
    CBAR();
    if (__all(ld32(R, uf, 0) != ~0u)) break;
    __builtin_amdgcn_s_sleep(16);
  }
  for (unsigned spins = 0; spins < SPINCAP; ++spins) {
    CBAR();
    if (__all(ld32(R, OFF_ZF + (unsigned)(m * 40960 + (lane & 31) * 1024 + 1020), 0) != ~0u)) break;
    __builtin_amdgcn_s_sleep(3);
  }
  RT(2);
  unsigned tq = 0, lev = PACE0, bud = SPINCAP, s = 0, su = 0, sy = 0, sp = 0, tt, sa, sb, sn, tn, ln, ue, t0, sx, sq, cw, sv, ua = ~0u, ub, uq, nr = 0, cv = 0;
  int ufl = -1;
  unsigned ev0 = 0, ev1 = 0, ev2 = 0, ev3 = 0, ec[7] = {0, 0, 0, 0, 0, 0, 0};
  int r, k;
  asm volatile(
      "29:\n\t" ULD0 ULD1 "s_mov_b32 %[t0], 0\n\ts_mov_b32 %[sb], 0\n\ts_mov_b32 %[ue], 2*" XSTR(SLOT_REAL) "\n\ts_waitcnt vmcnt(0)\n\t"
      "30:\n\t" FMZ DZERO UM0(0) UM0(1) UM0(2) UM0(3) UM0(4) UM0(5) "s_waitcnt vmcnt(28)\n\t"
      "buffer_store_dwordx4 %[one], %[mc], %[D], %[ue] offen sc1\n\ts_mov_b32 %[sx], 0\n\t"
      "v_max_i32 %[ufl], %[ufl], %[ua]\n\ts_add_u32 %[su], %[s], 1\n\ts_and_b32 %[su], %[su], " XSTR(RMASK) "\n\ts_mul_i32 %[su], %[su], 36*40960\n\t"
      "s_add_u32 %[sn], %[sp], " XSTR(SLOT_REAL) "\n\ts_and_b32 %[sn], %[sn], 7*" XSTR(SLOT_REAL) "\n\t"
      "s_lshr_b32 %[sq], %[sp], 5\n\ts_and_b32 %[sq], %[sq], " XSTR(CPY1) "\n\ts_xor_b32 %[sq], %[sq], %[own]\n\ts_add_u32 %[sq], %[sq], %[sp]\n\t"
      "s_cmp_lg_u32 %[s], 0\n\ts_cbranch_scc0 31f\n\t"
      "35:\n\tv_cmp_eq_u32 vcc, -1, %[sv]\n\ts_cbranch_vccz 32f\n\t" MCA(100) BUD("36f") "s_branch 32f\n\t"
      "36:\n\ts_add_u32 %[tq], %[tq], " XSTR(GRID) "+" XSTR(XT) "\n\ts_xor_b32 %[sa], %[sa], %[flp]\n\t"
      SPIN(tq, "37", "38", "39") "buffer_load_dword %[sv], %[sm], %[D], %[sa] offen sc1\n\ts_waitcnt vmcnt(0)\n\ts_branch 35b\n\t"
      "31:\n\tv_mov_b32 %[cw], 0\n\tbuffer_store_dword %[cw], %[rdy], %[D], 0 offen sc1\n\t"
      "64:\n\tbuffer_load_dword %[cw], %[wp], %[D], 0 offen sc1\n\tbuffer_load_dword %[ufl], %[uf], %[D], 0 offen sc1\n\t"
      "s_waitcnt vmcnt(0)\n\tv_cmp_gt_i32 vcc, 0, %[cw]\n\ts_cbranch_vccz 65f\n\t"
      "s_sleep 1\n\t" BUD("64b")
      "65:\n\ts_memrealtime vcc\n\ts_waitcnt lgkmcnt(0)\n\ts_sub_u32 %[tq], vcc_lo, " XSTR(DE0) "-" XSTR(D0) "\n\ts_branch 32f\n\t"
      "59:\n\ts_waitcnt lgkmcnt(0)\n\ts_lshr_b32 %[tt], %[lev], 8\n\ts_sub_u32 %[k], vcc_lo, %[tt]\n\ts_sub_u32 %[k], %[k], " XSTR(RTOL) "\n\t"
      "34:\n\ts_add_u32 %[tq], %[tq], " XSTR(GRID) "\n\ts_add_u32 %[t0], %[t0], " XSTR(XT) "\n\ts_sub_u32 %[r], %[tq], %[k]\n\ts_cmp_lt_i32 %[r], 0\n\ts_cbranch_scc1 34b\n\t"
      "32:\n\t"
      "v_cmp_ge_i32 vcc, %[s], %[ufl]\n\tv_mov_b32 %[uq], " XSTR(OOB) "\n\tv_cndmask_b32 %[uq], %[uq], %[uf], vcc\n\t"
      "s_and_b32 %[tt], %[lev], 255\n\ts_add_u32 %[tn], %[tq], %[tt]\n\t"
      "s_add_u32 %[tn], %[tn], %[t0]\n\t"
      "s_movk_i32 %[ln], " XSTR(PACEC) "\n\ts_cmp_lt_u32 %[s], " XSTR(S2) "-1\n\ts_cmovk_i32 %[ln], " XSTR(PACEB) "\n\t"
      "s_cmp_lt_u32 %[s], " XSTR(S1) "-1\n\ts_cmovk_i32 %[ln], " XSTR(PACEA) "\n\ts_cmp_lt_u32 %[s], " XSTR(S0) "-1\n\ts_cmovk_i32 %[ln], " XSTR(PACE0) "\n\t"
      "s_add_u32 %[sa], %[sn], " XSTR(CPY1) "\n\ts_xor_b32 %[sb], %[sn], " XSTR(CPY2) "\n\t"
      "s_lshr_b32 %[tt], %[lev], 8\n\ts_add_u32 %[tt], %[tt], %[tq]\n\t"
      "s_cmp_eq_u32 %[sx], 4\n\ts_cbranch_scc1 75f\n\t"
      UQ(0, 8) UMA(6) UMA(7) UMA(8) UQ(1, 11) UMA(9) UMA(10) UMA(11) UQ(2, 13) UMV(12) UMV(13) UQ(3, 15) UMV(14) UMV(15) "s_mov_b32 %[sx], 4\n\t" MEV(3)
      "75:\n\t" TRY("0", EM2_0, "40", "41", "42", "43")
      "51:\n\tbuffer_store_dword v61, %[mo], %[D], %[sn] offen sc1\n\tbuffer_store_dword v61, %[mo], %[D], %[sa] offen sc1\n\ts_memrealtime vcc\n\t" MEV(2) MCY(6)
      "buffer_store_dword v61, %[mo], %[D], %[sb] offen sc1\n\ts_add_u32 %[sb], %[sb], " XSTR(CPY1) "\n\tbuffer_store_dword v61, %[mo], %[D], %[sb] offen sc1\n\t"
      "s_waitcnt vmcnt(4) lgkmcnt(0)\n\ts_add_u32 %[k], vcc_lo, " XSTR(MGAP) "\n"
      "97:\n\ts_sub_u32 %[r], %[tn], %[k]\n\ts_cmp_lt_i32 %[r], 0\n\ts_cbranch_scc0 98f\n\ts_add_u32 %[tn], %[tn], " XSTR(GRID) "+" XSTR(XT) "\n\ts_branch 97b\n"
      "98:\n\t" MEVPUT MCYPUT MCN
      "s_add_u32 %[s], %[s], 1\n\ts_cmp_ge_u32 %[s], 63\n\ts_cbranch_scc1 60f\n\t"
      "v_max_i32 %[ufl], %[ufl], %[ub]\n\tv_cmp_gt_i32 vcc, %[s], %[ufl]\n\ts_mov_b32 %[sx], 1\n\ts_cbranch_vccnz 100f\n\t"
      "s_mov_b32 %[sx], 0\n\t" ULD0 "100:\n\t"
      "s_cmp_lg_u32 %[s], 1\n\ts_cbranch_scc1 67f\n\t"
      "66:\n\tbuffer_load_dword %[cw], %[an], %[D], 0 offen sc1\n\ts_waitcnt vmcnt(0)\n\tv_readfirstlane_b32 %[tn], %[cw]\n\t"
      "s_cmp_lg_u32 %[tn], -1\n\ts_cbranch_scc1 67f\n\t" BUD("66b")
      "67:\n\t"
      "s_xor_b32 %[ue], %[sn], 2*" XSTR(SLOT_REAL) "\n\ts_and_b32 %[ue], %[ue], 3*" XSTR(SLOT_REAL) "\n\ts_mov_b32 %[sp], %[sn]\n\t"
      "s_mov_b32 %[tq], %[tn]\n\ts_mov_b32 %[t0], 0\n\ts_mov_b32 %[lev], %[ln]\n\t"
      "v_cmp_ge_i32 vcc, %[s], %[ufl]\n\tv_mov_b32 %[ua], " XSTR(OOB) "\n\tv_cndmask_b32 %[ua], %[ua], %[uf], vcc\n\t"
      "s_lshr_b32 %[sa], %[sn], 5\n\ts_and_b32 %[sa], %[sa], " XSTR(CPY1) "\n\ts_xor_b32 %[sa], %[sa], %[own]\n\ts_add_u32 %[sa], %[sa], %[sn]\n\t"
      SPIN(tq, "52", "53", "54") "buffer_load_dword %[sv], %[sm], %[D], %[sa] offen sc1\n\t"
      "buffer_load_dword %[ua], %[ua], %[D], 0 offen sc1\n\t"
      "s_cmp_eq_u32 %[sx], 0\n\ts_cbranch_scc1 101f\n\t"
      "55:\n\ts_waitcnt vmcnt(0)\n\tv_max_i32 %[ufl], %[ufl], %[ua]\n\tv_cmp_gt_i32 vcc, %[s], %[ufl]\n\ts_cbranch_vccz 57f\n\t" MCA(100000)
      "buffer_load_dword %[ua], %[uf], %[D], 0 offen sc1\n\t" BUD("55b")
      "57:\n\t" ULD0 "101:\n\t" ULD1 YPO(0, 0]) YPO(1, 0]) YPO(2, 0]) YPO(3, 0]) YPO(4, 1]) YPO(5, 1]) YPO(6, 1]) YPO(7, 1])
      "s_xor_b32 %[sy], %[sy], " XSTR(YSLOT_REAL) "\n\ts_branch 30b\n\t"
      "58:\n\ts_cmp_lg_u32 %[s], 0\n\ts_cbranch_scc1 102f\n\ts_mov_b32 %[su], 0\n\ts_branch 29b\n102:\n\ts_memrealtime vcc\n\ts_xor_b32 %[sq], %[sq], %[flp]\n\t" DZERO "s_branch 59b\n\t"
      "50:\n\t" FML MCA(1) BUD("58b") "v_min_u32 v61, -2, v61\n\ts_branch 51b\n\t"
      "99:\n\ts_memrealtime vcc\n\ts_xor_b32 %[sq], %[sq], %[flp]\n\ts_branch 59b\n\t"
      "61:\n\t" FME MCA(1) BUD("99b") "s_branch 62b\n\t"
      "76:\n\ts_cmp_eq_u32 %[sx], 3\n\ts_cbranch_scc1 93f\n\ts_cmp_eq_u32 %[sx], 2\n\ts_cbranch_scc1 92f\n\ts_cmp_eq_u32 %[sx], 1\n\ts_cbranch_scc1 91f\n\t"
      UW("73", 6) UMA(6) UW("73", 7) UMA(7) UW("73", 8) UMA(8) "91:\n\t" UW("73", 9) UMA(9) UW("73", 10) UMA(10) UW("73", 11) UMA(11)
      "92:\n\t" UW("73", 12) UMV(12) UW("73", 13) UMV(13) "93:\n\t" UW("73", 14) UMV(14) UW("73", 15) UMV(15) MEV(3)
      "s_mov_b32 %[sx], 4\n\ts_nop 1\n\ts_branch 77b\n\t"
      "60:\n\ts_waitcnt vmcnt(0)"
      : [tq] "+&s"(tq), [lev] "+&s"(lev), [bud] "+&s"(bud), [s] "+&s"(s), [su] "+&s"(su), [sy] "+&s"(sy), [sp] "+&s"(sp), [tt] "=&s"(tt), [sa] "=&s"(sa),
        [sn] "=&s"(sn), [sb] "=&s"(sb), [tn] "=&s"(tn), [ln] "=&s"(ln), [ue] "=&s"(ue), [t0] "=&s"(t0), [sx] "=&s"(sx), [sq] "=&s"(sq), [cw] "=&v"(cw), [sv] "=&v"(sv), [ua] "+&v"(ua), [ub] "=&v"(ub), [uq] "=&v"(uq), [ufl] "+&v"(ufl), [r] "=&s"(r), [k] "=&s"(k), [nr] "+&s"(nr), [cv] "+&v"(cv),
        [ev0] "+&v"(ev0), [ev1] "+&v"(ev1), [ev2] "+&v"(ev2), [ev3] "+&v"(ev3) MCYO
      : [D] "s"(D), [u0] "v"(uo[0]), [u1] "v"(uo[1]), [u2] "v"(uo[2]), [u3] "v"(uo[3]), [u4] "v"(uo[4]), [u5] "v"(uo[5]), [u6] "v"(uo[6]), [u7] "v"(uo[7]), [e0] "v"(eo[0]), [e1] "v"(eo[1]), [e2] "v"(eo[2]),
        [e3] "v"(eo[3]), [y0] "v"(y0), [y1] "v"(y0 + 4096), [pm] "v"(pm), [al] "v"(al), [mo] "v"(mo), [mc] "v"(mc), [an] "v"(an), [wp] "v"(wp),
        [ie] "v"(ie), [ip] "v"(ip), [ia] "v"(ia), [sel] "v"(sel), [one] "v"(one), [sm] "v"(sm), [uf] "v"(uf), [es] "v"(lane < 32 ? (unsigned)nearoff(NEAR_E + lane * 512 + (lane & 1) * (64 - 512)) + fo : OOB), [rdy] "v"(lane == 0 ? (unsigned)(FL_MRDY + m * 4) : OOB), [own] "s"(own), [flp] "s"(CPY2 + CPY1)
      : "memory", "vcc", "scc", "s68", "s69", "s70", "s71", "s72", "s73", "s74", "s75", "s76", "s77", "s78", "s79", "s80", "s81", "s84", "s85", "s86", "s87", "s88", "s89", "s90", "s91",
        "v59", VR8("v", 6), VR8("v", 7), VR8("v", 8), VR8("v", 9), VR8("v", 10), VR8("v", 11), VR8("v", 12), VR8("v", 13), VR8("v", 14), VR8("v", 15),
        VR8("v", 16), VR8("v", 17), VR8("v", 18), VR8("v", 19), VR8("v", 20), VR8("v", 21), VR8("v", 22), VR8("v", 23), VR8("v", 24),
        "v250", "v251", "v252", "v253", "v254", "v255",
        "a0", "a1", "a2", "a3", "a4", "a5", "a6", "a7", "a8", "a9", VR8("a", 1), VR8("a", 2), VR8("a", 3), VR8("a", 4), VR8("a", 5), VR8("a", 6),
        VR8("a", 7), VR8("a", 8), VR8("a", 9), VR8("a", 10), VR8("a", 11), VR8("a", 12), VR8("a", 13), VR8("a", 14), VR8("a", 15),
        VR8("a", 16), VR8("a", 17), VR8("a", 18), VR8("a", 19), VR8("a", 20), VR8("a", 21), VR8("a", 22), VR8("a", 23), VR8("a", 24),
        "a250", "a251", "a252", "a253", "a254", "a255");
  RT(7);
}

#define XROW 1184
struct XL { rsrc_t X; unsigned vo; int wr, rd; };
__device__ __forceinline__ void x_init(XL& xl, const u16* x, int wave, int lane) {
  xl.X = __builtin_amdgcn_make_buffer_rsrc((void*)x, 0, S_LEN * DM * 2, 0x00020000);
  xl.vo = (unsigned)((wave * 8 + (lane >> 3)) * 1152 + (lane & 7) * 16);
  xl.wr = (wave * 8 + (lane >> 3)) * XROW + (lane & 7) * 16;
  xl.rd = (lane & 15) * XROW + (lane >> 4) * 16;
}
__device__ __forceinline__ u32x4 x_ld1(const XL& xl, int s, int k) { return __builtin_amdgcn_raw_buffer_load_b128(xl.X, xl.vo + k * 128, (unsigned)s * 36864u, 0); }
__device__ __forceinline__ void x_gload(u32x4 (&xr)[9], const XL& xl, int s) {
#pragma unroll
  for (int k = 0; k < 9; ++k) xr[k] = x_ld1(xl, s, k);
}
__device__ __forceinline__ void x_swrite(const u32x4 (&xr)[9], lds_char* buf, const XL& xl) {
#pragma unroll
  for (int k = 0; k < 9; ++k) *(lds_u32x4*)(buf + xl.wr + k * 128) = xr[k];
}
#define XF(buf, tt, ks) (*(lds_u32x4*)((buf) + (tt) * (16 * XROW) + (ks) * 64))
__device__ __forceinline__ void proj4(lds_char* buf, lds_char* nbuf, u32x4 (&xr)[9], const XL& xl, int snext, int wave, int lane,
                                      const u32x4 (&w0)[18], const u32x4 (&w1)[18], const u32x4 (&w2)[18], const u32x4 (&w3)[18],
                                      f32x4 (&acc)[4][2]) {
  u32x4 bb[3][2];
  buf += xl.rd; nbuf += xl.wr;
#pragma unroll
  for (int ks = 0; ks < 2; ++ks) { bb[ks][0] = XF(buf, 0, ks); bb[ks][1] = XF(buf, 1, ks); }
#pragma unroll
  for (int t = 0; t < 4; ++t) { acc[t][0] = (f32x4){0.f, 0.f, 0.f, 0.f}; acc[t][1] = acc[t][0]; }
#pragma unroll
  for (int ks = 0; ks < 18; ++ks) {
    const u32x4 b0 = bb[ks % 3][0], b1 = bb[ks % 3][1];
    SB();
    acc[0][0] = mfma(w0[ks], b0, acc[0][0]); SB();
    if (ks + 2 < 18) bb[(ks + 2) % 3][0] = XF(buf, 0, ks + 2);
    SB();
    acc[0][1] = mfma(w0[ks], b1, acc[0][1]); SB();
    if (ks + 2 < 18) bb[(ks + 2) % 3][1] = XF(buf, 1, ks + 2);
    SB();
    acc[1][0] = mfma(w1[ks], b0, acc[1][0]); SB();
    if ((ks & 1) == 0) *(lds_u32x4*)(nbuf + (ks >> 1) * 128) = xr[ks >> 1];
    SB();
    acc[1][1] = mfma(w1[ks], b1, acc[1][1]);
    acc[2][0] = mfma(w2[ks], b0, acc[2][0]); SB();
    if ((ks & 1) == 0) xr[ks >> 1] = x_ld1(xl, snext, ks >> 1);
    SB();
    acc[2][1] = mfma(w2[ks], b1, acc[2][1]);
    acc[3][0] = mfma(w3[ks], b0, acc[3][0]); acc[3][1] = mfma(w3[ks], b1, acc[3][1]);
    SB();
  }
}
#define PFL 13
#define STG0 6
#define STGD 0
#define NOST 0
#define NOFL 0
#define UFL 5
__device__ __forceinline__ int clampseg(int s) { return s < NSEG ? s : NSEG - 1; }

__device__ __forceinline__ void role_prj(const Args& a, rsrc_t R, const Go& go, lds_char* lds, const u16* W0, const u16* W1, unsigned o0, unsigned o1, unsigned flag,
                                         int tid, int wave, int lane) {
  const int l15 = lane & 15, g = lane >> 4;
  u32x4 w[2][2][18];
  u32x4 xr[9], xq[9];
  XL xl; x_init(xl, a.x, wave, lane);
  x_gload(xq, xl, 0);
  x_gload(xr, xl, 1);
  const unsigned fo = wave * 1024 + lane * 16;
  const unsigned pf = lane == 0 ? flag + wave * 4 : OOB;
  f32x4 acc[2][2][2];
  {
    u32x4 sg[PDEP][8];
    const rsrc_t B0 = __builtin_amdgcn_make_buffer_rsrc((void*)W0, 0, DM * HD * 2, 0x00020000), B1 = __builtin_amdgcn_make_buffer_rsrc((void*)W1, 0, DM * HD * 2, 0x00020000);
    const unsigned wof = (unsigned)((tid >> 4) * (HD * 2) + (tid & 15) * 16);
    auto gl = [&](int i, int k) {
      if (i < 10 && (i % 5 < 4 || k < 4)) { sg[i % PDEP][k] = __builtin_amdgcn_raw_buffer_load_b128(i < 5 ? B0 : B1, wof, (unsigned)(128 * (i % 5) + 16 * k) * (HD * 2), (NTM & 4) ? 2 : 0); SB(); }
    };
#pragma unroll
    for (int k = 0; k < 8 * PDEP; ++k) gl(k >> 3, k & 7);
    if (wave == 0) WALK(100); else if (wave == 1) WALK(101); else if (wave == 2) WALK(102); else WALK(103);
    RT(2);
    PADS(100);
    lds_char *x0p = lds + X0_OFF, *x1p = lds + X1_OFF;
    pin_v(x0p); pin_v(x1p);
    x_swrite(xq, x0p, xl);
    PAD();
    x_swrite(xr, x1p, xl);
    PAD();
    x0p += xl.rd; x1p += xl.rd;
    pin_v(x0p); pin_v(x1p);
    lds_char *wp = lds + (tid >> 4) * 288 + (tid & 15) * 16, *rp = lds + (g * 8 + (l15 >> 2)) * 288 + wave * 64 + (l15 & 3) * 8;
    pin_v(wp); pin_v(rp);
    auto put = [&](int i, int k) {
      if (i < 10 && (i % 5 < 4 || k < 4)) { *(lds_u32x4*)(wp + (i & 1) * STG_BYTES + k * (16 * 288)) = sg[i % PDEP][k]; SB(); }
    };
#pragma unroll
    for (int k = 0; k < 8; ++k) { put(0, k); gl(PDEP, k); }
    auto chunk = [&](auto IC) {
      constexpr int i = decltype(IC)::value, m = i / 5, c = i % 5, nk = c < 4 ? 4 : 2;
      if (c == 0) {
#pragma unroll
        for (int k = 0; k < 8; ++k) { acc[k >> 2][(k >> 1) & 1][k & 1] = (f32x4){0.f, 0.f, 0.f, 0.f}; pin_v(acc[k >> 2][(k >> 1) & 1][k & 1]); }
      }
      if (i == 4) { PADE(); PADS(101); } else if (i == 8) { PADE(); PADS(102); } else PAD();
      BAR();
      if (i == 0) RT(3);
      if (i == 2) RT(4);
      if (i == 4) RT(5);
      if (i == 6) RT(6);
      if (i == 7) RT(8);
      if (i == 8) RT(9);
      u32x4 xb[2][4];
      u32x2 wh[2][4];
      auto rd = [&](int kk, int j) {
        if (kk >= nk) return;
        if (j & 1) xb[kk & 1][j >> 1] = XF((j & 4) ? x1p : x0p, (j >> 1) & 1, 4 * c + kk);
        else wh[kk & 1][j >> 1] = trd(rp + (i & 1) * STG_BYTES + (kk * 32 + (j & 2) * 2) * 288 + (j >> 2) * 32);
        SB();
      };
#pragma unroll
      for (int j = 0; j < 8; ++j) rd(0, j);
#pragma unroll
      for (int k = 0; k < 8 * nk; ++k) {
        const int kk = k >> 3;
        f32x4& d = acc[(k >> 2) & 1][k & 1][(k >> 1) & 1];
        u32x4& f = w[m][k & 1][4 * c + kk];
        if ((k & 7) < 2) {
          f = (u32x4){wh[kk & 1][(k & 1) * 2][0], wh[kk & 1][(k & 1) * 2][1], wh[kk & 1][(k & 1) * 2 + 1][0], wh[kk & 1][(k & 1) * 2 + 1][1]};
          if (c < 4) pin_a(f); else pin_v(f);
        }
        if (c < 4) mfA(d, f, xb[kk & 1][(k >> 1) & 3]); else mfa(d, f, xb[kk & 1][(k >> 1) & 3]);
        SB();
        rd(kk + 1, k & 7);
        if (k % nk == 0) put(i + 1, k / nk);
        if (k % nk == 1) gl(i + 1 + PDEP, k / nk);
        if (k % 12 == 11 || k == 8 * nk - 1) PAD();
      }
      if (i == 6) x_gload(xq, xl, 2);
      if (i == 7) x_gload(xr, xl, 3);
      if (i == 6) {
        go_wait(R, go, lane);
        st32(0u, R, pf, 0);
      }
      SB();
      if (c == 4) {
        asm volatile("s_nop 7"); SB();
#pragma unroll
        for (int k = 0; k < 4; ++k) {
          PAD();
          const f32x4 (&e)[2][2] = acc[k >> 1];
          const int tt = k & 1;
          st128((u32x4){pk2(e[0][tt][0], e[0][tt][1]), pk2(e[0][tt][2], e[0][tt][3]), pk2(e[1][tt][0], e[1][tt][1]), pk2(e[1][tt][2], e[1][tt][3])}, R,
                (m ? o1 : o0) + fo + tt * 4096, (unsigned)(k >> 1) * 262144u);
        }
        SB();
      }
    };
#define CH(n) chunk(std::integral_constant<int, n>())
    CH(0); CH(1); CH(2); CH(3); CH(4); CH(5); CH(6); CH(7); CH(8); CH(9);
#undef CH
    BAR();
  }
  RT(1);
  PAD();
  x_swrite(xq, lds, xl);
  PAD();
  u32x4 bb[3][2];
  VMW(0);
  st32(3u, R, pf, 0);
  BAR();
#pragma unroll
  for (int ks = 0; ks < 2; ++ks) { bb[ks][0] = XF(lds + xl.rd, 0, ks); bb[ks][1] = XF(lds + xl.rd, 1, ks); }
  __builtin_amdgcn_s_waitcnt(0x0F70);
  lds_char *rd0 = lds + xl.rd, *rd1 = rd0 + STG_BYTES, *wr0 = lds + xl.wr, *wr1 = wr0 + STG_BYTES;
  unsigned oa = o0 + fo, ob = o1 + fo, tc0 = 0, tr0 = 0;
  pin_v(rd0); pin_v(rd1); pin_v(wr0); pin_v(wr1); pin_v(oa); pin_v(ob);
  auto half = [&](auto HC, int s) {
    constexpr int H = decltype(HC)::value & 1, P = decltype(HC)::value >> 1;
    lds_char* buf = P ? rd1 : rd0;
    lds_char* nrd = H ? (P ? rd0 : rd1) : buf;
    lds_char* nwr = P ? wr0 : wr1;
    const unsigned oo = H ? oa : ob;
    unsigned so, sx;
    f32x4 (&c)[2][2] = acc[H];
    f32x4 (&e)[2][2] = acc[H ^ 1];
    u32x4 o;
#define PM(t, tt) do { if (ks == 0) c[t][tt] = mfA0(w[H][t][0], bb[0][tt]); else if (ks < 16) mfA(c[t][tt], w[H][t][ks], bb[ks % 3][tt]); \
                       else mfa(c[t][tt], w[H][t][ks], bb[ks % 3][tt]); SB(); } while (0)
#define PR(tt) do { bb[(ks + 2) % 3][tt] = ks < 16 ? XF(buf, tt, ks + 2) : XF(nrd, tt, ks - 16); SB(); } while (0)
#pragma unroll
    for (int ks = 0; ks < 18; ++ks) {
      const int xi = H * 5 + ((ks - 7) >> 1);
      const bool xs = ks >= 7 && ks < 17 && xi < 9 && (H == 0 || ks < 15);
      if (H == 1 && ks == 16) { __builtin_amdgcn_s_waitcnt(0xC27F); __builtin_amdgcn_s_barrier(); SB(); }
      else {
        const int kw = (ks & 1) ? ks - 2 : ks - 1;
        if (kw >= 7 && kw < (H ? 15 : 17)) __builtin_amdgcn_s_waitcnt(0xC37F); else __builtin_amdgcn_s_waitcnt(0xC27F);
        SB();
      }
      PM(0, 0); PR(0);
      if (ks == 0) {
        so = (unsigned)(s + H - 1) * 262144u;
        sx = (unsigned)clampseg(s + 2) * 36864u;
        asm volatile("" : "+s"(so), "+s"(sx)); SB();
      }
      PM(0, 1); PR(1);
      PM(1, 0);
      if (ks == 1 || ks == 2) { o[(ks - 1) * 2] = pkv(e[ks - 1][0][0], e[ks - 1][0][1]); SB(); }
      if (ks == 4 || ks == 5) { o[(ks - 4) * 2] = pkv(e[ks - 4][1][0], e[ks - 4][1][1]); SB(); }
      if (ks == 3 && NOST < 2) { st128(o, R, oo, so); SB(); }
      if (ks == 6 && NOST < 2) { st128(o, R, oo + 4096, so); SB(); }
      if (xs && (ks & 1)) { *(lds_u32x4*)(nwr + xi * 128) = xr[xi < 9 ? xi : 0]; SB(); }
      if (xs && !(ks & 1)) { xr[xi < 9 ? xi : 0] = __builtin_amdgcn_raw_buffer_load_b128(xl.X, xl.vo + xi * 128, sx, 0); SB(); }
      PM(1, 1);
      if (ks % 3 == 0) { if (H == 1 && P == 0 && ks == 0) { PADE(); PADS(103); } else PAD(); SB(); }
      if (ks == 1 || ks == 2) { o[(ks - 1) * 2 + 1] = pkv(e[ks - 1][0][2], e[ks - 1][0][3]); SB(); }
      if (ks == 4 || ks == 5) { o[(ks - 4) * 2 + 1] = pkv(e[ks - 4][1][2], e[ks - 4][1][3]); SB(); }
      if (ks == PFL) {
        asm volatile("s_waitcnt vmcnt(%0)" :: "n"((H ? 8 : 7) + (PFL - 6) / 2) : "memory");
        st32((unsigned)max(2 * s + H - 2, 3), R, pf, 0); SB();
      }
      if (ks == 17 && (P == 0) == (H == 1) && UNLIKELY(s < 4)) { __builtin_amdgcn_s_waitcnt(0x0F70); st32((unsigned)(2 * s + H - 1), R, pf, 0); SB(); }
    }
#undef PM
#undef PR
  };
#pragma nounroll
  for (int s = 2; s < NSEG; ++s) {
    half(std::integral_constant<int, 0>(), s);
    half(std::integral_constant<int, 1>(), s);
    STEPRT(0, 0);
    ++s;
    half(std::integral_constant<int, 2>(), s);
    half(std::integral_constant<int, 3>(), s);
    STEPRT(0, 0);
  }
  PADE();
  mf_wait2(acc[1][0][0], acc[1][0][1]); mf_wait2(acc[1][1][0], acc[1][1][1]);
#pragma unroll
  for (int tt = 0; tt < 2; ++tt) {
    const u32x4 o = {pk2(acc[1][0][tt][0], acc[1][0][tt][1]), pk2(acc[1][0][tt][2], acc[1][0][tt][3]), pk2(acc[1][1][tt][0], acc[1][1][tt][1]), pk2(acc[1][1][tt][2], acc[1][1][tt][3])};
    st128(o, R, o1 + fo + tt * 4096, (unsigned)((NSEG - 1) & RMASK) * 262144u);
  }
  VMW(0);
  st32((unsigned)(2 * NSEG - 1), R, pf, 0);
  RT(7);
}

__device__ __forceinline__ void role_px(const Args& a, rsrc_t R, const Go& go, int pi, int wave, int lane) {
  const int l15 = lane & 15, g = lane >> 4;
  const int h = pi * 2 + (wave >> 1), qt = wave & 1;
  const int wz = h >> 3, tq = (h >> 1) & 3, hp = h & 1;
  const int gy = ((tq & 1) << 1) | (tq >> 1);
  const unsigned kmo = OFF_KMF + (unsigned)h * 4096u + lane * 16;
  const unsigned pmo = OFF_PXF + (unsigned)((qt * 40 + 32 + wz * 2 + hp) * 1024 + (gy * 16 + l15) * 16 + g * 4);
  const unsigned ppf = (lane == 0) ? (unsigned)(FL_PXPROG + h * 128 + qt * 4) : OOB;
  const unsigned qo = OFF_QF + (unsigned)h * 8192u + qt * 4096 + lane * 16;
  const unsigned k0o = OFF_KF + (unsigned)h * 8192u + lane * 16;
  const unsigned k1o = qt ? k0o + 4096 : OOB;
  const unsigned kmf = lane == 0 ? FL_KMPROG + h * 128 : OOB;
  const int kon = lane == 0 ? 0 : -NSEG;
  int fm = -1;
  RT(1);
  u32x4 qf[4], k0[4], k1[4];
  if (!go_wait(R, go, lane) || !wait_flag_c<16, 1>(R, kmf, kon, fm)) return;
#pragma unroll
  for (int ks = 0; ks < 4; ++ks) { qf[ks] = ld128(R, qo + ks * 1024, 0); k0[ks] = ld128(R, k0o + ks * 1024, 0); k1[ks] = ld128(R, k1o + ks * 1024, 0); }
  __builtin_amdgcn_s_waitcnt(0x0F70);
  for (int s = 0; s < NSEG - 1; ++s) {
    u32x4 km[4], qc[4];
    f32x4 G = {0.f, 0.f, 0.f, 0.f}, T0 = G, T1 = G;
#pragma unroll
    for (int ks = 0; ks < 4; ++ks) { T0 = mfma(k0[ks], qf[ks], T0); T1 = mfma(k1[ks], qf[ks], T1); qc[ks] = qf[ks]; }
    float mx, sx;
    px_mask(T0, T1, qt, lane);
    px_seg(T0, T1, mx, sx);
    if (!wait_flag_c<PXS, 1>(R, kmf, s + kon, fm)) return;
#pragma unroll
    for (int ks = 0; ks < 4; ++ks) km[ks] = ld128(R, kmo + ks * 1024, (unsigned)s * 131072u);
    const unsigned so = (unsigned)clampseg(s + 1) * 262144u;
#pragma unroll
    for (int ks = 0; ks < 4; ++ks) {
      qf[ks] = ld128(R, qo + ks * 1024, so);
      k0[ks] = ld128(R, k0o + ks * 1024, so);
      k1[ks] = ld128(R, k1o + ks * 1024, so);
    }
    CBAR();
    fm = (int)lda(R, kmf);
    VMW(17);
    st32((unsigned)(s - 1), R, ppf, 0);
    CBAR();
#pragma unroll
    for (int ks = 0; ks < 4; ++ks) G = mfma(km[ks], qc[ks], G);
    u32x4 px; unsigned pw;
    px_fin(G, T0, T1, mx, sx, px, pw);
    st128(px, R, OFF_PXF + (unsigned)((qt * 40 + h) * 1024 + lane * 16), (unsigned)s * 81920u);
    st32(pw, R, pmo, (unsigned)s * 81920u);
    CBAR();
  }
  VMW(0);
  st32((unsigned)(NSEG - 2), R, ppf, 0);
  RT(7);
}

__device__ __forceinline__ void role_vuw(const Args& a, rsrc_t R, const Go& go, lds_char* lds, int h, int tid, int wave, int lane) {
  const int l15 = lane & 15, g = lane >> 4;
  u32x4 wv[2][18], wo[9][4];
  u32x4 xr[9], xq[9];
  XL xl; x_init(xl, a.x, wave, lane);
  x_gload(xq, xl, 0);
  x_gload(xr, xl, 1);
  unsigned zo = OFF_ZF + (unsigned)(wave * 9 * 40960 + lane * 16) + h * 1024;
  const unsigned vpf = (lane == 0) ? (unsigned)(FL_UPROG + h * 128 + wave * 4) : OOB;
  f32x4 cc[2][2], av[2][2][2];
  {
    u32x4 sg[WDEP][9];
    const rsrc_t B0 = __builtin_amdgcn_make_buffer_rsrc((void*)(a.Wv + h * 128), 0, DM * HD * 2, 0x00020000);
    const rsrc_t B1 = __builtin_amdgcn_make_buffer_rsrc((void*)(a.Wo + (size_t)h * 128 * DM), 0, 128 * DM * 2, 0x00020000);
    const unsigned wof = (unsigned)((tid >> 4) * (HD * 2) + (tid & 15) * 16);
    auto gl = [&](int c, int k) {
      if (c < 5) { if (k < (c < 4 ? 8 : 4)) { sg[c % WDEP][k] = __builtin_amdgcn_raw_buffer_load_b128(B0, wof, (unsigned)(128 * c + 16 * k) * (HD * 2), (NTM & 8) ? 2 : 0); SB(); } }
      else if (c < 9) { sg[c % WDEP][k] = __builtin_amdgcn_raw_buffer_load_b128(B1, tid * 16, (unsigned)((c - 5) * 9 + k) * 4096, (NTM & 8) ? 2 : 0); SB(); }
    };
#pragma unroll
    for (int k = 0; k < 8 * WDEP; ++k) gl(k >> 3, k & 7);
    if (wave == 0) WALK(110); else if (wave == 1) WALK(111); else if (wave == 2) WALK(112); else WALK(113);
    RT(2);
    PADS(110);
    lds_char *x0p = lds + X0_OFF, *x1p = lds + X1_OFF;
    pin_v(x0p); pin_v(x1p);
    x_swrite(xq, x0p, xl);
    PAD();
    x_swrite(xr, x1p, xl);
    PAD();
    x0p += xl.rd; x1p += xl.rd;
    pin_v(x0p); pin_v(x1p);
    lds_char *wp = lds + (tid >> 4) * 288 + (tid & 15) * 16, *rp = lds + (g * 8 + (l15 >> 2)) * 288 + wave * 64 + (l15 & 3) * 8;
    pin_v(wp); pin_v(rp);
    auto put = [&](int c, int k) {
      const int u = tid + 256 * k;
      lds_char* buf = lds + (c & 1) * STG_BYTES;
      if (c < 5) { if (k < (c < 4 ? 8 : 4)) { *(lds_u32x4*)(wp + (c & 1) * STG_BYTES + k * (16 * 288)) = sg[c % WDEP][k]; SB(); } }
      else if (c < 9) { *(lds_u32x4*)(buf + (u / 72) * 1184 + (u % 72) * 16) = sg[c % WDEP][k]; SB(); }
    };
#pragma unroll
    for (int k = 0; k < 8; ++k) { put(0, k); gl(WDEP, k); }
    PAD();
#pragma unroll
    for (int k = 0; k < 8; ++k) { av[k >> 2][(k >> 1) & 1][k & 1] = (f32x4){0.f, 0.f, 0.f, 0.f}; pin_v(av[k >> 2][(k >> 1) & 1][k & 1]); }
    auto chunk = [&](auto CC) {
      constexpr int c = decltype(CC)::value, nk = c < 4 ? 4 : 2;
      lds_char* buf = lds + (c & 1) * STG_BYTES;
      if (c == 4) { PADE(); PADS(111); } else PAD();
      BAR();
      if (c == 0) RT(3);
      if (c == 2) RT(4);
      if (c == 4) RT(5);
      if (c == 6) RT(6);
      if (c < 5) {
        u32x4 xb[2][4];
        u32x2 wh[2][4];
        auto rd = [&](int kk, int j) {
          if (kk >= nk) return;
          if (j & 1) xb[kk & 1][j >> 1] = XF((j & 4) ? x1p : x0p, (j >> 1) & 1, 4 * c + kk);
          else wh[kk & 1][j >> 1] = trd(rp + (c & 1) * STG_BYTES + (kk * 32 + (j & 2) * 2) * 288 + (j >> 2) * 32);
          SB();
        };
#pragma unroll
        for (int j = 0; j < 8; ++j) rd(0, j);
#pragma unroll
        for (int k = 0; k < 8 * nk; ++k) {
          const int kk = k >> 3;
          u32x4& f = wv[k & 1][(4 * c + kk) % 18];
          if ((k & 7) < 2) {
            f = (u32x4){wh[kk & 1][(k & 1) * 2][0], wh[kk & 1][(k & 1) * 2][1], wh[kk & 1][(k & 1) * 2 + 1][0], wh[kk & 1][(k & 1) * 2 + 1][1]};
            pin_a(f);
          }
          mfA(av[(k >> 2) & 1][k & 1][(k >> 1) & 1], f, xb[kk & 1][(k >> 1) & 3]);
          SB();
          rd(kk + 1, k & 7);
          if (k % nk == 0) put(c + 1, k / nk);
          if (k % nk == 1) gl(c + 1 + WDEP, k / nk);
          if (k % 12 == 11 || k == 8 * nk - 1) PAD();
        }
        put(c + 1, 8);
        gl(c + 1 + WDEP, 8);
      } else {
#pragma unroll
        for (int ct = 0; ct < 9; ++ct) {
          lds_char* p = buf + (4 * g + (l15 >> 2)) * 1184 + (wave * 9 + ct) * 32 + (l15 & 3) * 8;
          const u32x2 v0 = trd(p), v1 = trd(p + 16 * 1184);
          wo[ct][(c + 3) % 4] = (u32x4){v0[0], v0[1], v1[0], v1[1]};
        }
        PAD();
        SB();
        if (c == 5) {
#pragma unroll
          for (int tt = 0; tt < 2; ++tt)
            *(lds_u32x4*)(lds + X0_OFF + tt * 4096 + wave * 1024 + lane * 16) =
                (u32x4){pk2(av[0][0][tt][0], av[0][0][tt][1]), pk2(av[0][0][tt][2], av[0][0][tt][3]), pk2(av[0][1][tt][0], av[0][1][tt][1]), pk2(av[0][1][tt][2], av[0][1][tt][3])};
        }
#pragma unroll
        for (int k = 0; k < 9; ++k) { put(c + 1, k); gl(c + 1 + WDEP, k); }
        PAD();
#pragma unroll
        for (int ct = 0; ct < 9; ++ct) { if (ct < 7) pin_a(wo[ct][(c + 3) % 4]); else pin_v(wo[ct][(c + 3) % 4]); }
      }
      if (c == UPO) {
#pragma unroll
        for (int k = 0; k < 9; ++k) st128((u32x4){~0u, ~0u, ~0u, ~0u}, R, zo, k * 40960);
      }
      if (c == UGO) { go_wait(R, go, lane); st32(~1u, R, vpf, 0); }
      if (c == 5) x_gload(xq, xl, 2);
      if (c == 6) x_gload(xr, xl, 3);
      SB();
    };
#define CH(n) chunk(std::integral_constant<int, n>())
    CH(0); CH(1); CH(2); CH(3); CH(4); CH(5); CH(6); CH(7); CH(8);
#undef CH
    BAR();
  }
  RT(1);
  x_swrite(xq, lds, xl);
  lds_char *rd0 = lds + xl.rd, *rd1 = rd0 + STG_BYTES, *wr0 = lds + xl.wr, *wr1 = wr0 + STG_BYTES;
  lds_char *vr = lds + 2 * STG_BYTES + lane * 16, *vw = vr + wave * 1024;
  pin_v(rd0); pin_v(rd1); pin_v(wr0); pin_v(wr1); pin_v(vr); pin_v(vw); pin_v(zo);
  u32x4 bb[3][2], vf[2][2][4];
#pragma unroll
  for (int k = 0; k < 4; ++k) cc[k >> 1][k & 1] = (f32x4){0.f, 0.f, 0.f, 0.f};
#pragma unroll
  for (int k = 0; k < 8; ++k) { vf[0][k >> 2][k & 3] = *(lds_u32x4*)(vr + k * 1024); vf[1][k >> 2][k & 3] = xq[0]; }
  BAR();
#pragma unroll
  for (int ks = 0; ks < 2; ++ks) { bb[ks][0] = XF(rd0, 0, ks); bb[ks][1] = XF(rd0, 1, ks); }
  __builtin_amdgcn_s_waitcnt(0x0F70);
  auto iter = [&](auto PC, int s) {
    constexpr int P = decltype(PC)::value & 1, W = 0, NOV = decltype(PC)::value >> 1;
    lds_char* buf = P ? rd1 : rd0;
    lds_char* nrd = P ? rd0 : rd1;
    lds_char* nwr = P ? wr0 : wr1;
    f32x4 (&c)[2][2] = av[P];
    f32x4 (&e)[2][2] = av[P ^ 1];
    u32x4 o, q;
    unsigned s1, s2, sx;
#define VM(ks, t, tt) do { if (NOV) break; if ((ks) == 0) c[t][tt] = mfA0(wv[t][ks], bb[(ks) % 3][tt]); else mfA(c[t][tt], wv[t][ks], bb[(ks) % 3][tt]); SB(); } while (0)
#define UM(k, tt) do { f32x4& d = cc[(gq + P) & 1][tt]; const u32x4& f = vf[gq < 2 ? P : P ^ 1][tt][k];                                      \
                       if (ct < 7) { if ((k) == 0) d = mfB0(f, wo[ct][k]); else mfB(d, f, wo[ct][k]); }                                       \
                       else { if ((k) == 0) d = mf0(f, wo[ct][k]); else mfa(d, f, wo[ct][k]); } SB(); } while (0)
#define BRD(ks, tt) do { if (NOV) break; bb[(ks) % 3][tt] = (ks) < 18 ? XF(buf, tt, (ks) % 18) : XF(nrd, tt, (ks) % 18); SB(); } while (0)
#define CV(j) do { if (NOV && (j) == 0) asm volatile("s_nop 3"); o[j] = pkv(cc[(gq + P + 1) & 1][(j) >> 1][((j) & 1) * 2], cc[(gq + P + 1) & 1][(j) >> 1][((j) & 1) * 2 + 1]); SB(); } while (0)
#define CE(j, tt) do { if (gq == 0) { q[j] = pkv(e[(j) >> 1][tt][((j) & 1) * 2], e[(j) >> 1][tt][((j) & 1) * 2 + 1]); SB(); } } while (0)
#define VR(j) do { if (gq == 1) { vf[P ^ 1][(j) & 1][(j) >> 1] = *(lds_u32x4*)(vr + P * 8192 + ((j) & 1) * 4096 + ((j) >> 1) * 1024); SB(); } } while (0)
#define LW(n) do { __builtin_amdgcn_s_waitcnt(0xC07F | ((n) << 8)); SB(); } while (0)
#pragma unroll
    for (int gq = NOV ? 2 : 0; gq < 9; ++gq) {
      const int ct = (gq + 7) % 9, k0 = 2 * gq, k1 = 2 * gq + 1, pt = (gq + 6) % 9;
      if (NOV) {} else if (gq == 0 || gq == 8) LW(2); else if (gq == 1) LW(3); else if (gq == 2) LW(8); else LW(3);
      if (gq == 8 && !NOV) { __builtin_amdgcn_s_barrier(); SB(); }
      VM(k0, 0, 0); BRD(k0 + 2, 0);
      if (gq == (NOV ? 2 : 0)) {
        s1 = (unsigned)(s - 1) < NSEG ? (unsigned)(s - 1) * ZS : OOB; s2 = s >= 2 ? (unsigned)(s - 2) * ZS : OOB; sx = (unsigned)clampseg(s + 2) * 36864u;
        s1 = __builtin_amdgcn_readfirstlane(s1); s2 = __builtin_amdgcn_readfirstlane(s2); sx = __builtin_amdgcn_readfirstlane(sx);
        asm volatile("" : "+s"(s1), "+s"(s2), "+s"(sx)); SB();
      }
      UM(0, 0); CV(0);
      VM(k0, 0, 1); BRD(k0 + 2, 1);
      UM(0, 1); CV(1);
      if (gq == 1) { LW(2); __builtin_amdgcn_s_barrier(); SB(); }
      VM(k0, 1, 0); CV(2); VR(0);
      UM(1, 0); CV(3); VR(1);
#define ST(n) do { if (!NOST && STG0 + W * STGD == n) { st128(o, R, zo, (gq < 3 ? s2 : s1) + pt * 40960); SB(); } } while (0)
      ST(5);
      VM(k0, 1, 1); ST(6);
      UM(1, 1); CE(0, 0); VR(2); ST(7);
      if (NOV) {} else if (gq == 1) LW(5); else LW(2);
      VM(k1, 0, 0); BRD(k1 + 2, 0); ST(8);
      UM(2, 0); CE(1, 0); VR(3); ST(9);
      if (!NOV && gq >= 2 && gq < 7) { *(lds_u32x4*)(nwr + (gq - 2) * 128) = xr[gq - 2]; SB(); }
      VM(k1, 0, 1); BRD(k1 + 2, 1); ST(10);
      UM(2, 1); CE(2, 0); CE(3, 0); VR(4); ST(11);
      if (!NOV && gq >= 2 && gq < 7) { xr[gq - 2] = __builtin_amdgcn_raw_buffer_load_b128(xl.X, xl.vo + (gq - 2) * 128, sx, 0); SB(); }
      VM(k1, 1, 0); VR(5); ST(12);
      if (gq == 0) { *(lds_u32x4*)(vw + P * 8192) = q; SB(); CE(0, 1); }
      if (!NOV && gq >= 3 && gq < 7) { *(lds_u32x4*)(nwr + (gq + 2) * 128) = xr[gq + 2]; SB(); }
      UM(3, 0); CE(1, 1); CE(2, 1); VR(6); ST(13);
      if (!NOV && gq == 0) { PADE(); if (P) PADS(113); else PADS(112); } else PAD();
      SB();
      VM(k1, 1, 1); CE(3, 1); VR(7); ST(14);
      if (!NOV && gq >= 3 && gq < 7) { xr[gq + 2] = __builtin_amdgcn_raw_buffer_load_b128(xl.X, xl.vo + (gq + 2) * 128, sx, 0); SB(); }
      UM(3, 1); ST(15);
#undef ST
      if (gq == 0) { *(lds_u32x4*)(vw + P * 8192 + 4096) = q; SB(); }
      if (!NOV && gq == 7 && !NOFL) { VMW(13); st32((unsigned)(s - 2), R, vpf, 0); SB(); }
      if (!NOV && gq == 3 && UNLIKELY(s == 3 || s == 4)) { __builtin_amdgcn_s_waitcnt(0x0F70); st32((unsigned)(s - 2), R, vpf, 0); SB(); }
    }
#undef VM
#undef UM
#undef BRD
#undef CV
#undef CE
#undef VR
#undef LW
  };
  RT(8);
  iter(std::integral_constant<int, 3>(), 1);
  {
    f32x4 d[3][2];
#pragma unroll
    for (int k = 0; k < 16; ++k) {
      f32x4& e = d[1 + (k >> 3)][k & 1];
      if (((k >> 1) & 3) == 0) e = mf0(vf[0][k & 1][0], wo[7 + (k >> 3)][0]); else mfa(e, vf[0][k & 1][(k >> 1) & 3], wo[7 + (k >> 3)][(k >> 1) & 3]);
    }
    d[0][0] = cc[1][0]; d[0][1] = cc[1][1];
    mf_wait2(d[2][0], d[2][1]); SB();
#pragma unroll
    for (int t = 0; t < 3; ++t)
      st128((u32x4){pk2(d[t][0][0], d[t][0][1]), pk2(d[t][0][2], d[t][0][3]), pk2(d[t][1][0], d[t][1][1]), pk2(d[t][1][2], d[t][1][3])}, R, zo, (6 + t) * 40960);
    SB();
  }
  RT(9);
#pragma nounroll
  for (int s = 2; s < NSEG + 2; s += 2) {
    iter(std::integral_constant<int, 0>(), s);
    iter(std::integral_constant<int, 1>(), s + 1);
  }
  PADE();
  RT(7);
}

#define RING_OFF (2 * STG_BYTES)
#define RING_STR 456
#define OST_OFF (RING_OFF + 64 * RING_STR)
__device__ __forceinline__ float dot2(unsigned a, unsigned b, float c) {
  return __builtin_amdgcn_fdot2_f32_bf16(__builtin_bit_cast(bf16x2, a), __builtin_bit_cast(bf16x2, b), c, false);
}
__device__ __forceinline__ float swp1(float x) { return __uint_as_float(__builtin_amdgcn_update_dpp(0u, __float_as_uint(x), 0xB1, 0xf, 0xf, false)); }
__device__ __forceinline__ void role_aw(const Args& a, rsrc_t R, const Go& go, lds_char* lds, int ai, int tid, int wave, int lane) {
  const int l15 = lane & 15, g = lane >> 4;
  const int ga = ai & 7, rr = ai >> 3;
  for (int k = 0; k < AWD; ++k) __builtin_amdgcn_s_sleep(32);
  u32x4 w[4][18];
  {
    u32x4 sg[3][7];
    unsigned gv[9], wo[7], lo[7];
    const rsrc_t W = __builtin_amdgcn_make_buffer_rsrc((void*)a.wqkv, 0, DM * 1728 * 2, 0x00020000);
    const rsrc_t G = __builtin_amdgcn_make_buffer_rsrc((void*)a.gw, 0, DM * 2, 0x00020000);
#pragma unroll
    for (int k = 0; k < 7; ++k) {
      const int u = tid + 256 * k, row = u / 27, cu = u % 27;
      wo[k] = row < 64 ? (unsigned)(row * 3456 + (cu / 9) * 1152 + ga * 144 + (cu % 9) * 16) : OOB;
      lo[k] = row < 64 ? (unsigned)(row * 544 + cu * 16) : (unsigned)((tid & 63) * 544 + 448);
    }
    auto gl = [&](u32x4 (&st)[7], int c) {
#pragma unroll
      for (int k = 0; k < 7; ++k) st[k] = __builtin_amdgcn_raw_buffer_load_b128(W, wo[k], (unsigned)c * (64u * 3456u), (NTM & 16) ? 2 : 0);
    };
#pragma unroll
    for (int c = 0; c < 9; ++c) gv[c] = __builtin_amdgcn_raw_buffer_load_b16(G, (unsigned)(tid & 63) * 2, (unsigned)c * 128u, 0);
    gl(sg[0], 0); if (ASLP > 0) __builtin_amdgcn_s_sleep(ASLP);
    gl(sg[1], 1); if (ASLP > 0) __builtin_amdgcn_s_sleep(ASLP);
    gl(sg[2], 2);
#pragma unroll
    for (int c = 0; c < 9; ++c) {
      lds_char* buf = lds + (c & 1) * STG_BYTES;
#pragma unroll
      for (int k = 0; k < 7; ++k) *(lds_u32x4*)(buf + lo[k]) = sg[c % 3][k];
      *(lds_u32x4*)(buf + (tid & 63) * 544 + 432) = (u32x4){gv[c], 0u, 0u, 0u};
      if (ASLP > 0) __builtin_amdgcn_s_sleep(ASLP);
      if (c + 3 < 9) gl(sg[c % 3], c + 3);
      BAR();
#pragma unroll
      for (int kk = 0; kk < 2; ++kk)
#pragma unroll
        for (int t = 0; t < 4; ++t) {
          lds_char* p = buf + (kk * 32 + g * 8 + (l15 >> 2)) * 544 + (wave * 64 + t * 16) * 2 + (l15 & 3) * 8;
          u32x2 v0 = trd(p), v1 = trd(p + 4 * 544);
          w[t][2 * c + kk] = (u32x4){v0[0], v0[1], v1[0], v1[1]};
        }
      SB();
#pragma unroll
      for (int k = 0; k < 8; ++k) { if (c < 7) pin_a(w[k & 3][2 * c + (k >> 2)]); else pin_v(w[k & 3][2 * c + (k >> 2)]); }
    }
    BAR();
  }
  RT(1);
  go_wait(R, go, lane);
  auto sgm = [&](int it) { const int j = it / 5; return min(16 * j + 4 * rr + it - 5 * j - 1, NSEG - 1); };
  const int i0 = rr > 0 ? 0 : 1;
  u32x4 xr[9];
  XL xl; x_init(xl, a.x, wave, lane);
  x_gload(xr, xl, sgm(i0));
  x_swrite(xr, lds + i0 * STG_BYTES, xl);
  x_gload(xr, xl, sgm(i0 + 1));
  lds_char* ring = lds + RING_OFF;
  lds_char* ost = lds + OST_OFF;
  const int t = tid >> 3, hh = (tid >> 1) & 3, half = tid & 1;
  const float sc = 0.23570226039551584f * 1.4426950408889634f;
  auto done = [&](int s) { __builtin_amdgcn_raw_ptr_buffer_atomic_add_i32(1, R, tid == 0 && s >= 0 ? (unsigned)(FL_APROG + s * 128) : OOB, 0, SC1); };
  const unsigned gto = (ga == 0 && wave == 3 && g == 2) ? OFF_GATE + l15 * 4 : OOB;
  int sp = -1;
  for (int it = i0; it < 20; ++it) {
    const int s = sgm(it);
    BAR();
    {
      f32x4 acc[4][2];
      proj4(lds + (it & 1) * STG_BYTES, lds + ((it + 1) & 1) * STG_BYTES, xr, xl, sgm(it + 2), wave, lane, w[0], w[1], w[2], w[3], acc);
      VMW(9);
#pragma unroll
      for (int tl = 0; tl < 4; ++tl)
#pragma unroll
        for (int tt = 0; tt < 2; ++tt) {
          const int col = wave * 64 + tl * 16 + g * 4;
          u32x2 o = {pk2(acc[tl][tt][0], acc[tl][tt][1]), pk2(acc[tl][tt][2], acc[tl][tt][3])};
          if (col < 216) *(__attribute__((address_space(3))) u32x2*)(ring + (((s & 1) * 32 + tt * 16 + l15) * RING_STR) + col * 2) = o;
        }
#pragma unroll
      for (int tt = 0; tt < 2; ++tt) {
        float z = acc[1][tt][0];
        float gv = __builtin_amdgcn_rcpf(1.f + __builtin_amdgcn_exp2f(-1.4426950408889634f * z));
        st32(__float_as_uint(gv), R, gto + tt * 64, (unsigned)s * 128u);
      }
    }
    BAR();
    done(sp);
    sp = it % 5 != 0 ? s : -1;
    if (it % 5 != 0) {
      const int row = s * 32 + t;
      unsigned q[9];
      lds_char* qp = ring + (row & 63) * RING_STR + hh * 36;
#pragma unroll
      for (int j = 0; j < 9; ++j) q[j] = *(lds_u32*)(qp + j * 4);
      float lg[4];
      lds_char* vp[4];
#pragma unroll
      for (int wi = 0; wi < 4; ++wi) {
        const int r2 = row - 2 * (half * 4 + wi);
        const bool ok = r2 >= 0;
        lds_char* kp = ring + ((ok ? r2 : row) & 63) * RING_STR + hh * 36 + 144;
        vp[wi] = kp + 144;
        float d = 0.f;
#pragma unroll
        for (int j = 0; j < 9; ++j) d = dot2(q[j], *(lds_u32*)(kp + j * 4), d);
        lg[wi] = ok ? d : -__builtin_inff();
      }
      float m = fmaxf(fmaxf(lg[0], lg[1]), fmaxf(lg[2], lg[3]));
      m = fmaxf(m, swp1(m));
      const float ms = -m * sc;
      float sum = 0.f;
#pragma unroll
      for (int wi = 0; wi < 4; ++wi) { lg[wi] = __builtin_amdgcn_exp2f(fmaf(lg[wi], sc, ms)); sum += lg[wi]; }
      sum += swp1(sum);
      const float inv = __builtin_amdgcn_rcpf(sum);
      const unsigned p01 = pk2(lg[0] * inv, lg[1] * inv), p23 = pk2(lg[2] * inv, lg[3] * inv);
#pragma unroll
      for (int j = 0; j < 9; ++j) {
        const unsigned v0 = *(lds_u32*)(vp[0] + j * 4), v1 = *(lds_u32*)(vp[1] + j * 4), v2 = *(lds_u32*)(vp[2] + j * 4), v3 = *(lds_u32*)(vp[3] + j * 4);
        float lo = dot2(__builtin_amdgcn_perm(v1, v0, 0x05040100u), p01, 0.f);
        lo = dot2(__builtin_amdgcn_perm(v3, v2, 0x05040100u), p23, lo);
        float hi = dot2(__builtin_amdgcn_perm(v1, v0, 0x07060302u), p01, 0.f);
        hi = dot2(__builtin_amdgcn_perm(v3, v2, 0x07060302u), p23, hi);
        lo += swp1(lo); hi += swp1(hi);
        if ((j & 1) == half) *(lds_u32*)(ost + t * 144 + hh * 36 + j * 4) = pk2(lo, hi);
      }
      BAR();
#pragma unroll
      for (int k = 0; k < 2; ++k) {
        const int u = tid + 256 * k;
        const bool ok = u < 288;
        const int ur = u & 31, uc = ok ? u >> 5 : 0, cc = ga * 9 + uc;
        u32x4 o = *(lds_u32x4*)(ost + ur * 144 + uc * 16);
        st128(o, R, ok ? OFF_OA + (unsigned)(((ur >> 4) * 18 + (cc >> 2)) * 1024 + ((cc & 3) * 16 + (ur & 15)) * 16) : OOB, (unsigned)s * 36864u);
      }
      CBAR();
    }
  }
  VMW(0);
  BAR();
  done(sp);
  RT(7);
}

__device__ __forceinline__ void role_out(const Args& a, rsrc_t R, const Go& go, lds_char* lds, int o, int tid, int wave, int lane) {
  const int l15 = lane & 15, g = lane >> 4;
  const int cb = o % 9, grp = o < 36 ? o / 9 : 3, send = o / 9 == 3 ? NSEG - 1 : NSEG, sst = o < 36 ? 4 : NSEG - 4;
  u32x4 wf[4][5];
  {
    u32x4 st[18];
#pragma unroll
    for (int k = 0; k < 18; ++k) { int u = tid + 256 * k; st[k] = *(const u32x4*)(a.wo + (size_t)(u >> 3) * DM + cb * 64 + (u & 7) * 8); }
#pragma unroll
    for (int k = 0; k < 18; ++k) { int u = tid + 256 * k; *(lds_u32x4*)(lds + (u >> 3) * 144 + (u & 7) * 16) = st[k]; }
    BAR();
#pragma unroll
    for (int i = 0; i < 5; ++i)
#pragma unroll
      for (int ct = 0; ct < 4; ++ct) {
        const int ks = wave + 4 * i;
        lds_char* p = lds + ((ks < 18 ? ks : 17) * 32 + g * 8 + (l15 >> 2)) * 144 + ct * 32 + (l15 & 3) * 8;
        u32x2 v0 = trd(p), v1 = trd(p + 4 * 144);
        wf[ct][i] = (ks < 18) ? (u32x4){v0[0], v0[1], v1[0], v1[1]} : (u32x4){0u, 0u, 0u, 0u};
        pin_a(wf[ct][i]);
      }
    BAR();
  }
  const unsigned zb = OFF_ZF + (unsigned)(4 * cb) * 40960u + lane * 16;
  const unsigned flk = lane == 32 ? FL_KMPROG + ((o * 4 + wave) & 31) * 128 : OOB;
  const bool isu = (lane & 24) == 8 && lane < 48;
  auto faddr = [&](int s) -> unsigned {
    return lane < 8 ? (lane == 0 ? FL_APROG + s * 128 : OOB)
                    : (isu ? FL_UPROG + (wave * 8 + (lane & 7)) * 128 + ((cb * 64 + (lane < 16 ? 0 : 63)) / 144) * 4
                           : (lane < 32 ? (s == NSEG - 1 ? FL_PXLAST + (wave * 16 + lane - 16) * 128 : FL_PXPROG + (wave * 8 + (lane & 7)) * 128 + ((lane >> 3) & 1) * 4)
                                        : (s + 3 < NSEG ? flk : FL_YPROG + (wave * 8 + (lane & 7)) * 16 + ((cb * 64 + (lane < 48 ? 0 : 63)) / 144) * 4 + (lane < 56 ? 0 : OOB))));
  };
  const int t = tid >> 3, n8 = tid & 7;
  const rsrc_t O = __builtin_amdgcn_make_buffer_rsrc(a.out, 0, S_LEN * DM * 2, 0x00020000);
  const i32x4 D = mkdesc(a.wb);
  int fv = -1;
  RT(1);
  u32x4 zu[4][8];
  int fu = -1;
  const unsigned fua = isu ? faddr(0) : OOB;
  auto zuld = [&](int s) -> bool {
    unsigned spins = 0;
    while (ballot(isu && fu < s) != 0) {
      if (++spins > SPINCAP) return false;
      if (spins > 1) __builtin_amdgcn_s_sleep(16);
      if (s == NSEG - 1 && spins > 1) __builtin_amdgcn_s_sleep(100);
      CBAR();
      fu = (int)ld32(R, fua, 0);
    }
#pragma unroll
    for (int ct = 0; ct < 4; ++ct)
#pragma unroll
      for (int hh = 0; hh < 8; ++hh)
        asm volatile("buffer_load_dwordx4 %0, %1, %2, %3 offen offset:%4 sc1" : "=a"(zu[ct][hh])
                     : "v"(zb + ct * 40960 + wave * 8192 + (hh >> 2) * 4096), "s"(D), "s"((unsigned)(s & RMASK) * ZS), "n"((hh & 3) * 1024) : "memory");
    SB();
    return true;
  };
  if ((o != 44 && o != 40 && !go_wait(R, go, lane)) || !zuld(grp)) return;
  unsigned acc_t[8] = {0, 0, 0, 0, 0, 0, 0, 0}, t0 = 0, t1 = 0, t2 = 0, t3 = 0, t4 = 0, t5 = 0, t6 = 0;
  for (int s = grp; s < send; s += sst) {
    TS(t0);
    const unsigned fa = faddr(s);
    const int need = isu || fa >= OOB ? -1 : (lane == 0 ? 7 : ((lane >= 32 && s + 3 < NSEG) ? s + 2 : s));
    const bool far = lane >= 8 && lane < 48 && (isu || (lane < 32 && s < NSEG - 1));
    unsigned spins = 0;
    while ((ballot(fv < need) & 0xFFull) != 0) {
      if (++spins > SPINCAP) return;
      if (spins > 1) __builtin_amdgcn_s_sleep(32);
      CBAR();
      const int nv = (int)lda(R, fv < need && lane < 8 ? fa : OOB);
      fv = lane < 8 ? max(fv, nv) : fv;
    }
    TS(t1);
    u32x4 zy[4][2], pf[2][10], of[2][5];
    float gt[2];
#pragma unroll
    for (int qt = 0; qt < 2; ++qt) {
#pragma unroll
      for (int i = 0; i < 5; ++i) {
        const int ks = wave + 4 * i;
        of[qt][i] = ld128(R, OFF_OA + (unsigned)((qt * 18 + (ks < 18 ? ks : 17)) * 1024 + lane * 16), (unsigned)s * 36864u);
      }
      gt[qt] = __uint_as_float(ld32(R, OFF_GATE + (qt * 16 + l15) * 4, (unsigned)s * 128u));
    }
    SB();
    f32x4 aa[4][2];
#pragma unroll
    for (int ct = 0; ct < 4; ++ct)
#pragma unroll
      for (int qt = 0; qt < 2; ++qt) {
        f32x4 acc = {0.f, 0.f, 0.f, 0.f};
#pragma unroll
        for (int i = 0; i < 5; ++i) acc = mfma(wf[ct][i], of[qt][i], acc);
        aa[ct][qt] = acc * gt[qt];
        pin_v(aa[ct][qt]);
      }
    SB();
#define FWAIT(m) do { spins = 0;                                                                                          \
      while ((ballot(fv < need) & (m)) != 0) {                                                                            \
        if (++spins > SPINCAP) return;                                                                                    \
        if (spins > 1) { if (s < OFAST) __builtin_amdgcn_s_sleep(OSLP); else __builtin_amdgcn_s_sleep(OSL2); }            \
        CBAR();                                                                                                           \
        const int n0 = (int)lda(R, fv < need && !far ? fa : OOB), n1 = (int)ld32(R, fv < need && far ? fa : OOB, 0), nv = far ? n1 : n0;                                                             \
        fv = max(fv, nv);                                                                                                 \
      } } while (0)
#define ZYLD(zv) do { _Pragma("unroll") for (int hp = 0; hp < 2; ++hp) _Pragma("unroll") for (int ct = 0; ct < 4; ++ct) { \
                        zy[ct][hp] = ld128(R, (zv) + hp * 1024, (unsigned)(s & RMASK) * ZS + ct * 40960 + 32768 + wave * 2048); SB(); } } while (0)
#define MFS(h0, h1) do { _Pragma("unroll") for (int hh = h0; hh < h1; ++hh) _Pragma("unroll") for (int qt = 0; qt < 2; ++qt)                       \
                        _Pragma("unroll") for (int ct = 0; ct < 4; ++ct) {                                                                         \
                        ab[ct][qt] = mfma(hh < 8 ? zu[ct][hh & 7] : zy[ct][hh & 1], pf[qt][hh], hh ? ab[ct][qt] : (f32x4){0.f, 0.f, 0.f, 0.f}); SB(); } } while (0)
    TS(t2);
    FWAIT(s == NSEG - 1 ? 0xFFull << 32 : 0ull);
    const bool yr = (ballot(fv < need) >> 32) == 0;
    ZYLD(yr ? zb : OOB);
    FWAIT(0xFFFFFFFFull);
    TS(t3);
#pragma unroll
    for (int hh = 0; hh < 10; ++hh)
#pragma unroll
      for (int qt = 0; qt < 2; ++qt) {
        pf[qt][hh] = ld128(R, OFF_PXF + (unsigned)((qt * 40 + (hh < 8 ? wave * 8 + hh : 24 + wave * 2 + hh)) * 1024 + lane * 16), (unsigned)(s & RMASK) * 81920u);
        SB();
      }
    CBAR();
    f32x4 ab[4][2];
    MFS(0, 8);
    TS(t4);
    const int sn = s + sst < send ? s + sst : s;
    if (sn != s && !zuld(sn)) return;
    if (!yr) { FWAIT(~0ull); ZYLD(zb); }
    CBAR();
    {
      const bool fn = lane >= 8 && lane < 48 && (isu || (lane < 32 && sn < NSEG - 1));
      const int v0 = (int)lda(R, fn ? OOB : faddr(sn)), v1 = (int)ld32(R, fn ? faddr(sn) : OOB, 0);
      fv = fn ? v1 : v0;
    }
    SB();
    MFS(8, 10);
    fu = isu ? fv : fu;
    TS(t5);
#undef FWAIT
#undef ZYLD
#undef MFS
#pragma unroll
    for (int qt = 0; qt < 2; ++qt)
#pragma unroll
      for (int ct = 0; ct < 4; ++ct)
        *(lds_f32x4*)(lds + wave * 8704 + (qt * 16 + l15) * 272 + (ct * 16 + g * 4) * 4) = aa[ct][qt] + ab[ct][qt] * (1.f - gt[qt]);
    BAR();
    f32x4 b0 = *(lds_f32x4*)(lds + t * 272 + n8 * 32), b1 = *(lds_f32x4*)(lds + t * 272 + n8 * 32 + 16);
#pragma unroll
    for (int w = 1; w < 4; ++w) { b0 += *(lds_f32x4*)(lds + w * 8704 + t * 272 + n8 * 32); b1 += *(lds_f32x4*)(lds + w * 8704 + t * 272 + n8 * 32 + 16); }
    u32x4 ov = {pk2(b0[0], b0[1]), pk2(b0[2], b0[3]), pk2(b1[0], b1[1]), pk2(b1[2], b1[3])};
    __builtin_amdgcn_raw_buffer_store_b128(ov, O, (unsigned)((s * 32 + t) * DM + cb * 64 + n8 * 8) * 2, 0, OAUX);
    BAR();
    if (s < 4) RT(2);
    TS(t6); ACC(0, 4 * (t1 - t0)); ACC(1, 4 * (t2 - t1)); ACC(2, 4 * (t3 - t2)); ACC(3, 4 * (t4 - t3)); ACC(4, 4 * (t5 - t4)); ACC(5, 4 * (t6 - t5));
    STEPRT(0, 0);
  }
  for (int k = 0; k < 8; ++k) SV(16 + k, acc_t[k]);
  RT(7);
}

#define LDS_BYTES (153600 + 256)
__device__ __forceinline__ void code_touch(u64 pc, unsigned len, unsigned off, unsigned ld) {
  const i32x4 P = {(int)(unsigned)pc, (int)(unsigned)(pc >> 32), (int)len, 0x00020000};
  asm volatile("s_mov_b32 m0, %2\n\ts_nop 0\n\tbuffer_load_dword %0, %1, 0 offen lds" :: "v"(off), "s"(P), "s"(ld) : "memory");
}
__global__ void __launch_bounds__(256, 1) mega(Args a) {
  __shared__ __attribute__((aligned(16))) char smem[LDS_BYTES];
  lds_char* lds = (lds_char*)smem;
  const int b = blockIdx.x, tid = threadIdx.x, lane = tid & 63, wave = __builtin_amdgcn_readfirstlane(tid >> 6);
  const int x = b & 7, sl = b >> 3;
  const rsrc_t R = __builtin_amdgcn_make_buffer_rsrc(a.wb, 0, (int)OFF_END, 0x00020000);
  const Go go = go_look(R, b + wave * 4);
  if (b == 244) go_lead(R, tid, wave, lane);
  {
    u64 pc; unsigned len;
    asm volatile("s_getpc_b64 %0\n.Lcode_here:\n\ts_mov_b32 %1, zz_code_end-.Lcode_here" : "=s"(pc), "=s"(len));
    if (wave == CPF) code_touch(pc, len, (unsigned)(sl * 8192 + lane * 128), (unsigned)(uintptr_t)lds + 153600);
  }
  RT(0);
  if (wave == 3 && (sl == 24 || sl == 29)) {
    unsigned d[4];
#pragma unroll
    for (int k = 0; k < 3; ++k) d[k] = ld32(R, (unsigned)(k * 64 + lane) << 20, 0);
    d[3] = *(const unsigned*)(a.out + min(lane, 2) * (1 << 19));
    asm volatile("" :: "v"(d[0]), "v"(d[1]), "v"(d[2]), "v"(d[3]));
  }
  if (x < 4 && wave == 1 && sl >= 16 && sl < 24) calibrate(R, go, x + (sl & 4), sl & 3, lane);
  if (x < 4) {
    if (sl < 16) for (int k = 0; k < QDL; ++k) __builtin_amdgcn_s_sleep(32);
    if (sl < 8) role_qkc(a, R, go, lds, x * 8 + sl, tid, wave, lane);
    else if (sl < 16) role_vcu(a, R, go, lds, x * 8 + sl - 8, tid, wave, lane);
    else if (sl < 25) role_mix(a, R, go, x * 9 + sl - 16, wave, lane);
    else role_aw(a, R, go, lds, x * 8 + sl - 25, tid, wave, lane);
  } else {
    const int y = x - 4;
    if (sl < 8) {
      const int h = y * 8 + sl;
      role_prj(a, R, go, lds, a.Wk + h * 128, a.Wq + h * 128, OFF_KF + h * 8192u, OFF_QF + h * 8192u, FL_PROG + h * 128, tid, wave, lane);
    } else if (sl < 16) role_vuw(a, R, go, lds, y * 8 + sl - 8, tid, wave, lane);
    else if (sl < 20) role_px(a, R, go, y * 4 + sl - 16, wave, lane);
    else {
      if (sl == 31) role_aw(a, R, go, lds, y * 8 + 7, tid, wave, lane);
      for (int k = 0; k < XPD; ++k) __builtin_amdgcn_s_sleep(32);
      if (sl == 29) {
        const rsrc_t X = __builtin_amdgcn_make_buffer_rsrc((void*)a.x, 0, S_LEN * DM * 2, 0x00020000);
#pragma unroll 1
        for (int k = 0; k < 3; ++k) {
          unsigned t[12];
#pragma unroll
          for (int j = 0; j < 12; ++j) t[j] = __builtin_amdgcn_raw_buffer_load_b32(X, (unsigned)(((k * 12 + j) * 16 + y * 4 + wave) * 64 + lane) * 64, 0, 0);
#pragma unroll
          for (int j = 0; j < 12; ++j) asm volatile("" :: "v"(t[j]));
        }
      }
      if (sl < 31 || y == 0) { BAR(); role_out(a, R, go, lds, sl < 29 ? y * 9 + sl - 20 : (sl < 31 ? 36 + (sl - 29) * 4 + y : 44), tid, wave, lane); }
    }
  }
}

extern "C" __device__ __attribute__((noinline, used, visibility("hidden"))) void zz_code_end() {}
extern "C" void kernel_launch(void* const* d_in, const int* in_sizes, int n_in, void* d_out, int out_size, void* d_ws,
                              size_t ws_size, hipStream_t stream) {
  char* wb = (char*)(((uintptr_t)d_ws + 0x1FFFFF) & ~(uintptr_t)0x1FFFFF);
  Args a;
  a.x = (const u16*)d_in[0]; a.mem0 = (const u16*)d_in[4]; a.Wq = (const u16*)d_in[5]; a.Wk = (const u16*)d_in[6]; a.Wv = (const u16*)d_in[7];
  a.Wo = (const u16*)d_in[8]; a.wqkv = (const u16*)d_in[1]; a.wo = (const u16*)d_in[2]; a.gw = (const u16*)d_in[10]; a.out = (u16*)d_out; a.wb = wb;
  mega<<<256, 256, 0, stream>>>(a);
}
```

```cpp
#define EFIRST
#define CSLP 1
#define RTOL 2
#define QDL 0
#define LTOL 16
#define GRID 48
#define XT 6
#define NOAT
#define AWD 8
#define XPD 8
#define NTM 15
#define FIXL
#define K2 68
#define T1D 100
#define DE0 100
#define DELTA0 172
#include <hip/hip_runtime.h>
#include <stdint.h>
#include <type_traits>

typedef unsigned short u16;
typedef __attribute__((ext_vector_type(2))) __bf16 bf16x2;
typedef __attribute__((ext_vector_type(4))) __bf16 bf16x4;
typedef __attribute__((ext_vector_type(8))) __bf16 bf16x8;
typedef __attribute__((ext_vector_type(16))) __bf16 bf16x16;
typedef __attribute__((ext_vector_type(2))) float f32x2;
typedef __attribute__((ext_vector_type(4))) float f32x4;
typedef __attribute__((ext_vector_type(2))) unsigned u32x2;
typedef __attribute__((ext_vector_type(4))) unsigned u32x4;
typedef __attribute__((ext_vector_type(8))) unsigned u32x8;
typedef __attribute__((ext_vector_type(4))) short s16x4;
typedef __attribute__((address_space(3))) s16x4 lds_s16x4;
typedef __attribute__((address_space(3))) u32x4 lds_u32x4;
typedef __attribute__((address_space(3))) f32x4 lds_f32x4;
typedef __attribute__((address_space(3))) unsigned lds_u32;
typedef __attribute__((address_space(3))) u32x2 lds_u32x2;
typedef __attribute__((address_space(3))) char lds_char;

#define S_LEN 2048
#define DM 576
#define NSEG 64
#define HD 4096

#define SLOT_REAL  131072
#define NEAR_PM    (8192 + 1024)
#define NEAR_AL    (8192 + 5120)
#define NEAR_E     16384
#define OFF_FLAGS  0x100000
#define FL_FLIP    (OFF_FLAGS + 0)
#define FL_WP      (OFF_FLAGS + 16384)
#define FL_AN      (OFF_FLAGS + 18432)
#define FL_MRDY    (OFF_FLAGS + 2048)
#define FL_PROG    (OFF_FLAGS + 4096)
#define FL_UPROG   (OFF_FLAGS + 8192)
#define FL_KMPROG  (OFF_FLAGS + 12288)
#define FL_YPROG   (OFF_FLAGS + 20480)
#define FL_PXPROG  (OFF_FLAGS + 24576)
#define FL_APROG   (OFF_FLAGS + 40960)
#define FL_PXLAST  (OFF_FLAGS + 32768)
#define FL_ACK     (OFF_FLAGS + 28672)
#define FLAG_BYTES 0xC000
#define FL_GO      (OFF_FLAGS + FLAG_BYTES)
#define NACK       945
#define REAL_CAL   0x140000
#define OFF_YR     0x200000
#define YSLOT_REAL 0x100000
#define CPY1       16384
#define CPY2       0x80000
#define OFF_KF   (4u << 20)
#define OFF_QF   (OFF_KF + (16u << 20))
#define OFF_KMF  (OFF_QF + (16u << 20))
#define OFF_PXF  (OFF_KMF + (8u << 20))
#define OFF_ZF   (OFF_PXF + (6u << 20))
#define ZS       (36u * 40960u)
#define OFF_OA   (OFF_ZF + (91u << 20))
#define OFF_GATE (OFF_OA + (3u << 20))
#define OFF_END  (OFF_GATE + (1u << 20))

#define RMASK 63
#define SPINCAP (1u << 19)
#define SC1 16

#define BAR() do { asm volatile("s_waitcnt lgkmcnt(0)" ::: "memory"); __builtin_amdgcn_s_barrier(); asm volatile("" ::: "memory"); } while (0)
#define VMW(n) asm volatile("s_waitcnt vmcnt(" #n ")" ::: "memory")
#define CBAR() asm volatile("" ::: "memory")
#define PADA
#define PADN "s_nop 0\n\t"
#define PADM
#define PADZ
#define PAD()
#define PADS(id)
#define PADE()
#define WALK(id)

#define NOSTEP 0
#define OFF_STAMP (200u << 20)
#define TS(v)
#define RT(i)
#define SV(i, v)
#define ACC(k, d)
#define STEPRT(j, f)
#define EVASM
#define EV(v)
#define EVK(v)
#define EVPUT(j, v)
#define EVPUTI(j, v)
#define EVDECL
#define EVDUMP()
#define CKDECL
#define CKS()
#define CKDUMP()
typedef unsigned long long u64;
struct Args {
  const u16* x; const u16* mem0; const u16* Wq; const u16* Wk; const u16* Wv; const u16* Wo;
  const u16* wqkv; const u16* wo; const u16* gw; u16* out; char* wb;
};

__device__ __forceinline__ float bf2f(u16 v) { return __uint_as_float(((unsigned)v) << 16); }
__device__ __forceinline__ unsigned pk2(float a, float b) {
  f32x2 v = {a, b};
  bf16x2 r = __builtin_convertvector(v, bf16x2);
  return __builtin_bit_cast(unsigned, r);
}
__device__ __forceinline__ int nearoff(int o) { return ((o >> 13) << 15) + (o & 8191); }
__device__ __forceinline__ unsigned own_cpy(int cls, int x) { return ((cls >> (2 * x)) & 2) ? CPY1 : 0u; }
__device__ __forceinline__ unsigned ywin(int cls, int m) {
  const int x = m / 9, row = (x >> 1) * 9 + m - 9 * x;
  const int se = (cls >> (4 * (x >> 1) + 1)) & 1, so = (cls >> (4 * (x >> 1) + 3)) & 1, sd = se == so ? x & 1 : ((x & 1) ? so : se);
  return (unsigned)(OFF_YR + row * 32768 + ((cls >> 8) & 1) * 8192 + ((sd ^ (cls >> 1) ^ (cls >> 9) ^ (row >> 4)) & 1) * 16384);
}
__device__ __forceinline__ u32x2 trd(lds_char* p) {
  s16x4 a = __builtin_amdgcn_ds_read_tr16_b64_v4i16((lds_s16x4*)p);
  return __builtin_bit_cast(u32x2, a);
}
__device__ __forceinline__ f32x4 mfma(u32x4 a, u32x4 b, f32x4 c) {
  return __builtin_amdgcn_mfma_f32_16x16x32_bf16(__builtin_bit_cast(bf16x8, a), __builtin_bit_cast(bf16x8, b), c, 0, 0, 0);
}
__device__ __forceinline__ f32x4 mfv0(u32x4 a, u32x4 b) {
  f32x4 d; asm volatile("s_nop 1\n\tv_mfma_f32_16x16x32_bf16 %0, %1, %2, 0" : "=&v"(d) : "v"(a), "v"(b)); return d;
}
__device__ __forceinline__ f32x4 mfvc(u32x4 a, u32x4 b, f32x4 c) {
  f32x4 d; asm volatile("s_nop 1\n\tv_mfma_f32_16x16x32_bf16 %0, %1, %2, %3" : "=&v"(d) : "v"(a), "v"(b), "v"(c)); return d;
}
__device__ __forceinline__ void mfva(f32x4& d, u32x4 a, u32x4 b) {
  asm volatile("s_nop 1\n\tv_mfma_f32_16x16x32_bf16 %0, %1, %2, %0" : "+v"(d) : "v"(a), "v"(b));
}
__device__ __forceinline__ void mf_wait(f32x4& d) { asm volatile("s_nop 7" : "+v"(d)); }
__device__ __forceinline__ void mf_wait2(f32x4& d, f32x4& e) { asm volatile("s_nop 7" : "+v"(d), "+v"(e)); }
__device__ __forceinline__ f32x4 smf(u32x4 a, u32x8 b, f32x4 c, int idx) {
  return __builtin_amdgcn_smfmac_f32_16x16x64_bf16(__builtin_bit_cast(bf16x8, a), __builtin_bit_cast(bf16x16, b), c, idx, 0, 0);
}
__device__ __forceinline__ float xmax(float x) {
  auto s = __builtin_amdgcn_permlane16_swap(__float_as_uint(x), __float_as_uint(x), false, false);
  x = fmaxf(__uint_as_float(s[0]), __uint_as_float(s[1]));
  auto t = __builtin_amdgcn_permlane32_swap(__float_as_uint(x), __float_as_uint(x), false, false);
  return fmaxf(__uint_as_float(t[0]), __uint_as_float(t[1]));
}
__device__ __forceinline__ float xsum(float x) {
  auto s = __builtin_amdgcn_permlane16_swap(__float_as_uint(x), __float_as_uint(x), false, false);
  x = __uint_as_float(s[0]) + __uint_as_float(s[1]);
  auto t = __builtin_amdgcn_permlane32_swap(__float_as_uint(x), __float_as_uint(x), false, false);
  return __uint_as_float(t[0]) + __uint_as_float(t[1]);
}
__device__ __forceinline__ f32x4 halfadd(f32x4 a, f32x4 b) {
  f32x4 o;
#pragma unroll
  for (int r = 0; r < 4; ++r) {
    auto s = __builtin_amdgcn_permlane32_swap(__float_as_uint(a[r]), __float_as_uint(b[r]), false, false);
    o[r] = __uint_as_float(s[0]) + __uint_as_float(s[1]);
  }
  return o;
}
template <class T> __device__ __forceinline__ void pin_a(T& v) { asm volatile("" : "+a"(v)); }
typedef __amdgpu_buffer_rsrc_t rsrc_t;
__device__ __forceinline__ u32x4 ld128(rsrc_t R, unsigned vo, unsigned so) { return __builtin_amdgcn_raw_buffer_load_b128(R, vo, so, SC1); }
__device__ __forceinline__ unsigned ld32(rsrc_t R, unsigned vo, unsigned so) { return __builtin_amdgcn_raw_buffer_load_b32(R, vo, so, SC1); }
__device__ __forceinline__ void st128(u32x4 v, rsrc_t R, unsigned vo, unsigned so) { __builtin_amdgcn_raw_buffer_store_b128(v, R, vo, so, SC1); }
__device__ __forceinline__ void st32(unsigned v, rsrc_t R, unsigned vo, unsigned so) { __builtin_amdgcn_raw_buffer_store_b32(v, R, vo, so, SC1); }
#define OOB 0x7ff00000
__device__ __forceinline__ unsigned lda(rsrc_t R, unsigned vo) { return ld32(R, vo, 0); }

struct Go { unsigned vo, v0; };
__device__ __forceinline__ Go go_look(rsrc_t R, int b) {
  Go g; g.vo = FL_GO + (b & 15) * 128; g.v0 = ld32(R, g.vo, 0); return g;
}
__device__ __forceinline__ bool go_wait(rsrc_t R, const Go& g, int lane) {
  for (unsigned spins = 0; spins < SPINCAP; ++spins) {
    CBAR();
    if (__all(ld32(R, g.vo, 0) != g.v0)) {
      __builtin_amdgcn_raw_ptr_buffer_atomic_add_i32(1, R, lane == 0 ? g.vo - FL_GO + FL_ACK : OOB, 0, SC1);
      return true;
    }
    __builtin_amdgcn_s_sleep(6);
  }
  return false;
}
__device__ __forceinline__ void go_lead(rsrc_t R, int tid, int wave, int lane) {
#pragma unroll
  for (int k = 0; k < FLAG_BYTES / 4096; ++k) st128((u32x4){~0u, ~0u, ~0u, ~0u}, R, OFF_FLAGS + k * 4096 + tid * 16, 0);
  VMW(0);
  BAR();
  if (wave != 0) return;
  for (unsigned k = 0; k < SPINCAP; ++k) {
    st32(k, R, lane < 16 ? FL_GO + lane * 128 : OOB, 0);
    int n = lane < 16 ? (int)ld32(R, FL_ACK + lane * 128, 0) + 1 : 0;
#pragma unroll
    for (int d = 1; d < 16; d <<= 1) n += __shfl_xor(n, d);
    if (__builtin_amdgcn_readfirstlane(n) >= NACK) break;
    __builtin_amdgcn_s_sleep(4);
  }
}
__device__ __forceinline__ bool wait_flag(rsrc_t R, unsigned vo, int need) {
  for (unsigned spins = 0; spins < SPINCAP; ++spins) {
    CBAR();
    int v = (int)ld32(R, vo, 0);
    if (__all(v >= need)) return true;
    __builtin_amdgcn_s_sleep(8);
  }
  return false;
}

template <int SLP, int AT = 0>
__device__ __forceinline__ bool wait_flag_c(rsrc_t R, unsigned vo, int need, int& cache) {
  if (__all(cache >= need)) return true;
  for (unsigned spins = 0; spins < SPINCAP; ++spins) {
    CBAR();
    cache = (int)(AT ? lda(R, vo) : ld32(R, vo, 0));
    if (__all(cache >= need)) return true;
    __builtin_amdgcn_s_sleep(SLP);
  }
  return false;
}
__device__ __forceinline__ unsigned umax3(unsigned a, unsigned b, unsigned c) { return max(max(a, b), c); }

template <int NTB> __device__ __forceinline__ u32x4 gld(const u16* p) {
  if (NTB) return __builtin_nontemporal_load((const u32x4*)p);
  return *(const u32x4*)p;
}
#define XSTR2(x) #x
#define XSTR(x) XSTR2(x)
#define LSLP 3
#define ESLP 0
#define MSLP 0
#define PXS 6
#define TSLP 2
#define XSLP 2
#define KSLP 10
#define OSLP 24
#define OSL2 4
#define UE 1
#define YL
#define CPF 3
#define PARK 58
#define OFAST 60
#define CPS 8
#define CSL 2
#define TSL 6
#define UGO 5
#define UPO 1
#define CALN 4
#define QDEP 4
#define PDEP 3
#define WDEP 3
#define VSLP 2
#define QLN 24
#define VLN 8
#define PLN 32
#define LMASK(n) ((n) >= 64 ? ~0ull : ((1ull << ((n) & 63)) - 1))
#define UP3 0
#define ORDT 0x73652410u
#define USLP 8
#define UT 0
#define UTD 1
#define EMF 0
#define EMV 0
#define MWPOS 1
#define DSLP "s_sleep 1\n\t"
typedef __attribute__((ext_vector_type(4))) int i32x4;
__device__ __forceinline__ u64 ballot(bool c) { return __builtin_amdgcn_ballot_w64(c); }
#define UNLIKELY(c) __builtin_expect(!!(c), 0)
#define DELTA 164
#define D1 98
#define DVQ 4
#define LEV0 1
#define LEV3 1
#define LEVMAX 7
#define CL0 8
#define HEAT 3
#define COOL0 4
#define PSLP 8
#define VK0 64
#define AWE 0
#define ASLP 0
#define LEVF 99
#define RX 0
#define VSL 6
#define D0 14
#define WLAST 41
#define UDL 8
#define MGAP 0
#define SMIN 8
#define DE 82
#define K1 4
#define PAD0 4
#define PADD 4
#define PADMAX 16
#define OAUX 0
__device__ __forceinline__ unsigned rt_now() {
  u64 t; asm volatile("s_memrealtime %0\n\ts_waitcnt lgkmcnt(0)" : "=s"(t) :: "memory"); return (unsigned)t;
}
#define S1 26
#define DEA 86
#define KA_ 70
#define S2 30
#define DEC 80
#define KC_ 66
#define S0 3
#define PACE0 ((DE0 << 8) | DELTA0)
#define PACEA ((DEA << 8) | (DEA + KA_))
#define PACEB ((DE << 8) | (DE + K2))
#define PACEC ((DEC << 8) | (DEC + KC_))
__device__ __forceinline__ void next_time(unsigned& t, int s) {
  t += (s < S0 ? PACE0 : (s < S1 ? PACEA : (s < S2 ? PACEB : PACEC))) & 255u;
  t = __builtin_amdgcn_readfirstlane(t);
  asm volatile("" : "+s"(t));
}
#define SNOP 1
#define SPIN(T, a, b, c) a ":\n\ts_memrealtime vcc\n\ts_waitcnt lgkmcnt(0)\n\ts_sub_u32 %[r], %[" #T "], vcc_lo\n\ts_sub_u32 %[k], %[r], 4\n\t"   \
               "s_cmp_lt_u32 %[k], 2000\n\ts_cbranch_scc1 " a "b\n\t"                                                                       \
               "s_sub_u32 %[k], %[r], 1\n\ts_cmp_lt_u32 %[k], 3\n\ts_cbranch_scc0 " c "f\n\t"                                               \
               b ":\n\ts_nop " XSTR(SNOP) "\n\ts_sub_u32 %[k], %[k], 1\n\ts_cbranch_scc0 " b "b\n\t" c ":\n\t"
__device__ __forceinline__ int spin_until(unsigned T) {
  int r, k;
  asm volatile(SPIN(T, "1", "2", "3") : [r] "=&s"(r), [k] "=&s"(k) : [T] "s"(T) : "memory", "vcc", "scc");
  return r;
}
#define KA "v_mov_b32 v240, v240\n\ts_nop 2\n\t"
#define KAN KA KA
#define EVA(n)
#define EVC(k)
#define BUD(l) "s_sub_u32 %[bud], %[bud], 1\n\ts_cbranch_scc0 " l "\n\ts_mov_b32 %[bud], 0\n\t"
__device__ __forceinline__ i32x4 mkdesc(char* wb) {
  const u64 p = (u64)wb;
  return (i32x4){(int)(unsigned)p, (int)(unsigned)(p >> 32), (int)OFF_END, 0x00020000};
}
template <class T> __device__ __forceinline__ void pin_v(T& v) { asm volatile("" : "+v"(v)); }
#define SB() __builtin_amdgcn_sched_barrier(0)
__device__ __forceinline__ unsigned pkv(float a, float b) {
  unsigned d; asm volatile("v_cvt_pk_bf16_f32 %0, %1, %2" : "=v"(d) : "v"(a), "v"(b)); return d;
}
__device__ __forceinline__ f32x4 mfB0(u32x4 a, u32x4 b) {
  f32x4 d; asm volatile("v_mfma_f32_16x16x32_bf16 %0, %1, %2, 0" : "=&v"(d) : "v"(a), "a"(b)); return d;
}
__device__ __forceinline__ void mfB(f32x4& d, u32x4 a, u32x4 b) {
  asm volatile("v_mfma_f32_16x16x32_bf16 %0, %1, %2, %0" : "+v"(d) : "v"(a), "a"(b));
}

template <int NTB>
__device__ __forceinline__ void qk_gload(u32x4 (&st)[8], const Args& a, int h, int c, int tid) {
#pragma unroll
  for (int k = 0; k < 8; ++k) {
    int u = tid + 256 * k; int row = u >> 5, cu = u & 31;
    const u16* W = (cu < 16) ? a.Wq : a.Wk;
    st[k] = gld<NTB>(W + (size_t)(64 * c + row) * HD + h * 128 + (cu & 15) * 8);
  }
}
__device__ __forceinline__ void qk_swrite(const u32x4 (&st)[8], lds_char* buf, int tid) {
#pragma unroll
  for (int k = 0; k < 8; ++k) { int u = tid + 256 * k; int row = u >> 5, cu = u & 31; *(lds_u32x4*)(buf + row * 544 + cu * 16) = st[k]; }
}
#define STG_BYTES 38912
#define X0_OFF (2 * STG_BYTES)
#define X1_OFF (X0_OFF + 37888)

__device__ __forceinline__ int cls_look(rsrc_t R, int lane) { return (int)ld32(R, FL_FLIP + (lane & 31) * 4, 0); }
__device__ __forceinline__ int rowsum(int v) {
  v += __builtin_amdgcn_update_dpp(v, v, 0x128, 0xf, 0xf, false);
  v += __builtin_amdgcn_update_dpp(v, v, 0x124, 0xf, 0xf, false);
  v += __builtin_amdgcn_update_dpp(v, v, 0x122, 0xf, 0xf, false);
  return v + __builtin_amdgcn_update_dpp(v, v, 0x121, 0xf, 0xf, false);
}
__device__ __forceinline__ int get_cls(rsrc_t R, int lane, int v = -1) {
  for (unsigned spins = 0; spins < SPINCAP; ++spins) {
    if (__all(v >= 0)) {
      const int n = rowsum((lane & 1) ? -v : v) > 0;
      const int d = rowsum((lane & 1) != n ? 0 : (((lane >> 1) ^ (lane >> 2)) & 1 ? -v : v)) > 0;
      const int c = n | (d << 1);
      return (__builtin_amdgcn_readlane(c, 0) * 0x55 ^ 0x88) | (__builtin_amdgcn_readlane(c, 16) << 8);
    }
    if (spins) __builtin_amdgcn_s_sleep(CPS);
    CBAR();
    v = cls_look(R, lane);
  }
  return -1;
}
__device__ __forceinline__ void calibrate(rsrc_t R, const Go& go, int r, int k, int lane) {
  unsigned best = 0xFFFFFFu;
  for (int it = 0; it < CALN; ++it) {
    unsigned long long t0, t1;
    asm volatile("s_waitcnt vmcnt(0)\n\ts_memtime %0\n\ts_waitcnt lgkmcnt(0)" : "=s"(t0) :: "memory");
    st32((unsigned)it, R, (lane == 0) ? (unsigned)((r >> 2) * OFF_YR + REAL_CAL + (r & 3) * 256 + k * 8192 + (it & 1) * 4096) : OOB, 0);
    asm volatile("s_waitcnt vmcnt(0)\n\ts_memtime %0\n\ts_waitcnt lgkmcnt(0)" : "=s"(t1) :: "memory");
    best = min(best, (unsigned)(t1 - t0));
  }
  if (go_wait(R, go, lane)) st32(best, R, lane == 0 ? (unsigned)(FL_FLIP + (r * 4 + k) * 4) : OOB, 0);
}
__device__ __forceinline__ unsigned get_t1(rsrc_t R, unsigned vo, unsigned& bud) {
  unsigned v = 0;
#pragma nounroll
  for (; bud != 0; --bud) {
    CBAR();
    v = __builtin_amdgcn_readfirstlane(ld32(R, vo, 0));
    if (v != ~0u) break;
    __builtin_amdgcn_s_sleep(2);
  }
  return v;
}

#define F8_OFF 18432
#define QSTG 34816
#define ISOT()
#define ISOQ(n)
__device__ __forceinline__ float vmax(float a, float b) { float d; asm("v_max_f32 %0, %1, %2" : "=v"(d) : "v"(a), "v"(b)); return d; }
__device__ __forceinline__ float vmax3(float a, float b, float c) { float d; asm("v_max3_f32 %0, %1, %2, %3" : "=v"(d) : "v"(a), "v"(b), "v"(c)); return d; }
__device__ __forceinline__ float max32(float x) {
  auto t = __builtin_amdgcn_permlane32_swap(__float_as_uint(x), __float_as_uint(x), false, false);
  return vmax(__uint_as_float(t[0]), __uint_as_float(t[1]));
}
__device__ __forceinline__ float max16(float x) {
  auto t = __builtin_amdgcn_permlane16_swap(__float_as_uint(x), __float_as_uint(x), false, false);
  return vmax(__uint_as_float(t[0]), __uint_as_float(t[1]));
}
__device__ __forceinline__ float sum32(float x) {
  auto t = __builtin_amdgcn_permlane32_swap(__float_as_uint(x), __float_as_uint(x), false, false);
  return __uint_as_float(t[0]) + __uint_as_float(t[1]);
}
template <int r> __device__ __forceinline__ void swp32(f32x4& a, f32x4& b) {
  auto t = __builtin_amdgcn_permlane32_swap(__float_as_uint(a[r]), __float_as_uint(b[r]), false, false);
  a[r] = __uint_as_float(t[0]); b[r] = __uint_as_float(t[1]);
}
__device__ __forceinline__ float vadd(float a, float b) { float d; asm("v_add_f32 %0, %1, %2" : "=v"(d) : "v"(a), "v"(b)); return d; }
__device__ __forceinline__ float max8(float x) {
  float d; asm("s_nop 1\n\tv_max_f32_dpp %0, %1, %1 row_ror:8 row_mask:0xf bank_mask:0xf" : "=&v"(d) : "v"(x)); return d;
}
__device__ __forceinline__ float sum8(float x) {
  float d; asm("s_nop 1\n\tv_add_f32_dpp %0, %1, %1 row_ror:8 row_mask:0xf bank_mask:0xf" : "=&v"(d) : "v"(x)); return d;
}
__device__ __forceinline__ float sum16(float x) {
  auto t = __builtin_amdgcn_permlane16_swap(__float_as_uint(x), __float_as_uint(x), false, false);
  return __uint_as_float(t[0]) + __uint_as_float(t[1]);
}
__device__ __forceinline__ void st64(u32x2 v, rsrc_t R, unsigned vo, unsigned so) { __builtin_amdgcn_raw_buffer_store_b64(v, R, vo, so, SC1); }
template <int N>
__device__ __forceinline__ void mem_issue2(i32x4 D, unsigned m0, unsigned m1, unsigned m2, unsigned so, unsigned l0, unsigned l1, unsigned l2, u64& ew) {
  asm volatile("s_mov_b32 m0, %[l0]\n\ts_barrier\n\t" EVASM
               "buffer_load_dwordx4 %[m0], %[D], %[so] offen sc1 lds\n\t"
               "s_mov_b32 m0, %[l1]\n\ts_nop 0\n\t"
               "buffer_load_dwordx4 %[m1], %[D], %[so] offen sc1 lds\n\t"
               ".if %[n] == 3\n\ts_mov_b32 m0, %[l2]\n\ts_nop 0\n\t"
               "buffer_load_dwordx4 %[m2], %[D], %[so] offen sc1 lds\n\t.endif"
               : [ew] "=&s"(ew)
               : [m0] "v"(m0), [m1] "v"(m1), [m2] "v"(m2), [D] "s"(D), [so] "s"(so), [l0] "s"(l0), [l1] "s"(l1), [l2] "s"(l2), [n] "n"(N)
               : "memory");
}
#define TRYB ".if %[id] >= 0\n\ts_cbranch_vccnz .Ltry%[id]\n.Lend%[id]:\n\t.endif\n\t"
#define TRYC "vcc", "scc", "v236", "v237", "v238", "v239", "v240"
template <int ID>
__device__ __forceinline__ void try_top(unsigned& nt, unsigned& ms, unsigned& tm, unsigned& rcnt, unsigned ia) {
  asm volatile(".Ltry%[id]:\n\ts_waitcnt lgkmcnt(0)\n\ts_cmp_ge_i32 %[nt], 2\n\ts_cbranch_scc1 1f\n"
               "2:\n\ts_nop 4\n\ts_xor_b32 %[ms], %[ms], %[fl]\n\ts_add_u32 %[tm], %[tm], " XSTR(GRID) "+" XSTR(XT) "\n\ts_add_u32 %[nt], %[nt], 1\n\t.subsection 1\n"
               "1:\n\tv_mov_b32 v240, 0\n\t"
               ".irp o, 0, 1024, 2048, 3072, 4096, 5120, 6144, 7168, " XSTR(F8_OFF) "\n\tds_read_b128 v[236:239], %[ia] offset:\\o\n\ts_waitcnt lgkmcnt(0)\n\t"
               "v_max3_u32 v240, v240, v236, v237\n\tv_max3_u32 v240, v240, v238, v239\n\t.endr\n\t"
               "v_cmp_eq_u32 vcc, -1, v240\n\ts_cbranch_vccz .Lend%[id]\n\ts_add_u32 %[rc], %[rc], 1\n\ts_cmp_lt_u32 %[rc], %[cap]\n\t"
               "s_cbranch_scc1 2b\n\ts_branch .Lend%[id]\n\t.subsection 0"
               : [nt] "+s"(nt), [ms] "+s"(ms), [tm] "+s"(tm), [rc] "+s"(rcnt)
               : [fl] "n"(CPY2 + CPY1), [id] "n"(ID), [ia] "v"(ia), [cap] "n"(SPINCAP / 2) : "memory", TRYC);
}
__device__ __forceinline__ void try_end(unsigned ia, unsigned nt, unsigned ms, unsigned tm, unsigned rcnt,
                                        unsigned m0, unsigned m1, unsigned m2, unsigned l0, unsigned l1, unsigned l2, i32x4 D) {
  asm volatile("" :: "s"(rcnt), "s"(nt), "s"(ms), "s"(tm), "v"(ia), "v"(m0), "v"(m1), "v"(m2), "s"(l0), "s"(l1), "s"(l2), "s"(D) : "memory");
}
__device__ __forceinline__ int spin_late(unsigned& t, unsigned d) {
  int r, k; unsigned T;
  asm volatile("s_add_u32 %[T], %[t], %[d]\n\t" SPIN(T, "1", "2", "3") : [r] "=&s"(r), [k] "=&s"(k), [T] "=&s"(T), [t] "+s"(t) : [d] "n"(d) : "memory", "vcc", "scc");
  return r;
}
__device__ __forceinline__ void move_on(unsigned& t, int r) {
  asm volatile("1:\n\ts_cmp_lt_i32 %1, -" XSTR(LTOL) "\n\ts_cbranch_scc0 2f\n\ts_add_u32 %0, %0, " XSTR(GRID) "+" XSTR(XT) "\n\ts_add_u32 %1, %1, " XSTR(GRID) "+" XSTR(XT) "\n\ts_branch 1b\n2:"
               : "+s"(t), "+s"(r) :: "scc");
}
__device__ __forceinline__ void gate_wait(rsrc_t R, unsigned vo, int need, int& cache, unsigned& bud) {
#pragma nounroll
  for (; bud != 0; --bud) {
    if (__all(cache >= need)) break;
    CBAR();
    cache = (int)ld32(R, vo, 0);
    __builtin_amdgcn_s_sleep(4);
  }
}
__device__ __forceinline__ unsigned um3v(unsigned a, unsigned b, unsigned c) {
  unsigned d; asm volatile("v_max3_u32 %0, %1, %2, %3" : "=v"(d) : "v"(a), "v"(b), "v"(c)); return d;
}
template <int OFF>
__device__ __forceinline__ void k_put(f32x4 k, unsigned ad) {
  unsigned t0, t1;
  asm volatile("v_cvt_pk_bf16_f32 %0, %2, %3\n\tv_cvt_pk_bf16_f32 %1, %4, %5\n\tds_write2_b32 %6, %0, %1 offset0:%7 offset1:%8"
               : "=&v"(t0), "=&v"(t1) : "v"(k[0]), "v"(k[1]), "v"(k[2]), "v"(k[3]), "v"(ad), "n"(OFF), "n"(OFF + 2) : "memory");
}
template <int OFF>
__device__ __forceinline__ void q_put(f32x4 k, unsigned ad) {
  unsigned t0, t1;
  asm volatile("v_cvt_pk_bf16_f32 %0, %2, %3\n\tv_cvt_pk_bf16_f32 %1, %4, %5\n\tds_write2_b32 %6, %0, %1 offset0:%7 offset1:%8"
               : "=&v"(t0), "=&v"(t1) : "v"(k[0]), "v"(k[1]), "v"(k[2]), "v"(k[3]), "v"(ad), "n"(OFF), "n"(OFF + 1) : "memory");
}
__device__ __forceinline__ f32x4 mf0(u32x4 a, u32x4 b) {
  f32x4 d; asm volatile("v_mfma_f32_16x16x32_bf16 %0, %1, %2, 0" : "=&v"(d) : "v"(a), "v"(b)); return d;
}
__device__ __forceinline__ void mfa(f32x4& d, u32x4 a, u32x4 b) {
  asm volatile("v_mfma_f32_16x16x32_bf16 %0, %1, %2, %0" : "+v"(d) : "v"(a), "v"(b));
}
__device__ __forceinline__ f32x4 mfA0(u32x4 a, u32x4 b) {
  f32x4 d; asm volatile("v_mfma_f32_16x16x32_bf16 %0, %1, %2, 0" : "=&v"(d) : "a"(a), "v"(b)); return d;
}
__device__ __forceinline__ void mfA(f32x4& d, u32x4 a, u32x4 b) {
  asm volatile("v_mfma_f32_16x16x32_bf16 %0, %1, %2, %0" : "+v"(d) : "a"(a), "v"(b));
}
#define DPPLO " row_ror:8 row_mask:0xf bank_mask:0x3\n\t"
#define DPPHI " row_ror:8 row_mask:0xf bank_mask:0xc\n\t"
#define DPPAL " row_ror:8 row_mask:0xf bank_mask:0xf\n\t"
#define SCK "0x3e0293ee"
#define SCN "0xbe0293ee"
__device__ __forceinline__ void px_mask(f32x4& T0, f32x4& T1, int qt, int lane) {
  const float ninf = -__builtin_inff();
#pragma unroll
  for (int r = 0; r < 4; ++r) {
    const bool diag = (4 * (lane >> 4) + r) <= (lane & 15);
    T0[r] = (qt == 1 || diag) ? T0[r] : ninf;
    T1[r] = (qt == 1 && diag) ? T1[r] : ninf;
  }
}
__device__ __forceinline__ void px_seg(f32x4& T0, f32x4& T1, float& mx, float& sx) {
  const float sc = 0.08838834764831845f * 1.4426950408889634f;
  mx = xmax(fmaxf(fmaxf(fmaxf(T0[0], T0[1]), fmaxf(T0[2], T0[3])), fmaxf(fmaxf(T1[0], T1[1]), fmaxf(T1[2], T1[3]))));
  const float ms = -mx * sc;
  sx = 0.f;
#pragma unroll
  for (int r = 0; r < 4; ++r) {
    T0[r] = __builtin_amdgcn_exp2f(fmaf(T0[r], sc, ms)); T1[r] = __builtin_amdgcn_exp2f(fmaf(T1[r], sc, ms));
    sx += T0[r] + T1[r];
  }
}
__device__ __forceinline__ void px_fin(f32x4 G, f32x4 T0, f32x4 T1, float mx, float sx, u32x4& px, unsigned& pw, int lane) {
  const float sc = 0.08838834764831845f * 1.4426950408889634f;
  float g0 = (lane & 16) ? G[2] : G[0], g1 = (lane & 16) ? G[3] : G[1];
  const float ms = -xmax(fmaxf(fmaxf(g0, g1), mx)) * sc;
  g0 = __builtin_amdgcn_exp2f(fmaf(g0, sc, ms)); g1 = __builtin_amdgcn_exp2f(fmaf(g1, sc, ms));
  const float f = __builtin_amdgcn_exp2f(fmaf(mx, sc, ms));
  asm volatile("" : "+v"(g0), "+v"(g1)); PAD();
  const float inv = __builtin_amdgcn_rcpf(xsum(fmaf(f, sx, g0 + g1))), fi = f * inv;
  px = (u32x4){pk2(T0[0] * fi, T0[1] * fi), pk2(T0[2] * fi, T0[3] * fi), pk2(T1[0] * fi, T1[1] * fi), pk2(T1[2] * fi, T1[3] * fi)};
  pw = pk2(g0 * inv, g1 * inv);
}
#define WPOS 4
#define QPOS 1
#define LPOS 1
#define QIM_OFF 8192
#define KIM_OFF 10240
#define FLG_OFF 17408
#define F8W 3
#define KX_OFF 36864
typedef __attribute__((ext_vector_type(16))) float f32x16;
typedef __attribute__((ext_vector_type(16))) unsigned u32x16;
__device__ __forceinline__ void ds_flag(unsigned ad, unsigned v) { asm volatile("ds_write_b32 %0, %1" :: "v"(ad), "v"(v) : "memory"); }
#define SMX(d, j, w) "v_smfmac_f32_16x16x64_bf16 v[" d "], %[a" #j "], %[" #w "], %[ix]\n\t"
#define CVX(d, x, y) "v_cvt_pk_bf16_f32 v" #d ", v" #x ", v" #y "\n\t"
#define PKA(d, b) "v_pk_add_f32 v[" #d ":" #d "+1], v[" #b ":" #b "+1], v[" #b "+2:" #b "+3]\n\t"
#define IMW(a, o) "ds_write_b32 " a ", v232 offset:" o "\n\t"
template <int T, int AG>
__device__ __forceinline__ void smo(f32x4& c, u32x4 a, u32x8 b, int ix) {
  if (T == 0 && AG) asm volatile(SMX("140:143", 0, w) : "+{v[140:143]}"(c) : [a0] "v"(a), [w] "a"(b), [ix] "v"(ix));
  if (T == 0 && !AG) asm volatile(SMX("140:143", 0, w) : "+{v[140:143]}"(c) : [a0] "v"(a), [w] "v"(b), [ix] "v"(ix));
  if (T == 1 && AG) asm volatile(SMX("144:147", 0, w) : "+{v[144:147]}"(c) : [a0] "v"(a), [w] "a"(b), [ix] "v"(ix));
  if (T == 1 && !AG) asm volatile(SMX("144:147", 0, w) : "+{v[144:147]}"(c) : [a0] "v"(a), [w] "v"(b), [ix] "v"(ix));
}
__device__ __forceinline__ void v_zero(f32x4& v0, f32x4& v1) {
  asm volatile("v_mov_b64 v[140:141], 0\n\tv_mov_b64 v[142:143], 0\n\tv_mov_b64 v[144:145], 0\n\tv_mov_b64 v[146:147], 0\n\ts_nop 1"
               : "={v[140:143]}"(v0), "={v[144:147]}"(v1));
}
__device__ __forceinline__ void k_zero(f32x4& k0, f32x4& k1, f32x4& k2) {
  asm volatile(".rept 6\n\tv_mov_b64 v[148+2*\\+:149+2*\\+], 0\n\t.endr" : "={v[148:151]}"(k0), "={v[152:155]}"(k1), "={v[156:159]}"(k2));
}
#define TZERO ".rept 6\n\tv_mov_b64 v[200+2*\\+:201+2*\\+], 0\n\t.endr\n\t"
__device__ __forceinline__ void t_zero(f32x4& k0, f32x16& T) {
  asm volatile("v_mov_b64 v[148:149], 0\n\tv_mov_b64 v[150:151], 0\n\t" TZERO : "={v[148:151]}"(k0), "={v[200:215]}"(T));
}
#define AD0(b) "v_add_f32 v230, v[" #b "], v[" #b "+2]\n\t"
#define AD1(b) "v_add_f32 v231, v[" #b "+1], v[" #b "+3]\n\t"
#define CVP CVX(232, 230, 231)
#define Q1(j) SMX("144:147", j, w##j)
template <int ID>
__device__ __forceinline__ void q_mid(const u32x4 (&af)[9], const u32x8 (&w)[9], int ix, unsigned qw, f32x4 q0, f32x4& q1) {
  asm volatile(Q1(3) Q1(4) AD0(140) Q1(5) AD1(140) "v_cmp_u_f32 vcc, v230, v231\n\t" Q1(6) CVP Q1(7) TRYB IMW("%[qw]", "0") Q1(8)
               : "+{v[144:147]}"(q1)
               : "{v[140:143]}"(q0), [a3] "v"(af[3]), [a4] "v"(af[4]), [a5] "v"(af[5]), [a6] "v"(af[6]), [a7] "v"(af[7]), [a8] "v"(af[8]),
                 [w3] "a"(w[3]), [w4] "a"(w[4]), [w5] "a"(w[5]), [w6] "a"(w[6]), [w7] "a"(w[7]), [w8] "v"(w[8]), [ix] "v"(ix), [qw] "v"(qw), [id] "n"(ID)
               : "memory", "v230", "v231", "v232", TRYC);
}
__device__ __forceinline__ void k_last(unsigned kw, f32x4 q1, unsigned fw, unsigned fv) {
  asm volatile("s_nop 7\n\t" PKA(230, 144) CVP IMW("%[kw]", "1024") "ds_write_b32 %[fw], %[fv]"
               :: "{v[144:147]}"(q1), [kw] "v"(kw), [fw] "v"(fw), [fv] "v"(fv) : "memory", "v230", "v231", "v232");
}
#define K0(j) SMX("148:151", j, u##j)
#define K1(j) SMX("152:155", j, u##j)
#define AF9 [a0] "v"(af[0]), [a1] "v"(af[1]), [a2] "v"(af[2]), [a3] "v"(af[3]), [a4] "v"(af[4]), [a5] "v"(af[5]), [a6] "v"(af[6]), [a7] "v"(af[7]), [a8] "v"(af[8])
#define WK9 [u0] "a"(wk[0]), [u1] "a"(wk[1]), [u2] "a"(wk[2]), [u3] "a"(wk[3]), [u4] "a"(wk[4]), [u5] "a"(wk[5]), [u6] "a"(wk[6]), [u7] "a"(wk[7]), [u8] "v"(wk[8])
__device__ __forceinline__ void k_one(const u32x4 (&af)[9], const u32x8 (&wk)[9], const u32x8 (&wp)[3], int ix, unsigned qw, f32x4 q1, f32x4& k0, f32x4& k2, float& t) {
  asm volatile(K0(8) K0(0) AD0(144) AD1(144) CVP K0(4) IMW("%[qw]", "1024") K0(1) K0(2) K0(3) K0(5) PADA K0(6) K0(7)
               SMX("156:159", 0, p0) SMX("156:159", 4, p1) AD0(148)
               : "+{v[148:151]}"(k0), "+{v[156:159]}"(k2), "=&{v230}"(t)
               : "{v[144:147]}"(q1), AF9, WK9, [p0] "v"(wp[0]), [p1] "v"(wp[1]), [ix] "v"(ix), [qw] "v"(qw) : "memory", "v231", "v232");
}
#define KB
template <int PO, int JC>
__device__ __forceinline__ void k_end(const u32x4 (&af)[9], const u32x8 (&wk)[9], u32x8 wp, int ix, f32x4 k0, f32x4 k1, f32x4 k2, float t, unsigned kw, unsigned fw, unsigned fv) {
  asm volatile(".if %[jc] == 3\n\t" SMX("156:159", 3, p2) ".endif\n\t.if %[jc] == 6\n\t" SMX("156:159", 6, p2) ".endif\n\t.if %[jc] == 8\n\t" SMX("156:159", 8, p2) ".endif\n\t"
               AD1(148) K1(8) CVP K1(0) IMW("%[kw]", "0") K1(4) AD0(156) K1(1) AD1(156) K1(2) CVP K1(3) IMW("%[kw]", "%[po]") K1(5) PADA K1(6) K1(7)
               "s_nop 7\n\t" PKA(230, 152) CVP IMW("%[kw]", "1024") KB "ds_write_b32 %[fw], %[fv]\n\t"
               :: "{v[148:151]}"(k0), "{v[152:155]}"(k1), "{v[156:159]}"(k2), "{v230}"(t), AF9, WK9, [p2] "v"(wp), [ix] "v"(ix), [kw] "v"(kw), [fw] "v"(fw),
                  [fv] "v"(fv), [po] "n"(PO), [jc] "n"(JC)
               : "memory", "v231", "v232");
}
__device__ __forceinline__ unsigned add3bf(unsigned a, unsigned b, unsigned c) {
  return pk2(__uint_as_float(a << 16) + __uint_as_float(b << 16) + __uint_as_float(c << 16),
             __uint_as_float(a & 0xffff0000u) + __uint_as_float(b & 0xffff0000u) + __uint_as_float(c & 0xffff0000u));
}
#define IS(n)
#define cKW "v112"
#define cKA "v113"
#define cQ0 "v114"
#define cQ1 "v115"
#define cQ3 "v116"
#define cFA "v117"
#define cEO "v118"
#define cLO "v119"
#define cC0 "v120"
#define cC1 "v121"
#define cC2 "v122"
#define cFW "v123"
#define cQW "v124"
#define cKX "v125"
#define TRD(r, a, o) "ds_read_b64_tr_b16 v[" #r ":" #r "+1], " a " offset:" #o "\n\t"
#define LGK(n) "s_waitcnt lgkmcnt(" #n ")\n\t"
#define SXM(t, c) "v_smfmac_f32_16x16x64_bf16 v[200+4*" #t ":203+4*" #t "], v[160+4*" #c ":163+4*" #c "], v[64+16*" #t "+8*" #c ":71+16*" #t "+8*" #c "], %[ix]\n\t"
#define SMM(c) "v_smfmac_f32_16x16x64_bf16 v[208:211], v[160+4*" #c ":163+4*" #c "], v[176+8*" #c ":183+8*" #c "], %[ix]\n\t"
#define QRE TRD(160, cQ0, 0) TRD(162, cQ1, 0)
#define QRL TRD(164, cQ0, 1024) TRD(166, cQ1, 1024)
#define CN(k)
#define QE 0
#define QL 1
#define KL -1
#define QP(n) ".if " XSTR(QE) " == " #n "\n\t" QRE ".endif\n\t"
#define QQ(n) ".if " XSTR(QL) " == " #n "\n\t" QRL ".endif\n\t"
template <int PH>
__device__ __forceinline__ void kx_get(u32x16 cst, u32x16& x0, u32x16& x1) {
#define KXR(k) "ds_read_b128 v[64+4*" #k ":67+4*" #k "], " cKX " offset:%[o]+1024*" #k "\n\tds_read_b128 v[80+4*" #k ":83+4*" #k "], " cKX " offset:%[o]+1024*" #k "+16\n\t"
  asm volatile(KXR(0) KXR(1) KXR(2) KXR(3)
               : "={v[64:79]}"(x0), "={v[80:95]}"(x1) : "{v[112:127]}"(cst), [o] "n"(KX_OFF + PH * 8192) : "memory");
}
#define NK 2
#define KIF(c, t) ".if " XSTR(NK) c "\n\t" t ".endif\n\t"
__device__ __forceinline__ void s_first(const u32x4 (&af)[9], const u32x8 (&wk)[9], int ix, u32x16 cst, u32x16 x0, u32x16 x1, f32x4 q1, f32x4& k0, f32x16& T, u32x8& Q) {
  asm volatile(K0(8) QP(0) K0(0) AD0(144) AD1(144) CVP K0(4) IMW(cQW, "1024") K0(1) QP(1) QQ(0) K0(2) QQ(1) K0(3) QQ(2) KIF(" == 0", K0(5))
               LGK(2) SXM(0, 0) SXM(1, 0) LGK(0) SXM(0, 1) SXM(1, 1)
               : "+{v[148:151]}"(k0), "+{v[200:215]}"(T), "=&{v[160:167]}"(Q)
               : "{v[144:147]}"(q1), [a0] "v"(af[0]), [a1] "v"(af[1]), [a2] "v"(af[2]), [a3] "v"(af[3]), [a4] "v"(af[4]), [a5] "v"(af[5]), [a8] "v"(af[8]),
                 [u0] "a"(wk[0]), [u1] "a"(wk[1]), [u2] "a"(wk[2]), [u3] "a"(wk[3]), [u4] "a"(wk[4]), [u5] "a"(wk[5]), [u8] "v"(wk[8]),
                 [ix] "v"(ix), "{v[112:127]}"(cst), "{v[64:79]}"(x0), "{v[80:95]}"(x1)
               : "memory", "v230", "v231", "v232");
}
#define DPPF " row_mask:0xf bank_mask:0xf\n\t"
#define DX1 " quad_perm:[1,0,3,2]" DPPF
#define DX2 " quad_perm:[2,3,0,1]" DPPF
#define DX4 " row_half_mirror" DPPF
__device__ __forceinline__ void el_phase(i32x4 D, const u32x4 (&af)[9], const u32x8 (&wk)[9], int ix, u32x16 cst, u32x16 x0, u32x16 x1, u32x8& Q, f32x4& k0, f32x16& T,
                                         unsigned& bud, u64& e1, u64& e4, unsigned& nr, u32x8 sc, unsigned fv) {
#define EST(s) "buffer_store_dwordx2 v[222:223], " cEO ", %[D], " s " offen sc1\n\t"
#define LST(s) "buffer_store_dword v227, " cLO ", %[D], " s " offen sc1\n\t"
#define EPART(KA, KB_)                                                                                                             \
               IS(4) KA "v_add_f32 v212, v200, v202\n\t" KB_ "v_add_f32 v213, v201, v203\n\tv_add_f32 v214, v204, v206\n\tv_add_f32 v215, v205, v207\n\t" \
               "v_max_f32 v216, v212, v214\n\tv_max_f32 v217, v213, v215\n\tv_mov_b32 v219, v212\n\t"                              \
               "v_max_f32_dpp v218, v216, v216" DPPLO "v_max_f32_dpp v218, v217, v217" DPPHI                                       \
               "v_mov_b32 v220, v214\n\ts_nop 0\n\t"                                                                             \
               "v_max_f32_dpp v218, v218, v218" DX1 "v_mov_b32_dpp v212, v213" DPPHI "v_mov_b32_dpp v214, v215" DPPHI              \
               "v_max_f32_dpp v218, v218, v218" DX2 "v_mov_b32_dpp v213, v219" DPPLO "v_mov_b32_dpp v215, v220" DPPLO              \
               "v_max_f32_dpp v218, v218, v218" DX4 IS(5)                                                                          \
               "v_mul_f32 v216, " SCN ", v218\n\t"                                                                                 \
               "v_pk_fma_f32 v[212:213], v[212:213], s[44:45], v[216:217] op_sel_hi:[1,0,0]\n\t"                                   \
               "v_pk_fma_f32 v[214:215], v[214:215], s[44:45], v[216:217] op_sel_hi:[1,0,0]\n\t"                                   \
               "v_exp_f32 v212, v212\n\tv_exp_f32 v214, v214\n\tv_exp_f32 v213, v213\n\tv_exp_f32 v215, v215\n\t"                  \
               CVX(222, 212, 214) CVX(223, 213, 215)
#define MPART(KA, KB_, KBW)                                                                                                        \
               KA EST("s40") TRD(168, cQ3, 0) TRD(192, cKA, 2048) EST("s41") EVA(4) IS(6) TRD(194, cKA, 2304) TRD(176, cKA, 0)      \
               KB_ TRD(178, cKA, 256) TRD(180, cKA, 512) TRD(182, cKA, 768)                                                        \
               "v_add_f32 v212, v212, v214\n\tv_add_f32 v213, v213, v215\n\tv_mov_b32 v225, " cC2 "\n\tv_add_f32 v212, v212, v213\n\t"  \
               LGK(4) PKA(230, 148) CVP "v_smfmac_f32_16x16x32_bf16 v[208:211], v[168:169], v[192:195], %[ix]\n\t"                 \
               IMW(cKW, "1024") TRD(190, cKA, 1792) KBW TRD(184, cKA, 1024) TRD(186, cKA, 1280) TRD(188, cKA, 1536) LGK(5) SMM(0)  \
               EST("s42") "ds_write_b32 " cFW ", %[fv]\n\t" LGK(1) SMM(1) IS(7)
#define GADD   "v_add_f32_dpp v220, v208, v210 quad_perm:[0,1,2,3] row_mask:0xf bank_mask:0x3\n\t"                                 \
               "v_add_f32_dpp v220, v209, v211 quad_perm:[0,1,2,3] row_mask:0xf bank_mask:0xc\n\t"
#define LEND   "v_add_f32_dpp v217, v217, v217" DX4 "v_rcp_f32 v218, v217\n\t" PADN                                                \
               "v_pk_mul_f32 v[224:225], v[224:225], v[218:219] op_sel_hi:[1,0]\n\t"                                               \
               CVX(227, 224, 225) IS(8) LST("s40") LST("s41") EVA(1) IS(9) LST("s42") EST("s43") LST("s43")
#define KBF
#define EVO
  asm volatile(EPART(K0(6) KIF(" == 2", "s_nop 1\n\t"), KIF(" < 2", K0(7)))
               MPART(KIF(" > 0", K0(5)), KIF(" == 2", K0(7)), KBF)
               "s_nop 1\n\tv_add_f32_dpp v212, v212, v212" DX1 "s_nop 4\n\t" GADD
               "v_fmamk_f32 v221, v220, " SCK ", v216\n14:\n\tv_exp_f32 v220, v221\n\tv_cmp_nge_f32 vcc, s45, v221\n\tv_add_f32 v217, v220, v212\n\t"
               "v_add_f32_dpp v217, v220, v217" DX1 "v_fma_f32 v224, v220, " cC0 ", " cC1 "\n\tv_fmac_f32_dpp v225, v220, " cC0 DX1
               "v_add_f32_dpp v217, v217, v217" DX2 "s_cmp_eq_u64 vcc, 0\n\ts_cselect_b64 exec, -1, 0\n\t" LEND "s_cbranch_scc0 19f\n"
               "13:\n\t.subsection 1\n19:\n\ts_mov_b64 exec, -1\n\t" CN(10000)
               "20:\n\tds_read_b32 v224, " cFA "\n\t" LGK(0) "v_cmp_gt_i32 vcc, %[fv], v224\n\ts_cbranch_vccz 21f\n\t" BUD("20b")
               "21:\n\t" QRE QRL TZERO LGK(0) SXM(0, 0) SXM(1, 0) SXM(0, 1) SXM(1, 1)
               EPART("s_nop 7\n\t", "") MPART("", "", "")
               "v_mov_b32 v213, v212\n\tv_add_f32_dpp v212, v212, v212" DX1 "s_nop 5\n\t" GADD
               "v_fmamk_f32 v221, v220, " SCK ", v216\n\tv_cmp_nge_f32 vcc, s45, v221\n\ts_cbranch_vccz 14b\n\t"
               "v_max_f32 v216, v220, v218\n\tv_mul_f32 v221, " SCK ", v218\n\ts_nop 0\n\t"
               "v_max_f32_dpp v216, v216, v216" DX1 "v_mul_f32 v220, " SCK ", v220\n\ts_nop 0\n\t"
               "v_max_f32_dpp v216, v216, v216" DX2 "s_nop 1\n\t"
               "v_max_f32_dpp v216, v216, v216" DX4
               "v_pk_fma_f32 v[220:221], v[216:217], s[44:45], v[220:221] op_sel_hi:[0,0,1] neg_lo:[1,0,0] neg_hi:[1,0,0]\n\t"
               "v_exp_f32 v221, v221\n\tv_exp_f32 v220, v220\n\t"
               "v_mul_f32 v224, v221, " cC1 "\n\tv_fma_f32 v217, v221, v213, v220\n\t"
               "v_mul_f32 v225, v221, " cC2 "\n\tv_fmac_f32 v224, v220, " cC0 "\n\t"
               "v_add_f32_dpp v217, v217, v217" DX1 "v_fmac_f32_dpp v225, v220, " cC0 DX1 "s_nop 0\n\t"
               "v_add_f32_dpp v217, v217, v217" DX2 "s_nop 1\n\t" LEND "s_branch 13b\n\t.subsection 0"
               : [bud] "+s"(bud), "+{v[160:167]}"(Q), "+{v[148:151]}"(k0), "+{v[200:215]}"(T) EVO
               : [a5] "v"(af[5]), [a6] "v"(af[6]), [a7] "v"(af[7]), [u5] "a"(wk[5]), [u6] "a"(wk[6]), [u7] "a"(wk[7]), [ix] "v"(ix),
                 "{v[112:127]}"(cst), "{v[64:79]}"(x0), "{v[80:95]}"(x1), [D] "s"(D), "{s[40:47]}"(sc), [fv] "v"(fv)
               : "memory", "vcc", "scc", "v168", "v169", "v176", "v177", "v178", "v179", "v180", "v181", "v182", "v183", "v184",
                 "v185", "v186", "v187", "v188", "v189", "v190", "v191", "v192", "v193", "v194", "v195",
                 "v216", "v217", "v218", "v219", "v220", "v221", "v222", "v223", "v224", "v225", "v227", "v230", "v231", "v232");
}
#define YMAX 0x42700000u
#define TWO 2
#define ARD(j, p) do { af[j] = *(lds_u32x4*)(p); SB(); } while (0)
#define SECOND(n, NXV) do { if (TWO == n) { asm volatile("s_waitcnt vmcnt(%0)" :: "n"(NXV) : "memory"); __builtin_amdgcn_s_barrier(); CBAR(); SB(); \
                         ARD(4, ard0 + 4096); ARD(5, ard1 + 4096); } if (TWO + 1 == n) { ARD(6, ard2 + 4096); ARD(7, ard3 + 4096); }               \
                         if (TWO + 2 == n) ARD(8, lds + F8_OFF + ar); } while (0)
#define HEAD(M, NXV, H7) do {                                                                                                                     \
    ARD(0, ard0); __builtin_amdgcn_s_barrier(); CBAR(); SB(); ARD(1, ard1); ARD(2, ard2); ARD(3, ard3);                                       \
    if (!TWO) { ARD(4, ard0 + 4096); ARD(5, ard1 + 4096); }                                                                                   \
    M(0, 0); SECOND(1, NXV); M(1, 0); SECOND(2, NXV); M(0, 1); SECOND(3, NXV); M(1, 1); SECOND(4, NXV); M(0, 2); SECOND(5, NXV);              \
    if (!(H7)) M(1, 2);                                                                                                                       \
    SECOND(6, NXV); M(0, 3); M(0, 4); M(0, 5); M(0, 6); M(0, 7); M(0, 8); } while (0)
__device__ __forceinline__ void role_qkc(const Args& a, rsrc_t R, const Go& go, lds_char* lds, int h, int tid, int hw, int lane) {
  const int wave = 3 - hw;
  const int l15 = lane & 15, g = lane >> 4;
  u32x8 wq[2][9], wk[2][9], wp[3];
  int cls = -1, kv0 = -1;
  const int wz = h >> 3, tq = (h >> 1) & 3, hp = h & 1, qi = 2 * g + (l15 >> 3), kq = l15 & 7;
  unsigned pxo = (unsigned)nearoff(NEAR_E + (h >> 1) * 1024 + ((kq >> 1) * 16 + 8 * (qi >> 2) + 4 * hp + (qi & 3)) * 16 + (kq & 1) * 8);
  unsigned plo = !(kq & 1) ? (unsigned)nearoff(NEAR_PM + wz * 1024 + ((2 * hp + (tq >> 1)) * 16 + 4 * (qi >> 1) + (kq & 2) + (qi & 1)) * 16 + (tq & 1) * 8 + (kq >> 2) * 4)
                           : (kq == 1 ? (unsigned)nearoff(NEAR_AL + wz * 256 + ((tq >> 1) * 8 + qi) * 16 + (2 * hp + (tq & 1)) * 4) : OOB);
  {
    u32x4 sg[QDEP][8];
#pragma unroll
    for (int c = 0; c < QDEP; ++c) qk_gload<NTM & 1>(sg[c], a, h, c, tid);
#define WK3(b) do { WALK(b + 2); WALK(b + 1); WALK(b); } while (0)
    if (wave == 0) WK3(200); else if (wave == 1) WK3(208); else if (wave == 2) WK3(216); else WK3(224);
    const int q = l15 >> 2;
    const int lrow = 16 * (g >> 1) + 4 * (g & 1) + 8 * (q >> 1) + (q & 1);
    auto wrd = [&](lds_char* p) {
      u32x2 v0 = trd(p), v1 = trd(p + 2 * 544), v2 = trd(p + 32 * 544), v3 = trd(p + 34 * 544);
      return (u32x8){v0[0], v0[1], v1[0], v1[1], v2[0], v2[1], v3[0], v3[1]};
    };
#pragma unroll
    for (int ph = 0; ph < 3; ++ph) {
#pragma unroll
      for (int i = 0; i < 4; ++i) {
        const int c = ph * 4 + i;
        if (c < 9) {
          qk_swrite(sg[c % QDEP], lds + i * QSTG, tid);
          if (c + QDEP < 9) qk_gload<NTM & 1>(sg[c % QDEP], a, h, c + QDEP, tid);
          CBAR();
        }
      }
      RT(8 + ph);
      if (ph == 2 && go_wait(R, go, lane)) {
        kv0 = (int)ld32(R, FL_PROG + h * 128 + (lane & 3) * 4, 0);
        cls = get_cls(R, lane, cls_look(R, lane));
        pxo += (unsigned)(cls & 1) * 8192u; plo += (unsigned)(cls & 1) * 8192u;
        if (wave < 3) {
#pragma unroll
          for (int k = 0; k < 4; ++k) {
            const unsigned so = wave * SLOT_REAL + (k & 1) * CPY1 + (k >> 1) * CPY2;
            st64((u32x2){~0u, ~0u}, R, pxo, so); st32(~0u, R, plo, so);
          }
        }
        CBAR();
        RT(11);
      }
      BAR();
#pragma unroll
      for (int i = 0; i < 4; ++i) {
        const int j = ph * 4 + i;
        if (j < 9) {
          lds_char* buf = lds + (ph < 2 ? ((i + wave) & 3) : 0) * QSTG + lrow * 544 + (l15 & 3) * 8;
#pragma unroll
          for (int t = 0; t < 4; ++t) {
            u32x8& w = t < 2 ? wq[t & 1][j] : wk[t & 1][j];
            w = wrd(buf + t * 128 + wave * 32);
          }
          if (j == 0 || j == 4) wp[j >> 2] = wrd(buf + 256 + 96);
        }
      }
      SB();
#pragma unroll
      for (int k = 0; k < 16; ++k) {
        const int j = ph * 4 + (k >> 2);
        if (j < 9) { u32x8& w = (k & 2) ? wk[k & 1][j] : wq[k & 1][j]; if (j < 8) pin_a(w); else pin_v(w); }
      }
      if (ph < 2) pin_v(wp[ph]);
      if (ph == 0 || wave == ph) wp[2] = wrd(lds + (ph < 2 ? 3 : 0) * QSTG + lrow * 544 + (l15 & 3) * 8 + 256 + 96);
      pin_v(wp[2]);
      BAR();
    }
  }
  RT(1);
  if (cls < 0) return;
  RT(2);
  auto run = [&](auto ROLEC) {
  constexpr int ROLE = decltype(ROLEC)::value;
  lds_char* img = lds;
  const unsigned fo = (unsigned)(cls & 1) * 8192u;
  const int r0 = ROLE * 1024 + lane * 16, r1 = r0 + 4096, r2 = 8192 + lane * 16;
  const unsigned m0 = nearoff(r0) + fo, m1 = nearoff(r1) + fo, m2 = nearoff(r2) + fo;
  const unsigned lb = (unsigned)(uintptr_t)lds, lw0 = lb + ROLE * 1024;
  const i32x4 D = mkdesc(a.wb);
  const int ar = g * 256 + ((l15 >> 1) & 1) * 128 + (2 * (l15 >> 2) + (l15 & 1)) * 16;
  lds_char* ard0 = img + ROLE * 1024 + ar;
  lds_char* ard1 = img + ((ROLE + 1) & 3) * 1024 + ar;
  lds_char* ard2 = img + ((ROLE + 2) & 3) * 1024 + ar;
  lds_char* ard3 = img + ((ROLE + 3) & 3) * 1024 + ar;
  const int gy = ((tq & 1) << 1) | (tq >> 1);
  const unsigned pcl = lane < 32 ? (unsigned)nearoff(NEAR_E + (h >> 1) * 1024 + ((lane >> 2) * 8 + 4 * hp + (lane & 3)) * 16) + fo
                     : (lane < 40 ? (unsigned)nearoff(NEAR_PM + wz * 1024 + ((2 * hp + (tq >> 1)) * 16 + (tq & 1) * 8 + (lane & 7)) * 16) + fo
                                  : (lane < 42 ? (unsigned)nearoff(NEAR_AL + wz * 256 + ((tq >> 1) * 8 + (2 * hp + (tq & 1)) * 2 + (lane & 1)) * 16) + fo : OOB));
  const unsigned kfo = OFF_KF + (unsigned)h * 8192u + lane * 16;
  constexpr int QT = ROLE >> 1;
  const unsigned kmo = OFF_KMF + (unsigned)h * 4096u + lane * 16;
  const unsigned pxs = OFF_PXF + (unsigned)((QT * 40 + h) * 1024 + lane * 16);
  const unsigned pws = OFF_PXF + (unsigned)((QT * 40 + 32 + wz * 2 + hp) * 1024 + (gy * 16 + l15) * 16 + g * 4);
  const unsigned kpf = FL_PROG + h * 128 + (lane & 3) * 4;
  const unsigned kmf = (lane == 0) ? (unsigned)(FL_KMPROG + h * 128) : OOB;
  const unsigned wpo = lane == 0 ? (unsigned)(FL_WP + h * 32) : OOB;
  const int idx = (lane & 2) ? 0xEEEE : 0x4444;
  const int jr = l15 >> 2;
  const unsigned qw = lb + QIM_OFF + (ROLE * 8 + ((2 * jr + (l15 & 1)) ^ ((ROLE & 1) << 1))) * 32 + g * 8 + (l15 & 2) * 2;
  const unsigned kw = lb + KIM_OFF + ROLE * 256 + l15 * 16 + g * 4;
  lds_char* krd = lds + KIM_OFF + (4 * g + jr) * 16 + (l15 & 2) * 4;
  unsigned fw = lb + FLG_OFF + ROLE * 4;
  pin_v(fw);
  lds_char* frd = lds + FLG_OFF + (lane & 3) * 4;
  const unsigned qa = lb + QIM_OFF + (16 * (g >> 1) + 4 * (g & 1) + (jr & 1) + 10 * (jr >> 1)) * 32 + (l15 & 3) * 8;
  u32x16 cst = {kw, lb + KIM_OFF + (4 * g + jr) * 16 + (l15 & 1) * 8, qa, qa ^ 64, lb + QIM_OFF + ((24 + 2 * g + (jr & 1)) ^ 2) * 32 + (l15 & 3) * 8,
                lb + FLG_OFF + min(lane & 3, 2) * 4, pxo, plo, (l15 & 1) ? 0u : 0x3f800000u, l15 == 1 ? 0x3f800000u : 0u, l15 == 9 ? 0x3f800000u : 0u, fw, qw,
                lb + (l15 >> 3) * 4096 + (g * 16 + 4 * (l15 & 3) + 2 * (jr & 1)) * 16, 0u, 0u};
  const unsigned own = own_cpy(cls, h >> 3);
  unsigned ima = lb + lane * 16;
  pin_v(ima);
  ds_flag(fw, 0u);
  ds_flag(fw + 16, 0u);
  auto poison = [&]() {
#pragma unroll
    for (int f = 0; f < 5; ++f) *(lds_u32x4*)(lds + QIM_OFF + f * 1024 + lane * 16) = (u32x4){~0u, ~0u, ~0u, ~0u};
    asm volatile("s_waitcnt lgkmcnt(0)" ::: "memory");
  };
  if (ROLE == 0) poison();

  int fl = kv0;
  u32x4 af0[3];
#pragma unroll
  for (int f = 0; f < 3; ++f) {
    const int u = (f == 2 ? 8 : f * 4 + ROLE) * 64 + lane;
    af0[f] = *(const u32x4*)(a.mem0 + (u & 7) * DM + (u >> 3) * 8);
  }
  auto k_dma = [&](int sg, int half) {
#pragma unroll
    for (int f = 0; f < 4; ++f)
      asm volatile("s_mov_b32 m0, %2\n\ts_nop 0\n\tbuffer_load_dwordx4 %0, %1, %3 offen offset:%4 sc1 lds"
                   :: "v"(kfo), "s"(D), "s"(lb + KX_OFF + (sg & 1) * 8192 + half * 4096), "s"((unsigned)(sg & RMASK) * 262144u + half * 4096), "n"(f * 1024) : "memory");
  };
  u32x4 qq[4], F[4];
#pragma unroll
  for (int f = 0; f < 4; ++f) { qq[f] = af0[0]; F[f] = af0[0]; }
  f32x4 T0 = {0.f, 0.f, 0.f, 0.f}, T1 = T0;
  float tmx = 0.f, tsx = 0.f;
  auto q_ld = [&](u32x4 (&qx)[4], int sg) {
#pragma unroll
    for (int f = 0; f < 4; ++f)
      asm volatile("buffer_load_dwordx4 %0, %1, %2, %3 offen offset:%4 sc1"
                   : "+v"(qx[f]) : "v"(kfo), "s"(D), "s"((unsigned)sg * 262144u + (OFF_QF - OFF_KF) + QT * 4096), "n"(f * 1024) : "memory");
  };
  auto t_part = [&](u32x4 (&qx)[4], int par, auto NC) {
    asm volatile("s_waitcnt vmcnt(%4)" : "+v"(qx[0]), "+v"(qx[1]), "+v"(qx[2]), "+v"(qx[3]) : "n"(decltype(NC)::value) : "memory");
    T0 = (f32x4){0.f, 0.f, 0.f, 0.f}; T1 = T0;
#pragma unroll
    for (int ks = 0; ks < 4; ++ks) {
      T0 = mfma(*(lds_u32x4*)(lds + KX_OFF + par * 8192 + ks * 1024 + lane * 16), qx[ks], T0);
      if (QT) T1 = mfma(*(lds_u32x4*)(lds + KX_OFF + par * 8192 + 4096 + ks * 1024 + lane * 16), qx[ks], T1);
    }
    pin_v(T0); pin_v(T1); PAD();
    px_mask(T0, T1, QT, lane);
    pin_v(T0); pin_v(T1); PAD();
    px_seg(T0, T1, tmx, tsx);
    pin_v(T0); pin_v(T1); pin_v(tmx); pin_v(tsx); PAD();
  };
  if (ROLE == 3) {
    const bool ok = wait_flag_c<8>(R, kpf, 0, fl);
    k_dma(0, 0); k_dma(0, 1);
    if (!ok) return;
  }
  VMW(0);

  unsigned iso_acc = 0, bud = SPINCAP, tm = 0, rcnt = 0;
  unsigned cv = 0;
  const unsigned cwo = FL_AN + (h & 15) * 128;
  EVDECL; CKDECL;
  auto k_get = [&](u32x4 (&F)[4], int s, bool parts) {
#pragma nounroll
    for (; bud != 0; --bud) {
      CBAR();
      if (__all((int)*(lds_u32*)frd > s)) break;
      __builtin_amdgcn_s_sleep(1);
    }
    CBAR();
#pragma unroll
    for (int f = 0; f < 4; ++f) { u32x2 lo = trd(krd + f * 512), hi = trd(krd + f * 512 + 256); F[f] = (u32x4){lo[0], lo[1], hi[0], hi[1]}; }
    if (!parts) return;
    const u32x2 p0 = trd(krd + 2048), p1 = trd(krd + 2304);
    F[1][2] = add3bf(F[1][2], p0[0], p1[0]); F[1][3] = add3bf(F[1][3], p0[1], p1[1]);
  };
  auto step = [&](auto PHC, int s) {
    constexpr int PH = decltype(PHC)::value & 1, FIRST = (decltype(PHC)::value >> 1) & 1, LAST = decltype(PHC)::value >> 2;
    constexpr int JC = ROLE == 0 ? 3 : (ROLE == 1 ? 6 : 8);
    u64 e0 = 0, e1 = 0, e3 = 0, e4 = 0; unsigned nr0 = 0;
    u32x4 af[9];
    f32x4 q0, q1, k0, k1, k2;
    PADS(200 + ROLE * 8 + decltype(PHC)::value);
    unsigned f3 = s + 1;
    pin_v(f3);
    constexpr int RD = ROLE == 3 && !LAST;
    f32x16 T;
    u32x16 x0, x1;
    if (RD) t_zero(k0, T); else if (!LAST) k_zero(k0, k1, k2);
    const u32x8 (&wf)[2][9] = LAST ? wk : wq;
#define MQ(t, j) do { smo<t, (j) < 8>(q##t, af[j], wf[t][j], idx); SB(); } while (0)
    lds_char* f8 = lds + F8_OFF + lane * 16;
    bud = __builtin_amdgcn_readfirstlane(bud);
    asm volatile("" : "+s"(bud));
    unsigned nt = 0; int rq = 0;
    tm = __builtin_amdgcn_readfirstlane(tm); rcnt = __builtin_amdgcn_readfirstlane(rcnt);
    asm volatile("" : "+s"(tm), "+s"(rcnt));
    if (RD) asm volatile("" : "+{v[112:127]}"(cst));
    unsigned p0 = (s & 7) * SLOT_REAL, p1 = p0 + CPY1, p2 = p0 ^ CPY2, p3 = p2 + CPY1;
    unsigned ms = (p2 + own) ^ ((s & 4) ? 0 : CPY1);
    u32x8 sc = {p0, p1, p2, p3, 0x3e0293eeu, YMAX, 0u, 0u};
    if (RD) asm volatile("" : "+{s[40:47]}"(sc));
    constexpr int NX = ROLE == 3 || LAST ? 0 : (ROLE == 0 ? 8 : 4);
    constexpr int ID = FIRST ? -1 : ROLE * 8 + decltype(PHC)::value;
    auto head = [&]() {
    v_zero(q0, q1);
    if (!FIRST) {
      int r = 0;
      if (ROLE == 1) r = spin_late(tm, 0);
      rq = r;
      mem_issue2<ROLE == F8W ? 3 : 2>(D, m0, m1, m2, ms, lw0, lw0 + 4096, lb + F8_OFF, e3);
      if (ROLE == 1) { move_on(tm, r); SB(); }
      PAD();
      if (RD) { kx_get<PH>(cst, x0, x1); asm volatile("s_waitcnt lgkmcnt(0)" ::: "memory"); }
      if (ROLE == 3) asm volatile("s_waitcnt vmcnt(%0)" :: "n"(TWO ? 1 + (F8W == 3) : 0) : "memory");
      else if (nt != 0) VMW(0);
      else {
      __builtin_amdgcn_s_sleep(XSLP);
      if (!LAST && (ROLE == 1 || ROLE == 2)) k_dma(s + 1, ROLE - 1);
      if (ROLE == 0 && !LAST) {
#pragma unroll
        for (int ks = 0; ks < 4; ++ks) st128(F[ks], R, kmo + ks * 1024, (unsigned)(s - 1) * 131072u);
      }
      if (ROLE == 0 && !LAST) {
        st128((u32x4){~0u, ~0u, ~0u, ~0u}, R, pcl, (unsigned)((s + 2) & 3) * SLOT_REAL);
        st128((u32x4){~0u, ~0u, ~0u, ~0u}, R, pcl, (unsigned)((s + 2) & 3) * SLOT_REAL + CPY1);
        st128((u32x4){~0u, ~0u, ~0u, ~0u}, R, pcl, (unsigned)((s + 2) & 3) * SLOT_REAL + CPY2);
        st128((u32x4){~0u, ~0u, ~0u, ~0u}, R, pcl, (unsigned)((s + 2) & 3) * SLOT_REAL + CPY2 + CPY1);
      }
      PAD();
      CBAR();
      asm volatile("s_waitcnt vmcnt(%0)" :: "n"(NX + (TWO ? 1 + (ROLE == F8W) : 0)) : "memory");
      }
    } else {
      *(lds_u32x4*)(img + r0) = af0[0]; *(lds_u32x4*)(img + r1) = af0[1]; *(lds_u32x4*)f8 = af0[2];
      asm volatile("s_waitcnt lgkmcnt(0)" ::: "memory");
    }
    SB();
    HEAD(MQ, NX, 0);
    if (s == 0) RT(3);
    if (s == 1) RT(4);
    if (s == 8) RT(5);
    if (s == 60) RT(6);
    ISOQ(1);
    q_mid<ID>(af, wf[1], idx, LAST ? kw : qw, q0, q1); SB();
    };
    if (!FIRST) {
      tm -= GRID + XT; nt = ~0u;
      try_top<ID>(nt, ms, tm, rcnt, ima); SB();
      head();
      try_end(ima, nt, ms, tm, rcnt, m0, m1, m2, lw0, lw0 + 4096, lb + F8_OFF, D); SB();
    } else
    {
      if (FIRST && RD) { kx_get<PH>(cst, x0, x1); asm volatile("s_waitcnt lgkmcnt(0)" ::: "memory"); }
      head();
    }
    ISOQ(2);
    EV(e0);
    if (LAST) k_last(kw, q1, fw, f3);
    if (ROLE < 3) {
      if (!LAST) {
        float t;
        k_one(af, wk[0], wp, idx, qw, q1, k0, k2, t); SB();
        ISOQ(4);
        if (ROLE == 1 && FIRST) { st32(f3, R, wpo, 0); SB(); }
        k_end<ROLE == 2 ? 256 : 2048, JC>(af, wk[1], wp[2], idx, k0, k1, k2, t, kw, fw, f3); SB();
      }
      ISOQ(6);
      PAD();
      if (ROLE == 1 && !LAST) {
        nr0 = nt != 0;
        if (!FIRST) next_time(tm, s);
      }
      if (ROLE == 0 && !LAST) {
        __builtin_amdgcn_s_sleep(TSLP);
        k_get(F, s, true);
        ds_flag(lb + FLG_OFF + 20, f3);
        pin_v(F[0]); pin_v(F[1]); pin_v(F[2]); pin_v(F[3]);
        VMW(0);
        st32((unsigned)(s - 1), R, kmf, 0);
        if (!PH && !FIRST && UNLIKELY(s == NSEG - 2)) {
#pragma unroll
          for (int ks = 0; ks < 4; ++ks) st128(F[ks], R, kmo + ks * 1024, (unsigned)s * 131072u);
          VMW(0);
          st32((unsigned)s, R, kmf, 0);
        }
      }
      if (ROLE == 2 && !LAST) {
        if (FIRST) { gate_wait(R, kpf, 2, fl, bud); k_dma(1, 0); k_dma(1, 1); }
        gate_wait(R, kpf, min(2 * s + 4 + (s >= NSEG - 4), 2 * NSEG - 1), fl, bud);
        fl = (int)ld32(R, kpf, 0);
        VMW(0);
        pin_v(fl);
      }
      if ((ROLE == 0 || ROLE == 2) && !PH && !FIRST && UNLIKELY(s == NSEG - 2)) {
        k_dma(s + 1, 0);
        q_ld(qq, s + 1);
        t_part(qq, 1, std::integral_constant<int, 0>());
      }
      if ((ROLE == 0 || ROLE == 2) && LAST) {
        f32x4 G = {0.f, 0.f, 0.f, 0.f};
        u32x4 px; unsigned pw;
        k_get(F, s, false);
#pragma unroll
        for (int ks = 0; ks < 4; ++ks) G = mfma(F[ks], qq[ks], G);
        px_fin(G, T0, T1, tmx, tsx, px, pw, lane);
        st128(px, R, pxs, (unsigned)s * 81920u); st32(pw, R, pws, (unsigned)s * 81920u);
        VMW(0);
        st32((unsigned)s, R, lane == 0 ? (unsigned)(FL_PXLAST + h * 256 + QT * 128) : OOB, 0);
      }
      CBAR();
      if (ROLE == 0) CKS();
    } else if (!LAST) {
      u32x8 Q;
      s_first(af, wk[1], idx, cst, x0, x1, q1, k0, T, Q); SB();
      el_phase(D, af, wk[1], idx, cst, x0, x1, Q, k0, T, bud, e1, e4, nr0, sc, f3);
      ds_flag(fw + 4, f3);
      EVPUT(2, e1); EVPUT(4, e4);
    }
    EVPUT(0, e0);
    EVPUTI(1, nr0); if (!FIRST) EVPUT(3, e3);
    if (ROLE == 1 && !LAST) {
#pragma nounroll
      for (; bud != 0; --bud) {
        CBAR();
        if (__all((int)*(lds_u32*)(lds + FLG_OFF + 16 + (lane & 1) * 4) > s)) break;
        __builtin_amdgcn_s_sleep(1);
      }
      poison();
      VMW(0);
      if (FIRST) { tm = get_t1(R, cwo, bud); asm volatile("" : "+s"(tm)); }
    }
    if (PH && !LAST && UNLIKELY(s == WLAST)) WALK(200 + ROLE * 8 + 5);
    PADE();
#undef MQ
  };
  step(std::integral_constant<int, 2>(), 0);
#pragma nounroll
  for (int s = 1; s < NSEG - 1; s += 2) {
    step(std::integral_constant<int, 1>(), s);
    step(std::integral_constant<int, 0>(), s + 1);
  }
  step(std::integral_constant<int, 5>(), NSEG - 1);
  EVDUMP();
  if (ROLE == 0) CKDUMP();
  };
  if (hw == 0) run(std::integral_constant<int, 3>());
  else if (hw == 1) run(std::integral_constant<int, 2>());
  else if (hw == 2) run(std::integral_constant<int, 1>());
  else run(std::integral_constant<int, 0>());
  RT(7);
  SV(15, cls);
}

#define VIM_OFF 8192
#define NOP(n) do { asm volatile("s_nop " #n); SB(); } while (0)
#define cVW "v112"
#define cRA "v113"
#define cRB "v114"
#define cHA "v115"
#define cHB "v116"
#define cVF "v117"
#define cVR "v118"
#define cIY "v119"
#define YP(p, c) "v_smfmac_f32_16x16x64_bf16 v[148+4*" #p ":151+4*" #p "], v[168+4*" #c "+4*(" #c ">>1):171+4*" #c "+4*(" #c ">>1)], %[w" #p #c "], " cIY "\n\t"
#define Y8(k) "v_smfmac_f32_16x16x64_bf16 v[164:167], v[176+12*" #k ":179+12*" #k "], %[w8" #k "], " cIY "\n\t"
#define HA(d, b) "v_add_f32 v" #d ", v[" b "], v[" b "+2]\n\t"
#define HB(d, b) "v_add_f32 v" #d ", v[" b "+1], v[" b "+3]\n\t"
#define YC(p, e) "v_cvt_pk_bf16_f32 v[192+2*" #p "+" #e "], v[148+4*" #p "+2*" #e "], v[149+4*" #p "+2*" #e "]\n\t"
#define YS(p) ".if %[st]\n\tbuffer_store_dwordx2 v[192+2*" #p ":193+2*" #p "], v[120+" #p "], %[D], %[ys] offen sc1\n\t.endif\n\t"
#define S16(a, b) "v_permlane16_swap_b32 v" #a ", v" #b "\n\t"
#define Z2(r) "v_mov_b64 v[" #r ":" #r "+1], 0\n\tv_mov_b64 v[" #r "+2:" #r "+3], 0\n\t"
#define YZERO Z2(148) Z2(152) Z2(156) Z2(160) Z2(164)
#define VR0 TRD(168, cRA, 0) TRD(170, cRB, 0)
#define VR1 TRD(172, cRA, 512) TRD(174, cRB, 512) TRD(176, cHA, 0) TRD(178, cHB, 0)
__device__ __forceinline__ void y_zero(f32x16& ya, f32x4& yc) {
  asm volatile(YZERO : "={v[148:163]}"(ya), "={v[164:167]}"(yc));
}
#define HV 0
#define VO 1
#define VR 1
#define YEO
#define YEN
template <int ID>
__device__ __forceinline__ void v_end(const u32x4 (&af)[9], const u32x8 (&w)[9], int ix, u32x16 cst, f32x4 v0, f32x4& v1, u32x16& A,
                                      const u32x8 (&wo)[4][4], f32x16& ya, u64& e1) {
#define VH0 HA(201, "140") HB(202, "140") CVX(203, 201, 202)
#define VH1 HA(201, "144") HB(202, "144") CVX(203, 201, 202)
#define VW0 "ds_write_b32 " cVW ", v203\n\t"
#define VW1 "ds_write_b32 " cVW ", v203 offset:1024\n\t"
#define VCM "v_cmp_u_f32 vcc, v201, v202\n\t"
  asm volatile(
               Q1(3) Q1(4) VH0 Q1(5) VW0 Q1(6) "s_barrier\n\t" VR0 Q1(7) TRD(172, cRA, 512) TRD(174, cRB, 512) Q1(8) TRD(176, cHA, 0) TRD(178, cHB, 0) VCM
               LGK(4) YP(0, 0) IS(16) VH1 YP(1, 0) VW1 TRYB
               : "+{v[144:147]}"(v1), "=&{v[168:183]}"(A), "+{v[148:163]}"(ya) YEO
               : "{v[140:143]}"(v0), [a2] "v"(af[2]), [a3] "v"(af[3]), [a4] "v"(af[4]), [a5] "v"(af[5]), [a6] "v"(af[6]), [a7] "v"(af[7]), [a8] "v"(af[8]),
                 [w2] "a"(w[2]), [w3] "a"(w[3]), [w4] "a"(w[4]), [w5] "a"(w[5]), [w6] "a"(w[6]), [w7] "a"(w[7]), [w8] "v"(w[8]), [ix] "v"(ix), "{v[112:127]}"(cst), [id] "n"(ID),
                 [w00] "a"(wo[0][0]), [w10] "a"(wo[1][0])
               : "memory", "v201", "v202", "v203", TRYC);
}
template <int ST>
__device__ __forceinline__ void y_all(const u32x8 (&wo)[4][4], const u32x8 (&w8)[2], u32x16 cst, f32x16 ya, f32x4 yc, u32x16 A, i32x4 D,
                                      unsigned ys, unsigned fv, u32x8& o, unsigned& o8, u64& e1, unsigned& nr) {
#define RH1 TRD(188, cHA, 1024) TRD(190, cHB, 1024)
#define RQ2 TRD(180, cRA, 1024) TRD(182, cRB, 1024)
#define RQ3 TRD(184, cRA, 1536) TRD(186, cRB, 1536)
#define VFL "ds_write_b32 " cVF ", %[fv]\n\t"
#define YK0 YP(2, 0) YP(3, 0) RH1 LGK(3) YP(0, 1) RQ2 Y8(0) RQ3 YP(1, 1) VFL IS(17)
#define YK1    LGK(5) Y8(1) IS(18) YP(2, 1) YP(3, 1) S16(164, 166) LGK(3) YP(0, 2) S16(165, 167) LGK(1) YP(0, 3) HA(201, "164") YP(1, 2) HB(202, "164") CVX(200, 201, 202) \
               YP(1, 3) ".if %[st]\n\tbuffer_store_dword v200, v124, %[D], %[ys] offen sc1\n\t.endif\n\t" YC(0, 0) YP(2, 2) YC(0, 1) YP(2, 3) YS(0) YC(1, 0)   \
               YP(3, 2) YC(1, 1) YP(3, 3) IS(19) YS(1) YC(2, 0) YC(2, 1) YS(2) "s_nop 5-2*%[st]\n\t" YC(3, 0) YC(3, 1) YS(3) IS(20) EVA(1)
  asm volatile(YK0 YK1
               "v_cmp_u_f32 vcc, v148, v150\n\tv_cmp_u_f32 s[46:47], v201, v202\n\ts_or_b64 vcc, vcc, s[46:47]\n\ts_cbranch_scc1 19f\n"
               "13:\n\t.subsection 1\n"
               "19:\n\tds_read_b32 v204, " cVR "\n\t" LGK(0) "v_cmp_gt_i32 vcc, %[fv], v204\n\ts_cbranch_vccnz 19b\n\t"
               CN(100) VR0 VR1 RH1 RQ2 RQ3 YZERO LGK(0) YP(0, 0) YP(1, 0) YP(2, 0) YP(3, 0) YP(0, 1) Y8(0) YP(1, 1) YK1 "s_branch 13b\n\t.subsection 0"
               : "=&{v[192:199]}"(o), "=&{v200}"(o8) YEO YEN
               : [w00] "a"(wo[0][0]), [w01] "a"(wo[0][1]), [w02] "a"(wo[0][2]), [w03] "a"(wo[0][3]), [w10] "a"(wo[1][0]), [w11] "a"(wo[1][1]),
                 [w12] "a"(wo[1][2]), [w13] "a"(wo[1][3]), [w20] "a"(wo[2][0]), [w21] "a"(wo[2][1]), [w22] "a"(wo[2][2]), [w23] "a"(wo[2][3]),
                 [w30] "a"(wo[3][0]), [w31] "a"(wo[3][1]), [w32] "a"(wo[3][2]), [w33] "a"(wo[3][3]), [w80] "v"(w8[0]), [w81] "v"(w8[1]),
                 "{v[112:127]}"(cst), "{v[148:163]}"(ya), "{v[164:167]}"(yc), "{v[168:183]}"(A),
                 [D] "s"(D), [ys] "s"(ys), [fv] "v"(fv), [st] "n"(ST)
               : "memory", "vcc", "scc", "s46", "s47", "v184", "v185", "v186", "v187", "v188", "v189", "v190", "v191", "v201", "v202", "v204");
}
__device__ __forceinline__ void role_vcu(const Args& a, rsrc_t R, const Go& go, lds_char* lds, int h, int tid, int wave, int lane) {
  const int l15 = lane & 15, g = lane >> 4;
  u32x8 wv[2][9], wo[4][4], w8[2];
  int cv0 = -1;
  {
    u32x4 st[36], so[36];
#pragma unroll
    for (int k = 0; k < 36; ++k) so[k] = gld<NTM & 2>(a.Wo + (size_t)h * 128 * DM + (tid + 256 * k) * 8);
    if (wave == 0) WK3(232); else if (wave == 1) WK3(240); else if (wave == 2) WK3(248); else WK3(256);
    const int q = l15 >> 2;
    const int lrow = 16 * (g >> 1) + 4 * (g & 1) + 8 * (q >> 1) + (q & 1), lrow2 = 8 * (g >> 1) + 4 * (g & 1) + (q & 1), up2 = (g & 1) ? 12 : 20;
    auto wrd = [&](lds_char* p, int rs, int up) {
      u32x2 v0 = trd(p), v1 = trd(p + 2 * rs), v2 = trd(p + up * rs), v3 = trd(p + (up + 2) * rs);
      return (u32x8){v0[0], v0[1], v1[0], v1[1], v2[0], v2[1], v3[0], v3[1]};
    };
#pragma unroll
    for (int ph = 0; ph < 4; ++ph) {
      if (ph < 2) {
#pragma unroll
        for (int k = 0; k < 18; ++k) { const int u = tid + 256 * k; *(lds_u32x4*)(lds + (u / 72) * 1184 + (u % 72) * 16) = so[ph * 18 + k]; }
#pragma unroll
        for (int k = ph * 18; k < ph * 18 + 18; ++k) { const int u = tid + 256 * k; st[k] = gld<NTM & 2>(a.Wv + (size_t)(u >> 4) * HD + h * 128 + (u & 15) * 8); }
      } else {
#pragma unroll
        for (int k = (ph - 2) * 16; k < (ph == 2 ? 16 : 36); ++k) { const int u = tid + 256 * k; *(lds_u32x4*)(lds + ((u >> 4) - (ph - 2) * 256) * 288 + (u & 15) * 16) = st[k]; }
      }
      if (ph == 3 && go_wait(R, go, lane)) { cv0 = cls_look(R, lane); CBAR(); }
      BAR();
      if (ph < 2) {
#pragma unroll
        for (int k = 0; k < 8; ++k) wo[k >> 1][2 * ph + (k & 1)] = wrd(lds + (lrow2 + 32 * (k & 1)) * 1184 + (wave * 9 + (k & 6) + (q >> 1)) * 32 + (l15 & 3) * 8, 1184, up2);
        w8[ph] = wrd(lds + (lrow2 + 32 * (q >> 1)) * 1184 + (wave * 9 + 8) * 32 + (l15 & 3) * 8, 1184, up2);
        SB();
#pragma unroll
        for (int k = 0; k < 8; ++k) pin_a(wo[k >> 1][2 * ph + (k & 1)]);
        pin_v(w8[ph]);
      } else {
#pragma unroll
        for (int k = 0; k < (ph == 2 ? 8 : 10); ++k) {
          const int i = k >> 1, t = k & 1;
          wv[t][(ph - 2) * 4 + i] = wrd(lds + ((i < 4 ? (i + wave) & 3 : 4) * 64 + lrow) * 288 + (64 * t + 16 * wave) * 2 + (l15 & 3) * 8, 288, 32);
        }
        SB();
#pragma unroll
        for (int k = 0; k < (ph == 2 ? 8 : 10); ++k) { if (k < 8) pin_a(wv[k & 1][(ph - 2) * 4 + (k >> 1)]); else pin_v(wv[k & 1][8]); }
      }
      BAR();
    }
  }
  RT(1);
  const int cls = get_cls(R, lane, cv0);
  if (cls < 0) return;
  RT(2);
  auto run = [&](auto ROLEC) {
  constexpr int ROLE = decltype(ROLEC)::value;
  lds_char* img = lds;
  const unsigned fo = (unsigned)(cls & 1) * 8192u;
  const int r0 = ROLE * 1024 + lane * 16, r1 = r0 + 4096, r2 = 8192 + lane * 16;
  const unsigned m0 = nearoff(r0) + fo, m1 = nearoff(r1) + fo, m2 = nearoff(r2) + fo;
  const unsigned lb = (unsigned)(uintptr_t)lds, lw0 = lb + ROLE * 1024;
  const i32x4 D = mkdesc(a.wb);
  const int ar = g * 256 + ((l15 >> 1) & 1) * 128 + (2 * (l15 >> 2) + (l15 & 1)) * 16;
  lds_char* ard0 = img + ROLE * 1024 + ar;
  lds_char* ard1 = img + ((ROLE + 1) & 3) * 1024 + ar;
  lds_char* ard2 = img + ((ROLE + 2) & 3) * 1024 + ar;
  lds_char* ard3 = img + ((ROLE + 3) & 3) * 1024 + ar;
  const int wz = h >> 3, tq = (h >> 1) & 3, hp = h & 1;
  const int gh = ((tq & 1) << 1) | (tq >> 1);
  unsigned yr[5], yz[5];
#pragma unroll
  for (int q = 0; q < 5; ++q) {
    const int m = ROLE * 9 + q * 2 + (q < 4 ? g & 1 : 0), o = l15 * 16 + (q < 4 ? (g >> 1) * 8 : g * 4);
    yr[q] = ywin(cls, m) + (unsigned)((wz * 2 + hp) * 1024 + tq * 256 + o);
    yz[q] = OFF_ZF + (unsigned)(m * 40960 + (32 + wz * 2 + hp) * 1024 + gh * 256 + o);
  }
  lds_char* vw = lds + VIM_OFF + (ROLE * 16 + l15) * 16 + g * 4;
  const unsigned ra = lb + VIM_OFF + (8 * g + 4 * (g >> 1) + (l15 >> 2)) * 16 + (l15 & 2) * 4, rh = ra + (l15 & 1) * 512;
  u32x16 cst = {(unsigned)(uintptr_t)vw, ra, ra ^ 64, rh, rh ^ 64, lb + FLG_OFF + ROLE * 4, lb + FLG_OFF + (lane & 3) * 4, (lane & 4) ? 0xEEEEu : 0x4444u,
                yr[0], yr[1], yr[2], yr[3], yr[4], 0u, 0u, 0u};
  ds_flag(lb + FLG_OFF + ROLE * 4, 0u);
  const unsigned own = own_cpy(cls, h >> 3);
  unsigned ima = lb + lane * 16;
  pin_v(ima);
  const int idx = (lane & 2) ? 0xEEEE : 0x4444;
  u32x4 af0[3];
#pragma unroll
  for (int f = 0; f < 3; ++f) {
    const int u = (f == 2 ? 8 : f * 4 + ROLE) * 64 + lane;
    af0[f] = *(const u32x4*)(a.mem0 + (u & 7) * DM + (u >> 3) * 8);
  }
  unsigned tm = 0, rcnt = 0, cv = 0, iso_acc = 0, o8 = 0, bud = SPINCAP;
  u32x8 o = {0u, 0u, 0u, 0u, 0u, 0u, 0u, 0u};
  auto y_copy = [&](int s) {
#pragma unroll
    for (int q = 0; q < 4; ++q) st64((u32x2){o[2 * q], o[2 * q + 1]}, R, yz[q], (unsigned)s * ZS);
    st32(o8, R, yz[4], (unsigned)s * ZS);
  };
  auto poison = [&]() { asm volatile("v_mov_b32 v203, -1\n\tds_write2st64_b32 " cVW ", v203, v203 offset1:4" :: "{v[112:127]}"(cst) : "memory", "v203"); };
  const unsigned cwo = FL_AN + ((h + 8) & 15) * 128;
  EVDECL;
  auto step = [&](auto PHC, int s) {
    constexpr int PH = decltype(PHC)::value & 1, FIRST = (decltype(PHC)::value >> 1) & 1, LAST = decltype(PHC)::value >> 2;
    u64 e0 = 0, e1 = 0, e3 = 0, e4 = 0, e5 = 0, e6 = 0;
    u32x4 af[9];
    f32x4 v0, v1, yc;
    f32x16 ya;
    u32x16 A;
    unsigned nsl = 0;
    PADS(232 + ROLE * 8 + decltype(PHC)::value);
    unsigned f3 = s + 1;
    pin_v(f3);
    asm volatile("" : "+{v[112:127]}"(cst));
    PAD();
    unsigned nt = 0;
    tm = __builtin_amdgcn_readfirstlane(tm); rcnt = __builtin_amdgcn_readfirstlane(rcnt);
    asm volatile("" : "+s"(tm), "+s"(rcnt));
#define MV(t, j) do { smo<t, (j) < 8>(v##t, af[j], wv[t][j], idx); SB(); } while (0)
    unsigned ms = ((((s & 7) * SLOT_REAL) ^ CPY2) + own) ^ ((s & 4) ? 0 : CPY1);
    constexpr int ID = FIRST ? -1 : 32 + ROLE * 8 + decltype(PHC)::value;
    auto head = [&]() {
      y_zero(ya, yc);
      v_zero(v0, v1);
      if (!FIRST) {
        int r = 0;
        if (ROLE == 1) r = spin_late(tm, DVQ);
        mem_issue2<ROLE == F8W ? 3 : 2>(D, m0, m1, m2, ms, lw0, lw0 + 4096, lb + F8_OFF, e3);
        if (ROLE == 1) { move_on(tm, r); SB(); }
        poison();
        PAD();
        if (LAST || nt != 0) VMW(0);
        else {
          __builtin_amdgcn_s_sleep(XSLP);
          y_copy(s - 1);
          CBAR();
          asm volatile("s_waitcnt vmcnt(%0)" :: "n"(5 + (TWO ? 1 + (ROLE == F8W) : 0)) : "memory");
        }
      } else {
        *(lds_u32x4*)(img + r0) = af0[0]; *(lds_u32x4*)(img + r1) = af0[1]; *(lds_u32x4*)(lds + F8_OFF + lane * 16) = af0[2];
        poison();
        asm volatile("s_waitcnt lgkmcnt(0)" ::: "memory");
      }
      SB();
      EV(e6);
      HEAD(MV, 5, HV);
      ISOQ(11);
      v_end<ID>(af, wv[1], idx, cst, v0, v1, A, wo, ya, e0); SB();
    };
    if (!FIRST) {
      tm -= GRID + XT; nt = ~0u;
      try_top<ID>(nt, ms, tm, rcnt, ima); SB();
      head();
      try_end(ima, nt, ms, tm, rcnt, m0, m1, m2, lw0, lw0 + 4096, lb + F8_OFF, D); SB();
    } else
    head();
    if (LAST) { st32((unsigned)(s - 1), R, lane == 0 ? (unsigned)(FL_YPROG + h * 16 + ROLE * 4) : OOB, 0); SB(); }
    EV(e4); ISOQ(13);
    y_all<!LAST>(wo, w8, cst, ya, yc, A, D, (unsigned)(s & 1) * YSLOT_REAL, f3, o, o8, e5, nsl); SB();
    ISOQ(15);
    PAD();
#undef MV
    if (FIRST) {
      VMW(0); BAR();
      st32(0u, R, tid == 0 ? (unsigned)(FL_WP + (32 + h) * 32) : OOB, 0);
    }
    if (LAST || (!PH && !FIRST && UNLIKELY(s == NSEG - 2))) y_copy(s);
    CBAR();
    if (ROLE == 1 && !LAST) {
      if (FIRST) { tm = get_t1(R, cwo, bud); asm volatile("" : "+s"(tm)); }
      else
      next_time(tm, s);
    }
    if (PH && !LAST && UNLIKELY(s == WLAST)) WALK(232 + ROLE * 8 + 5);
    PADE();
    EVPUT(0, e0); EVPUT(6, e6); EVPUT(4, e4);
    EVPUTI(1, nt); EVPUT(2, e1); if (!FIRST) EVPUT(3, e3);
  };
  step(std::integral_constant<int, 2>(), 0);
#pragma nounroll
  for (int s = 1; s < NSEG - 1; s += 2) {
    step(std::integral_constant<int, 1>(), s);
    step(std::integral_constant<int, 0>(), s + 1);
  }
  step(std::integral_constant<int, 5>(), NSEG - 1);
  VMW(0);
  st32((unsigned)(NSEG - 1), R, lane == 0 ? (unsigned)(FL_YPROG + h * 16 + ROLE * 4) : OOB, 0);
  EVDUMP();
  };
  if (wave == 0) run(std::integral_constant<int, 0>());
  else if (wave == 1) run(std::integral_constant<int, 1>());
  else if (wave == 2) run(std::integral_constant<int, 2>());
  else run(std::integral_constant<int, 3>());
  RT(7);
}

#define NG 4
#define UL(k, o) "buffer_load_dwordx4 v[128+4*" #k ":128+4*" #k "+3], %[u" #o ", %[D], %[su] offen offset:(" #k "&3)*1024 sc1\n\t"
#define UL4(c, k0, k1, k2, k3, o) ".if " #k0 c "4*" XSTR(NG) "\n\t" UL(k0, o) UL(k1, o) UL(k2, o) UL(k3, o) ".endif\n\t"
#define ULD(c) UL4(c, 0, 1, 2, 3, 0]) UL4(c, 4, 5, 6, 7, 1]) UL4(c, 8, 9, 10, 11, 2]) UL4(c, 12, 13, 14, 15, 3])   \
               UL4(c, 16, 17, 18, 19, 4]) UL4(c, 20, 21, 22, 23, 5]) UL4(c, 24, 25, 26, 27, 6]) UL4(c, 28, 29, 30, 31, 7])
#define ULD0 ULD(" < ")
#define ULD1 ULD(" >= ")
#define TB 12
#define UM(T, lo, hi, n) ".if " #T " >= " lo " && " #T " < " hi "\n\t.if !(" #T " & 1)\n\ts_waitcnt vmcnt(" n "+28-2*" #T ")\n\t.endif\n\t"   \
                         "v_swap_b32 v[129+8*" #T "], v[132+8*" #T "]\n\tv_swap_b32 v[131+8*" #T "], v[134+8*" #T "]\n\t.endif\n\t"
#define UMS(...) UM(0, __VA_ARGS__) UM(1, __VA_ARGS__) UM(2, __VA_ARGS__) UM(3, __VA_ARGS__) UM(4, __VA_ARGS__) UM(5, __VA_ARGS__) UM(6, __VA_ARGS__) UM(7, __VA_ARGS__) \
                 UM(8, __VA_ARGS__) UM(9, __VA_ARGS__) UM(10, __VA_ARGS__) UM(11, __VA_ARGS__) UM(12, __VA_ARGS__) UM(13, __VA_ARGS__) UM(14, __VA_ARGS__) UM(15, __VA_ARGS__)
#define TIMES "s_and_b32 %[tt], %[lev], 255\n\ts_add_u32 %[tn], %[tq], %[tt]\n\ts_add_u32 %[tn], %[tn], %[t0]\n\t" \
              "s_lshr_b32 %[tt], %[lev], 8\n\ts_add_u32 %[tt], %[tt], %[tq]\n\t"
#define DZERO ".rept 32\n\tv_mov_b64 v[64+2*\\+:65+2*\\+], 0\n\t.endr\n\tv_mov_b64 v[60:61], 0\n\tv_mov_b64 v[62:63], 0\n\t"
#define YLD(k, o) "buffer_load_dwordx4 a[64+4*" #k ":64+4*" #k "+3], %[y" #o ", %[D], %[sy] offen offset:(" #k "&3)*1024 sc1\n\t"
#define YPO(k, o) "buffer_store_dwordx4 %[one], %[y" #o ", %[D], %[sy] offen offset:(" #k "&3)*1024 sc1\n\t"
#define ELD(T, o) "buffer_load_dwordx4 a[4*" #T ":4*" #T "+3], %[e" #o ", %[D], %[sq] offen offset:(" #T "&3)*1024 sc1\n\t"
#define PLD(G) "buffer_load_dwordx4 a[96+4*" #G ":96+4*" #G "+3], %[pm], %[D], %[sq] offen offset:" #G "*1024 sc1\n\t"
#define ALD(G) "buffer_load_dwordx4 a[112+4*" #G ":112+4*" #G "+3], %[al], %[D], %[sq] offen offset:" #G "*256 sc1\n\t"
#define VW(X, n) "s_waitcnt vmcnt(1+" n ")\n\t"
#define EM(T) "v_smfmac_f32_16x16x64_bf16 v[64+4*" T ":64+4*" T "+3], a[4*" T ":4*" T "+3], v[128+8*" T ":128+8*" T "+7], %[ie]\n\t"
#define WC0(T) "v_cvt_pk_bf16_f32 v[64+2*" #T "], v[64+4*" #T "], v[64+4*" #T "+1]\n\t"
#define WC1(T) "v_cvt_pk_bf16_f32 v[65+2*" #T "], v[64+4*" #T "+2], v[64+4*" #T "+3]\n\t"
#define WCV(T) WC0(T) WC1(T)
#define EM2_0(T, C, o) VW("0", "30-" #T) EM(#T) WC0(C) EM("(" #T "+1)") WC1(C)

#define PMM(X, G) VW(X, "7-" #G) "v_smfmac_f32_16x16x64_bf16 v[60:63], a[96+4*" #G ":96+4*" #G "+3], a[64+8*" #G ":64+8*" #G "+7], %[ip]\n\t"
#define AMM(X, G) VW(X, "3-" #G) "v_smfmac_f32_16x16x64_bf16 v[60:63], a[112+4*" #G ":112+4*" #G "+3], v[64+8*" #G ":64+8*" #G "+7], %[ia]\n\t"
#define TRY(X, EM2, a, b, c, d)                                                                                                       \
      SPIN(tt, a, b, c) MEV(1) MCY(0)                                                                                                \
      "buffer_load_dword v59, %[es], %[D], %[sq] offen sc1\n\t" ELD(0, 0]) ELD(1, 0]) ELD(2, 0]) ELD(3, 0]) ELD(4, 1]) ELD(5, 1]) ELD(6, 1]) ELD(7, 1])                                        \
      ELD(8, 2]) ELD(9, 2]) ELD(10, 2]) ELD(11, 2]) ELD(12, 3]) ELD(13, 3]) ELD(14, 3]) ELD(15, 3])                                  \
      YLD(0, 0]) YLD(1, 0]) YLD(2, 0]) YLD(3, 0]) YLD(4, 1]) YLD(5, 1]) YLD(6, 1]) YLD(7, 1])                                        \
      PLD(0) PLD(1) PLD(2) PLD(3) ALD(0) ALD(1) ALD(2) ALD(3) MCY(1) "buffer_load_dword %[ub], %[uq], %[D], 0 offen sc1\n\ts_cmp_lg_u32 %[sx], 0\n\ts_cbranch_scc1 77f\n\t" UMS(XSTR(TB), "16", "43") "s_mov_b32 %[sx], 1\n77:\n\t"                                                                 \
      VW("0", "32") "v_cmp_eq_u32 vcc, -1, v59\n\t" VW("0", "30") "s_cbranch_vccnz 61f\n62:\n\t" EM("0") MCY(2) EM("1")                                                                                           \
      EM2(2, 0, 0]) EM2(4, 1, 0]) EM2(6, 2, 0]) EM2(8, 3, 0]) EM2(10, 4, 1]) EM2(12, 5, 1]) EM2(14, 6, 1]) MCY(3)                           \
      "s_cmp_lt_i32 %[r], -" XSTR(LTOL) "\n\ts_cbranch_scc0 " d "f\n\t"                              \
      "63:\n\ts_add_u32 %[tq], %[tq], " XSTR(GRID) "\n\ts_add_u32 %[t0], %[t0], " XSTR(XT) "\n\ts_add_u32 %[tn], %[tn], " XSTR(GRID) "+" XSTR(XT) "\n\ts_add_u32 %[r], %[r], " XSTR(GRID) "\n\t"   \
      "s_cmp_lt_i32 %[r], -" XSTR(LTOL) "\n\ts_cbranch_scc1 63b\n\t" MCA(1000)                                                        \
      d ":\n\t" WCV(7) WCV(8) WCV(9) WCV(10) WCV(11) WCV(12) WCV(13) WCV(14) WCV(15)                                                 \
      PMM(X, 0) MCY(4) PMM(X, 1) PMM(X, 2) PMM(X, 3) AMM(X, 0) AMM(X, 1) AMM(X, 2) AMM(X, 3) MCY(5)                                  \
      PADN "s_nop 6\n\t"                                                                                                             \
      "v_pk_add_f32 v[60:61], v[60:61], v[62:63]\n\t"                                                                                \
      "v_cvt_pk_bf16_f32 v62, v60, v61\n\tv_cmp_u_f32 vcc, v60, v61\n\ts_nop 0\n\t"                                                  \
      "v_mov_b32_dpp v63, v62 quad_perm:[1,0,3,2] row_mask:0xf bank_mask:0xf\n\t"                                                    \
      "v_perm_b32 v61, v63, v62, %[sel]\n\ts_cbranch_vccnz 50f\n\t"
#define MEV(n)
#define MEVPUT
#define MCY(n)
#define MCYPUT
#define MCYO
#define MCN
#define MCA(k)
#define FME
#define FML
#define FMZ
#define VR8(p, b) p #b "0", p #b "1", p #b "2", p #b "3", p #b "4", p #b "5", p #b "6", p #b "7", p #b "8", p #b "9"
__device__ __forceinline__ void role_mix(const Args& a, rsrc_t R, const Go& go, int m, int wave, int lane) {
  if (wave >= 2) {
    unsigned t[4];
#pragma unroll
    for (int k = 0; k < 4; ++k) {
      const int u = ((m * 2 + wave - 2) * 4 + k) * 64 + lane;
      t[k] = ld32(R, u < 8192 ? (unsigned)u * 128u : (u < 12800 ? OFF_YR + (unsigned)(u - 8192) * 128u : (u < 17408 ? OFF_YR + YSLOT_REAL + (unsigned)(u - 12800) * 128u : OOB)), 0);
    }
    asm volatile("" :: "v"(t[0]), "v"(t[1]), "v"(t[2]), "v"(t[3]));
  }
  if (wave != 0 && (wave != 2 || m != 0)) return;
  const int l15 = lane & 15, g = lane >> 4;
  if (!go_wait(R, go, lane)) return;
  RT(3);
  if (wave == 2) {
    for (int k = 0; k < TSL; ++k) __builtin_amdgcn_s_sleep(32);
    wait_flag(R, FL_MRDY + min(lane, 35) * 4, 0);
    int c = -1;
    wait_flag_c<1>(R, FL_WP + lane * 32, 0, c);
    st32((rt_now() + T1D) & ~1u, R, lane < 16 ? (unsigned)(FL_AN + lane * 128) : OOB, 0);
    return;
  }
  for (int k = 0; k < CSL; ++k) __builtin_amdgcn_s_sleep(32);
  const int cls = get_cls(R, lane);
  if (cls < 0) return;
  RT(4);
  const unsigned fo = (unsigned)(cls & 1) * 8192u, y0 = ywin(cls, m) + lane * 16;
  const u32x4 one = {~0u, ~0u, ~0u, ~0u};
  const unsigned mc = (unsigned)nearoff(m * 256 + l15 * 16) + fo + (g & 1) * CPY1 + (g >> 1) * CPY2;
#pragma unroll
  for (int k = 0; k < 8; ++k) st128(one, R, y0 + k * 1024, YSLOT_REAL);
  st128(one, R, mc, SLOT_REAL);
  RT(1);
  unsigned uo[8], eo[4];
#pragma unroll
  for (int k = 0; k < 8; ++k) uo[k] = OFF_ZF + (unsigned)(m * 40960 + k * 4096 + lane * 16);
#pragma unroll
  for (int k = 0; k < 4; ++k) {
    eo[k] = (unsigned)nearoff(NEAR_E + k * 4096 + lane * 16) + fo;
  }
  const unsigned pm = (unsigned)nearoff(NEAR_PM + lane * 16) + fo;
  const int qi = 2 * (l15 >> 2) + (l15 & 1);
  const unsigned al = !(l15 & 2) && (g & 1) == (qi >> 2) ? (unsigned)nearoff(NEAR_AL + ((g >> 1) * 8 + qi) * 16) + fo : OOB;
  const unsigned mo = (unsigned)nearoff((2 * m + (l15 >> 3)) * 128 + (2 * g + (l15 & 1)) * 16 + ((l15 & 7) >> 1) * 4) + fo;
  const unsigned sm = lane < 36 ? (unsigned)nearoff(lane * 256) + fo : OOB, own = own_cpy(cls, m / 9);
  const unsigned an = FL_AN + (m & 15) * 128, wp = FL_WP + lane * 32;
  const unsigned ie = (lane & 4) ? 0xEEEE : 0x4444, ip = (lane & 2) ? 0xEEEE : 0x4444, ia = ie;
  const unsigned sel = (lane & 1) ? 0x03020706u : 0x05040100u;
  const i32x4 D = mkdesc(a.wb);
  const unsigned uf = FL_UPROG + (lane & 31) * 128 + (m / 9) * 4;
  for (unsigned spins = 0; spins < SPINCAP; ++spins) {
    CBAR();
    if (__all(ld32(R, uf, 0) != ~0u)) break;
    __builtin_amdgcn_s_sleep(16);
  }
  for (unsigned spins = 0; spins < SPINCAP; ++spins) {
    CBAR();
    if (__all(ld32(R, OFF_ZF + (unsigned)(m * 40960 + (lane & 31) * 1024 + 1020), 0) != ~0u)) break;
    __builtin_amdgcn_s_sleep(3);
  }
  RT(2);
  unsigned tq = 0, lev = PACE0, bud = SPINCAP, s = 0, su = 0, sy = 0, sp = 0, tt, sa, sb, sc, sn, tn, ln, ue, t0, sx, sq, cw, sv, ua = ~0u, ub, uq, nr = 0, cv = 0;
  int ufl = -1;
  unsigned ev0 = 0, ev1 = 0, ev2 = 0, ev3 = 0, ec[7] = {0, 0, 0, 0, 0, 0, 0};
  int r, k;
  asm volatile(
      "29:\n\t" ULD0 ULD1 "s_mov_b32 %[t0], 0\n\ts_mov_b32 %[sb], 0\n\ts_mov_b32 %[ue], 2*" XSTR(SLOT_REAL) "\n\ts_waitcnt vmcnt(0)\n\t"
      "30:\n\t" FMZ DZERO
      "s_add_u32 %[su], %[s], 1\n\ts_and_b32 %[su], %[su], " XSTR(RMASK) "\n\ts_mul_i32 %[su], %[su], 36*40960\n\t"
      "s_add_u32 %[sn], %[sp], " XSTR(SLOT_REAL) "\n\ts_and_b32 %[sn], %[sn], 7*" XSTR(SLOT_REAL) "\n\t"
      "s_lshr_b32 %[sq], %[sp], 5\n\ts_and_b32 %[sq], %[sq], " XSTR(CPY1) "\n\ts_xor_b32 %[sq], %[sq], %[own]\n\ts_add_u32 %[sq], %[sq], %[sp]\n\t"
      "s_movk_i32 %[ln], " XSTR(PACEC) "\n\ts_cmp_lt_u32 %[s], " XSTR(S2) "-1\n\ts_cmovk_i32 %[ln], " XSTR(PACEB) "\n\t"
      "s_cmp_lt_u32 %[s], " XSTR(S1) "-1\n\ts_cmovk_i32 %[ln], " XSTR(PACEA) "\n\ts_cmp_lt_u32 %[s], " XSTR(S0) "-1\n\ts_cmovk_i32 %[ln], " XSTR(PACE0) "\n\t"
      "s_add_u32 %[sc], %[sn], " XSTR(CPY1) "\n\ts_xor_b32 %[sb], %[sn], " XSTR(CPY2) "\n\ts_mov_b32 %[sx], 0\n\t" TIMES
      UMS("0", "2*" XSTR(NG), "8") "s_cmp_lg_u32 %[s], 0\n\t"
      "buffer_store_dwordx4 %[one], %[mc], %[D], %[ue] offen sc1\n\tv_max_i32 %[ufl], %[ufl], %[ua]\n\ts_cbranch_scc0 31f\n\t"
      "v_cmp_eq_u32 vcc, -1, %[sv]\n\ts_cbranch_vccnz 35f\n"
      "33:\n\tv_cmp_ge_i32 vcc, %[s], %[ufl]\n\tv_mov_b32 %[uq], " XSTR(OOB) "\n\tv_cndmask_b32 %[uq], %[uq], %[uf], vcc\n\t"
      UMS("2*" XSTR(NG), XSTR(TB), "9") MEV(3)
      "75:\n\t" TRY("0", EM2_0, "40", "41", "42", "43")
      "51:\n\tbuffer_store_dword v61, %[mo], %[D], %[sn] offen sc1\n\tbuffer_store_dword v61, %[mo], %[D], %[sc] offen sc1\n\ts_memrealtime vcc\n\t" MEV(2) MCY(6)
      "buffer_store_dword v61, %[mo], %[D], %[sb] offen sc1\n\ts_add_u32 %[sb], %[sb], " XSTR(CPY1) "\n\tbuffer_store_dword v61, %[mo], %[D], %[sb] offen sc1\n\t"
      "s_waitcnt vmcnt(4) lgkmcnt(0)\n\ts_add_u32 %[k], vcc_lo, " XSTR(MGAP) "\n"
      "97:\n\ts_sub_u32 %[r], %[tn], %[k]\n\ts_cmp_lt_i32 %[r], 0\n\ts_cbranch_scc0 98f\n\ts_add_u32 %[tn], %[tn], " XSTR(GRID) "+" XSTR(XT) "\n\ts_branch 97b\n"
      "98:\n\t" MEVPUT MCYPUT MCN
      "s_add_u32 %[s], %[s], 1\n\ts_cmp_ge_u32 %[s], 63\n\ts_cbranch_scc1 60f\n\t"
      "v_max_i32 %[ufl], %[ufl], %[ub]\n\tv_cmp_gt_i32 vcc, %[s], %[ufl]\n\ts_mov_b32 %[sx], 1\n\ts_cbranch_vccnz 100f\n\t"
      "s_mov_b32 %[sx], 0\n\t" ULD0 "100:\n\t"
      "s_cmp_lg_u32 %[s], 1\n\ts_cbranch_scc1 67f\n\t"
      "66:\n\tbuffer_load_dword %[cw], %[an], %[D], 0 offen sc1\n\ts_waitcnt vmcnt(0)\n\tv_readfirstlane_b32 %[tn], %[cw]\n\t"
      "s_cmp_lg_u32 %[tn], -1\n\ts_cbranch_scc1 67f\n\t" BUD("66b")
      "67:\n\t"
      "s_xor_b32 %[ue], %[sn], 2*" XSTR(SLOT_REAL) "\n\ts_and_b32 %[ue], %[ue], 3*" XSTR(SLOT_REAL) "\n\ts_mov_b32 %[sp], %[sn]\n\t"
      "s_mov_b32 %[tq], %[tn]\n\ts_mov_b32 %[t0], 0\n\ts_mov_b32 %[lev], %[ln]\n\t"
      "v_cmp_ge_i32 vcc, %[s], %[ufl]\n\tv_mov_b32 %[ua], " XSTR(OOB) "\n\tv_cndmask_b32 %[ua], %[ua], %[uf], vcc\n\t"
      "s_lshr_b32 %[sa], %[sn], 5\n\ts_and_b32 %[sa], %[sa], " XSTR(CPY1) "\n\ts_xor_b32 %[sa], %[sa], %[own]\n\ts_add_u32 %[sa], %[sa], %[sn]\n\t"
      SPIN(tq, "52", "53", "54") "buffer_load_dword %[sv], %[sm], %[D], %[sa] offen sc1\n\t"
      "buffer_load_dword %[ua], %[ua], %[D], 0 offen sc1\n\t"
      "s_cmp_eq_u32 %[sx], 0\n\ts_cbranch_scc1 101f\n\t"
      "55:\n\ts_waitcnt vmcnt(0)\n\tv_max_i32 %[ufl], %[ufl], %[ua]\n\tv_cmp_gt_i32 vcc, %[s], %[ufl]\n\ts_cbranch_vccz 57f\n\t" MCA(100000)
      "buffer_load_dword %[ua], %[uf], %[D], 0 offen sc1\n\t" BUD("55b")
      "57:\n\t" ULD0 "101:\n\t" ULD1 YPO(0, 0]) YPO(1, 0]) YPO(2, 0]) YPO(3, 0]) YPO(4, 1]) YPO(5, 1]) YPO(6, 1]) YPO(7, 1])
      "s_xor_b32 %[sy], %[sy], " XSTR(YSLOT_REAL) "\n\ts_branch 30b\n\t"
      "35:\n\t" MCA(100) BUD("36f") "s_branch 32f\n\t"
      "36:\n\ts_add_u32 %[tq], %[tq], " XSTR(GRID) "+" XSTR(XT) "\n\ts_xor_b32 %[sa], %[sa], %[flp]\n\t"
      SPIN(tq, "37", "38", "39") "buffer_load_dword %[sv], %[sm], %[D], %[sa] offen sc1\n\ts_waitcnt vmcnt(0)\n\t"
      "v_cmp_eq_u32 vcc, -1, %[sv]\n\ts_cbranch_vccnz 35b\n"
      "32:\n\t" TIMES "s_branch 33b\n"
      "31:\n\tv_mov_b32 %[cw], 0\n\tbuffer_store_dword %[cw], %[rdy], %[D], 0 offen sc1\n\t"
      "64:\n\tbuffer_load_dword %[cw], %[wp], %[D], 0 offen sc1\n\tbuffer_load_dword %[ufl], %[uf], %[D], 0 offen sc1\n\t"
      "s_waitcnt vmcnt(0)\n\tv_cmp_gt_i32 vcc, 0, %[cw]\n\ts_cbranch_vccz 65f\n\t"
      "s_sleep 1\n\t" BUD("64b")
      "65:\n\ts_memrealtime vcc\n\ts_waitcnt lgkmcnt(0)\n\ts_sub_u32 %[tq], vcc_lo, " XSTR(DE0) "-" XSTR(D0) "\n\ts_branch 32b\n\t"
      "59:\n\ts_waitcnt lgkmcnt(0)\n\ts_lshr_b32 %[tt], %[lev], 8\n\ts_sub_u32 %[k], vcc_lo, %[tt]\n\ts_sub_u32 %[k], %[k], " XSTR(RTOL) "\n\t"
      "34:\n\ts_add_u32 %[tq], %[tq], " XSTR(GRID) "\n\ts_add_u32 %[t0], %[t0], " XSTR(XT) "\n\ts_sub_u32 %[r], %[tq], %[k]\n\ts_cmp_lt_i32 %[r], 0\n\ts_cbranch_scc1 34b\n\t"
      TIMES "s_branch 75b\n\t"
      "58:\n\ts_cmp_lg_u32 %[s], 0\n\ts_cbranch_scc1 102f\n\ts_mov_b32 %[su], 0\n\ts_branch 29b\n102:\n\ts_memrealtime vcc\n\ts_xor_b32 %[sq], %[sq], %[flp]\n\t" DZERO "s_branch 59b\n\t"
      "50:\n\t" FML MCA(1) BUD("58b") "v_min_u32 v61, -2, v61\n\ts_branch 51b\n\t"
      "99:\n\ts_memrealtime vcc\n\ts_xor_b32 %[sq], %[sq], %[flp]\n\ts_branch 59b\n\t"
      "61:\n\t" FME MCA(1) BUD("99b") "s_branch 62b\n\t"
      "60:\n\ts_waitcnt vmcnt(0)"
      : [tq] "+&s"(tq), [lev] "+&s"(lev), [bud] "+&s"(bud), [s] "+&s"(s), [su] "+&s"(su), [sy] "+&s"(sy), [sp] "+&s"(sp), [tt] "=&s"(tt), [sa] "=&s"(sa),
        [sn] "=&s"(sn), [sb] "=&s"(sb), [sc] "=&s"(sc), [tn] "=&s"(tn), [ln] "=&s"(ln), [ue] "=&s"(ue), [t0] "=&s"(t0), [sx] "=&s"(sx), [sq] "=&s"(sq), [cw] "=&v"(cw), [sv] "=&v"(sv), [ua] "+&v"(ua), [ub] "=&v"(ub), [uq] "=&v"(uq), [ufl] "+&v"(ufl), [r] "=&s"(r), [k] "=&s"(k), [nr] "+&s"(nr), [cv] "+&v"(cv),
        [ev0] "+&v"(ev0), [ev1] "+&v"(ev1), [ev2] "+&v"(ev2), [ev3] "+&v"(ev3) MCYO
      : [D] "s"(D), [u0] "v"(uo[0]), [u1] "v"(uo[1]), [u2] "v"(uo[2]), [u3] "v"(uo[3]), [u4] "v"(uo[4]), [u5] "v"(uo[5]), [u6] "v"(uo[6]), [u7] "v"(uo[7]), [e0] "v"(eo[0]), [e1] "v"(eo[1]), [e2] "v"(eo[2]),
        [e3] "v"(eo[3]), [y0] "v"(y0), [y1] "v"(y0 + 4096), [pm] "v"(pm), [al] "v"(al), [mo] "v"(mo), [mc] "v"(mc), [an] "v"(an), [wp] "v"(wp),
        [ie] "v"(ie), [ip] "v"(ip), [ia] "v"(ia), [sel] "v"(sel), [one] "v"(one), [sm] "v"(sm), [uf] "v"(uf), [es] "v"(lane < 32 ? (unsigned)nearoff(NEAR_E + lane * 512 + (lane & 1) * (64 - 512)) + fo : OOB), [rdy] "v"(lane == 0 ? (unsigned)(FL_MRDY + m * 4) : OOB), [own] "s"(own), [flp] "s"(CPY2 + CPY1)
      : "memory", "vcc", "scc", "s68", "s69", "s70", "s71", "s72", "s73", "s74", "s75", "s76", "s77", "s78", "s79", "s80", "s81", "s84", "s85", "s86", "s87", "s88", "s89", "s90", "s91",
        "v59", VR8("v", 6), VR8("v", 7), VR8("v", 8), VR8("v", 9), VR8("v", 10), VR8("v", 11), VR8("v", 12), VR8("v", 13), VR8("v", 14), VR8("v", 15),
        VR8("v", 16), VR8("v", 17), VR8("v", 18), VR8("v", 19), VR8("v", 20), VR8("v", 21), VR8("v", 22), VR8("v", 23), VR8("v", 24),
        "v250", "v251", "v252", "v253", "v254", "v255",
        "a0", "a1", "a2", "a3", "a4", "a5", "a6", "a7", "a8", "a9", VR8("a", 1), VR8("a", 2), VR8("a", 3), VR8("a", 4), VR8("a", 5), VR8("a", 6),
        VR8("a", 7), VR8("a", 8), VR8("a", 9), VR8("a", 10), VR8("a", 11), VR8("a", 12), VR8("a", 13), VR8("a", 14), VR8("a", 15),
        VR8("a", 16), VR8("a", 17), VR8("a", 18), VR8("a", 19), VR8("a", 20), VR8("a", 21), VR8("a", 22), VR8("a", 23), VR8("a", 24),
        "a250", "a251", "a252", "a253", "a254", "a255");
  RT(7);
}

#define XROW 1184
struct XL { rsrc_t X; unsigned vo; int wr, rd; };
__device__ __forceinline__ void x_init(XL& xl, const u16* x, int wave, int lane) {
  xl.X = __builtin_amdgcn_make_buffer_rsrc((void*)x, 0, S_LEN * DM * 2, 0x00020000);
  xl.vo = (unsigned)((wave * 8 + (lane >> 3)) * 1152 + (lane & 7) * 16);
  xl.wr = (wave * 8 + (lane >> 3)) * XROW + (lane & 7) * 16;
  xl.rd = (lane & 15) * XROW + (lane >> 4) * 16;
}
__device__ __forceinline__ u32x4 x_ld1(const XL& xl, int s, int k) { return __builtin_amdgcn_raw_buffer_load_b128(xl.X, xl.vo + k * 128, (unsigned)s * 36864u, 0); }
__device__ __forceinline__ void x_gload(u32x4 (&xr)[9], const XL& xl, int s) {
#pragma unroll
  for (int k = 0; k < 9; ++k) xr[k] = x_ld1(xl, s, k);
}
__device__ __forceinline__ void x_swrite(const u32x4 (&xr)[9], lds_char* buf, const XL& xl) {
#pragma unroll
  for (int k = 0; k < 9; ++k) *(lds_u32x4*)(buf + xl.wr + k * 128) = xr[k];
}
#define XF(buf, tt, ks) (*(lds_u32x4*)((buf) + (tt) * (16 * XROW) + (ks) * 64))
__device__ __forceinline__ void proj4(lds_char* buf, lds_char* nbuf, u32x4 (&xr)[9], const XL& xl, int snext, int wave, int lane,
                                      const u32x4 (&w0)[18], const u32x4 (&w1)[18], const u32x4 (&w2)[18], const u32x4 (&w3)[18],
                                      f32x4 (&acc)[4][2]) {
  u32x4 bb[3][2];
  buf += xl.rd; nbuf += xl.wr;
#pragma unroll
  for (int ks = 0; ks < 2; ++ks) { bb[ks][0] = XF(buf, 0, ks); bb[ks][1] = XF(buf, 1, ks); }
#pragma unroll
  for (int t = 0; t < 4; ++t) { acc[t][0] = (f32x4){0.f, 0.f, 0.f, 0.f}; acc[t][1] = acc[t][0]; }
#pragma unroll
  for (int ks = 0; ks < 18; ++ks) {
    const u32x4 b0 = bb[ks % 3][0], b1 = bb[ks % 3][1];
    SB();
    acc[0][0] = mfma(w0[ks], b0, acc[0][0]); SB();
    if (ks + 2 < 18) bb[(ks + 2) % 3][0] = XF(buf, 0, ks + 2);
    SB();
    acc[0][1] = mfma(w0[ks], b1, acc[0][1]); SB();
    if (ks + 2 < 18) bb[(ks + 2) % 3][1] = XF(buf, 1, ks + 2);
    SB();
    acc[1][0] = mfma(w1[ks], b0, acc[1][0]); SB();
    if ((ks & 1) == 0) *(lds_u32x4*)(nbuf + (ks >> 1) * 128) = xr[ks >> 1];
    SB();
    acc[1][1] = mfma(w1[ks], b1, acc[1][1]);
    acc[2][0] = mfma(w2[ks], b0, acc[2][0]); SB();
    if ((ks & 1) == 0) xr[ks >> 1] = x_ld1(xl, snext, ks >> 1);
    SB();
    acc[2][1] = mfma(w2[ks], b1, acc[2][1]);
    acc[3][0] = mfma(w3[ks], b0, acc[3][0]); acc[3][1] = mfma(w3[ks], b1, acc[3][1]);
    SB();
  }
}
#define PFL 13
#define STG0 6
#define STGD 0
#define NOST 0
#define NOFL 0
#define UFL 5
__device__ __forceinline__ int clampseg(int s) { return s < NSEG ? s : NSEG - 1; }

__device__ __forceinline__ void role_prj(const Args& a, rsrc_t R, const Go& go, lds_char* lds, const u16* W0, const u16* W1, unsigned o0, unsigned o1, unsigned flag,
                                         int tid, int wave, int lane) {
  const int l15 = lane & 15, g = lane >> 4;
  u32x4 w[2][2][18];
  u32x4 xr[9], xq[9];
  XL xl; x_init(xl, a.x, wave, lane);
  x_gload(xq, xl, 0);
  x_gload(xr, xl, 1);
  const unsigned fo = wave * 1024 + lane * 16;
  const unsigned pf = lane == 0 ? flag + wave * 4 : OOB;
  f32x4 acc[2][2][2];
  {
    u32x4 sg[PDEP][8];
    const rsrc_t B0 = __builtin_amdgcn_make_buffer_rsrc((void*)W0, 0, DM * HD * 2, 0x00020000), B1 = __builtin_amdgcn_make_buffer_rsrc((void*)W1, 0, DM * HD * 2, 0x00020000);
    const unsigned wof = (unsigned)((tid >> 4) * (HD * 2) + (tid & 15) * 16);
    auto gl = [&](int i, int k) {
      if (i < 10 && (i % 5 < 4 || k < 4)) { sg[i % PDEP][k] = __builtin_amdgcn_raw_buffer_load_b128(i < 5 ? B0 : B1, wof, (unsigned)(128 * (i % 5) + 16 * k) * (HD * 2), (NTM & 4) ? 2 : 0); SB(); }
    };
#pragma unroll
    for (int k = 0; k < 8 * PDEP; ++k) gl(k >> 3, k & 7);
    if (wave == 0) WALK(100); else if (wave == 1) WALK(101); else if (wave == 2) WALK(102); else WALK(103);
    RT(2);
    PADS(100);
    lds_char *x0p = lds + X0_OFF, *x1p = lds + X1_OFF;
    pin_v(x0p); pin_v(x1p);
    x_swrite(xq, x0p, xl);
    PAD();
    x_swrite(xr, x1p, xl);
    PAD();
    x0p += xl.rd; x1p += xl.rd;
    pin_v(x0p); pin_v(x1p);
    lds_char *wp = lds + (tid >> 4) * 288 + (tid & 15) * 16, *rp = lds + (g * 8 + (l15 >> 2)) * 288 + wave * 64 + (l15 & 3) * 8;
    pin_v(wp); pin_v(rp);
    auto put = [&](int i, int k) {
      if (i < 10 && (i % 5 < 4 || k < 4)) { *(lds_u32x4*)(wp + (i & 1) * STG_BYTES + k * (16 * 288)) = sg[i % PDEP][k]; SB(); }
    };
#pragma unroll
    for (int k = 0; k < 8; ++k) { put(0, k); gl(PDEP, k); }
    auto chunk = [&](auto IC) {
      constexpr int i = decltype(IC)::value, m = i / 5, c = i % 5, nk = c < 4 ? 4 : 2;
      if (c == 0) {
#pragma unroll
        for (int k = 0; k < 8; ++k) { acc[k >> 2][(k >> 1) & 1][k & 1] = (f32x4){0.f, 0.f, 0.f, 0.f}; pin_v(acc[k >> 2][(k >> 1) & 1][k & 1]); }
      }
      if (i == 4) { PADE(); PADS(101); } else if (i == 8) { PADE(); PADS(102); } else PAD();
      BAR();
      if (i == 0) RT(3);
      if (i == 2) RT(4);
      if (i == 4) RT(5);
      if (i == 6) RT(6);
      if (i == 7) RT(8);
      if (i == 8) RT(9);
      u32x4 xb[2][4];
      u32x2 wh[2][4];
      auto rd = [&](int kk, int j) {
        if (kk >= nk) return;
        if (j & 1) xb[kk & 1][j >> 1] = XF((j & 4) ? x1p : x0p, (j >> 1) & 1, 4 * c + kk);
        else wh[kk & 1][j >> 1] = trd(rp + (i & 1) * STG_BYTES + (kk * 32 + (j & 2) * 2) * 288 + (j >> 2) * 32);
        SB();
      };
#pragma unroll
      for (int j = 0; j < 8; ++j) rd(0, j);
#pragma unroll
      for (int k = 0; k < 8 * nk; ++k) {
        const int kk = k >> 3;
        f32x4& d = acc[(k >> 2) & 1][k & 1][(k >> 1) & 1];
        u32x4& f = w[m][k & 1][4 * c + kk];
        if ((k & 7) < 2) {
          f = (u32x4){wh[kk & 1][(k & 1) * 2][0], wh[kk & 1][(k & 1) * 2][1], wh[kk & 1][(k & 1) * 2 + 1][0], wh[kk & 1][(k & 1) * 2 + 1][1]};
          if (c < 4) pin_a(f); else pin_v(f);
        }
        if (c < 4) mfA(d, f, xb[kk & 1][(k >> 1) & 3]); else mfa(d, f, xb[kk & 1][(k >> 1) & 3]);
        SB();
        rd(kk + 1, k & 7);
        if (k % nk == 0) put(i + 1, k / nk);
        if (k % nk == 1) gl(i + 1 + PDEP, k / nk);
        if (k % 12 == 11 || k == 8 * nk - 1) PAD();
      }
      if (i == 6) x_gload(xq, xl, 2);
      if (i == 7) x_gload(xr, xl, 3);
      if (i == 6) {
        go_wait(R, go, lane);
        st32(0u, R, pf, 0);
      }
      SB();
      if (c == 4) {
        asm volatile("s_nop 7"); SB();
#pragma unroll
        for (int k = 0; k < 4; ++k) {
          PAD();
          const f32x4 (&e)[2][2] = acc[k >> 1];
          const int tt = k & 1;
          st128((u32x4){pk2(e[0][tt][0], e[0][tt][1]), pk2(e[0][tt][2], e[0][tt][3]), pk2(e[1][tt][0], e[1][tt][1]), pk2(e[1][tt][2], e[1][tt][3])}, R,
                (m ? o1 : o0) + fo + tt * 4096, (unsigned)(k >> 1) * 262144u);
        }
        SB();
      }
    };
#define CH(n) chunk(std::integral_constant<int, n>())
    CH(0); CH(1); CH(2); CH(3); CH(4); CH(5); CH(6); CH(7); CH(8); CH(9);
#undef CH
    BAR();
  }
  RT(1);
  PAD();
  x_swrite(xq, lds, xl);
  PAD();
  u32x4 bb[3][2];
  VMW(0);
  st32(3u, R, pf, 0);
  BAR();
#pragma unroll
  for (int ks = 0; ks < 2; ++ks) { bb[ks][0] = XF(lds + xl.rd, 0, ks); bb[ks][1] = XF(lds + xl.rd, 1, ks); }
  __builtin_amdgcn_s_waitcnt(0x0F70);
  lds_char *rd0 = lds + xl.rd, *rd1 = rd0 + STG_BYTES, *wr0 = lds + xl.wr, *wr1 = wr0 + STG_BYTES;
  unsigned oa = o0 + fo, ob = o1 + fo, tc0 = 0, tr0 = 0;
  pin_v(rd0); pin_v(rd1); pin_v(wr0); pin_v(wr1); pin_v(oa); pin_v(ob);
  auto half = [&](auto HC, int s) {
    constexpr int H = decltype(HC)::value & 1, P = decltype(HC)::value >> 1;
    lds_char* buf = P ? rd1 : rd0;
    lds_char* nrd = H ? (P ? rd0 : rd1) : buf;
    lds_char* nwr = P ? wr0 : wr1;
    const unsigned oo = H ? oa : ob;
    unsigned so, sx;
    f32x4 (&c)[2][2] = acc[H];
    f32x4 (&e)[2][2] = acc[H ^ 1];
    u32x4 o;
#define PM(t, tt) do { if (ks == 0) c[t][tt] = mfA0(w[H][t][0], bb[0][tt]); else if (ks < 16) mfA(c[t][tt], w[H][t][ks], bb[ks % 3][tt]); \
                       else mfa(c[t][tt], w[H][t][ks], bb[ks % 3][tt]); SB(); } while (0)
#define PR(tt) do { bb[(ks + 2) % 3][tt] = ks < 16 ? XF(buf, tt, ks + 2) : XF(nrd, tt, ks - 16); SB(); } while (0)
#pragma unroll
    for (int ks = 0; ks < 18; ++ks) {
      const int xi = H * 5 + ((ks - 7) >> 1);
      const bool xs = ks >= 7 && ks < 17 && xi < 9 && (H == 0 || ks < 15);
      if (H == 1 && ks == 16) { __builtin_amdgcn_s_waitcnt(0xC27F); __builtin_amdgcn_s_barrier(); SB(); }
      else {
        const int kw = (ks & 1) ? ks - 2 : ks - 1;
        if (kw >= 7 && kw < (H ? 15 : 17)) __builtin_amdgcn_s_waitcnt(0xC37F); else __builtin_amdgcn_s_waitcnt(0xC27F);
        SB();
      }
      PM(0, 0); PR(0);
      if (ks == 0) {
        so = (unsigned)(s + H - 1) * 262144u;
        sx = (unsigned)clampseg(s + 2) * 36864u;
        asm volatile("" : "+s"(so), "+s"(sx)); SB();
      }
      PM(0, 1); PR(1);
      PM(1, 0);
      if (ks == 1 || ks == 2) { o[(ks - 1) * 2] = pkv(e[ks - 1][0][0], e[ks - 1][0][1]); SB(); }
      if (ks == 4 || ks == 5) { o[(ks - 4) * 2] = pkv(e[ks - 4][1][0], e[ks - 4][1][1]); SB(); }
      if (ks == 3 && NOST < 2) { st128(o, R, oo, so); SB(); }
      if (ks == 6 && NOST < 2) { st128(o, R, oo + 4096, so); SB(); }
      if (xs && (ks & 1)) { *(lds_u32x4*)(nwr + xi * 128) = xr[xi < 9 ? xi : 0]; SB(); }
      if (xs && !(ks & 1)) { xr[xi < 9 ? xi : 0] = __builtin_amdgcn_raw_buffer_load_b128(xl.X, xl.vo + xi * 128, sx, 0); SB(); }
      PM(1, 1);
      if (ks % 3 == 0) { if (H == 1 && P == 0 && ks == 0) { PADE(); PADS(103); } else PAD(); SB(); }
      if (ks == 1 || ks == 2) { o[(ks - 1) * 2 + 1] = pkv(e[ks - 1][0][2], e[ks - 1][0][3]); SB(); }
      if (ks == 4 || ks == 5) { o[(ks - 4) * 2 + 1] = pkv(e[ks - 4][1][2], e[ks - 4][1][3]); SB(); }
      if (ks == PFL) {
        asm volatile("s_waitcnt vmcnt(%0)" :: "n"((H ? 8 : 7) + (PFL - 6) / 2) : "memory");
        st32((unsigned)max(2 * s + H - 2, 3), R, pf, 0); SB();
      }
      if (ks == 17 && (P == 0) == (H == 1) && UNLIKELY(s < 4)) { __builtin_amdgcn_s_waitcnt(0x0F70); st32((unsigned)(2 * s + H - 1), R, pf, 0); SB(); }
    }
#undef PM
#undef PR
  };
#pragma nounroll
  for (int s = 2; s < NSEG; ++s) {
    half(std::integral_constant<int, 0>(), s);
    half(std::integral_constant<int, 1>(), s);
    STEPRT(0, 0);
    ++s;
    half(std::integral_constant<int, 2>(), s);
    half(std::integral_constant<int, 3>(), s);
    STEPRT(0, 0);
  }
  PADE();
  mf_wait2(acc[1][0][0], acc[1][0][1]); mf_wait2(acc[1][1][0], acc[1][1][1]);
#pragma unroll
  for (int tt = 0; tt < 2; ++tt) {
    const u32x4 o = {pk2(acc[1][0][tt][0], acc[1][0][tt][1]), pk2(acc[1][0][tt][2], acc[1][0][tt][3]), pk2(acc[1][1][tt][0], acc[1][1][tt][1]), pk2(acc[1][1][tt][2], acc[1][1][tt][3])};
    st128(o, R, o1 + fo + tt * 4096, (unsigned)((NSEG - 1) & RMASK) * 262144u);
  }
  VMW(0);
  st32((unsigned)(2 * NSEG - 1), R, pf, 0);
  RT(7);
}

__device__ __forceinline__ void role_px(const Args& a, rsrc_t R, const Go& go, int pi, int wave, int lane) {
  const int l15 = lane & 15, g = lane >> 4;
  const int h = pi * 2 + (wave >> 1), qt = wave & 1;
  const int wz = h >> 3, tq = (h >> 1) & 3, hp = h & 1;
  const int gy = ((tq & 1) << 1) | (tq >> 1);
  const unsigned kmo = OFF_KMF + (unsigned)h * 4096u + lane * 16;
  const unsigned pmo = OFF_PXF + (unsigned)((qt * 40 + 32 + wz * 2 + hp) * 1024 + (gy * 16 + l15) * 16 + g * 4);
  const unsigned ppf = (lane == 0) ? (unsigned)(FL_PXPROG + h * 128 + qt * 4) : OOB;
  const unsigned qo = OFF_QF + (unsigned)h * 8192u + qt * 4096 + lane * 16;
  const unsigned k0o = OFF_KF + (unsigned)h * 8192u + lane * 16;
  const unsigned k1o = qt ? k0o + 4096 : OOB;
  const unsigned kmf = lane == 0 ? FL_KMPROG + h * 128 : OOB;
  const int kon = lane == 0 ? 0 : -NSEG;
  int fm = -1;
  RT(1);
  u32x4 qf[4], k0[4], k1[4];
  if (!go_wait(R, go, lane) || !wait_flag_c<16, 1>(R, kmf, kon, fm)) return;
#pragma unroll
  for (int ks = 0; ks < 4; ++ks) { qf[ks] = ld128(R, qo + ks * 1024, 0); k0[ks] = ld128(R, k0o + ks * 1024, 0); k1[ks] = ld128(R, k1o + ks * 1024, 0); }
  __builtin_amdgcn_s_waitcnt(0x0F70);
  for (int s = 0; s < NSEG - 1; ++s) {
    u32x4 km[4], qc[4];
    f32x4 G = {0.f, 0.f, 0.f, 0.f}, T0 = G, T1 = G;
#pragma unroll
    for (int ks = 0; ks < 4; ++ks) { T0 = mfma(k0[ks], qf[ks], T0); T1 = mfma(k1[ks], qf[ks], T1); qc[ks] = qf[ks]; }
    float mx, sx;
    px_mask(T0, T1, qt, lane);
    px_seg(T0, T1, mx, sx);
    if (!wait_flag_c<PXS, 1>(R, kmf, s + kon, fm)) return;
#pragma unroll
    for (int ks = 0; ks < 4; ++ks) km[ks] = ld128(R, kmo + ks * 1024, (unsigned)s * 131072u);
    const unsigned so = (unsigned)clampseg(s + 1) * 262144u;
#pragma unroll
    for (int ks = 0; ks < 4; ++ks) {
      qf[ks] = ld128(R, qo + ks * 1024, so);
      k0[ks] = ld128(R, k0o + ks * 1024, so);
      k1[ks] = ld128(R, k1o + ks * 1024, so);
    }
    CBAR();
    fm = (int)lda(R, kmf);
    VMW(17);
    st32((unsigned)(s - 1), R, ppf, 0);
    CBAR();
#pragma unroll
    for (int ks = 0; ks < 4; ++ks) G = mfma(km[ks], qc[ks], G);
    u32x4 px; unsigned pw;
    px_fin(G, T0, T1, mx, sx, px, pw, lane);
    st128(px, R, OFF_PXF + (unsigned)((qt * 40 + h) * 1024 + lane * 16), (unsigned)s * 81920u);
    st32(pw, R, pmo, (unsigned)s * 81920u);
    CBAR();
  }
  VMW(0);
  st32((unsigned)(NSEG - 2), R, ppf, 0);
  RT(7);
}

__device__ __forceinline__ void role_vuw(const Args& a, rsrc_t R, const Go& go, lds_char* lds, int h, int tid, int wave, int lane) {
  const int l15 = lane & 15, g = lane >> 4;
  u32x4 wv[2][18], wo[9][4];
  u32x4 xr[9], xq[9];
  XL xl; x_init(xl, a.x, wave, lane);
  x_gload(xq, xl, 0);
  x_gload(xr, xl, 1);
  unsigned zo = OFF_ZF + (unsigned)(wave * 9 * 40960 + lane * 16) + h * 1024;
  const unsigned vpf = (lane == 0) ? (unsigned)(FL_UPROG + h * 128 + wave * 4) : OOB;
  f32x4 cc[2][2], av[2][2][2];
  {
    u32x4 sg[WDEP][9];
    const rsrc_t B0 = __builtin_amdgcn_make_buffer_rsrc((void*)(a.Wv + h * 128), 0, DM * HD * 2, 0x00020000);
    const rsrc_t B1 = __builtin_amdgcn_make_buffer_rsrc((void*)(a.Wo + (size_t)h * 128 * DM), 0, 128 * DM * 2, 0x00020000);
    const unsigned wof = (unsigned)((tid >> 4) * (HD * 2) + (tid & 15) * 16);
    auto gl = [&](int c, int k) {
      if (c < 5) { if (k < (c < 4 ? 8 : 4)) { sg[c % WDEP][k] = __builtin_amdgcn_raw_buffer_load_b128(B0, wof, (unsigned)(128 * c + 16 * k) * (HD * 2), (NTM & 8) ? 2 : 0); SB(); } }
      else if (c < 9) { sg[c % WDEP][k] = __builtin_amdgcn_raw_buffer_load_b128(B1, tid * 16, (unsigned)((c - 5) * 9 + k) * 4096, (NTM & 8) ? 2 : 0); SB(); }
    };
#pragma unroll
    for (int k = 0; k < 8 * WDEP; ++k) gl(k >> 3, k & 7);
    if (wave == 0) WALK(110); else if (wave == 1) WALK(111); else if (wave == 2) WALK(112); else WALK(113);
    RT(2);
    PADS(110);
    lds_char *x0p = lds + X0_OFF, *x1p = lds + X1_OFF;
    pin_v(x0p); pin_v(x1p);
    x_swrite(xq, x0p, xl);
    PAD();
    x_swrite(xr, x1p, xl);
    PAD();
    x0p += xl.rd; x1p += xl.rd;
    pin_v(x0p); pin_v(x1p);
    lds_char *wp = lds + (tid >> 4) * 288 + (tid & 15) * 16, *rp = lds + (g * 8 + (l15 >> 2)) * 288 + wave * 64 + (l15 & 3) * 8;
    pin_v(wp); pin_v(rp);
    auto put = [&](int c, int k) {
      const int u = tid + 256 * k;
      lds_char* buf = lds + (c & 1) * STG_BYTES;
      if (c < 5) { if (k < (c < 4 ? 8 : 4)) { *(lds_u32x4*)(wp + (c & 1) * STG_BYTES + k * (16 * 288)) = sg[c % WDEP][k]; SB(); } }
      else if (c < 9) { *(lds_u32x4*)(buf + (u / 72) * 1184 + (u % 72) * 16) = sg[c % WDEP][k]; SB(); }
    };
#pragma unroll
    for (int k = 0; k < 8; ++k) { put(0, k); gl(WDEP, k); }
    PAD();
#pragma unroll
    for (int k = 0; k < 8; ++k) { av[k >> 2][(k >> 1) & 1][k & 1] = (f32x4){0.f, 0.f, 0.f, 0.f}; pin_v(av[k >> 2][(k >> 1) & 1][k & 1]); }
    auto chunk = [&](auto CC) {
      constexpr int c = decltype(CC)::value, nk = c < 4 ? 4 : 2;
      lds_char* buf = lds + (c & 1) * STG_BYTES;
      if (c == 4) { PADE(); PADS(111); } else PAD();
      BAR();
      if (c == 0) RT(3);
      if (c == 2) RT(4);
      if (c == 4) RT(5);
      if (c == 6) RT(6);
      if (c < 5) {
        u32x4 xb[2][4];
        u32x2 wh[2][4];
        auto rd = [&](int kk, int j) {
          if (kk >= nk) return;
          if (j & 1) xb[kk & 1][j >> 1] = XF((j & 4) ? x1p : x0p, (j >> 1) & 1, 4 * c + kk);
          else wh[kk & 1][j >> 1] = trd(rp + (c & 1) * STG_BYTES + (kk * 32 + (j & 2) * 2) * 288 + (j >> 2) * 32);
          SB();
        };
#pragma unroll
        for (int j = 0; j < 8; ++j) rd(0, j);
#pragma unroll
        for (int k = 0; k < 8 * nk; ++k) {
          const int kk = k >> 3;
          u32x4& f = wv[k & 1][(4 * c + kk) % 18];
          if ((k & 7) < 2) {
            f = (u32x4){wh[kk & 1][(k & 1) * 2][0], wh[kk & 1][(k & 1) * 2][1], wh[kk & 1][(k & 1) * 2 + 1][0], wh[kk & 1][(k & 1) * 2 + 1][1]};
            pin_a(f);
          }
          mfA(av[(k >> 2) & 1][k & 1][(k >> 1) & 1], f, xb[kk & 1][(k >> 1) & 3]);
          SB();
          rd(kk + 1, k & 7);
          if (k % nk == 0) put(c + 1, k / nk);
          if (k % nk == 1) gl(c + 1 + WDEP, k / nk);
          if (k % 12 == 11 || k == 8 * nk - 1) PAD();
        }
        put(c + 1, 8);
        gl(c + 1 + WDEP, 8);
      } else {
#pragma unroll
        for (int ct = 0; ct < 9; ++ct) {
          lds_char* p = buf + (4 * g + (l15 >> 2)) * 1184 + (wave * 9 + ct) * 32 + (l15 & 3) * 8;
          const u32x2 v0 = trd(p), v1 = trd(p + 16 * 1184);
          wo[ct][(c + 3) % 4] = (u32x4){v0[0], v0[1], v1[0], v1[1]};
        }
        PAD();
        SB();
        if (c == 5) {
#pragma unroll
          for (int tt = 0; tt < 2; ++tt)
            *(lds_u32x4*)(lds + X0_OFF + tt * 4096 + wave * 1024 + lane * 16) =
                (u32x4){pk2(av[0][0][tt][0], av[0][0][tt][1]), pk2(av[0][0][tt][2], av[0][0][tt][3]), pk2(av[0][1][tt][0], av[0][1][tt][1]), pk2(av[0][1][tt][2], av[0][1][tt][3])};
        }
#pragma unroll
        for (int k = 0; k < 9; ++k) { put(c + 1, k); gl(c + 1 + WDEP, k); }
        PAD();
#pragma unroll
        for (int ct = 0; ct < 9; ++ct) { if (ct < 7) pin_a(wo[ct][(c + 3) % 4]); else pin_v(wo[ct][(c + 3) % 4]); }
      }
      if (c == UPO) {
#pragma unroll
        for (int k = 0; k < 9; ++k) st128((u32x4){~0u, ~0u, ~0u, ~0u}, R, zo, k * 40960);
      }
      if (c == UGO) { go_wait(R, go, lane); st32(~1u, R, vpf, 0); }
      if (c == 5) x_gload(xq, xl, 2);
      if (c == 6) x_gload(xr, xl, 3);
      SB();
    };
#define CH(n) chunk(std::integral_constant<int, n>())
    CH(0); CH(1); CH(2); CH(3); CH(4); CH(5); CH(6); CH(7); CH(8);
#undef CH
    BAR();
  }
  RT(1);
  x_swrite(xq, lds, xl);
  lds_char *rd0 = lds + xl.rd, *rd1 = rd0 + STG_BYTES, *wr0 = lds + xl.wr, *wr1 = wr0 + STG_BYTES;
  lds_char *vr = lds + 2 * STG_BYTES + lane * 16, *vw = vr + wave * 1024;
  pin_v(rd0); pin_v(rd1); pin_v(wr0); pin_v(wr1); pin_v(vr); pin_v(vw); pin_v(zo);
  u32x4 bb[3][2], vf[2][2][4];
#pragma unroll
  for (int k = 0; k < 4; ++k) cc[k >> 1][k & 1] = (f32x4){0.f, 0.f, 0.f, 0.f};
#pragma unroll
  for (int k = 0; k < 8; ++k) { vf[0][k >> 2][k & 3] = *(lds_u32x4*)(vr + k * 1024); vf[1][k >> 2][k & 3] = xq[0]; }
  BAR();
#pragma unroll
  for (int ks = 0; ks < 2; ++ks) { bb[ks][0] = XF(rd0, 0, ks); bb[ks][1] = XF(rd0, 1, ks); }
  __builtin_amdgcn_s_waitcnt(0x0F70);
  auto iter = [&](auto PC, int s) {
    constexpr int P = decltype(PC)::value & 1, W = 0, NOV = decltype(PC)::value >> 1;
    lds_char* buf = P ? rd1 : rd0;
    lds_char* nrd = P ? rd0 : rd1;
    lds_char* nwr = P ? wr0 : wr1;
    f32x4 (&c)[2][2] = av[P];
    f32x4 (&e)[2][2] = av[P ^ 1];
    u32x4 o, q;
    unsigned s1, s2, sx;
#define VM(ks, t, tt) do { if (NOV) break; if ((ks) == 0) c[t][tt] = mfA0(wv[t][ks], bb[(ks) % 3][tt]); else mfA(c[t][tt], wv[t][ks], bb[(ks) % 3][tt]); SB(); } while (0)
#define UM(k, tt) do { f32x4& d = cc[(gq + P) & 1][tt]; const u32x4& f = vf[gq < 2 ? P : P ^ 1][tt][k];                                      \
                       if (ct < 7) { if ((k) == 0) d = mfB0(f, wo[ct][k]); else mfB(d, f, wo[ct][k]); }                                       \
                       else { if ((k) == 0) d = mf0(f, wo[ct][k]); else mfa(d, f, wo[ct][k]); } SB(); } while (0)
#define BRD(ks, tt) do { if (NOV) break; bb[(ks) % 3][tt] = (ks) < 18 ? XF(buf, tt, (ks) % 18) : XF(nrd, tt, (ks) % 18); SB(); } while (0)
#define CV(j) do { if (NOV && (j) == 0) asm volatile("s_nop 3"); o[j] = pkv(cc[(gq + P + 1) & 1][(j) >> 1][((j) & 1) * 2], cc[(gq + P + 1) & 1][(j) >> 1][((j) & 1) * 2 + 1]); SB(); } while (0)
#define CE(j, tt) do { if (gq == 0) { q[j] = pkv(e[(j) >> 1][tt][((j) & 1) * 2], e[(j) >> 1][tt][((j) & 1) * 2 + 1]); SB(); } } while (0)
#define VR(j) do { if (gq == 1) { vf[P ^ 1][(j) & 1][(j) >> 1] = *(lds_u32x4*)(vr + P * 8192 + ((j) & 1) * 4096 + ((j) >> 1) * 1024); SB(); } } while (0)
#define LW(n) do { __builtin_amdgcn_s_waitcnt(0xC07F | ((n) << 8)); SB(); } while (0)
#pragma unroll
    for (int gq = NOV ? 2 : 0; gq < 9; ++gq) {
      const int ct = (gq + 7) % 9, k0 = 2 * gq, k1 = 2 * gq + 1, pt = (gq + 6) % 9;
      if (NOV) {} else if (gq == 0 || gq == 8) LW(2); else if (gq == 1) LW(3); else if (gq == 2) LW(8); else LW(3);
      if (gq == 8 && !NOV) { __builtin_amdgcn_s_barrier(); SB(); }
      VM(k0, 0, 0); BRD(k0 + 2, 0);
      if (gq == (NOV ? 2 : 0)) {
        s1 = (unsigned)(s - 1) < NSEG ? (unsigned)(s - 1) * ZS : OOB; s2 = s >= 2 ? (unsigned)(s - 2) * ZS : OOB; sx = (unsigned)clampseg(s + 2) * 36864u;
        s1 = __builtin_amdgcn_readfirstlane(s1); s2 = __builtin_amdgcn_readfirstlane(s2); sx = __builtin_amdgcn_readfirstlane(sx);
        asm volatile("" : "+s"(s1), "+s"(s2), "+s"(sx)); SB();
      }
      UM(0, 0); CV(0);
      VM(k0, 0, 1); BRD(k0 + 2, 1);
      UM(0, 1); CV(1);
      if (gq == 1) { LW(2); __builtin_amdgcn_s_barrier(); SB(); }
      VM(k0, 1, 0); CV(2); VR(0);
      UM(1, 0); CV(3); VR(1);
#define ST(n) do { if (!NOST && STG0 + W * STGD == n) { st128(o, R, zo, (gq < 3 ? s2 : s1) + pt * 40960); SB(); } } while (0)
      ST(5);
      VM(k0, 1, 1); ST(6);
      UM(1, 1); CE(0, 0); VR(2); ST(7);
      if (NOV) {} else if (gq == 1) LW(5); else LW(2);
      VM(k1, 0, 0); BRD(k1 + 2, 0); ST(8);
      UM(2, 0); CE(1, 0); VR(3); ST(9);
      if (!NOV && gq >= 2 && gq < 7) { *(lds_u32x4*)(nwr + (gq - 2) * 128) = xr[gq - 2]; SB(); }
      VM(k1, 0, 1); BRD(k1 + 2, 1); ST(10);
      UM(2, 1); CE(2, 0); CE(3, 0); VR(4); ST(11);
      if (!NOV && gq >= 2 && gq < 7) { xr[gq - 2] = __builtin_amdgcn_raw_buffer_load_b128(xl.X, xl.vo + (gq - 2) * 128, sx, 0); SB(); }
      VM(k1, 1, 0); VR(5); ST(12);
      if (gq == 0) { *(lds_u32x4*)(vw + P * 8192) = q; SB(); CE(0, 1); }
      if (!NOV && gq >= 3 && gq < 7) { *(lds_u32x4*)(nwr + (gq + 2) * 128) = xr[gq + 2]; SB(); }
      UM(3, 0); CE(1, 1); CE(2, 1); VR(6); ST(13);
      if (!NOV && gq == 0) { PADE(); if (P) PADS(113); else PADS(112); } else PAD();
      SB();
      VM(k1, 1, 1); CE(3, 1); VR(7); ST(14);
      if (!NOV && gq >= 3 && gq < 7) { xr[gq + 2] = __builtin_amdgcn_raw_buffer_load_b128(xl.X, xl.vo + (gq + 2) * 128, sx, 0); SB(); }
      UM(3, 1); ST(15);
#undef ST
      if (gq == 0) { *(lds_u32x4*)(vw + P * 8192 + 4096) = q; SB(); }
      if (!NOV && gq == 7 && !NOFL) { VMW(13); st32((unsigned)(s - 2), R, vpf, 0); SB(); }
      if (!NOV && gq == 3 && UNLIKELY(s == 3 || s == 4)) { __builtin_amdgcn_s_waitcnt(0x0F70); st32((unsigned)(s - 2), R, vpf, 0); SB(); }
    }
#undef VM
#undef UM
#undef BRD
#undef CV
#undef CE
#undef VR
#undef LW
  };
  RT(8);
  iter(std::integral_constant<int, 3>(), 1);
  {
    f32x4 d[3][2];
#pragma unroll
    for (int k = 0; k < 16; ++k) {
      f32x4& e = d[1 + (k >> 3)][k & 1];
      if (((k >> 1) & 3) == 0) e = mf0(vf[0][k & 1][0], wo[7 + (k >> 3)][0]); else mfa(e, vf[0][k & 1][(k >> 1) & 3], wo[7 + (k >> 3)][(k >> 1) & 3]);
    }
    d[0][0] = cc[1][0]; d[0][1] = cc[1][1];
    mf_wait2(d[2][0], d[2][1]); SB();
#pragma unroll
    for (int t = 0; t < 3; ++t)
      st128((u32x4){pk2(d[t][0][0], d[t][0][1]), pk2(d[t][0][2], d[t][0][3]), pk2(d[t][1][0], d[t][1][1]), pk2(d[t][1][2], d[t][1][3])}, R, zo, (6 + t) * 40960);
    SB();
  }
  RT(9);
#pragma nounroll
  for (int s = 2; s < NSEG + 2; s += 2) {
    iter(std::integral_constant<int, 0>(), s);
    iter(std::integral_constant<int, 1>(), s + 1);
  }
  PADE();
  RT(7);
}

#define RING_OFF (2 * STG_BYTES)
#define RING_STR 456
#define OST_OFF (RING_OFF + 64 * RING_STR)
__device__ __forceinline__ float dot2(unsigned a, unsigned b, float c) {
  return __builtin_amdgcn_fdot2_f32_bf16(__builtin_bit_cast(bf16x2, a), __builtin_bit_cast(bf16x2, b), c, false);
}
__device__ __forceinline__ float swp1(float x) { return __uint_as_float(__builtin_amdgcn_update_dpp(0u, __float_as_uint(x), 0xB1, 0xf, 0xf, false)); }
__device__ __forceinline__ void role_aw(const Args& a, rsrc_t R, const Go& go, lds_char* lds, int ai, int tid, int wave, int lane) {
  const int l15 = lane & 15, g = lane >> 4;
  const int ga = ai & 7, rr = ai >> 3;
  for (int k = 0; k < AWD; ++k) __builtin_amdgcn_s_sleep(32);
  u32x4 w[4][18];
  {
    u32x4 sg[3][7];
    unsigned gv[9], wo[7], lo[7];
    const rsrc_t W = __builtin_amdgcn_make_buffer_rsrc((void*)a.wqkv, 0, DM * 1728 * 2, 0x00020000);
    const rsrc_t G = __builtin_amdgcn_make_buffer_rsrc((void*)a.gw, 0, DM * 2, 0x00020000);
#pragma unroll
    for (int k = 0; k < 7; ++k) {
      const int u = tid + 256 * k, row = u / 27, cu = u % 27;
      wo[k] = row < 64 ? (unsigned)(row * 3456 + (cu / 9) * 1152 + ga * 144 + (cu % 9) * 16) : OOB;
      lo[k] = row < 64 ? (unsigned)(row * 544 + cu * 16) : (unsigned)((tid & 63) * 544 + 448);
    }
    auto gl = [&](u32x4 (&st)[7], int c) {
#pragma unroll
      for (int k = 0; k < 7; ++k) st[k] = __builtin_amdgcn_raw_buffer_load_b128(W, wo[k], (unsigned)c * (64u * 3456u), (NTM & 16) ? 2 : 0);
    };
#pragma unroll
    for (int c = 0; c < 9; ++c) gv[c] = __builtin_amdgcn_raw_buffer_load_b16(G, (unsigned)(tid & 63) * 2, (unsigned)c * 128u, 0);
    gl(sg[0], 0); if (ASLP > 0) __builtin_amdgcn_s_sleep(ASLP);
    gl(sg[1], 1); if (ASLP > 0) __builtin_amdgcn_s_sleep(ASLP);
    gl(sg[2], 2);
#pragma unroll
    for (int c = 0; c < 9; ++c) {
      lds_char* buf = lds + (c & 1) * STG_BYTES;
#pragma unroll
      for (int k = 0; k < 7; ++k) *(lds_u32x4*)(buf + lo[k]) = sg[c % 3][k];
      *(lds_u32x4*)(buf + (tid & 63) * 544 + 432) = (u32x4){gv[c], 0u, 0u, 0u};
      if (ASLP > 0) __builtin_amdgcn_s_sleep(ASLP);
      if (c + 3 < 9) gl(sg[c % 3], c + 3);
      BAR();
#pragma unroll
      for (int kk = 0; kk < 2; ++kk)
#pragma unroll
        for (int t = 0; t < 4; ++t) {
          lds_char* p = buf + (kk * 32 + g * 8 + (l15 >> 2)) * 544 + (wave * 64 + t * 16) * 2 + (l15 & 3) * 8;
          u32x2 v0 = trd(p), v1 = trd(p + 4 * 544);
          w[t][2 * c + kk] = (u32x4){v0[0], v0[1], v1[0], v1[1]};
        }
      SB();
#pragma unroll
      for (int k = 0; k < 8; ++k) { if (c < 7) pin_a(w[k & 3][2 * c + (k >> 2)]); else pin_v(w[k & 3][2 * c + (k >> 2)]); }
    }
    BAR();
  }
  RT(1);
  go_wait(R, go, lane);
  auto sgm = [&](int it) { const int j = it / 5; return min(16 * j + 4 * rr + it - 5 * j - 1, NSEG - 1); };
  const int i0 = rr > 0 ? 0 : 1;
  u32x4 xr[9];
  XL xl; x_init(xl, a.x, wave, lane);
  x_gload(xr, xl, sgm(i0));
  x_swrite(xr, lds + i0 * STG_BYTES, xl);
  x_gload(xr, xl, sgm(i0 + 1));
  lds_char* ring = lds + RING_OFF;
  lds_char* ost = lds + OST_OFF;
  const int t = tid >> 3, hh = (tid >> 1) & 3, half = tid & 1;
  const float sc = 0.23570226039551584f * 1.4426950408889634f;
  auto done = [&](int s) { __builtin_amdgcn_raw_ptr_buffer_atomic_add_i32(1, R, tid == 0 && s >= 0 ? (unsigned)(FL_APROG + s * 128) : OOB, 0, SC1); };
  const unsigned gto = (ga == 0 && wave == 3 && g == 2) ? OFF_GATE + l15 * 4 : OOB;
  int sp = -1;
  for (int it = i0; it < 20; ++it) {
    const int s = sgm(it);
    BAR();
    {
      f32x4 acc[4][2];
      proj4(lds + (it & 1) * STG_BYTES, lds + ((it + 1) & 1) * STG_BYTES, xr, xl, sgm(it + 2), wave, lane, w[0], w[1], w[2], w[3], acc);
      VMW(9);
#pragma unroll
      for (int tl = 0; tl < 4; ++tl)
#pragma unroll
        for (int tt = 0; tt < 2; ++tt) {
          const int col = wave * 64 + tl * 16 + g * 4;
          u32x2 o = {pk2(acc[tl][tt][0], acc[tl][tt][1]), pk2(acc[tl][tt][2], acc[tl][tt][3])};
          if (col < 216) *(__attribute__((address_space(3))) u32x2*)(ring + (((s & 1) * 32 + tt * 16 + l15) * RING_STR) + col * 2) = o;
        }
#pragma unroll
      for (int tt = 0; tt < 2; ++tt) {
        float z = acc[1][tt][0];
        float gv = __builtin_amdgcn_rcpf(1.f + __builtin_amdgcn_exp2f(-1.4426950408889634f * z));
        st32(__float_as_uint(gv), R, gto + tt * 64, (unsigned)s * 128u);
      }
    }
    BAR();
    done(sp);
    sp = it % 5 != 0 ? s : -1;
    if (it % 5 != 0) {
      const int row = s * 32 + t;
      unsigned q[9];
      lds_char* qp = ring + (row & 63) * RING_STR + hh * 36;
#pragma unroll
      for (int j = 0; j < 9; ++j) q[j] = *(lds_u32*)(qp + j * 4);
      float lg[4];
      lds_char* vp[4];
#pragma unroll
      for (int wi = 0; wi < 4; ++wi) {
        const int r2 = row - 2 * (half * 4 + wi);
        const bool ok = r2 >= 0;
        lds_char* kp = ring + ((ok ? r2 : row) & 63) * RING_STR + hh * 36 + 144;
        vp[wi] = kp + 144;
        float d = 0.f;
#pragma unroll
        for (int j = 0; j < 9; ++j) d = dot2(q[j], *(lds_u32*)(kp + j * 4), d);
        lg[wi] = ok ? d : -__builtin_inff();
      }
      float m = fmaxf(fmaxf(lg[0], lg[1]), fmaxf(lg[2], lg[3]));
      m = fmaxf(m, swp1(m));
      const float ms = -m * sc;
      float sum = 0.f;
#pragma unroll
      for (int wi = 0; wi < 4; ++wi) { lg[wi] = __builtin_amdgcn_exp2f(fmaf(lg[wi], sc, ms)); sum += lg[wi]; }
      sum += swp1(sum);
      const float inv = __builtin_amdgcn_rcpf(sum);
      const unsigned p01 = pk2(lg[0] * inv, lg[1] * inv), p23 = pk2(lg[2] * inv, lg[3] * inv);
#pragma unroll
      for (int j = 0; j < 9; ++j) {
        const unsigned v0 = *(lds_u32*)(vp[0] + j * 4), v1 = *(lds_u32*)(vp[1] + j * 4), v2 = *(lds_u32*)(vp[2] + j * 4), v3 = *(lds_u32*)(vp[3] + j * 4);
        float lo = dot2(__builtin_amdgcn_perm(v1, v0, 0x05040100u), p01, 0.f);
        lo = dot2(__builtin_amdgcn_perm(v3, v2, 0x05040100u), p23, lo);
        float hi = dot2(__builtin_amdgcn_perm(v1, v0, 0x07060302u), p01, 0.f);
        hi = dot2(__builtin_amdgcn_perm(v3, v2, 0x07060302u), p23, hi);
        lo += swp1(lo); hi += swp1(hi);
        if ((j & 1) == half) *(lds_u32*)(ost + t * 144 + hh * 36 + j * 4) = pk2(lo, hi);
      }
      BAR();
#pragma unroll
      for (int k = 0; k < 2; ++k) {
        const int u = tid + 256 * k;
        const bool ok = u < 288;
        const int ur = u & 31, uc = ok ? u >> 5 : 0, cc = ga * 9 + uc;
        u32x4 o = *(lds_u32x4*)(ost + ur * 144 + uc * 16);
        st128(o, R, ok ? OFF_OA + (unsigned)(((ur >> 4) * 18 + (cc >> 2)) * 1024 + ((cc & 3) * 16 + (ur & 15)) * 16) : OOB, (unsigned)s * 36864u);
      }
      CBAR();
    }
  }
  VMW(0);
  BAR();
  done(sp);
  RT(7);
}

__device__ __forceinline__ void role_out(const Args& a, rsrc_t R, const Go& go, lds_char* lds, int o, int tid, int wave, int lane) {
  const int l15 = lane & 15, g = lane >> 4;
  const int cb = o % 9, grp = o < 36 ? o / 9 : 3, send = o / 9 == 3 ? NSEG - 1 : NSEG, sst = o < 36 ? 4 : NSEG - 4;
  u32x4 wf[4][5];
  {
    u32x4 st[18];
#pragma unroll
    for (int k = 0; k < 18; ++k) { int u = tid + 256 * k; st[k] = *(const u32x4*)(a.wo + (size_t)(u >> 3) * DM + cb * 64 + (u & 7) * 8); }
#pragma unroll
    for (int k = 0; k < 18; ++k) { int u = tid + 256 * k; *(lds_u32x4*)(lds + (u >> 3) * 144 + (u & 7) * 16) = st[k]; }
    BAR();
#pragma unroll
    for (int i = 0; i < 5; ++i)
#pragma unroll
      for (int ct = 0; ct < 4; ++ct) {
        const int ks = wave + 4 * i;
        lds_char* p = lds + ((ks < 18 ? ks : 17) * 32 + g * 8 + (l15 >> 2)) * 144 + ct * 32 + (l15 & 3) * 8;
        u32x2 v0 = trd(p), v1 = trd(p + 4 * 144);
        wf[ct][i] = (ks < 18) ? (u32x4){v0[0], v0[1], v1[0], v1[1]} : (u32x4){0u, 0u, 0u, 0u};
        pin_a(wf[ct][i]);
      }
    BAR();
  }
  const unsigned zb = OFF_ZF + (unsigned)(4 * cb) * 40960u + lane * 16;
  const unsigned flk = lane == 32 ? FL_KMPROG + ((o * 4 + wave) & 31) * 128 : OOB;
  const bool isu = (lane & 24) == 8 && lane < 48;
  auto faddr = [&](int s) -> unsigned {
    return lane < 8 ? (lane == 0 ? FL_APROG + s * 128 : OOB)
                    : (isu ? FL_UPROG + (wave * 8 + (lane & 7)) * 128 + ((cb * 64 + (lane < 16 ? 0 : 63)) / 144) * 4
                           : (lane < 32 ? (s == NSEG - 1 ? FL_PXLAST + (wave * 16 + lane - 16) * 128 : FL_PXPROG + (wave * 8 + (lane & 7)) * 128 + ((lane >> 3) & 1) * 4)
                                        : (s + 3 < NSEG ? flk : FL_YPROG + (wave * 8 + (lane & 7)) * 16 + ((cb * 64 + (lane < 48 ? 0 : 63)) / 144) * 4 + (lane < 56 ? 0 : OOB))));
  };
  const int t = tid >> 3, n8 = tid & 7;
  const rsrc_t O = __builtin_amdgcn_make_buffer_rsrc(a.out, 0, S_LEN * DM * 2, 0x00020000);
  const i32x4 D = mkdesc(a.wb);
  int fv = -1;
  RT(1);
  u32x4 zu[4][8];
  int fu = -1;
  const unsigned fua = isu ? faddr(0) : OOB;
  auto zuld = [&](int s) -> bool {
    unsigned spins = 0;
    while (ballot(isu && fu < s) != 0) {
      if (++spins > SPINCAP) return false;
      if (spins > 1) __builtin_amdgcn_s_sleep(16);
      if (s == NSEG - 1 && spins > 1) __builtin_amdgcn_s_sleep(100);
      CBAR();
      fu = (int)ld32(R, fua, 0);
    }
#pragma unroll
    for (int ct = 0; ct < 4; ++ct)
#pragma unroll
      for (int hh = 0; hh < 8; ++hh)
        asm volatile("buffer_load_dwordx4 %0, %1, %2, %3 offen offset:%4 sc1" : "=a"(zu[ct][hh])
                     : "v"(zb + ct * 40960 + wave * 8192 + (hh >> 2) * 4096), "s"(D), "s"((unsigned)(s & RMASK) * ZS), "n"((hh & 3) * 1024) : "memory");
    SB();
    return true;
  };
  if ((o != 44 && o != 40 && !go_wait(R, go, lane)) || !zuld(grp)) return;
  unsigned acc_t[8] = {0, 0, 0, 0, 0, 0, 0, 0}, t0 = 0, t1 = 0, t2 = 0, t3 = 0, t4 = 0, t5 = 0, t6 = 0;
  for (int s = grp; s < send; s += sst) {
    TS(t0);
    const unsigned fa = faddr(s);
    const int need = isu || fa >= OOB ? -1 : (lane == 0 ? 7 : ((lane >= 32 && s + 3 < NSEG) ? s + 2 : s));
    const bool far = lane >= 8 && lane < 48 && (isu || (lane < 32 && s < NSEG - 1));
    unsigned spins = 0;
    while ((ballot(fv < need) & 0xFFull) != 0) {
      if (++spins > SPINCAP) return;
      if (spins > 1) __builtin_amdgcn_s_sleep(32);
      CBAR();
      const int nv = (int)lda(R, fv < need && lane < 8 ? fa : OOB);
      fv = lane < 8 ? max(fv, nv) : fv;
    }
    TS(t1);
    u32x4 zy[4][2], pf[2][10], of[2][5];
    float gt[2];
#pragma unroll
    for (int qt = 0; qt < 2; ++qt) {
#pragma unroll
      for (int i = 0; i < 5; ++i) {
        const int ks = wave + 4 * i;
        of[qt][i] = ld128(R, OFF_OA + (unsigned)((qt * 18 + (ks < 18 ? ks : 17)) * 1024 + lane * 16), (unsigned)s * 36864u);
      }
      gt[qt] = __uint_as_float(ld32(R, OFF_GATE + (qt * 16 + l15) * 4, (unsigned)s * 128u));
    }
    SB();
    f32x4 aa[4][2];
#pragma unroll
    for (int ct = 0; ct < 4; ++ct)
#pragma unroll
      for (int qt = 0; qt < 2; ++qt) {
        f32x4 acc = {0.f, 0.f, 0.f, 0.f};
#pragma unroll
        for (int i = 0; i < 5; ++i) acc = mfma(wf[ct][i], of[qt][i], acc);
        aa[ct][qt] = acc * gt[qt];
        pin_v(aa[ct][qt]);
      }
    SB();
#define FWAIT(m) do { spins = 0;                                                                                          \
      while ((ballot(fv < need) & (m)) != 0) {                                                                            \
        if (++spins > SPINCAP) return;                                                                                    \
        if (spins > 1) { if (s < OFAST) __builtin_amdgcn_s_sleep(OSLP); else __builtin_amdgcn_s_sleep(OSL2); }            \
        CBAR();                                                                                                           \
        const int n0 = (int)lda(R, fv < need && !far ? fa : OOB), n1 = (int)ld32(R, fv < need && far ? fa : OOB, 0), nv = far ? n1 : n0;                                                             \
        fv = max(fv, nv);                                                                                                 \
      } } while (0)
#define ZYLD(zv) do { _Pragma("unroll") for (int hp = 0; hp < 2; ++hp) _Pragma("unroll") for (int ct = 0; ct < 4; ++ct) { \
                        zy[ct][hp] = ld128(R, (zv) + hp * 1024, (unsigned)(s & RMASK) * ZS + ct * 40960 + 32768 + wave * 2048); SB(); } } while (0)
#define MFS(h0, h1) do { _Pragma("unroll") for (int hh = h0; hh < h1; ++hh) _Pragma("unroll") for (int qt = 0; qt < 2; ++qt)                       \
                        _Pragma("unroll") for (int ct = 0; ct < 4; ++ct) {                                                                         \
                        ab[ct][qt] = mfma(hh < 8 ? zu[ct][hh & 7] : zy[ct][hh & 1], pf[qt][hh], hh ? ab[ct][qt] : (f32x4){0.f, 0.f, 0.f, 0.f}); SB(); } } while (0)
    TS(t2);
    FWAIT(s == NSEG - 1 ? 0xFFull << 32 : 0ull);
    const bool yr = (ballot(fv < need) >> 32) == 0;
    ZYLD(yr ? zb : OOB);
    FWAIT(0xFFFFFFFFull);
    TS(t3);
#pragma unroll
    for (int hh = 0; hh < 10; ++hh)
#pragma unroll
      for (int qt = 0; qt < 2; ++qt) {
        pf[qt][hh] = ld128(R, OFF_PXF + (unsigned)((qt * 40 + (hh < 8 ? wave * 8 + hh : 24 + wave * 2 + hh)) * 1024 + lane * 16), (unsigned)(s & RMASK) * 81920u);
        SB();
      }
    CBAR();
    f32x4 ab[4][2];
    MFS(0, 8);
    TS(t4);
    const int sn = s + sst < send ? s + sst : s;
    if (sn != s && !zuld(sn)) return;
    if (!yr) { FWAIT(~0ull); ZYLD(zb); }
    CBAR();
    {
      const bool fn = lane >= 8 && lane < 48 && (isu || (lane < 32 && sn < NSEG - 1));
      const int v0 = (int)lda(R, fn ? OOB : faddr(sn)), v1 = (int)ld32(R, fn ? faddr(sn) : OOB, 0);
      fv = fn ? v1 : v0;
    }
    SB();
    MFS(8, 10);
    fu = isu ? fv : fu;
    TS(t5);
#undef FWAIT
#undef ZYLD
#undef MFS
#pragma unroll
    for (int qt = 0; qt < 2; ++qt)
#pragma unroll
      for (int ct = 0; ct < 4; ++ct)
        *(lds_f32x4*)(lds + wave * 8704 + (qt * 16 + l15) * 272 + (ct * 16 + g * 4) * 4) = aa[ct][qt] + ab[ct][qt] * (1.f - gt[qt]);
    BAR();
    f32x4 b0 = *(lds_f32x4*)(lds + t * 272 + n8 * 32), b1 = *(lds_f32x4*)(lds + t * 272 + n8 * 32 + 16);
#pragma unroll
    for (int w = 1; w < 4; ++w) { b0 += *(lds_f32x4*)(lds + w * 8704 + t * 272 + n8 * 32); b1 += *(lds_f32x4*)(lds + w * 8704 + t * 272 + n8 * 32 + 16); }
    u32x4 ov = {pk2(b0[0], b0[1]), pk2(b0[2], b0[3]), pk2(b1[0], b1[1]), pk2(b1[2], b1[3])};
    __builtin_amdgcn_raw_buffer_store_b128(ov, O, (unsigned)((s * 32 + t) * DM + cb * 64 + n8 * 8) * 2, 0, OAUX);
    BAR();
    if (s < 4) RT(2);
    TS(t6); ACC(0, 4 * (t1 - t0)); ACC(1, 4 * (t2 - t1)); ACC(2, 4 * (t3 - t2)); ACC(3, 4 * (t4 - t3)); ACC(4, 4 * (t5 - t4)); ACC(5, 4 * (t6 - t5));
    STEPRT(0, 0);
  }
  for (int k = 0; k < 8; ++k) SV(16 + k, acc_t[k]);
  RT(7);
}

#define LDS_BYTES (153600 + 256)
__device__ __forceinline__ void code_touch(u64 pc, unsigned len, unsigned off, unsigned ld) {
  const i32x4 P = {(int)(unsigned)pc, (int)(unsigned)(pc >> 32), (int)len, 0x00020000};
  asm volatile("s_mov_b32 m0, %2\n\ts_nop 0\n\tbuffer_load_dword %0, %1, 0 offen lds" :: "v"(off), "s"(P), "s"(ld) : "memory");
}
__global__ void __launch_bounds__(256, 1) mega(Args a) {
  __shared__ __attribute__((aligned(16))) char smem[LDS_BYTES];
  lds_char* lds = (lds_char*)smem;
  const int b = blockIdx.x, tid = threadIdx.x, lane = tid & 63, wave = __builtin_amdgcn_readfirstlane(tid >> 6);
  const int x = b & 7, sl = b >> 3;
  const rsrc_t R = __builtin_amdgcn_make_buffer_rsrc(a.wb, 0, (int)OFF_END, 0x00020000);
  const Go go = go_look(R, b + wave * 4);
  if (b == 244) go_lead(R, tid, wave, lane);
  {
    u64 pc; unsigned len;
    asm volatile("s_getpc_b64 %0\n.Lcode_here:\n\ts_mov_b32 %1, zz_code_end-.Lcode_here" : "=s"(pc), "=s"(len));
    if (wave == CPF) code_touch(pc, len, (unsigned)(sl * 8192 + lane * 128), (unsigned)(uintptr_t)lds + 153600);
  }
  RT(0);
  if (wave == 3 && (sl == 24 || sl == 29)) {
    unsigned d[4];
#pragma unroll
    for (int k = 0; k < 3; ++k) d[k] = ld32(R, (unsigned)(k * 64 + lane) << 20, 0);
    d[3] = *(const unsigned*)(a.out + min(lane, 2) * (1 << 19));
    asm volatile("" :: "v"(d[0]), "v"(d[1]), "v"(d[2]), "v"(d[3]));
  }
  if (x < 4 && wave == 1 && sl >= 16 && sl < 24) calibrate(R, go, x + (sl & 4), sl & 3, lane);
  if (x < 4) {
    if (sl < 16) for (int k = 0; k < QDL; ++k) __builtin_amdgcn_s_sleep(32);
    if (sl < 8) role_qkc(a, R, go, lds, x * 8 + sl, tid, wave, lane);
    else if (sl < 16) role_vcu(a, R, go, lds, x * 8 + sl - 8, tid, wave, lane);
    else if (sl < 25) role_mix(a, R, go, x * 9 + sl - 16, wave, lane);
    else role_aw(a, R, go, lds, x * 8 + sl - 25, tid, wave, lane);
  } else {
    const int y = x - 4;
    if (sl < 8) {
      const int h = y * 8 + sl;
      role_prj(a, R, go, lds, a.Wk + h * 128, a.Wq + h * 128, OFF_KF + h * 8192u, OFF_QF + h * 8192u, FL_PROG + h * 128, tid, wave, lane);
    } else if (sl < 16) role_vuw(a, R, go, lds, y * 8 + sl - 8, tid, wave, lane);
    else if (sl < 20) role_px(a, R, go, y * 4 + sl - 16, wave, lane);
    else {
      if (sl == 31) role_aw(a, R, go, lds, y * 8 + 7, tid, wave, lane);
      for (int k = 0; k < XPD; ++k) __builtin_amdgcn_s_sleep(32);
      if (sl == 29) {
        const rsrc_t X = __builtin_amdgcn_make_buffer_rsrc((void*)a.x, 0, S_LEN * DM * 2, 0x00020000);
#pragma unroll 1
        for (int k = 0; k < 3; ++k) {
          unsigned t[12];
#pragma unroll
          for (int j = 0; j < 12; ++j) t[j] = __builtin_amdgcn_raw_buffer_load_b32(X, (unsigned)(((k * 12 + j) * 16 + y * 4 + wave) * 64 + lane) * 64, 0, 0);
#pragma unroll
          for (int j = 0; j < 12; ++j) asm volatile("" :: "v"(t[j]));
        }
      }
      if (sl < 31 || y == 0) { BAR(); role_out(a, R, go, lds, sl < 29 ? y * 9 + sl - 20 : (sl < 31 ? 36 + (sl - 29) * 4 + y : 44), tid, wave, lane); }
    }
  }
}

extern "C" __device__ __attribute__((noinline, used, visibility("hidden"))) void zz_code_end() {}
extern "C" void kernel_launch(void* const* d_in, const int* in_sizes, int n_in, void* d_out, int out_size, void* d_ws,
                              size_t ws_size, hipStream_t stream) {
  char* wb = (char*)(((uintptr_t)d_ws + 0x1FFFFF) & ~(uintptr_t)0x1FFFFF);
  Args a;
  a.x = (const u16*)d_in[0]; a.mem0 = (const u16*)d_in[4]; a.Wq = (const u16*)d_in[5]; a.Wk = (const u16*)d_in[6]; a.Wv = (const u16*)d_in[7];
  a.Wo = (const u16*)d_in[8]; a.wqkv = (const u16*)d_in[1]; a.wo = (const u16*)d_in[2]; a.gw = (const u16*)d_in[10]; a.out = (u16*)d_out; a.wb = wb;
  mega<<<256, 256, 0, stream>>>(a);
}
```

```cpp
#define S1 26
#define DEA 84
#define KA_ 68
#define S2 30
#define DEC 80
#define KC_ 64
#define DE 82
#define K2 66
#define EFIRST
#define CSLP 1
#define RTOL 2
#define QDL 0
#define LTOL 16
#define GRID 48
#define XT 6
#define NOAT
#define AWD 8
#define XPD 8
#define NTM 15
#define FIXL
#define T1D 100
#define DE0 100
#define DELTA0 172
#include <hip/hip_runtime.h>
#include <stdint.h>
#include <type_traits>

typedef unsigned short u16;
typedef __attribute__((ext_vector_type(2))) __bf16 bf16x2;
typedef __attribute__((ext_vector_type(4))) __bf16 bf16x4;
typedef __attribute__((ext_vector_type(8))) __bf16 bf16x8;
typedef __attribute__((ext_vector_type(16))) __bf16 bf16x16;
typedef __attribute__((ext_vector_type(2))) float f32x2;
typedef __attribute__((ext_vector_type(4))) float f32x4;
typedef __attribute__((ext_vector_type(2))) unsigned u32x2;
typedef __attribute__((ext_vector_type(4))) unsigned u32x4;
typedef __attribute__((ext_vector_type(8))) unsigned u32x8;
typedef __attribute__((ext_vector_type(4))) short s16x4;
typedef __attribute__((address_space(3))) s16x4 lds_s16x4;
typedef __attribute__((address_space(3))) u32x4 lds_u32x4;
typedef __attribute__((address_space(3))) f32x4 lds_f32x4;
typedef __attribute__((address_space(3))) unsigned lds_u32;
typedef __attribute__((address_space(3))) u32x2 lds_u32x2;
typedef __attribute__((address_space(3))) char lds_char;

#define S_LEN 2048
#define DM 576
#define NSEG 64
#define HD 4096

#define SLOT_REAL  131072
#define NEAR_PM    (8192 + 1024)
#define NEAR_AL    (8192 + 5120)
#define NEAR_E     16384
#define OFF_FLAGS  0x100000
#define FL_FLIP    (OFF_FLAGS + 0)
#define FL_WP      (OFF_FLAGS + 16384)
#define FL_AN      (OFF_FLAGS + 18432)
#define FL_MRDY    (OFF_FLAGS + 2048)
#define FL_PROG    (OFF_FLAGS + 4096)
#define FL_UPROG   (OFF_FLAGS + 8192)
#define FL_KMPROG  (OFF_FLAGS + 12288)
#define FL_YPROG   (OFF_FLAGS + 20480)
#define FL_PXPROG  (OFF_FLAGS + 24576)
#define FL_APROG   (OFF_FLAGS + 40960)
#define FL_PXLAST  (OFF_FLAGS + 32768)
#define FL_ACK     (OFF_FLAGS + 28672)
#define FLAG_BYTES 0xC000
#define FL_GO      (OFF_FLAGS + FLAG_BYTES)
#define NACK       945
#define REAL_CAL   0x140000
#define OFF_YR     0x200000
#define YSLOT_REAL 0x100000
#define CPY1       16384
#define CPY2       0x80000
#define OFF_KF   (4u << 20)
#define OFF_QF   (OFF_KF + (16u << 20))
#define OFF_KMF  (OFF_QF + (16u << 20))
#define OFF_PXF  (OFF_KMF + (8u << 20))
#define OFF_ZF   (OFF_PXF + (6u << 20))
#define ZS       (36u * 40960u)
#define OFF_OA   (OFF_ZF + (91u << 20))
#define OFF_GATE (OFF_OA + (3u << 20))
#define OFF_END  (OFF_GATE + (1u << 20))

#define RMASK 63
#define SPINCAP (1u << 19)
#define SC1 16

#define BAR() do { asm volatile("s_waitcnt lgkmcnt(0)" ::: "memory"); __builtin_amdgcn_s_barrier(); asm volatile("" ::: "memory"); } while (0)
#define VMW(n) asm volatile("s_waitcnt vmcnt(" #n ")" ::: "memory")
#define CBAR() asm volatile("" ::: "memory")
#define PADA
#define PADN "s_nop 0\n\t"
#define PADM
#define PADZ
#define PAD()
#define PADS(id)
#define PADE()
#define WALK(id)

#define NOSTEP 0
#define OFF_STAMP (200u << 20)
#define TS(v)
#define RT(i)
#define SV(i, v)
#define ACC(k, d)
#define STEPRT(j, f)
#define EVASM
#define EV(v)
#define EVK(v)
#define EVPUT(j, v)
#define EVPUTI(j, v)
#define EVDECL
#define EVDUMP()
#define CKDECL
#define CKS()
#define CKDUMP()
typedef unsigned long long u64;
struct Args {
  const u16* x; const u16* mem0; const u16* Wq; const u16* Wk; const u16* Wv; const u16* Wo;
  const u16* wqkv; const u16* wo; const u16* gw; u16* out; char* wb;
};

__device__ __forceinline__ float bf2f(u16 v) { return __uint_as_float(((unsigned)v) << 16); }
__device__ __forceinline__ unsigned pk2(float a, float b) {
  f32x2 v = {a, b};
  bf16x2 r = __builtin_convertvector(v, bf16x2);
  return __builtin_bit_cast(unsigned, r);
}
__device__ __forceinline__ int nearoff(int o) { return ((o >> 13) << 15) + (o & 8191); }
__device__ __forceinline__ unsigned own_cpy(int cls, int x) { return ((cls >> (2 * x)) & 2) ? CPY1 : 0u; }
__device__ __forceinline__ unsigned ywin(int cls, int m) {
  const int x = m / 9, row = (x >> 1) * 9 + m - 9 * x;
  const int se = (cls >> (4 * (x >> 1) + 1)) & 1, so = (cls >> (4 * (x >> 1) + 3)) & 1, sd = se == so ? x & 1 : ((x & 1) ? so : se);
  return (unsigned)(OFF_YR + row * 32768 + ((cls >> 8) & 1) * 8192 + ((sd ^ (cls >> 1) ^ (cls >> 9) ^ (row >> 4)) & 1) * 16384);
}
__device__ __forceinline__ u32x2 trd(lds_char* p) {
  s16x4 a = __builtin_amdgcn_ds_read_tr16_b64_v4i16((lds_s16x4*)p);
  return __builtin_bit_cast(u32x2, a);
}
__device__ __forceinline__ f32x4 mfma(u32x4 a, u32x4 b, f32x4 c) {
  return __builtin_amdgcn_mfma_f32_16x16x32_bf16(__builtin_bit_cast(bf16x8, a), __builtin_bit_cast(bf16x8, b), c, 0, 0, 0);
}
__device__ __forceinline__ f32x4 mfv0(u32x4 a, u32x4 b) {
  f32x4 d; asm volatile("s_nop 1\n\tv_mfma_f32_16x16x32_bf16 %0, %1, %2, 0" : "=&v"(d) : "v"(a), "v"(b)); return d;
}
__device__ __forceinline__ f32x4 mfvc(u32x4 a, u32x4 b, f32x4 c) {
  f32x4 d; asm volatile("s_nop 1\n\tv_mfma_f32_16x16x32_bf16 %0, %1, %2, %3" : "=&v"(d) : "v"(a), "v"(b), "v"(c)); return d;
}
__device__ __forceinline__ void mfva(f32x4& d, u32x4 a, u32x4 b) {
  asm volatile("s_nop 1\n\tv_mfma_f32_16x16x32_bf16 %0, %1, %2, %0" : "+v"(d) : "v"(a), "v"(b));
}
__device__ __forceinline__ void mf_wait(f32x4& d) { asm volatile("s_nop 7" : "+v"(d)); }
__device__ __forceinline__ void mf_wait2(f32x4& d, f32x4& e) { asm volatile("s_nop 7" : "+v"(d), "+v"(e)); }
__device__ __forceinline__ f32x4 smf(u32x4 a, u32x8 b, f32x4 c, int idx) {
  return __builtin_amdgcn_smfmac_f32_16x16x64_bf16(__builtin_bit_cast(bf16x8, a), __builtin_bit_cast(bf16x16, b), c, idx, 0, 0);
}
__device__ __forceinline__ float xmax(float x) {
  auto s = __builtin_amdgcn_permlane16_swap(__float_as_uint(x), __float_as_uint(x), false, false);
  x = fmaxf(__uint_as_float(s[0]), __uint_as_float(s[1]));
  auto t = __builtin_amdgcn_permlane32_swap(__float_as_uint(x), __float_as_uint(x), false, false);
  return fmaxf(__uint_as_float(t[0]), __uint_as_float(t[1]));
}
__device__ __forceinline__ float xsum(float x) {
  auto s = __builtin_amdgcn_permlane16_swap(__float_as_uint(x), __float_as_uint(x), false, false);
  x = __uint_as_float(s[0]) + __uint_as_float(s[1]);
  auto t = __builtin_amdgcn_permlane32_swap(__float_as_uint(x), __float_as_uint(x), false, false);
  return __uint_as_float(t[0]) + __uint_as_float(t[1]);
}
__device__ __forceinline__ f32x4 halfadd(f32x4 a, f32x4 b) {
  f32x4 o;
#pragma unroll
  for (int r = 0; r < 4; ++r) {
    auto s = __builtin_amdgcn_permlane32_swap(__float_as_uint(a[r]), __float_as_uint(b[r]), false, false);
    o[r] = __uint_as_float(s[0]) + __uint_as_float(s[1]);
  }
  return o;
}
template <class T> __device__ __forceinline__ void pin_a(T& v) { asm volatile("" : "+a"(v)); }
typedef __amdgpu_buffer_rsrc_t rsrc_t;
__device__ __forceinline__ u32x4 ld128(rsrc_t R, unsigned vo, unsigned so) { return __builtin_amdgcn_raw_buffer_load_b128(R, vo, so, SC1); }
__device__ __forceinline__ unsigned ld32(rsrc_t R, unsigned vo, unsigned so) { return __builtin_amdgcn_raw_buffer_load_b32(R, vo, so, SC1); }
__device__ __forceinline__ void st128(u32x4 v, rsrc_t R, unsigned vo, unsigned so) { __builtin_amdgcn_raw_buffer_store_b128(v, R, vo, so, SC1); }
__device__ __forceinline__ void st32(unsigned v, rsrc_t R, unsigned vo, unsigned so) { __builtin_amdgcn_raw_buffer_store_b32(v, R, vo, so, SC1); }
#define OOB 0x7ff00000
__device__ __forceinline__ unsigned lda(rsrc_t R, unsigned vo) { return ld32(R, vo, 0); }

struct Go { unsigned vo, v0; };
__device__ __forceinline__ Go go_look(rsrc_t R, int b) {
  Go g; g.vo = FL_GO + (b & 15) * 128; g.v0 = ld32(R, g.vo, 0); return g;
}
__device__ __forceinline__ bool go_wait(rsrc_t R, const Go& g, int lane) {
  for (unsigned spins = 0; spins < SPINCAP; ++spins) {
    CBAR();
    if (__all(ld32(R, g.vo, 0) != g.v0)) {
      __builtin_amdgcn_raw_ptr_buffer_atomic_add_i32(1, R, lane == 0 ? g.vo - FL_GO + FL_ACK : OOB, 0, SC1);
      return true;
    }
    __builtin_amdgcn_s_sleep(6);
  }
  return false;
}
__device__ __forceinline__ void go_lead(rsrc_t R, int tid, int wave, int lane) {
#pragma unroll
  for (int k = 0; k < FLAG_BYTES / 4096; ++k) st128((u32x4){~0u, ~0u, ~0u, ~0u}, R, OFF_FLAGS + k * 4096 + tid * 16, 0);
  VMW(0);
  BAR();
  if (wave != 0) return;
  for (unsigned k = 0; k < SPINCAP; ++k) {
    st32(k, R, lane < 16 ? FL_GO + lane * 128 : OOB, 0);
    int n = lane < 16 ? (int)ld32(R, FL_ACK + lane * 128, 0) + 1 : 0;
#pragma unroll
    for (int d = 1; d < 16; d <<= 1) n += __shfl_xor(n, d);
    if (__builtin_amdgcn_readfirstlane(n) >= NACK) break;
    __builtin_amdgcn_s_sleep(4);
  }
}
__device__ __forceinline__ bool wait_flag(rsrc_t R, unsigned vo, int need) {
  for (unsigned spins = 0; spins < SPINCAP; ++spins) {
    CBAR();
    int v = (int)ld32(R, vo, 0);
    if (__all(v >= need)) return true;
    __builtin_amdgcn_s_sleep(8);
  }
  return false;
}

template <int SLP, int AT = 0>
__device__ __forceinline__ bool wait_flag_c(rsrc_t R, unsigned vo, int need, int& cache) {
  if (__all(cache >= need)) return true;
  for (unsigned spins = 0; spins < SPINCAP; ++spins) {
    CBAR();
    cache = (int)(AT ? lda(R, vo) : ld32(R, vo, 0));
    if (__all(cache >= need)) return true;
    __builtin_amdgcn_s_sleep(SLP);
  }
  return false;
}
__device__ __forceinline__ unsigned umax3(unsigned a, unsigned b, unsigned c) { return max(max(a, b), c); }

template <int NTB> __device__ __forceinline__ u32x4 gld(const u16* p) {
  if (NTB) return __builtin_nontemporal_load((const u32x4*)p);
  return *(const u32x4*)p;
}
#define XSTR2(x) #x
#define XSTR(x) XSTR2(x)
#define LSLP 3
#define ESLP 0
#define MSLP 0
#define PXS 6
#define TSLP 2
#define XSLP 2
#define KSLP 10
#define OSLP 24
#define OSL2 4
#define UE 1
#define YL
#define CPF 3
#define PARK 58
#define OFAST 60
#define CPS 8
#define CSL 2
#define TSL 6
#define UGO 5
#define UPO 1
#define CALN 4
#define QDEP 4
#define PDEP 3
#define WDEP 3
#define VSLP 2
#define QLN 24
#define VLN 8
#define PLN 32
#define LMASK(n) ((n) >= 64 ? ~0ull : ((1ull << ((n) & 63)) - 1))
#define UP3 0
#define ORDT 0x73652410u
#define USLP 8
#define UT 0
#define UTD 1
#define EMF 0
#define EMV 0
#define MWPOS 1
#define DSLP "s_sleep 1\n\t"
typedef __attribute__((ext_vector_type(4))) int i32x4;
__device__ __forceinline__ u64 ballot(bool c) { return __builtin_amdgcn_ballot_w64(c); }
#define UNLIKELY(c) __builtin_expect(!!(c), 0)
#define DELTA 164
#define D1 98
#define DVQ 4
#define LEV0 1
#define LEV3 1
#define LEVMAX 7
#define CL0 8
#define HEAT 3
#define COOL0 4
#define PSLP 8
#define VK0 64
#define AWE 0
#define ASLP 0
#define LEVF 99
#define RX 0
#define VSL 6
#define D0 14
#define WLAST 41
#define UDL 8
#define MGAP 0
#define SMIN 8
#define K1 4
#define PAD0 4
#define PADD 4
#define PADMAX 16
#define OAUX 0
__device__ __forceinline__ unsigned rt_now() {
  u64 t; asm volatile("s_memrealtime %0\n\ts_waitcnt lgkmcnt(0)" : "=s"(t) :: "memory"); return (unsigned)t;
}
#define S0 3
#define PACE0 ((DE0 << 8) | DELTA0)
#define PACEA ((DEA << 8) | (DEA + KA_))
#define PACEB ((DE << 8) | (DE + K2))
#define PACEC ((DEC << 8) | (DEC + KC_))
__device__ __forceinline__ void next_time(unsigned& t, int s) {
  t += (s < S0 ? PACE0 : (s < S1 ? PACEA : (s < S2 ? PACEB : PACEC))) & 255u;
  t = __builtin_amdgcn_readfirstlane(t);
  asm volatile("" : "+s"(t));
}
#define SNOP 1
#define SPIN(T, a, b, c) a ":\n\ts_memrealtime vcc\n\ts_waitcnt lgkmcnt(0)\n\ts_sub_u32 %[r], %[" #T "], vcc_lo\n\ts_sub_u32 %[k], %[r], 4\n\t"   \
               "s_cmp_lt_u32 %[k], 2000\n\ts_cbranch_scc1 " a "b\n\t"                                                                       \
               "s_sub_u32 %[k], %[r], 1\n\ts_cmp_lt_u32 %[k], 3\n\ts_cbranch_scc0 " c "f\n\t"                                               \
               b ":\n\ts_nop " XSTR(SNOP) "\n\ts_sub_u32 %[k], %[k], 1\n\ts_cbranch_scc0 " b "b\n\t" c ":\n\t"
__device__ __forceinline__ int spin_until(unsigned T) {
  int r, k;
  asm volatile(SPIN(T, "1", "2", "3") : [r] "=&s"(r), [k] "=&s"(k) : [T] "s"(T) : "memory", "vcc", "scc");
  return r;
}
#define KA "v_mov_b32 v240, v240\n\ts_nop 2\n\t"
#define KAN KA KA
#define EVA(n)
#define EVC(k)
#define BUD(l) "s_sub_u32 %[bud], %[bud], 1\n\ts_cbranch_scc0 " l "\n\ts_mov_b32 %[bud], 0\n\t"
__device__ __forceinline__ i32x4 mkdesc(char* wb) {
  const u64 p = (u64)wb;
  return (i32x4){(int)(unsigned)p, (int)(unsigned)(p >> 32), (int)OFF_END, 0x00020000};
}
template <class T> __device__ __forceinline__ void pin_v(T& v) { asm volatile("" : "+v"(v)); }
#define SB() __builtin_amdgcn_sched_barrier(0)
__device__ __forceinline__ unsigned pkv(float a, float b) {
  unsigned d; asm volatile("v_cvt_pk_bf16_f32 %0, %1, %2" : "=v"(d) : "v"(a), "v"(b)); return d;
}
__device__ __forceinline__ f32x4 mfB0(u32x4 a, u32x4 b) {
  f32x4 d; asm volatile("v_mfma_f32_16x16x32_bf16 %0, %1, %2, 0" : "=&v"(d) : "v"(a), "a"(b)); return d;
}
__device__ __forceinline__ void mfB(f32x4& d, u32x4 a, u32x4 b) {
  asm volatile("v_mfma_f32_16x16x32_bf16 %0, %1, %2, %0" : "+v"(d) : "v"(a), "a"(b));
}

template <int NTB>
__device__ __forceinline__ void qk_gload(u32x4 (&st)[8], const Args& a, int h, int c, int tid) {
#pragma unroll
  for (int k = 0; k < 8; ++k) {
    int u = tid + 256 * k; int row = u >> 5, cu = u & 31;
    const u16* W = (cu < 16) ? a.Wq : a.Wk;
    st[k] = gld<NTB>(W + (size_t)(64 * c + row) * HD + h * 128 + (cu & 15) * 8);
  }
}
__device__ __forceinline__ void qk_swrite(const u32x4 (&st)[8], lds_char* buf, int tid) {
#pragma unroll
  for (int k = 0; k < 8; ++k) { int u = tid + 256 * k; int row = u >> 5, cu = u & 31; *(lds_u32x4*)(buf + row * 544 + cu * 16) = st[k]; }
}
#define STG_BYTES 38912
#define X0_OFF (2 * STG_BYTES)
#define X1_OFF (X0_OFF + 37888)

__device__ __forceinline__ int cls_look(rsrc_t R, int lane) { return (int)ld32(R, FL_FLIP + (lane & 31) * 4, 0); }
__device__ __forceinline__ int rowsum(int v) {
  v += __builtin_amdgcn_update_dpp(v, v, 0x128, 0xf, 0xf, false);
  v += __builtin_amdgcn_update_dpp(v, v, 0x124, 0xf, 0xf, false);
  v += __builtin_amdgcn_update_dpp(v, v, 0x122, 0xf, 0xf, false);
  return v + __builtin_amdgcn_update_dpp(v, v, 0x121, 0xf, 0xf, false);
}
__device__ __forceinline__ int get_cls(rsrc_t R, int lane, int v = -1) {
  for (unsigned spins = 0; spins < SPINCAP; ++spins) {
    if (__all(v >= 0)) {
      const int n = rowsum((lane & 1) ? -v : v) > 0;
      const int d = rowsum((lane & 1) != n ? 0 : (((lane >> 1) ^ (lane >> 2)) & 1 ? -v : v)) > 0;
      const int c = n | (d << 1);
      return (__builtin_amdgcn_readlane(c, 0) * 0x55 ^ 0x88) | (__builtin_amdgcn_readlane(c, 16) << 8);
    }
    if (spins) __builtin_amdgcn_s_sleep(CPS);
    CBAR();
    v = cls_look(R, lane);
  }
  return -1;
}
__device__ __forceinline__ void calibrate(rsrc_t R, const Go& go, int r, int k, int lane) {
  unsigned best = 0xFFFFFFu;
  for (int it = 0; it < CALN; ++it) {
    unsigned long long t0, t1;
    asm volatile("s_waitcnt vmcnt(0)\n\ts_memtime %0\n\ts_waitcnt lgkmcnt(0)" : "=s"(t0) :: "memory");
    st32((unsigned)it, R, (lane == 0) ? (unsigned)((r >> 2) * OFF_YR + REAL_CAL + (r & 3) * 256 + k * 8192 + (it & 1) * 4096) : OOB, 0);
    asm volatile("s_waitcnt vmcnt(0)\n\ts_memtime %0\n\ts_waitcnt lgkmcnt(0)" : "=s"(t1) :: "memory");
    best = min(best, (unsigned)(t1 - t0));
  }
  if (go_wait(R, go, lane)) st32(best, R, lane == 0 ? (unsigned)(FL_FLIP + (r * 4 + k) * 4) : OOB, 0);
}
__device__ __forceinline__ unsigned get_t1(rsrc_t R, unsigned vo, unsigned& bud) {
  unsigned v = 0;
#pragma nounroll
  for (; bud != 0; --bud) {
    CBAR();
    v = __builtin_amdgcn_readfirstlane(ld32(R, vo, 0));
    if (v != ~0u) break;
    __builtin_amdgcn_s_sleep(2);
  }
  return v;
}

#define F8_OFF 18432
#define QSTG 34816
#define ISOT()
#define ISOQ(n)
__device__ __forceinline__ float vmax(float a, float b) { float d; asm("v_max_f32 %0, %1, %2" : "=v"(d) : "v"(a), "v"(b)); return d; }
__device__ __forceinline__ float vmax3(float a, float b, float c) { float d; asm("v_max3_f32 %0, %1, %2, %3" : "=v"(d) : "v"(a), "v"(b), "v"(c)); return d; }
__device__ __forceinline__ float max32(float x) {
  auto t = __builtin_amdgcn_permlane32_swap(__float_as_uint(x), __float_as_uint(x), false, false);
  return vmax(__uint_as_float(t[0]), __uint_as_float(t[1]));
}
__device__ __forceinline__ float max16(float x) {
  auto t = __builtin_amdgcn_permlane16_swap(__float_as_uint(x), __float_as_uint(x), false, false);
  return vmax(__uint_as_float(t[0]), __uint_as_float(t[1]));
}
__device__ __forceinline__ float sum32(float x) {
  auto t = __builtin_amdgcn_permlane32_swap(__float_as_uint(x), __float_as_uint(x), false, false);
  return __uint_as_float(t[0]) + __uint_as_float(t[1]);
}
template <int r> __device__ __forceinline__ void swp32(f32x4& a, f32x4& b) {
  auto t = __builtin_amdgcn_permlane32_swap(__float_as_uint(a[r]), __float_as_uint(b[r]), false, false);
  a[r] = __uint_as_float(t[0]); b[r] = __uint_as_float(t[1]);
}
__device__ __forceinline__ float vadd(float a, float b) { float d; asm("v_add_f32 %0, %1, %2" : "=v"(d) : "v"(a), "v"(b)); return d; }
__device__ __forceinline__ float max8(float x) {
  float d; asm("s_nop 1\n\tv_max_f32_dpp %0, %1, %1 row_ror:8 row_mask:0xf bank_mask:0xf" : "=&v"(d) : "v"(x)); return d;
}
__device__ __forceinline__ float sum8(float x) {
  float d; asm("s_nop 1\n\tv_add_f32_dpp %0, %1, %1 row_ror:8 row_mask:0xf bank_mask:0xf" : "=&v"(d) : "v"(x)); return d;
}
__device__ __forceinline__ float sum16(float x) {
  auto t = __builtin_amdgcn_permlane16_swap(__float_as_uint(x), __float_as_uint(x), false, false);
  return __uint_as_float(t[0]) + __uint_as_float(t[1]);
}
__device__ __forceinline__ void st64(u32x2 v, rsrc_t R, unsigned vo, unsigned so) { __builtin_amdgcn_raw_buffer_store_b64(v, R, vo, so, SC1); }
template <int N>
__device__ __forceinline__ void mem_issue2(i32x4 D, unsigned m0, unsigned m1, unsigned m2, unsigned so, unsigned l0, unsigned l1, unsigned l2, u64& ew) {
  asm volatile("s_mov_b32 m0, %[l0]\n\ts_barrier\n\t" EVASM
               "buffer_load_dwordx4 %[m0], %[D], %[so] offen sc1 lds\n\t"
               "s_mov_b32 m0, %[l1]\n\ts_nop 0\n\t"
               "buffer_load_dwordx4 %[m1], %[D], %[so] offen sc1 lds\n\t"
               ".if %[n] == 3\n\ts_mov_b32 m0, %[l2]\n\ts_nop 0\n\t"
               "buffer_load_dwordx4 %[m2], %[D], %[so] offen sc1 lds\n\t.endif"
               : [ew] "=&s"(ew)
               : [m0] "v"(m0), [m1] "v"(m1), [m2] "v"(m2), [D] "s"(D), [so] "s"(so), [l0] "s"(l0), [l1] "s"(l1), [l2] "s"(l2), [n] "n"(N)
               : "memory");
}
#define TRYB ".if %[id] >= 0\n\ts_cbranch_vccnz .Ltry%[id]\n.Lend%[id]:\n\t.endif\n\t"
#define TRYC "vcc", "scc", "v236", "v237", "v238", "v239", "v240"
template <int ID>
__device__ __forceinline__ void try_top(unsigned& nt, unsigned& ms, unsigned& tm, unsigned& rcnt, unsigned ia) {
  asm volatile(".Ltry%[id]:\n\ts_waitcnt lgkmcnt(0)\n\ts_cmp_ge_i32 %[nt], 2\n\ts_cbranch_scc1 1f\n"
               "2:\n\ts_nop 4\n\ts_xor_b32 %[ms], %[ms], %[fl]\n\ts_add_u32 %[tm], %[tm], " XSTR(GRID) "+" XSTR(XT) "\n\ts_add_u32 %[nt], %[nt], 1\n\t.subsection 1\n"
               "1:\n\tv_mov_b32 v240, 0\n\t"
               ".irp o, 0, 1024, 2048, 3072, 4096, 5120, 6144, 7168, " XSTR(F8_OFF) "\n\tds_read_b128 v[236:239], %[ia] offset:\\o\n\ts_waitcnt lgkmcnt(0)\n\t"
               "v_max3_u32 v240, v240, v236, v237\n\tv_max3_u32 v240, v240, v238, v239\n\t.endr\n\t"
               "v_cmp_eq_u32 vcc, -1, v240\n\ts_cbranch_vccz .Lend%[id]\n\ts_add_u32 %[rc], %[rc], 1\n\ts_cmp_lt_u32 %[rc], %[cap]\n\t"
               "s_cbranch_scc1 2b\n\ts_branch .Lend%[id]\n\t.subsection 0"
               : [nt] "+s"(nt), [ms] "+s"(ms), [tm] "+s"(tm), [rc] "+s"(rcnt)
               : [fl] "n"(CPY2 + CPY1), [id] "n"(ID), [ia] "v"(ia), [cap] "n"(SPINCAP / 2) : "memory", TRYC);
}
__device__ __forceinline__ void try_end(unsigned ia, unsigned nt, unsigned ms, unsigned tm, unsigned rcnt,
                                        unsigned m0, unsigned m1, unsigned m2, unsigned l0, unsigned l1, unsigned l2, i32x4 D) {
  asm volatile("" :: "s"(rcnt), "s"(nt), "s"(ms), "s"(tm), "v"(ia), "v"(m0), "v"(m1), "v"(m2), "s"(l0), "s"(l1), "s"(l2), "s"(D) : "memory");
}
__device__ __forceinline__ int spin_late(unsigned& t, unsigned d) {
  int r, k; unsigned T;
  asm volatile("s_add_u32 %[T], %[t], %[d]\n\t" SPIN(T, "1", "2", "3") : [r] "=&s"(r), [k] "=&s"(k), [T] "=&s"(T), [t] "+s"(t) : [d] "n"(d) : "memory", "vcc", "scc");
  return r;
}
__device__ __forceinline__ void move_on(unsigned& t, int r) {
  asm volatile("1:\n\ts_cmp_lt_i32 %1, -" XSTR(LTOL) "\n\ts_cbranch_scc0 2f\n\ts_add_u32 %0, %0, " XSTR(GRID) "+" XSTR(XT) "\n\ts_add_u32 %1, %1, " XSTR(GRID) "+" XSTR(XT) "\n\ts_branch 1b\n2:"
               : "+s"(t), "+s"(r) :: "scc");
}
__device__ __forceinline__ void gate_wait(rsrc_t R, unsigned vo, int need, int& cache, unsigned& bud) {
#pragma nounroll
  for (; bud != 0; --bud) {
    if (__all(cache >= need)) break;
    CBAR();
    cache = (int)ld32(R, vo, 0);
    __builtin_amdgcn_s_sleep(4);
  }
}
__device__ __forceinline__ unsigned um3v(unsigned a, unsigned b, unsigned c) {
  unsigned d; asm volatile("v_max3_u32 %0, %1, %2, %3" : "=v"(d) : "v"(a), "v"(b), "v"(c)); return d;
}
template <int OFF>
__device__ __forceinline__ void k_put(f32x4 k, unsigned ad) {
  unsigned t0, t1;
  asm volatile("v_cvt_pk_bf16_f32 %0, %2, %3\n\tv_cvt_pk_bf16_f32 %1, %4, %5\n\tds_write2_b32 %6, %0, %1 offset0:%7 offset1:%8"
               : "=&v"(t0), "=&v"(t1) : "v"(k[0]), "v"(k[1]), "v"(k[2]), "v"(k[3]), "v"(ad), "n"(OFF), "n"(OFF + 2) : "memory");
}
template <int OFF>
__device__ __forceinline__ void q_put(f32x4 k, unsigned ad) {
  unsigned t0, t1;
  asm volatile("v_cvt_pk_bf16_f32 %0, %2, %3\n\tv_cvt_pk_bf16_f32 %1, %4, %5\n\tds_write2_b32 %6, %0, %1 offset0:%7 offset1:%8"
               : "=&v"(t0), "=&v"(t1) : "v"(k[0]), "v"(k[1]), "v"(k[2]), "v"(k[3]), "v"(ad), "n"(OFF), "n"(OFF + 1) : "memory");
}
__device__ __forceinline__ f32x4 mf0(u32x4 a, u32x4 b) {
  f32x4 d; asm volatile("v_mfma_f32_16x16x32_bf16 %0, %1, %2, 0" : "=&v"(d) : "v"(a), "v"(b)); return d;
}
__device__ __forceinline__ void mfa(f32x4& d, u32x4 a, u32x4 b) {
  asm volatile("v_mfma_f32_16x16x32_bf16 %0, %1, %2, %0" : "+v"(d) : "v"(a), "v"(b));
}
__device__ __forceinline__ f32x4 mfA0(u32x4 a, u32x4 b) {
  f32x4 d; asm volatile("v_mfma_f32_16x16x32_bf16 %0, %1, %2, 0" : "=&v"(d) : "a"(a), "v"(b)); return d;
}
__device__ __forceinline__ void mfA(f32x4& d, u32x4 a, u32x4 b) {
  asm volatile("v_mfma_f32_16x16x32_bf16 %0, %1, %2, %0" : "+v"(d) : "a"(a), "v"(b));
}
#define DPPLO " row_ror:8 row_mask:0xf bank_mask:0x3\n\t"
#define DPPHI " row_ror:8 row_mask:0xf bank_mask:0xc\n\t"
#define DPPAL " row_ror:8 row_mask:0xf bank_mask:0xf\n\t"
#define SCK "0x3e0293ee"
#define SCN "0xbe0293ee"
__device__ __forceinline__ void px_mask(f32x4& T0, f32x4& T1, int qt, int lane) {
  const float ninf = -__builtin_inff();
#pragma unroll
  for (int r = 0; r < 4; ++r) {
    const bool diag = (4 * (lane >> 4) + r) <= (lane & 15);
    T0[r] = (qt == 1 || diag) ? T0[r] : ninf;
    T1[r] = (qt == 1 && diag) ? T1[r] : ninf;
  }
}
__device__ __forceinline__ void px_seg(f32x4& T0, f32x4& T1, float& mx, float& sx) {
  const float sc = 0.08838834764831845f * 1.4426950408889634f;
  mx = xmax(fmaxf(fmaxf(fmaxf(T0[0], T0[1]), fmaxf(T0[2], T0[3])), fmaxf(fmaxf(T1[0], T1[1]), fmaxf(T1[2], T1[3]))));
  const float ms = -mx * sc;
  sx = 0.f;
#pragma unroll
  for (int r = 0; r < 4; ++r) {
    T0[r] = __builtin_amdgcn_exp2f(fmaf(T0[r], sc, ms)); T1[r] = __builtin_amdgcn_exp2f(fmaf(T1[r], sc, ms));
    sx += T0[r] + T1[r];
  }
}
__device__ __forceinline__ void px_fin(f32x4 G, f32x4 T0, f32x4 T1, float mx, float sx, u32x4& px, unsigned& pw, int lane) {
  const float sc = 0.08838834764831845f * 1.4426950408889634f;
  float g0 = (lane & 16) ? G[2] : G[0], g1 = (lane & 16) ? G[3] : G[1];
  const float ms = -xmax(fmaxf(fmaxf(g0, g1), mx)) * sc;
  g0 = __builtin_amdgcn_exp2f(fmaf(g0, sc, ms)); g1 = __builtin_amdgcn_exp2f(fmaf(g1, sc, ms));
  const float f = __builtin_amdgcn_exp2f(fmaf(mx, sc, ms));
  asm volatile("" : "+v"(g0), "+v"(g1)); PAD();
  const float inv = __builtin_amdgcn_rcpf(xsum(fmaf(f, sx, g0 + g1))), fi = f * inv;
  px = (u32x4){pk2(T0[0] * fi, T0[1] * fi), pk2(T0[2] * fi, T0[3] * fi), pk2(T1[0] * fi, T1[1] * fi), pk2(T1[2] * fi, T1[3] * fi)};
  pw = pk2(g0 * inv, g1 * inv);
}
#define WPOS 4
#define QPOS 1
#define LPOS 1
#define QIM_OFF 8192
#define KIM_OFF 10240
#define FLG_OFF 17408
#define F8W 3
#define KX_OFF 36864
typedef __attribute__((ext_vector_type(16))) float f32x16;
typedef __attribute__((ext_vector_type(16))) unsigned u32x16;
__device__ __forceinline__ void ds_flag(unsigned ad, unsigned v) { asm volatile("ds_write_b32 %0, %1" :: "v"(ad), "v"(v) : "memory"); }
#define SMX(d, j, w) "v_smfmac_f32_16x16x64_bf16 v[" d "], %[a" #j "], %[" #w "], %[ix]\n\t"
#define CVX(d, x, y) "v_cvt_pk_bf16_f32 v" #d ", v" #x ", v" #y "\n\t"
#define PKA(d, b) "v_pk_add_f32 v[" #d ":" #d "+1], v[" #b ":" #b "+1], v[" #b "+2:" #b "+3]\n\t"
#define IMW(a, o) "ds_write_b32 " a ", v232 offset:" o "\n\t"
template <int T, int AG>
__device__ __forceinline__ void smo(f32x4& c, u32x4 a, u32x8 b, int ix) {
  if (T == 0 && AG) asm volatile(SMX("140:143", 0, w) : "+{v[140:143]}"(c) : [a0] "v"(a), [w] "a"(b), [ix] "v"(ix));
  if (T == 0 && !AG) asm volatile(SMX("140:143", 0, w) : "+{v[140:143]}"(c) : [a0] "v"(a), [w] "v"(b), [ix] "v"(ix));
  if (T == 1 && AG) asm volatile(SMX("144:147", 0, w) : "+{v[144:147]}"(c) : [a0] "v"(a), [w] "a"(b), [ix] "v"(ix));
  if (T == 1 && !AG) asm volatile(SMX("144:147", 0, w) : "+{v[144:147]}"(c) : [a0] "v"(a), [w] "v"(b), [ix] "v"(ix));
}
__device__ __forceinline__ void v_zero(f32x4& v0, f32x4& v1) {
  asm volatile("v_mov_b64 v[140:141], 0\n\tv_mov_b64 v[142:143], 0\n\tv_mov_b64 v[144:145], 0\n\tv_mov_b64 v[146:147], 0\n\ts_nop 1"
               : "={v[140:143]}"(v0), "={v[144:147]}"(v1));
}
__device__ __forceinline__ void k_zero(f32x4& k0, f32x4& k1, f32x4& k2) {
  asm volatile(".rept 6\n\tv_mov_b64 v[148+2*\\+:149+2*\\+], 0\n\t.endr" : "={v[148:151]}"(k0), "={v[152:155]}"(k1), "={v[156:159]}"(k2));
}
#define TZERO ".rept 6\n\tv_mov_b64 v[200+2*\\+:201+2*\\+], 0\n\t.endr\n\t"
__device__ __forceinline__ void t_zero(f32x4& k0, f32x16& T) {
  asm volatile("v_mov_b64 v[148:149], 0\n\tv_mov_b64 v[150:151], 0\n\t" TZERO : "={v[148:151]}"(k0), "={v[200:215]}"(T));
}
#define AD0(b) "v_add_f32 v230, v[" #b "], v[" #b "+2]\n\t"
#define AD1(b) "v_add_f32 v231, v[" #b "+1], v[" #b "+3]\n\t"
#define CVP CVX(232, 230, 231)
#define Q1(j) SMX("144:147", j, w##j)
template <int ID>
__device__ __forceinline__ void q_mid(const u32x4 (&af)[9], const u32x8 (&w)[9], int ix, unsigned qw, f32x4 q0, f32x4& q1) {
  asm volatile(Q1(3) Q1(4) AD0(140) Q1(5) AD1(140) "v_cmp_u_f32 vcc, v230, v231\n\t" Q1(6) CVP Q1(7) TRYB IMW("%[qw]", "0") Q1(8)
               : "+{v[144:147]}"(q1)
               : "{v[140:143]}"(q0), [a3] "v"(af[3]), [a4] "v"(af[4]), [a5] "v"(af[5]), [a6] "v"(af[6]), [a7] "v"(af[7]), [a8] "v"(af[8]),
                 [w3] "a"(w[3]), [w4] "a"(w[4]), [w5] "a"(w[5]), [w6] "a"(w[6]), [w7] "a"(w[7]), [w8] "v"(w[8]), [ix] "v"(ix), [qw] "v"(qw), [id] "n"(ID)
               : "memory", "v230", "v231", "v232", TRYC);
}
__device__ __forceinline__ void k_last(unsigned kw, f32x4 q1, unsigned fw, unsigned fv) {
  asm volatile("s_nop 7\n\t" PKA(230, 144) CVP IMW("%[kw]", "1024") "ds_write_b32 %[fw], %[fv]"
               :: "{v[144:147]}"(q1), [kw] "v"(kw), [fw] "v"(fw), [fv] "v"(fv) : "memory", "v230", "v231", "v232");
}
#define K0(j) SMX("148:151", j, u##j)
#define K1(j) SMX("152:155", j, u##j)
#define AF9 [a0] "v"(af[0]), [a1] "v"(af[1]), [a2] "v"(af[2]), [a3] "v"(af[3]), [a4] "v"(af[4]), [a5] "v"(af[5]), [a6] "v"(af[6]), [a7] "v"(af[7]), [a8] "v"(af[8])
#define WK9 [u0] "a"(wk[0]), [u1] "a"(wk[1]), [u2] "a"(wk[2]), [u3] "a"(wk[3]), [u4] "a"(wk[4]), [u5] "a"(wk[5]), [u6] "a"(wk[6]), [u7] "a"(wk[7]), [u8] "v"(wk[8])
__device__ __forceinline__ void k_one(const u32x4 (&af)[9], const u32x8 (&wk)[9], const u32x8 (&wp)[3], int ix, unsigned qw, f32x4 q1, f32x4& k0, f32x4& k2, float& t) {
  asm volatile(K0(8) K0(0) AD0(144) AD1(144) CVP K0(4) IMW("%[qw]", "1024") K0(1) K0(2) K0(3) K0(5) PADA K0(6) K0(7)
               SMX("156:159", 0, p0) SMX("156:159", 4, p1) AD0(148)
               : "+{v[148:151]}"(k0), "+{v[156:159]}"(k2), "=&{v230}"(t)
               : "{v[144:147]}"(q1), AF9, WK9, [p0] "v"(wp[0]), [p1] "v"(wp[1]), [ix] "v"(ix), [qw] "v"(qw) : "memory", "v231", "v232");
}
#define KB
template <int PO, int JC>
__device__ __forceinline__ void k_end(const u32x4 (&af)[9], const u32x8 (&wk)[9], u32x8 wp, int ix, f32x4 k0, f32x4 k1, f32x4 k2, float t, unsigned kw, unsigned fw, unsigned fv) {
  asm volatile(".if %[jc] == 3\n\t" SMX("156:159", 3, p2) ".endif\n\t.if %[jc] == 6\n\t" SMX("156:159", 6, p2) ".endif\n\t.if %[jc] == 8\n\t" SMX("156:159", 8, p2) ".endif\n\t"
               AD1(148) K1(8) CVP K1(0) IMW("%[kw]", "0") K1(4) AD0(156) K1(1) AD1(156) K1(2) CVP K1(3) IMW("%[kw]", "%[po]") K1(5) PADA K1(6) K1(7)
               "s_nop 7\n\t" PKA(230, 152) CVP IMW("%[kw]", "1024") KB "ds_write_b32 %[fw], %[fv]\n\t"
               :: "{v[148:151]}"(k0), "{v[152:155]}"(k1), "{v[156:159]}"(k2), "{v230}"(t), AF9, WK9, [p2] "v"(wp), [ix] "v"(ix), [kw] "v"(kw), [fw] "v"(fw),
                  [fv] "v"(fv), [po] "n"(PO), [jc] "n"(JC)
               : "memory", "v231", "v232");
}
__device__ __forceinline__ unsigned add3bf(unsigned a, unsigned b, unsigned c) {
  return pk2(__uint_as_float(a << 16) + __uint_as_float(b << 16) + __uint_as_float(c << 16),
             __uint_as_float(a & 0xffff0000u) + __uint_as_float(b & 0xffff0000u) + __uint_as_float(c & 0xffff0000u));
}
#define IS(n)
#define cKW "v112"
#define cKA "v113"
#define cQ0 "v114"
#define cQ1 "v115"
#define cQ3 "v116"
#define cFA "v117"
#define cEO "v118"
#define cLO "v119"
#define cC0 "v120"
#define cC1 "v121"
#define cC2 "v122"
#define cFW "v123"
#define cQW "v124"
#define cKX "v125"
#define TRD(r, a, o) "ds_read_b64_tr_b16 v[" #r ":" #r "+1], " a " offset:" #o "\n\t"
#define LGK(n) "s_waitcnt lgkmcnt(" #n ")\n\t"
#define SXM(t, c) "v_smfmac_f32_16x16x64_bf16 v[200+4*" #t ":203+4*" #t "], v[160+4*" #c ":163+4*" #c "], v[64+16*" #t "+8*" #c ":71+16*" #t "+8*" #c "], %[ix]\n\t"
#define SMM(c) "v_smfmac_f32_16x16x64_bf16 v[208:211], v[160+4*" #c ":163+4*" #c "], v[176+8*" #c ":183+8*" #c "], %[ix]\n\t"
#define QRE TRD(160, cQ0, 0) TRD(162, cQ1, 0)
#define QRL TRD(164, cQ0, 1024) TRD(166, cQ1, 1024)
#define CN(k)
#define QE 0
#define QL 1
#define KL -1
#define QP(n) ".if " XSTR(QE) " == " #n "\n\t" QRE ".endif\n\t"
#define QQ(n) ".if " XSTR(QL) " == " #n "\n\t" QRL ".endif\n\t"
template <int PH>
__device__ __forceinline__ void kx_get(u32x16 cst, u32x16& x0, u32x16& x1) {
#define KXR(k) "ds_read_b128 v[64+4*" #k ":67+4*" #k "], " cKX " offset:%[o]+1024*" #k "\n\tds_read_b128 v[80+4*" #k ":83+4*" #k "], " cKX " offset:%[o]+1024*" #k "+16\n\t"
  asm volatile(KXR(0) KXR(1) KXR(2) KXR(3)
               : "={v[64:79]}"(x0), "={v[80:95]}"(x1) : "{v[112:127]}"(cst), [o] "n"(KX_OFF + PH * 8192) : "memory");
}
#define NK 2
#define KIF(c, t) ".if " XSTR(NK) c "\n\t" t ".endif\n\t"
__device__ __forceinline__ void s_first(const u32x4 (&af)[9], const u32x8 (&wk)[9], int ix, u32x16 cst, u32x16 x0, u32x16 x1, f32x4 q1, f32x4& k0, f32x16& T, u32x8& Q) {
  asm volatile(K0(8) QP(0) K0(0) AD0(144) AD1(144) CVP K0(4) IMW(cQW, "1024") K0(1) QP(1) QQ(0) K0(2) QQ(1) K0(3) QQ(2) KIF(" == 0", K0(5))
               LGK(2) SXM(0, 0) SXM(1, 0) LGK(0) SXM(0, 1) SXM(1, 1)
               : "+{v[148:151]}"(k0), "+{v[200:215]}"(T), "=&{v[160:167]}"(Q)
               : "{v[144:147]}"(q1), [a0] "v"(af[0]), [a1] "v"(af[1]), [a2] "v"(af[2]), [a3] "v"(af[3]), [a4] "v"(af[4]), [a5] "v"(af[5]), [a8] "v"(af[8]),
                 [u0] "a"(wk[0]), [u1] "a"(wk[1]), [u2] "a"(wk[2]), [u3] "a"(wk[3]), [u4] "a"(wk[4]), [u5] "a"(wk[5]), [u8] "v"(wk[8]),
                 [ix] "v"(ix), "{v[112:127]}"(cst), "{v[64:79]}"(x0), "{v[80:95]}"(x1)
               : "memory", "v230", "v231", "v232");
}
#define DPPF " row_mask:0xf bank_mask:0xf\n\t"
#define DX1 " quad_perm:[1,0,3,2]" DPPF
#define DX2 " quad_perm:[2,3,0,1]" DPPF
#define DX4 " row_half_mirror" DPPF
__device__ __forceinline__ void el_phase(i32x4 D, const u32x4 (&af)[9], const u32x8 (&wk)[9], int ix, u32x16 cst, u32x16 x0, u32x16 x1, u32x8& Q, f32x4& k0, f32x16& T,
                                         unsigned& bud, u64& e1, u64& e4, unsigned& nr, u32x8 sc, unsigned fv) {
#define EST(s) "buffer_store_dwordx2 v[222:223], " cEO ", %[D], " s " offen sc1\n\t"
#define LST(s) "buffer_store_dword v227, " cLO ", %[D], " s " offen sc1\n\t"
#define EPART(KA, KB_)                                                                                                             \
               IS(4) KA "v_add_f32 v212, v200, v202\n\t" KB_ "v_add_f32 v213, v201, v203\n\tv_add_f32 v214, v204, v206\n\tv_add_f32 v215, v205, v207\n\t" \
               "v_max_f32 v216, v212, v214\n\tv_max_f32 v217, v213, v215\n\tv_mov_b32 v219, v212\n\t"                              \
               "v_max_f32_dpp v218, v216, v216" DPPLO "v_max_f32_dpp v218, v217, v217" DPPHI                                       \
               "v_mov_b32 v220, v214\n\ts_nop 0\n\t"                                                                             \
               "v_max_f32_dpp v218, v218, v218" DX1 "v_mov_b32_dpp v212, v213" DPPHI "v_mov_b32_dpp v214, v215" DPPHI              \
               "v_max_f32_dpp v218, v218, v218" DX2 "v_mov_b32_dpp v213, v219" DPPLO "v_mov_b32_dpp v215, v220" DPPLO              \
               "v_max_f32_dpp v218, v218, v218" DX4 IS(5)                                                                          \
               "v_mul_f32 v216, " SCN ", v218\n\t"                                                                                 \
               "v_pk_fma_f32 v[212:213], v[212:213], s[44:45], v[216:217] op_sel_hi:[1,0,0]\n\t"                                   \
               "v_pk_fma_f32 v[214:215], v[214:215], s[44:45], v[216:217] op_sel_hi:[1,0,0]\n\t"                                   \
               "v_exp_f32 v212, v212\n\tv_exp_f32 v214, v214\n\tv_exp_f32 v213, v213\n\tv_exp_f32 v215, v215\n\t"                  \
               CVX(222, 212, 214) CVX(223, 213, 215)
#define MPART(KA, KB_, KBW)                                                                                                        \
               KA EST("s40") TRD(168, cQ3, 0) TRD(192, cKA, 2048) EST("s41") EVA(4) IS(6) TRD(194, cKA, 2304) TRD(176, cKA, 0)      \
               KB_ TRD(178, cKA, 256) TRD(180, cKA, 512) TRD(182, cKA, 768)                                                        \
               "v_add_f32 v212, v212, v214\n\tv_add_f32 v213, v213, v215\n\tv_mov_b32 v225, " cC2 "\n\tv_add_f32 v212, v212, v213\n\t"  \
               LGK(4) PKA(230, 148) CVP "v_smfmac_f32_16x16x32_bf16 v[208:211], v[168:169], v[192:195], %[ix]\n\t"                 \
               IMW(cKW, "1024") TRD(190, cKA, 1792) KBW TRD(184, cKA, 1024) TRD(186, cKA, 1280) TRD(188, cKA, 1536) LGK(5) SMM(0)  \
               EST("s42") "ds_write_b32 " cFW ", %[fv]\n\t" LGK(1) SMM(1) IS(7)
#define GADD   "v_add_f32_dpp v220, v208, v210 quad_perm:[0,1,2,3] row_mask:0xf bank_mask:0x3\n\t"                                 \
               "v_add_f32_dpp v220, v209, v211 quad_perm:[0,1,2,3] row_mask:0xf bank_mask:0xc\n\t"
#define LEND   "v_add_f32_dpp v217, v217, v217" DX4 "v_rcp_f32 v218, v217\n\t" PADN                                                \
               "v_pk_mul_f32 v[224:225], v[224:225], v[218:219] op_sel_hi:[1,0]\n\t"                                               \
               CVX(227, 224, 225) IS(8) LST("s40") LST("s41") EVA(1) IS(9) LST("s42") EST("s43") LST("s43")
#define KBF
#define EVO
  asm volatile(EPART(K0(6) KIF(" == 2", "s_nop 1\n\t"), KIF(" < 2", K0(7)))
               MPART(KIF(" > 0", K0(5)), KIF(" == 2", K0(7)), KBF)
               "s_nop 1\n\tv_add_f32_dpp v212, v212, v212" DX1 "s_nop 4\n\t" GADD
               "v_fmamk_f32 v221, v220, " SCK ", v216\n14:\n\tv_exp_f32 v220, v221\n\tv_cmp_nge_f32 vcc, s45, v221\n\tv_add_f32 v217, v220, v212\n\t"
               "v_add_f32_dpp v217, v220, v217" DX1 "v_fma_f32 v224, v220, " cC0 ", " cC1 "\n\tv_fmac_f32_dpp v225, v220, " cC0 DX1
               "v_add_f32_dpp v217, v217, v217" DX2 "s_cmp_eq_u64 vcc, 0\n\ts_cselect_b64 exec, -1, 0\n\t" LEND "s_cbranch_scc0 19f\n"
               "13:\n\t.subsection 1\n19:\n\ts_mov_b64 exec, -1\n\t" CN(10000)
               "20:\n\tds_read_b32 v224, " cFA "\n\t" LGK(0) "v_cmp_gt_i32 vcc, %[fv], v224\n\ts_cbranch_vccz 21f\n\t" BUD("20b")
               "21:\n\t" QRE QRL TZERO LGK(0) SXM(0, 0) SXM(1, 0) SXM(0, 1) SXM(1, 1)
               EPART("s_nop 7\n\t", "") MPART("", "", "")
               "v_mov_b32 v213, v212\n\tv_add_f32_dpp v212, v212, v212" DX1 "s_nop 5\n\t" GADD
               "v_fmamk_f32 v221, v220, " SCK ", v216\n\tv_cmp_nge_f32 vcc, s45, v221\n\ts_cbranch_vccz 14b\n\t"
               "v_max_f32 v216, v220, v218\n\tv_mul_f32 v221, " SCK ", v218\n\ts_nop 0\n\t"
               "v_max_f32_dpp v216, v216, v216" DX1 "v_mul_f32 v220, " SCK ", v220\n\ts_nop 0\n\t"
               "v_max_f32_dpp v216, v216, v216" DX2 "s_nop 1\n\t"
               "v_max_f32_dpp v216, v216, v216" DX4
               "v_pk_fma_f32 v[220:221], v[216:217], s[44:45], v[220:221] op_sel_hi:[0,0,1] neg_lo:[1,0,0] neg_hi:[1,0,0]\n\t"
               "v_exp_f32 v221, v221\n\tv_exp_f32 v220, v220\n\t"
               "v_mul_f32 v224, v221, " cC1 "\n\tv_fma_f32 v217, v221, v213, v220\n\t"
               "v_mul_f32 v225, v221, " cC2 "\n\tv_fmac_f32 v224, v220, " cC0 "\n\t"
               "v_add_f32_dpp v217, v217, v217" DX1 "v_fmac_f32_dpp v225, v220, " cC0 DX1 "s_nop 0\n\t"
               "v_add_f32_dpp v217, v217, v217" DX2 "s_nop 1\n\t" LEND "s_branch 13b\n\t.subsection 0"
               : [bud] "+s"(bud), "+{v[160:167]}"(Q), "+{v[148:151]}"(k0), "+{v[200:215]}"(T) EVO
               : [a5] "v"(af[5]), [a6] "v"(af[6]), [a7] "v"(af[7]), [u5] "a"(wk[5]), [u6] "a"(wk[6]), [u7] "a"(wk[7]), [ix] "v"(ix),
                 "{v[112:127]}"(cst), "{v[64:79]}"(x0), "{v[80:95]}"(x1), [D] "s"(D), "{s[40:47]}"(sc), [fv] "v"(fv)
               : "memory", "vcc", "scc", "v168", "v169", "v176", "v177", "v178", "v179", "v180", "v181", "v182", "v183", "v184",
                 "v185", "v186", "v187", "v188", "v189", "v190", "v191", "v192", "v193", "v194", "v195",
                 "v216", "v217", "v218", "v219", "v220", "v221", "v222", "v223", "v224", "v225", "v227", "v230", "v231", "v232");
}
#define YMAX 0x42700000u
#define TWO 2
#define ARD(j, p) do { af[j] = *(lds_u32x4*)(p); SB(); } while (0)
#define SECOND(n, NXV) do { if (TWO == n) { asm volatile("s_waitcnt vmcnt(%0)" :: "n"(NXV) : "memory"); __builtin_amdgcn_s_barrier(); CBAR(); SB(); \
                         ARD(4, ard0 + 4096); ARD(5, ard1 + 4096); } if (TWO + 1 == n) { ARD(6, ard2 + 4096); ARD(7, ard3 + 4096); }               \
                         if (TWO + 2 == n) ARD(8, lds + F8_OFF + ar); } while (0)
#define HEAD(M, NXV, H7) do {                                                                                                                     \
    ARD(0, ard0); __builtin_amdgcn_s_barrier(); CBAR(); SB(); ARD(1, ard1); ARD(2, ard2); ARD(3, ard3);                                       \
    if (!TWO) { ARD(4, ard0 + 4096); ARD(5, ard1 + 4096); }                                                                                   \
    M(0, 0); SECOND(1, NXV); M(1, 0); SECOND(2, NXV); M(0, 1); SECOND(3, NXV); M(1, 1); SECOND(4, NXV); M(0, 2); SECOND(5, NXV);              \
    if (!(H7)) M(1, 2);                                                                                                                       \
    SECOND(6, NXV); M(0, 3); M(0, 4); M(0, 5); M(0, 6); M(0, 7); M(0, 8); } while (0)
__device__ __forceinline__ void role_qkc(const Args& a, rsrc_t R, const Go& go, lds_char* lds, int h, int tid, int hw, int lane) {
  const int wave = 3 - hw;
  const int l15 = lane & 15, g = lane >> 4;
  u32x8 wq[2][9], wk[2][9], wp[3];
  int cls = -1, kv0 = -1;
  const int wz = h >> 3, tq = (h >> 1) & 3, hp = h & 1, qi = 2 * g + (l15 >> 3), kq = l15 & 7;
  unsigned pxo = (unsigned)nearoff(NEAR_E + (h >> 1) * 1024 + ((kq >> 1) * 16 + 8 * (qi >> 2) + 4 * hp + (qi & 3)) * 16 + (kq & 1) * 8);
  unsigned plo = !(kq & 1) ? (unsigned)nearoff(NEAR_PM + wz * 1024 + ((2 * hp + (tq >> 1)) * 16 + 4 * (qi >> 1) + (kq & 2) + (qi & 1)) * 16 + (tq & 1) * 8 + (kq >> 2) * 4)
                           : (kq == 1 ? (unsigned)nearoff(NEAR_AL + wz * 256 + ((tq >> 1) * 8 + qi) * 16 + (2 * hp + (tq & 1)) * 4) : OOB);
  {
    u32x4 sg[QDEP][8];
#pragma unroll
    for (int c = 0; c < QDEP; ++c) qk_gload<NTM & 1>(sg[c], a, h, c, tid);
#define WK3(b) do { WALK(b + 2); WALK(b + 1); WALK(b); } while (0)
    if (wave == 0) WK3(200); else if (wave == 1) WK3(208); else if (wave == 2) WK3(216); else WK3(224);
    const int q = l15 >> 2;
    const int lrow = 16 * (g >> 1) + 4 * (g & 1) + 8 * (q >> 1) + (q & 1);
    auto wrd = [&](lds_char* p) {
      u32x2 v0 = trd(p), v1 = trd(p + 2 * 544), v2 = trd(p + 32 * 544), v3 = trd(p + 34 * 544);
      return (u32x8){v0[0], v0[1], v1[0], v1[1], v2[0], v2[1], v3[0], v3[1]};
    };
#pragma unroll
    for (int ph = 0; ph < 3; ++ph) {
#pragma unroll
      for (int i = 0; i < 4; ++i) {
        const int c = ph * 4 + i;
        if (c < 9) {
          qk_swrite(sg[c % QDEP], lds + i * QSTG, tid);
          if (c + QDEP < 9) qk_gload<NTM & 1>(sg[c % QDEP], a, h, c + QDEP, tid);
          CBAR();
        }
      }
      RT(8 + ph);
      if (ph == 2 && go_wait(R, go, lane)) {
        kv0 = (int)ld32(R, FL_PROG + h * 128 + (lane & 3) * 4, 0);
        cls = get_cls(R, lane, cls_look(R, lane));
        pxo += (unsigned)(cls & 1) * 8192u; plo += (unsigned)(cls & 1) * 8192u;
        if (wave < 3) {
#pragma unroll
          for (int k = 0; k < 4; ++k) {
            const unsigned so = wave * SLOT_REAL + (k & 1) * CPY1 + (k >> 1) * CPY2;
            st64((u32x2){~0u, ~0u}, R, pxo, so); st32(~0u, R, plo, so);
          }
        }
        CBAR();
        RT(11);
      }
      BAR();
#pragma unroll
      for (int i = 0; i < 4; ++i) {
        const int j = ph * 4 + i;
        if (j < 9) {
          lds_char* buf = lds + (ph < 2 ? ((i + wave) & 3) : 0) * QSTG + lrow * 544 + (l15 & 3) * 8;
#pragma unroll
          for (int t = 0; t < 4; ++t) {
            u32x8& w = t < 2 ? wq[t & 1][j] : wk[t & 1][j];
            w = wrd(buf + t * 128 + wave * 32);
          }
          if (j == 0 || j == 4) wp[j >> 2] = wrd(buf + 256 + 96);
        }
      }
      SB();
#pragma unroll
      for (int k = 0; k < 16; ++k) {
        const int j = ph * 4 + (k >> 2);
        if (j < 9) { u32x8& w = (k & 2) ? wk[k & 1][j] : wq[k & 1][j]; if (j < 8) pin_a(w); else pin_v(w); }
      }
      if (ph < 2) pin_v(wp[ph]);
      if (ph == 0 || wave == ph) wp[2] = wrd(lds + (ph < 2 ? 3 : 0) * QSTG + lrow * 544 + (l15 & 3) * 8 + 256 + 96);
      pin_v(wp[2]);
      BAR();
    }
  }
  RT(1);
  if (cls < 0) return;
  RT(2);
  auto run = [&](auto ROLEC) {
  constexpr int ROLE = decltype(ROLEC)::value;
  lds_char* img = lds;
  const unsigned fo = (unsigned)(cls & 1) * 8192u;
  const int r0 = ROLE * 1024 + lane * 16, r1 = r0 + 4096, r2 = 8192 + lane * 16;
  const unsigned m0 = nearoff(r0) + fo, m1 = nearoff(r1) + fo, m2 = nearoff(r2) + fo;
  const unsigned lb = (unsigned)(uintptr_t)lds, lw0 = lb + ROLE * 1024;
  const i32x4 D = mkdesc(a.wb);
  const int ar = g * 256 + ((l15 >> 1) & 1) * 128 + (2 * (l15 >> 2) + (l15 & 1)) * 16;
  lds_char* ard0 = img + ROLE * 1024 + ar;
  lds_char* ard1 = img + ((ROLE + 1) & 3) * 1024 + ar;
  lds_char* ard2 = img + ((ROLE + 2) & 3) * 1024 + ar;
  lds_char* ard3 = img + ((ROLE + 3) & 3) * 1024 + ar;
  const int gy = ((tq & 1) << 1) | (tq >> 1);
  const unsigned pcl = lane < 32 ? (unsigned)nearoff(NEAR_E + (h >> 1) * 1024 + ((lane >> 2) * 8 + 4 * hp + (lane & 3)) * 16) + fo
                     : (lane < 40 ? (unsigned)nearoff(NEAR_PM + wz * 1024 + ((2 * hp + (tq >> 1)) * 16 + (tq & 1) * 8 + (lane & 7)) * 16) + fo
                                  : (lane < 42 ? (unsigned)nearoff(NEAR_AL + wz * 256 + ((tq >> 1) * 8 + (2 * hp + (tq & 1)) * 2 + (lane & 1)) * 16) + fo : OOB));
  const unsigned kfo = OFF_KF + (unsigned)h * 8192u + lane * 16;
  constexpr int QT = ROLE >> 1;
  const unsigned kmo = OFF_KMF + (unsigned)h * 4096u + lane * 16;
  const unsigned pxs = OFF_PXF + (unsigned)((QT * 40 + h) * 1024 + lane * 16);
  const unsigned pws = OFF_PXF + (unsigned)((QT * 40 + 32 + wz * 2 + hp) * 1024 + (gy * 16 + l15) * 16 + g * 4);
  const unsigned kpf = FL_PROG + h * 128 + (lane & 3) * 4;
  const unsigned kmf = (lane == 0) ? (unsigned)(FL_KMPROG + h * 128) : OOB;
  const unsigned wpo = lane == 0 ? (unsigned)(FL_WP + h * 32) : OOB;
  const int idx = (lane & 2) ? 0xEEEE : 0x4444;
  const int jr = l15 >> 2;
  const unsigned qw = lb + QIM_OFF + (ROLE * 8 + ((2 * jr + (l15 & 1)) ^ ((ROLE & 1) << 1))) * 32 + g * 8 + (l15 & 2) * 2;
  const unsigned kw = lb + KIM_OFF + ROLE * 256 + l15 * 16 + g * 4;
  lds_char* krd = lds + KIM_OFF + (4 * g + jr) * 16 + (l15 & 2) * 4;
  unsigned fw = lb + FLG_OFF + ROLE * 4;
  pin_v(fw);
  lds_char* frd = lds + FLG_OFF + (lane & 3) * 4;
  const unsigned qa = lb + QIM_OFF + (16 * (g >> 1) + 4 * (g & 1) + (jr & 1) + 10 * (jr >> 1)) * 32 + (l15 & 3) * 8;
  u32x16 cst = {kw, lb + KIM_OFF + (4 * g + jr) * 16 + (l15 & 1) * 8, qa, qa ^ 64, lb + QIM_OFF + ((24 + 2 * g + (jr & 1)) ^ 2) * 32 + (l15 & 3) * 8,
                lb + FLG_OFF + min(lane & 3, 2) * 4, pxo, plo, (l15 & 1) ? 0u : 0x3f800000u, l15 == 1 ? 0x3f800000u : 0u, l15 == 9 ? 0x3f800000u : 0u, fw, qw,
                lb + (l15 >> 3) * 4096 + (g * 16 + 4 * (l15 & 3) + 2 * (jr & 1)) * 16, 0u, 0u};
  const unsigned own = own_cpy(cls, h >> 3);
  unsigned ima = lb + lane * 16;
  pin_v(ima);
  ds_flag(fw, 0u);
  ds_flag(fw + 16, 0u);
  auto poison = [&]() {
#pragma unroll
    for (int f = 0; f < 5; ++f) *(lds_u32x4*)(lds + QIM_OFF + f * 1024 + lane * 16) = (u32x4){~0u, ~0u, ~0u, ~0u};
    asm volatile("s_waitcnt lgkmcnt(0)" ::: "memory");
  };
  if (ROLE == 0) poison();

  int fl = kv0;
  u32x4 af0[3];
#pragma unroll
  for (int f = 0; f < 3; ++f) {
    const int u = (f == 2 ? 8 : f * 4 + ROLE) * 64 + lane;
    af0[f] = *(const u32x4*)(a.mem0 + (u & 7) * DM + (u >> 3) * 8);
  }
  auto k_dma = [&](int sg, int half) {
#pragma unroll
    for (int f = 0; f < 4; ++f)
      asm volatile("s_mov_b32 m0, %2\n\ts_nop 0\n\tbuffer_load_dwordx4 %0, %1, %3 offen offset:%4 sc1 lds"
                   :: "v"(kfo), "s"(D), "s"(lb + KX_OFF + (sg & 1) * 8192 + half * 4096), "s"((unsigned)(sg & RMASK) * 262144u + half * 4096), "n"(f * 1024) : "memory");
  };
  u32x4 qq[4], F[4];
#pragma unroll
  for (int f = 0; f < 4; ++f) { qq[f] = af0[0]; F[f] = af0[0]; }
  f32x4 T0 = {0.f, 0.f, 0.f, 0.f}, T1 = T0;
  float tmx = 0.f, tsx = 0.f;
  auto q_ld = [&](u32x4 (&qx)[4], int sg) {
#pragma unroll
    for (int f = 0; f < 4; ++f)
      asm volatile("buffer_load_dwordx4 %0, %1, %2, %3 offen offset:%4 sc1"
                   : "+v"(qx[f]) : "v"(kfo), "s"(D), "s"((unsigned)sg * 262144u + (OFF_QF - OFF_KF) + QT * 4096), "n"(f * 1024) : "memory");
  };
  auto t_part = [&](u32x4 (&qx)[4], int par, auto NC) {
    asm volatile("s_waitcnt vmcnt(%4)" : "+v"(qx[0]), "+v"(qx[1]), "+v"(qx[2]), "+v"(qx[3]) : "n"(decltype(NC)::value) : "memory");
    T0 = (f32x4){0.f, 0.f, 0.f, 0.f}; T1 = T0;
#pragma unroll
    for (int ks = 0; ks < 4; ++ks) {
      T0 = mfma(*(lds_u32x4*)(lds + KX_OFF + par * 8192 + ks * 1024 + lane * 16), qx[ks], T0);
      if (QT) T1 = mfma(*(lds_u32x4*)(lds + KX_OFF + par * 8192 + 4096 + ks * 1024 + lane * 16), qx[ks], T1);
    }
    pin_v(T0); pin_v(T1); PAD();
    px_mask(T0, T1, QT, lane);
    pin_v(T0); pin_v(T1); PAD();
    px_seg(T0, T1, tmx, tsx);
    pin_v(T0); pin_v(T1); pin_v(tmx); pin_v(tsx); PAD();
  };
  if (ROLE == 3) {
    const bool ok = wait_flag_c<8>(R, kpf, 0, fl);
    k_dma(0, 0); k_dma(0, 1);
    if (!ok) return;
  }
  VMW(0);

  unsigned iso_acc = 0, bud = SPINCAP, tm = 0, rcnt = 0;
  unsigned cv = 0;
  const unsigned cwo = FL_AN + (h & 15) * 128;
  EVDECL; CKDECL;
  auto k_get = [&](u32x4 (&F)[4], int s, bool parts) {
#pragma nounroll
    for (; bud != 0; --bud) {
      CBAR();
      if (__all((int)*(lds_u32*)frd > s)) break;
      __builtin_amdgcn_s_sleep(1);
    }
    CBAR();
#pragma unroll
    for (int f = 0; f < 4; ++f) { u32x2 lo = trd(krd + f * 512), hi = trd(krd + f * 512 + 256); F[f] = (u32x4){lo[0], lo[1], hi[0], hi[1]}; }
    if (!parts) return;
    const u32x2 p0 = trd(krd + 2048), p1 = trd(krd + 2304);
    F[1][2] = add3bf(F[1][2], p0[0], p1[0]); F[1][3] = add3bf(F[1][3], p0[1], p1[1]);
  };
  auto step = [&](auto PHC, int s) {
    constexpr int PH = decltype(PHC)::value & 1, FIRST = (decltype(PHC)::value >> 1) & 1, LAST = decltype(PHC)::value >> 2;
    constexpr int JC = ROLE == 0 ? 3 : (ROLE == 1 ? 6 : 8);
    u64 e0 = 0, e1 = 0, e3 = 0, e4 = 0; unsigned nr0 = 0;
    u32x4 af[9];
    f32x4 q0, q1, k0, k1, k2;
    PADS(200 + ROLE * 8 + decltype(PHC)::value);
    unsigned f3 = s + 1;
    pin_v(f3);
    constexpr int RD = ROLE == 3 && !LAST;
    f32x16 T;
    u32x16 x0, x1;
    if (RD) t_zero(k0, T); else if (!LAST) k_zero(k0, k1, k2);
    const u32x8 (&wf)[2][9] = LAST ? wk : wq;
#define MQ(t, j) do { smo<t, (j) < 8>(q##t, af[j], wf[t][j], idx); SB(); } while (0)
    lds_char* f8 = lds + F8_OFF + lane * 16;
    bud = __builtin_amdgcn_readfirstlane(bud);
    asm volatile("" : "+s"(bud));
    unsigned nt = 0; int rq = 0;
    tm = __builtin_amdgcn_readfirstlane(tm); rcnt = __builtin_amdgcn_readfirstlane(rcnt);
    asm volatile("" : "+s"(tm), "+s"(rcnt));
    if (RD) asm volatile("" : "+{v[112:127]}"(cst));
    unsigned p0 = (s & 7) * SLOT_REAL, p1 = p0 + CPY1, p2 = p0 ^ CPY2, p3 = p2 + CPY1;
    unsigned ms = (p2 + own) ^ ((s & 4) ? 0 : CPY1);
    u32x8 sc = {p0, p1, p2, p3, 0x3e0293eeu, YMAX, 0u, 0u};
    if (RD) asm volatile("" : "+{s[40:47]}"(sc));
    constexpr int NX = ROLE == 3 || LAST ? 0 : (ROLE == 0 ? 8 : 4);
    constexpr int ID = FIRST ? -1 : ROLE * 8 + decltype(PHC)::value;
    auto head = [&]() {
    v_zero(q0, q1);
    if (!FIRST) {
      int r = 0;
      if (ROLE == 1) r = spin_late(tm, 0);
      rq = r;
      mem_issue2<ROLE == F8W ? 3 : 2>(D, m0, m1, m2, ms, lw0, lw0 + 4096, lb + F8_OFF, e3);
      if (ROLE == 1) { move_on(tm, r); SB(); }
      PAD();
      if (RD) { kx_get<PH>(cst, x0, x1); asm volatile("s_waitcnt lgkmcnt(0)" ::: "memory"); }
      if (ROLE == 3) asm volatile("s_waitcnt vmcnt(%0)" :: "n"(TWO ? 1 + (F8W == 3) : 0) : "memory");
      else if (nt != 0) VMW(0);
      else {
      __builtin_amdgcn_s_sleep(XSLP);
      if (!LAST && (ROLE == 1 || ROLE == 2)) k_dma(s + 1, ROLE - 1);
      if (ROLE == 0 && !LAST) {
#pragma unroll
        for (int ks = 0; ks < 4; ++ks) st128(F[ks], R, kmo + ks * 1024, (unsigned)(s - 1) * 131072u);
      }
      if (ROLE == 0 && !LAST) {
        st128((u32x4){~0u, ~0u, ~0u, ~0u}, R, pcl, (unsigned)((s + 2) & 3) * SLOT_REAL);
        st128((u32x4){~0u, ~0u, ~0u, ~0u}, R, pcl, (unsigned)((s + 2) & 3) * SLOT_REAL + CPY1);
        st128((u32x4){~0u, ~0u, ~0u, ~0u}, R, pcl, (unsigned)((s + 2) & 3) * SLOT_REAL + CPY2);
        st128((u32x4){~0u, ~0u, ~0u, ~0u}, R, pcl, (unsigned)((s + 2) & 3) * SLOT_REAL + CPY2 + CPY1);
      }
      PAD();
      CBAR();
      asm volatile("s_waitcnt vmcnt(%0)" :: "n"(NX + (TWO ? 1 + (ROLE == F8W) : 0)) : "memory");
      }
    } else {
      *(lds_u32x4*)(img + r0) = af0[0]; *(lds_u32x4*)(img + r1) = af0[1]; *(lds_u32x4*)f8 = af0[2];
      asm volatile("s_waitcnt lgkmcnt(0)" ::: "memory");
    }
    SB();
    HEAD(MQ, NX, 0);
    if (s == 0) RT(3);
    if (s == 1) RT(4);
    if (s == 8) RT(5);
    if (s == 60) RT(6);
    ISOQ(1);
    q_mid<ID>(af, wf[1], idx, LAST ? kw : qw, q0, q1); SB();
    };
    if (!FIRST) {
      tm -= GRID + XT; nt = ~0u;
      try_top<ID>(nt, ms, tm, rcnt, ima); SB();
      head();
      try_end(ima, nt, ms, tm, rcnt, m0, m1, m2, lw0, lw0 + 4096, lb + F8_OFF, D); SB();
    } else
    {
      if (FIRST && RD) { kx_get<PH>(cst, x0, x1); asm volatile("s_waitcnt lgkmcnt(0)" ::: "memory"); }
      head();
    }
    ISOQ(2);
    EV(e0);
    if (LAST) k_last(kw, q1, fw, f3);
    if (ROLE < 3) {
      if (!LAST) {
        float t;
        k_one(af, wk[0], wp, idx, qw, q1, k0, k2, t); SB();
        ISOQ(4);
        if (ROLE == 1 && FIRST) { st32(f3, R, wpo, 0); SB(); }
        k_end<ROLE == 2 ? 256 : 2048, JC>(af, wk[1], wp[2], idx, k0, k1, k2, t, kw, fw, f3); SB();
      }
      ISOQ(6);
      PAD();
      if (ROLE == 1 && !LAST) {
        nr0 = nt != 0;
        if (!FIRST) next_time(tm, s);
      }
      if (ROLE == 0 && !LAST) {
        __builtin_amdgcn_s_sleep(TSLP);
        k_get(F, s, true);
        ds_flag(lb + FLG_OFF + 20, f3);
        pin_v(F[0]); pin_v(F[1]); pin_v(F[2]); pin_v(F[3]);
        VMW(0);
        st32((unsigned)(s - 1), R, kmf, 0);
        if (!PH && !FIRST && UNLIKELY(s == NSEG - 2)) {
#pragma unroll
          for (int ks = 0; ks < 4; ++ks) st128(F[ks], R, kmo + ks * 1024, (unsigned)s * 131072u);
          VMW(0);
          st32((unsigned)s, R, kmf, 0);
        }
      }
      if (ROLE == 2 && !LAST) {
        if (FIRST) { gate_wait(R, kpf, 2, fl, bud); k_dma(1, 0); k_dma(1, 1); }
        gate_wait(R, kpf, min(2 * s + 4 + (s >= NSEG - 4), 2 * NSEG - 1), fl, bud);
        fl = (int)ld32(R, kpf, 0);
        VMW(0);
        pin_v(fl);
      }
      if ((ROLE == 0 || ROLE == 2) && !PH && !FIRST && UNLIKELY(s == NSEG - 2)) {
        k_dma(s + 1, 0);
        q_ld(qq, s + 1);
        t_part(qq, 1, std::integral_constant<int, 0>());
      }
      if ((ROLE == 0 || ROLE == 2) && LAST) {
        f32x4 G = {0.f, 0.f, 0.f, 0.f};
        u32x4 px; unsigned pw;
        k_get(F, s, false);
#pragma unroll
        for (int ks = 0; ks < 4; ++ks) G = mfma(F[ks], qq[ks], G);
        px_fin(G, T0, T1, tmx, tsx, px, pw, lane);
        st128(px, R, pxs, (unsigned)s * 81920u); st32(pw, R, pws, (unsigned)s * 81920u);
        VMW(0);
        st32((unsigned)s, R, lane == 0 ? (unsigned)(FL_PXLAST + h * 256 + QT * 128) : OOB, 0);
      }
      CBAR();
      if (ROLE == 0) CKS();
    } else if (!LAST) {
      u32x8 Q;
      s_first(af, wk[1], idx, cst, x0, x1, q1, k0, T, Q); SB();
      el_phase(D, af, wk[1], idx, cst, x0, x1, Q, k0, T, bud, e1, e4, nr0, sc, f3);
      ds_flag(fw + 4, f3);
      EVPUT(2, e1); EVPUT(4, e4);
    }
    EVPUT(0, e0);
    EVPUTI(1, nr0); if (!FIRST) EVPUT(3, e3);
    if (ROLE == 1 && !LAST) {
#pragma nounroll
      for (; bud != 0; --bud) {
        CBAR();
        if (__all((int)*(lds_u32*)(lds + FLG_OFF + 16 + (lane & 1) * 4) > s)) break;
        __builtin_amdgcn_s_sleep(1);
      }
      poison();
      VMW(0);
      if (FIRST) { tm = get_t1(R, cwo, bud); asm volatile("" : "+s"(tm)); }
    }
    if (PH && !LAST && UNLIKELY(s == WLAST)) WALK(200 + ROLE * 8 + 5);
    PADE();
#undef MQ
  };
  step(std::integral_constant<int, 2>(), 0);
#pragma nounroll
  for (int s = 1; s < NSEG - 1; s += 2) {
    step(std::integral_constant<int, 1>(), s);
    step(std::integral_constant<int, 0>(), s + 1);
  }
  step(std::integral_constant<int, 5>(), NSEG - 1);
  EVDUMP();
  if (ROLE == 0) CKDUMP();
  };
  if (hw == 0) run(std::integral_constant<int, 3>());
  else if (hw == 1) run(std::integral_constant<int, 2>());
  else if (hw == 2) run(std::integral_constant<int, 1>());
  else run(std::integral_constant<int, 0>());
  RT(7);
  SV(15, cls);
}

#define VIM_OFF 8192
#define NOP(n) do { asm volatile("s_nop " #n); SB(); } while (0)
#define cVW "v112"
#define cRA "v113"
#define cRB "v114"
#define cHA "v115"
#define cHB "v116"
#define cVF "v117"
#define cVR "v118"
#define cIY "v119"
#define YP(p, c) "v_smfmac_f32_16x16x64_bf16 v[148+4*" #p ":151+4*" #p "], v[168+4*" #c "+4*(" #c ">>1):171+4*" #c "+4*(" #c ">>1)], %[w" #p #c "], " cIY "\n\t"
#define Y8(k) "v_smfmac_f32_16x16x64_bf16 v[164:167], v[176+12*" #k ":179+12*" #k "], %[w8" #k "], " cIY "\n\t"
#define HA(d, b) "v_add_f32 v" #d ", v[" b "], v[" b "+2]\n\t"
#define HB(d, b) "v_add_f32 v" #d ", v[" b "+1], v[" b "+3]\n\t"
#define YC(p, e) "v_cvt_pk_bf16_f32 v[192+2*" #p "+" #e "], v[148+4*" #p "+2*" #e "], v[149+4*" #p "+2*" #e "]\n\t"
#define YS(p) ".if %[st]\n\tbuffer_store_dwordx2 v[192+2*" #p ":193+2*" #p "], v[120+" #p "], %[D], %[ys] offen sc1\n\t.endif\n\t"
#define S16(a, b) "v_permlane16_swap_b32 v" #a ", v" #b "\n\t"
#define Z2(r) "v_mov_b64 v[" #r ":" #r "+1], 0\n\tv_mov_b64 v[" #r "+2:" #r "+3], 0\n\t"
#define YZERO Z2(148) Z2(152) Z2(156) Z2(160) Z2(164)
#define VR0 TRD(168, cRA, 0) TRD(170, cRB, 0)
#define VR1 TRD(172, cRA, 512) TRD(174, cRB, 512) TRD(176, cHA, 0) TRD(178, cHB, 0)
__device__ __forceinline__ void y_zero(f32x16& ya, f32x4& yc) {
  asm volatile(YZERO : "={v[148:163]}"(ya), "={v[164:167]}"(yc));
}
#define HV 0
#define VO 1
#define VR 1
#define YEO
#define YEN
template <int ID>
__device__ __forceinline__ void v_end(const u32x4 (&af)[9], const u32x8 (&w)[9], int ix, u32x16 cst, f32x4 v0, f32x4& v1, u32x16& A,
                                      const u32x8 (&wo)[4][4], f32x16& ya, u64& e1) {
#define VH0 HA(201, "140") HB(202, "140") CVX(203, 201, 202)
#define VH1 HA(201, "144") HB(202, "144") CVX(203, 201, 202)
#define VW0 "ds_write_b32 " cVW ", v203\n\t"
#define VW1 "ds_write_b32 " cVW ", v203 offset:1024\n\t"
#define VCM "v_cmp_u_f32 vcc, v201, v202\n\t"
  asm volatile(
               Q1(3) Q1(4) VH0 Q1(5) VW0 Q1(6) "s_barrier\n\t" VR0 Q1(7) TRD(172, cRA, 512) TRD(174, cRB, 512) Q1(8) TRD(176, cHA, 0) TRD(178, cHB, 0) VCM
               LGK(4) YP(0, 0) IS(16) VH1 YP(1, 0) VW1 TRYB
               : "+{v[144:147]}"(v1), "=&{v[168:183]}"(A), "+{v[148:163]}"(ya) YEO
               : "{v[140:143]}"(v0), [a2] "v"(af[2]), [a3] "v"(af[3]), [a4] "v"(af[4]), [a5] "v"(af[5]), [a6] "v"(af[6]), [a7] "v"(af[7]), [a8] "v"(af[8]),
                 [w2] "a"(w[2]), [w3] "a"(w[3]), [w4] "a"(w[4]), [w5] "a"(w[5]), [w6] "a"(w[6]), [w7] "a"(w[7]), [w8] "v"(w[8]), [ix] "v"(ix), "{v[112:127]}"(cst), [id] "n"(ID),
                 [w00] "a"(wo[0][0]), [w10] "a"(wo[1][0])
               : "memory", "v201", "v202", "v203", TRYC);
}
template <int ST>
__device__ __forceinline__ void y_all(const u32x8 (&wo)[4][4], const u32x8 (&w8)[2], u32x16 cst, f32x16 ya, f32x4 yc, u32x16 A, i32x4 D,
                                      unsigned ys, unsigned fv, u32x8& o, unsigned& o8, u64& e1, unsigned& nr) {
#define RH1 TRD(188, cHA, 1024) TRD(190, cHB, 1024)
#define RQ2 TRD(180, cRA, 1024) TRD(182, cRB, 1024)
#define RQ3 TRD(184, cRA, 1536) TRD(186, cRB, 1536)
#define VFL "ds_write_b32 " cVF ", %[fv]\n\t"
#define YK0 YP(2, 0) YP(3, 0) RH1 LGK(3) YP(0, 1) RQ2 Y8(0) RQ3 YP(1, 1) VFL IS(17)
#define YK1    LGK(5) Y8(1) IS(18) YP(2, 1) YP(3, 1) S16(164, 166) LGK(3) YP(0, 2) S16(165, 167) LGK(1) YP(0, 3) HA(201, "164") YP(1, 2) HB(202, "164") CVX(200, 201, 202) \
               YP(1, 3) ".if %[st]\n\tbuffer_store_dword v200, v124, %[D], %[ys] offen sc1\n\t.endif\n\t" YC(0, 0) YP(2, 2) YC(0, 1) YP(2, 3) YS(0) YC(1, 0)   \
               YP(3, 2) YC(1, 1) YP(3, 3) IS(19) YS(1) YC(2, 0) YC(2, 1) YS(2) "s_nop 5-2*%[st]\n\t" YC(3, 0) YC(3, 1) YS(3) IS(20) EVA(1)
  asm volatile(YK0 YK1
               "v_cmp_u_f32 vcc, v148, v150\n\tv_cmp_u_f32 s[46:47], v201, v202\n\ts_or_b64 vcc, vcc, s[46:47]\n\ts_cbranch_scc1 19f\n"
               "13:\n\t.subsection 1\n"
               "19:\n\tds_read_b32 v204, " cVR "\n\t" LGK(0) "v_cmp_gt_i32 vcc, %[fv], v204\n\ts_cbranch_vccnz 19b\n\t"
               CN(100) VR0 VR1 RH1 RQ2 RQ3 YZERO LGK(0) YP(0, 0) YP(1, 0) YP(2, 0) YP(3, 0) YP(0, 1) Y8(0) YP(1, 1) YK1 "s_branch 13b\n\t.subsection 0"
               : "=&{v[192:199]}"(o), "=&{v200}"(o8) YEO YEN
               : [w00] "a"(wo[0][0]), [w01] "a"(wo[0][1]), [w02] "a"(wo[0][2]), [w03] "a"(wo[0][3]), [w10] "a"(wo[1][0]), [w11] "a"(wo[1][1]),
                 [w12] "a"(wo[1][2]), [w13] "a"(wo[1][3]), [w20] "a"(wo[2][0]), [w21] "a"(wo[2][1]), [w22] "a"(wo[2][2]), [w23] "a"(wo[2][3]),
                 [w30] "a"(wo[3][0]), [w31] "a"(wo[3][1]), [w32] "a"(wo[3][2]), [w33] "a"(wo[3][3]), [w80] "v"(w8[0]), [w81] "v"(w8[1]),
                 "{v[112:127]}"(cst), "{v[148:163]}"(ya), "{v[164:167]}"(yc), "{v[168:183]}"(A),
                 [D] "s"(D), [ys] "s"(ys), [fv] "v"(fv), [st] "n"(ST)
               : "memory", "vcc", "scc", "s46", "s47", "v184", "v185", "v186", "v187", "v188", "v189", "v190", "v191", "v201", "v202", "v204");
}
__device__ __forceinline__ void role_vcu(const Args& a, rsrc_t R, const Go& go, lds_char* lds, int h, int tid, int wave, int lane) {
  const int l15 = lane & 15, g = lane >> 4;
  u32x8 wv[2][9], wo[4][4], w8[2];
  int cv0 = -1;
  {
    u32x4 st[36], so[36];
#pragma unroll
    for (int k = 0; k < 36; ++k) so[k] = gld<NTM & 2>(a.Wo + (size_t)h * 128 * DM + (tid + 256 * k) * 8);
    if (wave == 0) WK3(232); else if (wave == 1) WK3(240); else if (wave == 2) WK3(248); else WK3(256);
    const int q = l15 >> 2;
    const int lrow = 16 * (g >> 1) + 4 * (g & 1) + 8 * (q >> 1) + (q & 1), lrow2 = 8 * (g >> 1) + 4 * (g & 1) + (q & 1), up2 = (g & 1) ? 12 : 20;
    auto wrd = [&](lds_char* p, int rs, int up) {
      u32x2 v0 = trd(p), v1 = trd(p + 2 * rs), v2 = trd(p + up * rs), v3 = trd(p + (up + 2) * rs);
      return (u32x8){v0[0], v0[1], v1[0], v1[1], v2[0], v2[1], v3[0], v3[1]};
    };
#pragma unroll
    for (int ph = 0; ph < 4; ++ph) {
      if (ph < 2) {
#pragma unroll
        for (int k = 0; k < 18; ++k) { const int u = tid + 256 * k; *(lds_u32x4*)(lds + (u / 72) * 1184 + (u % 72) * 16) = so[ph * 18 + k]; }
#pragma unroll
        for (int k = ph * 18; k < ph * 18 + 18; ++k) { const int u = tid + 256 * k; st[k] = gld<NTM & 2>(a.Wv + (size_t)(u >> 4) * HD + h * 128 + (u & 15) * 8); }
      } else {
#pragma unroll
        for (int k = (ph - 2) * 16; k < (ph == 2 ? 16 : 36); ++k) { const int u = tid + 256 * k; *(lds_u32x4*)(lds + ((u >> 4) - (ph - 2) * 256) * 288 + (u & 15) * 16) = st[k]; }
      }
      if (ph == 3 && go_wait(R, go, lane)) { cv0 = cls_look(R, lane); CBAR(); }
      BAR();
      if (ph < 2) {
#pragma unroll
        for (int k = 0; k < 8; ++k) wo[k >> 1][2 * ph + (k & 1)] = wrd(lds + (lrow2 + 32 * (k & 1)) * 1184 + (wave * 9 + (k & 6) + (q >> 1)) * 32 + (l15 & 3) * 8, 1184, up2);
        w8[ph] = wrd(lds + (lrow2 + 32 * (q >> 1)) * 1184 + (wave * 9 + 8) * 32 + (l15 & 3) * 8, 1184, up2);
        SB();
#pragma unroll
        for (int k = 0; k < 8; ++k) pin_a(wo[k >> 1][2 * ph + (k & 1)]);
        pin_v(w8[ph]);
      } else {
#pragma unroll
        for (int k = 0; k < (ph == 2 ? 8 : 10); ++k) {
          const int i = k >> 1, t = k & 1;
          wv[t][(ph - 2) * 4 + i] = wrd(lds + ((i < 4 ? (i + wave) & 3 : 4) * 64 + lrow) * 288 + (64 * t + 16 * wave) * 2 + (l15 & 3) * 8, 288, 32);
        }
        SB();
#pragma unroll
        for (int k = 0; k < (ph == 2 ? 8 : 10); ++k) { if (k < 8) pin_a(wv[k & 1][(ph - 2) * 4 + (k >> 1)]); else pin_v(wv[k & 1][8]); }
      }
      BAR();
    }
  }
  RT(1);
  const int cls = get_cls(R, lane, cv0);
  if (cls < 0) return;
  RT(2);
  auto run = [&](auto ROLEC) {
  constexpr int ROLE = decltype(ROLEC)::value;
  lds_char* img = lds;
  const unsigned fo = (unsigned)(cls & 1) * 8192u;
  const int r0 = ROLE * 1024 + lane * 16, r1 = r0 + 4096, r2 = 8192 + lane * 16;
  const unsigned m0 = nearoff(r0) + fo, m1 = nearoff(r1) + fo, m2 = nearoff(r2) + fo;
  const unsigned lb = (unsigned)(uintptr_t)lds, lw0 = lb + ROLE * 1024;
  const i32x4 D = mkdesc(a.wb);
  const int ar = g * 256 + ((l15 >> 1) & 1) * 128 + (2 * (l15 >> 2) + (l15 & 1)) * 16;
  lds_char* ard0 = img + ROLE * 1024 + ar;
  lds_char* ard1 = img + ((ROLE + 1) & 3) * 1024 + ar;
  lds_char* ard2 = img + ((ROLE + 2) & 3) * 1024 + ar;
  lds_char* ard3 = img + ((ROLE + 3) & 3) * 1024 + ar;
  const int wz = h >> 3, tq = (h >> 1) & 3, hp = h & 1;
  const int gh = ((tq & 1) << 1) | (tq >> 1);
  unsigned yr[5], yz[5];
#pragma unroll
  for (int q = 0; q < 5; ++q) {
    const int m = ROLE * 9 + q * 2 + (q < 4 ? g & 1 : 0), o = l15 * 16 + (q < 4 ? (g >> 1) * 8 : g * 4);
    yr[q] = ywin(cls, m) + (unsigned)((wz * 2 + hp) * 1024 + tq * 256 + o);
    yz[q] = OFF_ZF + (unsigned)(m * 40960 + (32 + wz * 2 + hp) * 1024 + gh * 256 + o);
  }
  lds_char* vw = lds + VIM_OFF + (ROLE * 16 + l15) * 16 + g * 4;
  const unsigned ra = lb + VIM_OFF + (8 * g + 4 * (g >> 1) + (l15 >> 2)) * 16 + (l15 & 2) * 4, rh = ra + (l15 & 1) * 512;
  u32x16 cst = {(unsigned)(uintptr_t)vw, ra, ra ^ 64, rh, rh ^ 64, lb + FLG_OFF + ROLE * 4, lb + FLG_OFF + (lane & 3) * 4, (lane & 4) ? 0xEEEEu : 0x4444u,
                yr[0], yr[1], yr[2], yr[3], yr[4], 0u, 0u, 0u};
  ds_flag(lb + FLG_OFF + ROLE * 4, 0u);
  const unsigned own = own_cpy(cls, h >> 3);
  unsigned ima = lb + lane * 16;
  pin_v(ima);
  const int idx = (lane & 2) ? 0xEEEE : 0x4444;
  u32x4 af0[3];
#pragma unroll
  for (int f = 0; f < 3; ++f) {
    const int u = (f == 2 ? 8 : f * 4 + ROLE) * 64 + lane;
    af0[f] = *(const u32x4*)(a.mem0 + (u & 7) * DM + (u >> 3) * 8);
  }
  unsigned tm = 0, rcnt = 0, cv = 0, iso_acc = 0, o8 = 0, bud = SPINCAP;
  u32x8 o = {0u, 0u, 0u, 0u, 0u, 0u, 0u, 0u};
  auto y_copy = [&](int s) {
#pragma unroll
    for (int q = 0; q < 4; ++q) st64((u32x2){o[2 * q], o[2 * q + 1]}, R, yz[q], (unsigned)s * ZS);
    st32(o8, R, yz[4], (unsigned)s * ZS);
  };
  auto poison = [&]() { asm volatile("v_mov_b32 v203, -1\n\tds_write2st64_b32 " cVW ", v203, v203 offset1:4" :: "{v[112:127]}"(cst) : "memory", "v203"); };
  const unsigned cwo = FL_AN + ((h + 8) & 15) * 128;
  EVDECL;
  auto step = [&](auto PHC, int s) {
    constexpr int PH = decltype(PHC)::value & 1, FIRST = (decltype(PHC)::value >> 1) & 1, LAST = decltype(PHC)::value >> 2;
    u64 e0 = 0, e1 = 0, e3 = 0, e4 = 0, e5 = 0, e6 = 0;
    u32x4 af[9];
    f32x4 v0, v1, yc;
    f32x16 ya;
    u32x16 A;
    unsigned nsl = 0;
    PADS(232 + ROLE * 8 + decltype(PHC)::value);
    unsigned f3 = s + 1;
    pin_v(f3);
    asm volatile("" : "+{v[112:127]}"(cst));
    PAD();
    unsigned nt = 0;
    tm = __builtin_amdgcn_readfirstlane(tm); rcnt = __builtin_amdgcn_readfirstlane(rcnt);
    asm volatile("" : "+s"(tm), "+s"(rcnt));
#define MV(t, j) do { smo<t, (j) < 8>(v##t, af[j], wv[t][j], idx); SB(); } while (0)
    unsigned ms = ((((s & 7) * SLOT_REAL) ^ CPY2) + own) ^ ((s & 4) ? 0 : CPY1);
    constexpr int ID = FIRST ? -1 : 32 + ROLE * 8 + decltype(PHC)::value;
    auto head = [&]() {
      y_zero(ya, yc);
      v_zero(v0, v1);
      if (!FIRST) {
        int r = 0;
        if (ROLE == 1) r = spin_late(tm, DVQ);
        mem_issue2<ROLE == F8W ? 3 : 2>(D, m0, m1, m2, ms, lw0, lw0 + 4096, lb + F8_OFF, e3);
        if (ROLE == 1) { move_on(tm, r); SB(); }
        poison();
        PAD();
        if (LAST || nt != 0) VMW(0);
        else {
          __builtin_amdgcn_s_sleep(XSLP);
          y_copy(s - 1);
          CBAR();
          asm volatile("s_waitcnt vmcnt(%0)" :: "n"(5 + (TWO ? 1 + (ROLE == F8W) : 0)) : "memory");
        }
      } else {
        *(lds_u32x4*)(img + r0) = af0[0]; *(lds_u32x4*)(img + r1) = af0[1]; *(lds_u32x4*)(lds + F8_OFF + lane * 16) = af0[2];
        poison();
        asm volatile("s_waitcnt lgkmcnt(0)" ::: "memory");
      }
      SB();
      EV(e6);
      HEAD(MV, 5, HV);
      ISOQ(11);
      v_end<ID>(af, wv[1], idx, cst, v0, v1, A, wo, ya, e0); SB();
    };
    if (!FIRST) {
      tm -= GRID + XT; nt = ~0u;
      try_top<ID>(nt, ms, tm, rcnt, ima); SB();
      head();
      try_end(ima, nt, ms, tm, rcnt, m0, m1, m2, lw0, lw0 + 4096, lb + F8_OFF, D); SB();
    } else
    head();
    if (LAST) { st32((unsigned)(s - 1), R, lane == 0 ? (unsigned)(FL_YPROG + h * 16 + ROLE * 4) : OOB, 0); SB(); }
    EV(e4); ISOQ(13);
    y_all<!LAST>(wo, w8, cst, ya, yc, A, D, (unsigned)(s & 1) * YSLOT_REAL, f3, o, o8, e5, nsl); SB();
    ISOQ(15);
    PAD();
#undef MV
    if (FIRST) {
      VMW(0); BAR();
      st32(0u, R, tid == 0 ? (unsigned)(FL_WP + (32 + h) * 32) : OOB, 0);
    }
    if (LAST || (!PH && !FIRST && UNLIKELY(s == NSEG - 2))) y_copy(s);
    CBAR();
    if (ROLE == 1 && !LAST) {
      if (FIRST) { tm = get_t1(R, cwo, bud); asm volatile("" : "+s"(tm)); }
      else
      next_time(tm, s);
    }
    if (PH && !LAST && UNLIKELY(s == WLAST)) WALK(232 + ROLE * 8 + 5);
    PADE();
    EVPUT(0, e0); EVPUT(6, e6); EVPUT(4, e4);
    EVPUTI(1, nt); EVPUT(2, e1); if (!FIRST) EVPUT(3, e3);
  };
  step(std::integral_constant<int, 2>(), 0);
#pragma nounroll
  for (int s = 1; s < NSEG - 1; s += 2) {
    step(std::integral_constant<int, 1>(), s);
    step(std::integral_constant<int, 0>(), s + 1);
  }
  step(std::integral_constant<int, 5>(), NSEG - 1);
  VMW(0);
  st32((unsigned)(NSEG - 1), R, lane == 0 ? (unsigned)(FL_YPROG + h * 16 + ROLE * 4) : OOB, 0);
  EVDUMP();
  };
  if (wave == 0) run(std::integral_constant<int, 0>());
  else if (wave == 1) run(std::integral_constant<int, 1>());
  else if (wave == 2) run(std::integral_constant<int, 2>());
  else run(std::integral_constant<int, 3>());
  RT(7);
}

#define NG 4
#define UL(k, o) "buffer_load_dwordx4 v[128+4*" #k ":128+4*" #k "+3], %[u" #o ", %[D], %[su] offen offset:(" #k "&3)*1024 sc1\n\t"
#define UL4(c, k0, k1, k2, k3, o) ".if " #k0 c "4*" XSTR(NG) "\n\t" UL(k0, o) UL(k1, o) UL(k2, o) UL(k3, o) ".endif\n\t"
#define ULD(c) UL4(c, 0, 1, 2, 3, 0]) UL4(c, 4, 5, 6, 7, 1]) UL4(c, 8, 9, 10, 11, 2]) UL4(c, 12, 13, 14, 15, 3])   \
               UL4(c, 16, 17, 18, 19, 4]) UL4(c, 20, 21, 22, 23, 5]) UL4(c, 24, 25, 26, 27, 6]) UL4(c, 28, 29, 30, 31, 7])
#define ULD0 ULD(" < ")
#define ULD1 ULD(" >= ")
#define TB 12
#define UM(T, lo, hi, n) ".if " #T " >= " lo " && " #T " < " hi "\n\t.if !(" #T " & 1)\n\ts_waitcnt vmcnt(" n "+28-2*" #T ")\n\t.endif\n\t"   \
                         "v_swap_b32 v[129+8*" #T "], v[132+8*" #T "]\n\tv_swap_b32 v[131+8*" #T "], v[134+8*" #T "]\n\t.endif\n\t"
#define UMS(...) UM(0, __VA_ARGS__) UM(1, __VA_ARGS__) UM(2, __VA_ARGS__) UM(3, __VA_ARGS__) UM(4, __VA_ARGS__) UM(5, __VA_ARGS__) UM(6, __VA_ARGS__) UM(7, __VA_ARGS__) \
                 UM(8, __VA_ARGS__) UM(9, __VA_ARGS__) UM(10, __VA_ARGS__) UM(11, __VA_ARGS__) UM(12, __VA_ARGS__) UM(13, __VA_ARGS__) UM(14, __VA_ARGS__) UM(15, __VA_ARGS__)
#define TIMES "s_and_b32 %[tt], %[lev], 255\n\ts_add_u32 %[tn], %[tq], %[tt]\n\ts_add_u32 %[tn], %[tn], %[t0]\n\t" \
              "s_lshr_b32 %[tt], %[lev], 8\n\ts_add_u32 %[tt], %[tt], %[tq]\n\t"
#define DZERO ".rept 32\n\tv_mov_b64 v[64+2*\\+:65+2*\\+], 0\n\t.endr\n\tv_mov_b64 v[60:61], 0\n\tv_mov_b64 v[62:63], 0\n\t"
#define YLD(k, o) "buffer_load_dwordx4 a[64+4*" #k ":64+4*" #k "+3], %[y" #o ", %[D], %[sy] offen offset:(" #k "&3)*1024 sc1\n\t"
#define YPO(k, o) "buffer_store_dwordx4 %[one], %[y" #o ", %[D], %[sy] offen offset:(" #k "&3)*1024 sc1\n\t"
#define ELD(T, o) "buffer_load_dwordx4 a[4*" #T ":4*" #T "+3], %[e" #o ", %[D], %[sq] offen offset:(" #T "&3)*1024 sc1\n\t"
#define PLD(G) "buffer_load_dwordx4 a[96+4*" #G ":96+4*" #G "+3], %[pm], %[D], %[sq] offen offset:" #G "*1024 sc1\n\t"
#define ALD(G) "buffer_load_dwordx4 a[112+4*" #G ":112+4*" #G "+3], %[al], %[D], %[sq] offen offset:" #G "*256 sc1\n\t"
#define VW(X, n) "s_waitcnt vmcnt(1+" n ")\n\t"
#define EM(T) "v_smfmac_f32_16x16x64_bf16 v[64+4*" T ":64+4*" T "+3], a[4*" T ":4*" T "+3], v[128+8*" T ":128+8*" T "+7], %[ie]\n\t"
#define WC0(T) "v_cvt_pk_bf16_f32 v[64+2*" #T "], v[64+4*" #T "], v[64+4*" #T "+1]\n\t"
#define WC1(T) "v_cvt_pk_bf16_f32 v[65+2*" #T "], v[64+4*" #T "+2], v[64+4*" #T "+3]\n\t"
#define WCV(T) WC0(T) WC1(T)
#define EM2_0(T, C, o) VW("0", "30-" #T) EM(#T) WC0(C) EM("(" #T "+1)") WC1(C)

#define PMM(X, G) VW(X, "7-" #G) "v_smfmac_f32_16x16x64_bf16 v[60:63], a[96+4*" #G ":96+4*" #G "+3], a[64+8*" #G ":64+8*" #G "+7], %[ip]\n\t"
#define AMM(X, G) VW(X, "3-" #G) "v_smfmac_f32_16x16x64_bf16 v[60:63], a[112+4*" #G ":112+4*" #G "+3], v[64+8*" #G ":64+8*" #G "+7], %[ia]\n\t"
#define TRY(X, EM2, a, b, c, d)                                                                                                       \
      SPIN(tt, a, b, c) MEV(1) MCY(0)                                                                                                \
      "buffer_load_dword v59, %[es], %[D], %[sq] offen sc1\n\t" ELD(0, 0]) ELD(1, 0]) ELD(2, 0]) ELD(3, 0]) ELD(4, 1]) ELD(5, 1]) ELD(6, 1]) ELD(7, 1])                                        \
      ELD(8, 2]) ELD(9, 2]) ELD(10, 2]) ELD(11, 2]) ELD(12, 3]) ELD(13, 3]) ELD(14, 3]) ELD(15, 3])                                  \
      YLD(0, 0]) YLD(1, 0]) YLD(2, 0]) YLD(3, 0]) YLD(4, 1]) YLD(5, 1]) YLD(6, 1]) YLD(7, 1])                                        \
      PLD(0) PLD(1) PLD(2) PLD(3) ALD(0) ALD(1) ALD(2) ALD(3) MCY(1) "buffer_load_dword %[ub], %[uq], %[D], 0 offen sc1\n\ts_cmp_lg_u32 %[sx], 0\n\ts_cbranch_scc1 77f\n\t" UMS(XSTR(TB), "16", "43") "s_mov_b32 %[sx], 1\n77:\n\t"                                                                 \
      VW("0", "32") "v_cmp_eq_u32 vcc, -1, v59\n\t" VW("0", "30") "s_cbranch_vccnz 61f\n62:\n\t" EM("0") MCY(2) EM("1")                                                                                           \
      EM2(2, 0, 0]) EM2(4, 1, 0]) EM2(6, 2, 0]) EM2(8, 3, 0]) EM2(10, 4, 1]) EM2(12, 5, 1]) EM2(14, 6, 1]) MCY(3)                           \
      "s_cmp_lt_i32 %[r], -" XSTR(LTOL) "\n\ts_cbranch_scc0 " d "f\n\t"                              \
      "63:\n\ts_add_u32 %[tq], %[tq], " XSTR(GRID) "\n\ts_add_u32 %[t0], %[t0], " XSTR(XT) "\n\ts_add_u32 %[tn], %[tn], " XSTR(GRID) "+" XSTR(XT) "\n\ts_add_u32 %[r], %[r], " XSTR(GRID) "\n\t"   \
      "s_cmp_lt_i32 %[r], -" XSTR(LTOL) "\n\ts_cbranch_scc1 63b\n\t" MCA(1000)                                                        \
      d ":\n\t" WCV(7) WCV(8) WCV(9) WCV(10) WCV(11) WCV(12) WCV(13) WCV(14) WCV(15)                                                 \
      PMM(X, 0) MCY(4) PMM(X, 1) PMM(X, 2) PMM(X, 3) AMM(X, 0) AMM(X, 1) AMM(X, 2) AMM(X, 3) MCY(5)                                  \
      PADN "s_nop 6\n\t"                                                                                                             \
      "v_pk_add_f32 v[60:61], v[60:61], v[62:63]\n\t"                                                                                \
      "v_cvt_pk_bf16_f32 v62, v60, v61\n\tv_cmp_u_f32 vcc, v60, v61\n\ts_nop 0\n\t"                                                  \
      "v_mov_b32_dpp v63, v62 quad_perm:[1,0,3,2] row_mask:0xf bank_mask:0xf\n\t"                                                    \
      "v_perm_b32 v61, v63, v62, %[sel]\n\ts_cbranch_vccnz 50f\n\t"
#define MEV(n)
#define MEVPUT
#define MCY(n)
#define MCYPUT
#define MCYO
#define MCN
#define MCA(k)
#define FME
#define FML
#define FMZ
#define VR8(p, b) p #b "0", p #b "1", p #b "2", p #b "3", p #b "4", p #b "5", p #b "6", p #b "7", p #b "8", p #b "9"
__device__ __forceinline__ void role_mix(const Args& a, rsrc_t R, const Go& go, int m, int wave, int lane) {
  if (wave >= 2) {
    unsigned t[4];
#pragma unroll
    for (int k = 0; k < 4; ++k) {
      const int u = ((m * 2 + wave - 2) * 4 + k) * 64 + lane;
      t[k] = ld32(R, u < 8192 ? (unsigned)u * 128u : (u < 12800 ? OFF_YR + (unsigned)(u - 8192) * 128u : (u < 17408 ? OFF_YR + YSLOT_REAL + (unsigned)(u - 12800) * 128u : OOB)), 0);
    }
    asm volatile("" :: "v"(t[0]), "v"(t[1]), "v"(t[2]), "v"(t[3]));
  }
  if (wave != 0 && (wave != 2 || m != 0)) return;
  const int l15 = lane & 15, g = lane >> 4;
  if (!go_wait(R, go, lane)) return;
  RT(3);
  if (wave == 2) {
    for (int k = 0; k < TSL; ++k) __builtin_amdgcn_s_sleep(32);
    wait_flag(R, FL_MRDY + min(lane, 35) * 4, 0);
    int c = -1;
    wait_flag_c<1>(R, FL_WP + lane * 32, 0, c);
    st32((rt_now() + T1D) & ~1u, R, lane < 16 ? (unsigned)(FL_AN + lane * 128) : OOB, 0);
    return;
  }
  for (int k = 0; k < CSL; ++k) __builtin_amdgcn_s_sleep(32);
  const int cls = get_cls(R, lane);
  if (cls < 0) return;
  RT(4);
  const unsigned fo = (unsigned)(cls & 1) * 8192u, y0 = ywin(cls, m) + lane * 16;
  const u32x4 one = {~0u, ~0u, ~0u, ~0u};
  const unsigned mc = (unsigned)nearoff(m * 256 + l15 * 16) + fo + (g & 1) * CPY1 + (g >> 1) * CPY2;
#pragma unroll
  for (int k = 0; k < 8; ++k) st128(one, R, y0 + k * 1024, YSLOT_REAL);
  st128(one, R, mc, SLOT_REAL);
  RT(1);
  unsigned uo[8], eo[4];
#pragma unroll
  for (int k = 0; k < 8; ++k) uo[k] = OFF_ZF + (unsigned)(m * 40960 + k * 4096 + lane * 16);
#pragma unroll
  for (int k = 0; k < 4; ++k) {
    eo[k] = (unsigned)nearoff(NEAR_E + k * 4096 + lane * 16) + fo;
  }
  const unsigned pm = (unsigned)nearoff(NEAR_PM + lane * 16) + fo;
  const int qi = 2 * (l15 >> 2) + (l15 & 1);
  const unsigned al = !(l15 & 2) && (g & 1) == (qi >> 2) ? (unsigned)nearoff(NEAR_AL + ((g >> 1) * 8 + qi) * 16) + fo : OOB;
  const unsigned mo = (unsigned)nearoff((2 * m + (l15 >> 3)) * 128 + (2 * g + (l15 & 1)) * 16 + ((l15 & 7) >> 1) * 4) + fo;
  const unsigned sm = lane < 36 ? (unsigned)nearoff(lane * 256) + fo : OOB, own = own_cpy(cls, m / 9);
  const unsigned an = FL_AN + (m & 15) * 128, wp = FL_WP + lane * 32;
  const unsigned ie = (lane & 4) ? 0xEEEE : 0x4444, ip = (lane & 2) ? 0xEEEE : 0x4444, ia = ie;
  const unsigned sel = (lane & 1) ? 0x03020706u : 0x05040100u;
  const i32x4 D = mkdesc(a.wb);
  const unsigned uf = FL_UPROG + (lane & 31) * 128 + (m / 9) * 4;
  for (unsigned spins = 0; spins < SPINCAP; ++spins) {
    CBAR();
    if (__all(ld32(R, uf, 0) != ~0u)) break;
    __builtin_amdgcn_s_sleep(16);
  }
  for (unsigned spins = 0; spins < SPINCAP; ++spins) {
    CBAR();
    if (__all(ld32(R, OFF_ZF + (unsigned)(m * 40960 + (lane & 31) * 1024 + 1020), 0) != ~0u)) break;
    __builtin_amdgcn_s_sleep(3);
  }
  RT(2);
  unsigned tq = 0, lev = PACE0, bud = SPINCAP, s = 0, su = 0, sy = 0, sp = 0, tt, sa, sb, sc, sn, tn, ln, ue, t0, sx, sq, cw, sv, ua = ~0u, ub, uq, nr = 0, cv = 0;
  int ufl = -1;
  unsigned ev0 = 0, ev1 = 0, ev2 = 0, ev3 = 0, ec[7] = {0, 0, 0, 0, 0, 0, 0};
  int r, k;
  asm volatile(
      "29:\n\t" ULD0 ULD1 "s_mov_b32 %[t0], 0\n\ts_mov_b32 %[sb], 0\n\ts_mov_b32 %[ue], 2*" XSTR(SLOT_REAL) "\n\ts_waitcnt vmcnt(0)\n\t"
      "30:\n\t" FMZ DZERO
      "s_add_u32 %[su], %[s], 1\n\ts_and_b32 %[su], %[su], " XSTR(RMASK) "\n\ts_mul_i32 %[su], %[su], 36*40960\n\t"
      "s_add_u32 %[sn], %[sp], " XSTR(SLOT_REAL) "\n\ts_and_b32 %[sn], %[sn], 7*" XSTR(SLOT_REAL) "\n\t"
      "s_lshr_b32 %[sq], %[sp], 5\n\ts_and_b32 %[sq], %[sq], " XSTR(CPY1) "\n\ts_xor_b32 %[sq], %[sq], %[own]\n\ts_add_u32 %[sq], %[sq], %[sp]\n\t"
      "s_movk_i32 %[ln], " XSTR(PACEC) "\n\ts_cmp_lt_u32 %[s], " XSTR(S2) "-1\n\ts_cmovk_i32 %[ln], " XSTR(PACEB) "\n\t"
      "s_cmp_lt_u32 %[s], " XSTR(S1) "-1\n\ts_cmovk_i32 %[ln], " XSTR(PACEA) "\n\ts_cmp_lt_u32 %[s], " XSTR(S0) "-1\n\ts_cmovk_i32 %[ln], " XSTR(PACE0) "\n\t"
      "s_add_u32 %[sc], %[sn], " XSTR(CPY1) "\n\ts_xor_b32 %[sb], %[sn], " XSTR(CPY2) "\n\ts_mov_b32 %[sx], 0\n\t" TIMES
      UMS("0", "2*" XSTR(NG), "8") "s_cmp_lg_u32 %[s], 0\n\t"
      "buffer_store_dwordx4 %[one], %[mc], %[D], %[ue] offen sc1\n\tv_max_i32 %[ufl], %[ufl], %[ua]\n\ts_cbranch_scc0 31f\n\t"
      "v_cmp_eq_u32 vcc, -1, %[sv]\n\ts_cbranch_vccnz 35f\n"
      "33:\n\tv_cmp_ge_i32 vcc, %[s], %[ufl]\n\tv_mov_b32 %[uq], " XSTR(OOB) "\n\tv_cndmask_b32 %[uq], %[uq], %[uf], vcc\n\t"
      UMS("2*" XSTR(NG), XSTR(TB), "9") MEV(3)
      "75:\n\t" TRY("0", EM2_0, "40", "41", "42", "43")
      "51:\n\tbuffer_store_dword v61, %[mo], %[D], %[sn] offen sc1\n\tbuffer_store_dword v61, %[mo], %[D], %[sc] offen sc1\n\ts_memrealtime vcc\n\t" MEV(2) MCY(6)
      "buffer_store_dword v61, %[mo], %[D], %[sb] offen sc1\n\ts_add_u32 %[sb], %[sb], " XSTR(CPY1) "\n\tbuffer_store_dword v61, %[mo], %[D], %[sb] offen sc1\n\t"
      "s_waitcnt vmcnt(4) lgkmcnt(0)\n\ts_add_u32 %[k], vcc_lo, " XSTR(MGAP) "\n"
      "97:\n\ts_sub_u32 %[r], %[tn], %[k]\n\ts_cmp_lt_i32 %[r], 0\n\ts_cbranch_scc0 98f\n\ts_add_u32 %[tn], %[tn], " XSTR(GRID) "+" XSTR(XT) "\n\ts_branch 97b\n"
      "98:\n\t" MEVPUT MCYPUT MCN
      "s_add_u32 %[s], %[s], 1\n\ts_cmp_ge_u32 %[s], 63\n\ts_cbranch_scc1 60f\n\t"
      "v_max_i32 %[ufl], %[ufl], %[ub]\n\tv_cmp_gt_i32 vcc, %[s], %[ufl]\n\ts_mov_b32 %[sx], 1\n\ts_cbranch_vccnz 100f\n\t"
      "s_mov_b32 %[sx], 0\n\t" ULD0 "100:\n\t"
      "s_cmp_lg_u32 %[s], 1\n\ts_cbranch_scc1 67f\n\t"
      "66:\n\tbuffer_load_dword %[cw], %[an], %[D], 0 offen sc1\n\ts_waitcnt vmcnt(0)\n\tv_readfirstlane_b32 %[tn], %[cw]\n\t"
      "s_cmp_lg_u32 %[tn], -1\n\ts_cbranch_scc1 67f\n\t" BUD("66b")
      "67:\n\t"
      "s_xor_b32 %[ue], %[sn], 2*" XSTR(SLOT_REAL) "\n\ts_and_b32 %[ue], %[ue], 3*" XSTR(SLOT_REAL) "\n\ts_mov_b32 %[sp], %[sn]\n\t"
      "s_mov_b32 %[tq], %[tn]\n\ts_mov_b32 %[t0], 0\n\ts_mov_b32 %[lev], %[ln]\n\t"
      "v_cmp_ge_i32 vcc, %[s], %[ufl]\n\tv_mov_b32 %[ua], " XSTR(OOB) "\n\tv_cndmask_b32 %[ua], %[ua], %[uf], vcc\n\t"
      "s_lshr_b32 %[sa], %[sn], 5\n\ts_and_b32 %[sa], %[sa], " XSTR(CPY1) "\n\ts_xor_b32 %[sa], %[sa], %[own]\n\ts_add_u32 %[sa], %[sa], %[sn]\n\t"
      SPIN(tq, "52", "53", "54") "buffer_load_dword %[sv], %[sm], %[D], %[sa] offen sc1\n\t"
      "buffer_load_dword %[ua], %[ua], %[D], 0 offen sc1\n\t"
      "s_cmp_eq_u32 %[sx], 0\n\ts_cbranch_scc1 101f\n\t"
      "55:\n\ts_waitcnt vmcnt(0)\n\tv_max_i32 %[ufl], %[ufl], %[ua]\n\tv_cmp_gt_i32 vcc, %[s], %[ufl]\n\ts_cbranch_vccz 57f\n\t" MCA(100000)
      "buffer_load_dword %[ua], %[uf], %[D], 0 offen sc1\n\t" BUD("55b")
      "57:\n\t" ULD0 "101:\n\t" ULD1 YPO(0, 0]) YPO(1, 0]) YPO(2, 0]) YPO(3, 0]) YPO(4, 1]) YPO(5, 1]) YPO(6, 1]) YPO(7, 1])
      "s_xor_b32 %[sy], %[sy], " XSTR(YSLOT_REAL) "\n\ts_branch 30b\n\t"
      "35:\n\t" MCA(100) BUD("36f") "s_branch 32f\n\t"
      "36:\n\ts_add_u32 %[tq], %[tq], " XSTR(GRID) "+" XSTR(XT) "\n\ts_xor_b32 %[sa], %[sa], %[flp]\n\t"
      SPIN(tq, "37", "38", "39") "buffer_load_dword %[sv], %[sm], %[D], %[sa] offen sc1\n\ts_waitcnt vmcnt(0)\n\t"
      "v_cmp_eq_u32 vcc, -1, %[sv]\n\ts_cbranch_vccnz 35b\n"
      "32:\n\t" TIMES "s_branch 33b\n"
      "31:\n\tv_mov_b32 %[cw], 0\n\tbuffer_store_dword %[cw], %[rdy], %[D], 0 offen sc1\n\t"
      "64:\n\tbuffer_load_dword %[cw], %[wp], %[D], 0 offen sc1\n\tbuffer_load_dword %[ufl], %[uf], %[D], 0 offen sc1\n\t"
      "s_waitcnt vmcnt(0)\n\tv_cmp_gt_i32 vcc, 0, %[cw]\n\ts_cbranch_vccz 65f\n\t"
      "s_sleep 1\n\t" BUD("64b")
      "65:\n\ts_memrealtime vcc\n\ts_waitcnt lgkmcnt(0)\n\ts_sub_u32 %[tq], vcc_lo, " XSTR(DE0) "-" XSTR(D0) "\n\ts_branch 32b\n\t"
      "59:\n\ts_waitcnt lgkmcnt(0)\n\ts_lshr_b32 %[tt], %[lev], 8\n\ts_sub_u32 %[k], vcc_lo, %[tt]\n\ts_sub_u32 %[k], %[k], " XSTR(RTOL) "\n\t"
      "34:\n\ts_add_u32 %[tq], %[tq], " XSTR(GRID) "\n\ts_add_u32 %[t0], %[t0], " XSTR(XT) "\n\ts_sub_u32 %[r], %[tq], %[k]\n\ts_cmp_lt_i32 %[r], 0\n\ts_cbranch_scc1 34b\n\t"
      TIMES "s_branch 75b\n\t"
      "58:\n\ts_cmp_lg_u32 %[s], 0\n\ts_cbranch_scc1 102f\n\ts_mov_b32 %[su], 0\n\ts_branch 29b\n102:\n\ts_memrealtime vcc\n\ts_xor_b32 %[sq], %[sq], %[flp]\n\t" DZERO "s_branch 59b\n\t"
      "50:\n\t" FML MCA(1) BUD("58b") "v_min_u32 v61, -2, v61\n\ts_branch 51b\n\t"
      "99:\n\ts_memrealtime vcc\n\ts_xor_b32 %[sq], %[sq], %[flp]\n\ts_branch 59b\n\t"
      "61:\n\t" FME MCA(1) BUD("99b") "s_branch 62b\n\t"
      "60:\n\ts_waitcnt vmcnt(0)"
      : [tq] "+&s"(tq), [lev] "+&s"(lev), [bud] "+&s"(bud), [s] "+&s"(s), [su] "+&s"(su), [sy] "+&s"(sy), [sp] "+&s"(sp), [tt] "=&s"(tt), [sa] "=&s"(sa),
        [sn] "=&s"(sn), [sb] "=&s"(sb), [sc] "=&s"(sc), [tn] "=&s"(tn), [ln] "=&s"(ln), [ue] "=&s"(ue), [t0] "=&s"(t0), [sx] "=&s"(sx), [sq] "=&s"(sq), [cw] "=&v"(cw), [sv] "=&v"(sv), [ua] "+&v"(ua), [ub] "=&v"(ub), [uq] "=&v"(uq), [ufl] "+&v"(ufl), [r] "=&s"(r), [k] "=&s"(k), [nr] "+&s"(nr), [cv] "+&v"(cv),
        [ev0] "+&v"(ev0), [ev1] "+&v"(ev1), [ev2] "+&v"(ev2), [ev3] "+&v"(ev3) MCYO
      : [D] "s"(D), [u0] "v"(uo[0]), [u1] "v"(uo[1]), [u2] "v"(uo[2]), [u3] "v"(uo[3]), [u4] "v"(uo[4]), [u5] "v"(uo[5]), [u6] "v"(uo[6]), [u7] "v"(uo[7]), [e0] "v"(eo[0]), [e1] "v"(eo[1]), [e2] "v"(eo[2]),
        [e3] "v"(eo[3]), [y0] "v"(y0), [y1] "v"(y0 + 4096), [pm] "v"(pm), [al] "v"(al), [mo] "v"(mo), [mc] "v"(mc), [an] "v"(an), [wp] "v"(wp),
        [ie] "v"(ie), [ip] "v"(ip), [ia] "v"(ia), [sel] "v"(sel), [one] "v"(one), [sm] "v"(sm), [uf] "v"(uf), [es] "v"(lane < 32 ? (unsigned)nearoff(NEAR_E + lane * 512 + (lane & 1) * (64 - 512)) + fo : OOB), [rdy] "v"(lane == 0 ? (unsigned)(FL_MRDY + m * 4) : OOB), [own] "s"(own), [flp] "s"(CPY2 + CPY1)
      : "memory", "vcc", "scc", "s68", "s69", "s70", "s71", "s72", "s73", "s74", "s75", "s76", "s77", "s78", "s79", "s80", "s81", "s84", "s85", "s86", "s87", "s88", "s89", "s90", "s91",
        "v59", VR8("v", 6), VR8("v", 7), VR8("v", 8), VR8("v", 9), VR8("v", 10), VR8("v", 11), VR8("v", 12), VR8("v", 13), VR8("v", 14), VR8("v", 15),
        VR8("v", 16), VR8("v", 17), VR8("v", 18), VR8("v", 19), VR8("v", 20), VR8("v", 21), VR8("v", 22), VR8("v", 23), VR8("v", 24),
        "v250", "v251", "v252", "v253", "v254", "v255",
        "a0", "a1", "a2", "a3", "a4", "a5", "a6", "a7", "a8", "a9", VR8("a", 1), VR8("a", 2), VR8("a", 3), VR8("a", 4), VR8("a", 5), VR8("a", 6),
        VR8("a", 7), VR8("a", 8), VR8("a", 9), VR8("a", 10), VR8("a", 11), VR8("a", 12), VR8("a", 13), VR8("a", 14), VR8("a", 15),
        VR8("a", 16), VR8("a", 17), VR8("a", 18), VR8("a", 19), VR8("a", 20), VR8("a", 21), VR8("a", 22), VR8("a", 23), VR8("a", 24),
        "a250", "a251", "a252", "a253", "a254", "a255");
  RT(7);
}

struct XL { rsrc_t X; i32x4 D; unsigned vo; int wr, rd; };
struct XP { lds_char* p[2]; };
__device__ __forceinline__ void x_init(XL& xl, const u16* x, int wave, int lane) {
  xl.X = __builtin_amdgcn_make_buffer_rsrc((void*)x, 0, S_LEN * DM * 2, 0x00020000);
  xl.D = (i32x4){(int)(unsigned)(u64)x, (int)(unsigned)((u64)x >> 32), S_LEN * DM * 2, 0x00020000};
  xl.vo = (unsigned)((wave * 8 + (lane >> 3)) * 1152 + ((lane ^ (lane >> 3)) & 7) * 16);
  xl.wr = wave * 9216 + lane * 16;
  xl.rd = ((lane >> 3) & 1) * 9216 + (lane & 7) * 128 + ((lane >> 4) ^ (lane & 7)) * 16;
}
__device__ __forceinline__ XP x_at(lds_char* buf, const XL& xl) {
  XP r = {{buf + xl.rd, buf + (xl.rd ^ 64)}};
  pin_v(r.p[0]); pin_v(r.p[1]);
  return r;
}
__device__ __forceinline__ void x_m0(unsigned b, int k) { asm volatile("s_add_u32 m0, %0, %1" :: "s"(b), "n"(k * 896) : "scc"); }
__device__ __forceinline__ void x_dma(const XL& xl, unsigned so, int k) {
  asm volatile("buffer_load_dwordx4 %0, %1, %2 offen offset:%3 lds" :: "v"(xl.vo), "s"(xl.D), "s"(so), "n"(k * 128) : "memory");
}
__device__ __forceinline__ void x_move(lds_char*& p, unsigned d) { asm volatile("v_add_u32 %0, %1, %0" : "+v"(p) : "s"(d)); }
__device__ __forceinline__ u32x4 x_ld1(const XL& xl, int s, int k) { return __builtin_amdgcn_raw_buffer_load_b128(xl.X, xl.vo + k * 128, (unsigned)s * 36864u, 0); }
__device__ __forceinline__ void x_gload(u32x4 (&xr)[9], const XL& xl, int s) {
#pragma unroll
  for (int k = 0; k < 9; ++k) xr[k] = x_ld1(xl, s, k);
}
__device__ __forceinline__ void x_swrite(const u32x4 (&xr)[9], lds_char* buf, const XL& xl) {
#pragma unroll
  for (int k = 0; k < 9; ++k) *(lds_u32x4*)(buf + xl.wr + k * 1024) = xr[k];
}
#define XF(buf, tt, ks) (*(lds_u32x4*)((buf).p[(ks) & 1] + (tt) * 18432 + ((ks) >> 1) * 1024))
__device__ __forceinline__ void proj4(lds_char* cbuf, lds_char* nbuf, u32x4 (&xr)[9], const XL& xl, int snext, int wave, int lane,
                                      const u32x4 (&w0)[18], const u32x4 (&w1)[18], const u32x4 (&w2)[18], const u32x4 (&w3)[18],
                                      f32x4 (&acc)[4][2]) {
  u32x4 bb[3][2];
  const XP buf = x_at(cbuf, xl);
  nbuf += xl.wr;
#pragma unroll
  for (int ks = 0; ks < 2; ++ks) { bb[ks][0] = XF(buf, 0, ks); bb[ks][1] = XF(buf, 1, ks); }
#pragma unroll
  for (int t = 0; t < 4; ++t) { acc[t][0] = (f32x4){0.f, 0.f, 0.f, 0.f}; acc[t][1] = acc[t][0]; }
#pragma unroll
  for (int ks = 0; ks < 18; ++ks) {
    const u32x4 b0 = bb[ks % 3][0], b1 = bb[ks % 3][1];
    SB();
    acc[0][0] = mfma(w0[ks], b0, acc[0][0]); SB();
    if (ks + 2 < 18) bb[(ks + 2) % 3][0] = XF(buf, 0, ks + 2);
    SB();
    acc[0][1] = mfma(w0[ks], b1, acc[0][1]); SB();
    if (ks + 2 < 18) bb[(ks + 2) % 3][1] = XF(buf, 1, ks + 2);
    SB();
    acc[1][0] = mfma(w1[ks], b0, acc[1][0]); SB();
    if ((ks & 1) == 0) *(lds_u32x4*)(nbuf + (ks >> 1) * 1024) = xr[ks >> 1];
    SB();
    acc[1][1] = mfma(w1[ks], b1, acc[1][1]);
    acc[2][0] = mfma(w2[ks], b0, acc[2][0]); SB();
    if ((ks & 1) == 0) xr[ks >> 1] = x_ld1(xl, snext, ks >> 1);
    SB();
    acc[2][1] = mfma(w2[ks], b1, acc[2][1]);
    acc[3][0] = mfma(w3[ks], b0, acc[3][0]); acc[3][1] = mfma(w3[ks], b1, acc[3][1]);
    SB();
  }
}
#define PFL 13
#define STG0 6
#define STGD 0
#define NOST 0
#define NOFL 0
#define FG 7
#define UFL 5
__device__ __forceinline__ int clampseg(int s) { return s < NSEG ? s : NSEG - 1; }

__device__ __forceinline__ void role_prj(const Args& a, rsrc_t R, const Go& go, lds_char* lds, const u16* W0, const u16* W1, unsigned o0, unsigned o1, unsigned flag,
                                         int tid, int wave, int lane) {
  const int l15 = lane & 15, g = lane >> 4;
  u32x4 w[2][2][18];
  u32x4 xr[9], xq[9];
  XL xl; x_init(xl, a.x, wave, lane);
  x_gload(xq, xl, 0);
  x_gload(xr, xl, 1);
  const unsigned fo = wave * 1024 + lane * 16;
  const unsigned pf = lane == 0 ? flag + wave * 4 : OOB;
  f32x4 acc[2][2][2];
  {
    u32x4 sg[PDEP][8];
    const rsrc_t B0 = __builtin_amdgcn_make_buffer_rsrc((void*)W0, 0, DM * HD * 2, 0x00020000), B1 = __builtin_amdgcn_make_buffer_rsrc((void*)W1, 0, DM * HD * 2, 0x00020000);
    const unsigned wof = (unsigned)((tid >> 4) * (HD * 2) + (tid & 15) * 16);
    auto gl = [&](int i, int k) {
      if (i < 10 && (i % 5 < 4 || k < 4)) { sg[i % PDEP][k] = __builtin_amdgcn_raw_buffer_load_b128(i < 5 ? B0 : B1, wof, (unsigned)(128 * (i % 5) + 16 * k) * (HD * 2), (NTM & 4) ? 2 : 0); SB(); }
    };
#pragma unroll
    for (int k = 0; k < 8 * PDEP; ++k) gl(k >> 3, k & 7);
    if (wave == 0) WALK(100); else if (wave == 1) WALK(101); else if (wave == 2) WALK(102); else WALK(103);
    RT(2);
    PADS(100);
    lds_char *x0p = lds + X0_OFF, *x1p = lds + X1_OFF;
    pin_v(x0p); pin_v(x1p);
    x_swrite(xq, x0p, xl);
    PAD();
    x_swrite(xr, x1p, xl);
    PAD();
    const XP x0r = x_at(x0p, xl), x1r = x_at(x1p, xl);
    lds_char *wp = lds + (tid >> 4) * 288 + (tid & 15) * 16, *rp = lds + (g * 8 + (l15 >> 2)) * 288 + wave * 64 + (l15 & 3) * 8;
    pin_v(wp); pin_v(rp);
    auto put = [&](int i, int k) {
      if (i < 10 && (i % 5 < 4 || k < 4)) { *(lds_u32x4*)(wp + (i & 1) * STG_BYTES + k * (16 * 288)) = sg[i % PDEP][k]; SB(); }
    };
#pragma unroll
    for (int k = 0; k < 8; ++k) { put(0, k); gl(PDEP, k); }
    auto chunk = [&](auto IC) {
      constexpr int i = decltype(IC)::value, m = i / 5, c = i % 5, nk = c < 4 ? 4 : 2;
      if (c == 0) {
#pragma unroll
        for (int k = 0; k < 8; ++k) { acc[k >> 2][(k >> 1) & 1][k & 1] = (f32x4){0.f, 0.f, 0.f, 0.f}; pin_v(acc[k >> 2][(k >> 1) & 1][k & 1]); }
      }
      if (i == 4) { PADE(); PADS(101); } else if (i == 8) { PADE(); PADS(102); } else PAD();
      BAR();
      if (i == 0) RT(3);
      if (i == 2) RT(4);
      if (i == 4) RT(5);
      if (i == 6) RT(6);
      if (i == 7) RT(8);
      if (i == 8) RT(9);
      u32x4 xb[2][4];
      u32x2 wh[2][4];
      auto rd = [&](int kk, int j) {
        if (kk >= nk) return;
        if (j & 1) xb[kk & 1][j >> 1] = XF((j & 4) ? x1r : x0r, (j >> 1) & 1, 4 * c + kk);
        else wh[kk & 1][j >> 1] = trd(rp + (i & 1) * STG_BYTES + (kk * 32 + (j & 2) * 2) * 288 + (j >> 2) * 32);
        SB();
      };
#pragma unroll
      for (int j = 0; j < 8; ++j) rd(0, j);
#pragma unroll
      for (int k = 0; k < 8 * nk; ++k) {
        const int kk = k >> 3;
        f32x4& d = acc[(k >> 2) & 1][k & 1][(k >> 1) & 1];
        u32x4& f = w[m][k & 1][4 * c + kk];
        if ((k & 7) < 2) {
          f = (u32x4){wh[kk & 1][(k & 1) * 2][0], wh[kk & 1][(k & 1) * 2][1], wh[kk & 1][(k & 1) * 2 + 1][0], wh[kk & 1][(k & 1) * 2 + 1][1]};
          if (c < 4) pin_a(f); else pin_v(f);
        }
        if (c < 4) mfA(d, f, xb[kk & 1][(k >> 1) & 3]); else mfa(d, f, xb[kk & 1][(k >> 1) & 3]);
        SB();
        rd(kk + 1, k & 7);
        if (k % nk == 0) put(i + 1, k / nk);
        if (k % nk == 1) gl(i + 1 + PDEP, k / nk);
        if (k % 12 == 11 || k == 8 * nk - 1) PAD();
      }
      if (i == 6) x_gload(xq, xl, 2);
      if (i == 7) x_gload(xr, xl, 3);
      if (i == 6) {
        go_wait(R, go, lane);
        st32(0u, R, pf, 0);
      }
      SB();
      if (c == 4) {
        asm volatile("s_nop 7"); SB();
#pragma unroll
        for (int k = 0; k < 4; ++k) {
          PAD();
          const f32x4 (&e)[2][2] = acc[k >> 1];
          const int tt = k & 1;
          st128((u32x4){pk2(e[0][tt][0], e[0][tt][1]), pk2(e[0][tt][2], e[0][tt][3]), pk2(e[1][tt][0], e[1][tt][1]), pk2(e[1][tt][2], e[1][tt][3])}, R,
                (m ? o1 : o0) + fo + tt * 4096, (unsigned)(k >> 1) * 262144u);
        }
        SB();
      }
    };
#define CH(n) chunk(std::integral_constant<int, n>())
    CH(0); CH(1); CH(2); CH(3); CH(4); CH(5); CH(6); CH(7); CH(8); CH(9);
#undef CH
    BAR();
  }
  RT(1);
  PAD();
  x_swrite(xq, lds, xl);
  x_swrite(xr, lds + STG_BYTES, xl);
  PAD();
  u32x4 bb[3][2];
  XP rd0 = x_at(lds, xl), rd1 = x_at(lds + STG_BYTES, xl);
  unsigned b0 = 0, b1 = STG_BYTES, b2 = X1_OFF;
  const unsigned xw = (unsigned)(uintptr_t)lds + __builtin_amdgcn_readfirstlane(wave) * 9216;
  VMW(0);
  st32(3u, R, pf, 0);
  BAR();
#pragma unroll
  for (int ks = 0; ks < 2; ++ks) { bb[ks][0] = XF(rd0, 0, ks); bb[ks][1] = XF(rd0, 1, ks); }
  __builtin_amdgcn_s_waitcnt(0x0F70);
  unsigned oa = o0 + fo, ob = o1 + fo, tc0 = 0, tr0 = 0;
  pin_v(oa); pin_v(ob);
  auto half = [&](auto HC, int s) {
    constexpr int H = decltype(HC)::value & 1, P = decltype(HC)::value >> 1;
    XP& buf = P ? rd1 : rd0;
    XP& nrd = H ? (P ? rd0 : rd1) : buf;
    const unsigned oo = H ? oa : ob;
    unsigned so, sx, xm, xd;
    f32x4 (&c)[2][2] = acc[H];
    f32x4 (&e)[2][2] = acc[H ^ 1];
    u32x4 o;
#define PM(t, tt) do { if (ks == 0) c[t][tt] = mfA0(w[H][t][0], bb[0][tt]); else if (ks < 16) mfA(c[t][tt], w[H][t][ks], bb[ks % 3][tt]); \
                       else mfa(c[t][tt], w[H][t][ks], bb[ks % 3][tt]); SB(); } while (0)
#define PR(tt) do { bb[(ks + 2) % 3][tt] = ks < 16 ? XF(buf, tt, ks + 2) : XF(nrd, tt, ks - 16); SB(); } while (0)
#pragma unroll
    for (int ks = 0; ks < 18; ++ks) {
      const int xi = H * 5 + ((ks - 7) >> 1);
      const bool xs = ks >= 7 && ks < 17 && xi < 9 && (H == 0 || ks < 15);
      if (H == 1 && ks == 16) { __builtin_amdgcn_s_waitcnt(0xC27F); __builtin_amdgcn_s_barrier(); SB(); }
      else { __builtin_amdgcn_s_waitcnt(0xC27F); SB(); }
      PM(0, 0); PR(0);
      if (ks == 0) {
        so = (unsigned)(s + H - 1) * 262144u;
        sx = (unsigned)clampseg(s + 2) * 36864u;
        asm volatile("" : "+s"(so), "+s"(sx)); SB();
      }
      if (ks == 1) {
        xm = xw + b2; xd = b2 - b0;
        if (H) { const unsigned t = b0; b0 = b1; b1 = b2; b2 = t; }
        asm volatile("" : "+s"(xm), "+s"(xd), "+s"(b0), "+s"(b1), "+s"(b2)); SB();
      }
      PM(0, 1); PR(1);
      PM(1, 0);
      if (ks == 1 || ks == 2) { o[(ks - 1) * 2] = pkv(e[ks - 1][0][0], e[ks - 1][0][1]); SB(); }
      if (ks == 4 || ks == 5) { o[(ks - 4) * 2] = pkv(e[ks - 4][1][0], e[ks - 4][1][1]); SB(); }
      if (ks == 3 && NOST < 2) { st128(o, R, oo, so); SB(); }
      if (ks == 6 && NOST < 2) { st128(o, R, oo + 4096, so); SB(); }
      if (xs && (ks & 1)) { x_m0(xm, xi); SB(); }
      if (xs && !(ks & 1)) { x_dma(xl, sx, xi); SB(); }
      if (H && ks >= 16) { x_move(buf.p[ks & 1], xd); SB(); }
      PM(1, 1);
      if (ks % 3 == 0) { if (H == 1 && P == 0 && ks == 0) { PADE(); PADS(103); } else PAD(); SB(); }
      if (ks == 1 || ks == 2) { o[(ks - 1) * 2 + 1] = pkv(e[ks - 1][0][2], e[ks - 1][0][3]); SB(); }
      if (ks == 4 || ks == 5) { o[(ks - 4) * 2 + 1] = pkv(e[ks - 4][1][2], e[ks - 4][1][3]); SB(); }
      if (ks == PFL) {
        asm volatile("s_waitcnt vmcnt(%0)" :: "n"((H ? 8 : 7) + (PFL - 6) / 2) : "memory");
        st32((unsigned)max(2 * s + H - 2, 3), R, pf, 0); SB();
      }
      if (ks == 17 && (P == 0) == (H == 1) && UNLIKELY(s < 4)) { __builtin_amdgcn_s_waitcnt(0x0F70); st32((unsigned)(2 * s + H - 1), R, pf, 0); SB(); }
    }
#undef PM
#undef PR
  };
#pragma nounroll
  for (int s = 2; s < NSEG; ++s) {
    half(std::integral_constant<int, 0>(), s);
    half(std::integral_constant<int, 1>(), s);
    STEPRT(0, 0);
    ++s;
    half(std::integral_constant<int, 2>(), s);
    half(std::integral_constant<int, 3>(), s);
    STEPRT(0, 0);
  }
  PADE();
  mf_wait2(acc[1][0][0], acc[1][0][1]); mf_wait2(acc[1][1][0], acc[1][1][1]);
#pragma unroll
  for (int tt = 0; tt < 2; ++tt) {
    const u32x4 o = {pk2(acc[1][0][tt][0], acc[1][0][tt][1]), pk2(acc[1][0][tt][2], acc[1][0][tt][3]), pk2(acc[1][1][tt][0], acc[1][1][tt][1]), pk2(acc[1][1][tt][2], acc[1][1][tt][3])};
    st128(o, R, o1 + fo + tt * 4096, (unsigned)((NSEG - 1) & RMASK) * 262144u);
  }
  VMW(0);
  st32((unsigned)(2 * NSEG - 1), R, pf, 0);
  RT(7);
}

__device__ __forceinline__ void role_px(const Args& a, rsrc_t R, const Go& go, int pi, int wave, int lane) {
  const int l15 = lane & 15, g = lane >> 4;
  const int h = pi * 2 + (wave >> 1), qt = wave & 1;
  const int wz = h >> 3, tq = (h >> 1) & 3, hp = h & 1;
  const int gy = ((tq & 1) << 1) | (tq >> 1);
  const unsigned kmo = OFF_KMF + (unsigned)h * 4096u + lane * 16;
  const unsigned pmo = OFF_PXF + (unsigned)((qt * 40 + 32 + wz * 2 + hp) * 1024 + (gy * 16 + l15) * 16 + g * 4);
  const unsigned ppf = (lane == 0) ? (unsigned)(FL_PXPROG + h * 128 + qt * 4) : OOB;
  const unsigned qo = OFF_QF + (unsigned)h * 8192u + qt * 4096 + lane * 16;
  const unsigned k0o = OFF_KF + (unsigned)h * 8192u + lane * 16;
  const unsigned k1o = qt ? k0o + 4096 : OOB;
  const unsigned kmf = lane == 0 ? FL_KMPROG + h * 128 : OOB;
  const int kon = lane == 0 ? 0 : -NSEG;
  int fm = -1;
  RT(1);
  u32x4 qf[4], k0[4], k1[4];
  if (!go_wait(R, go, lane) || !wait_flag_c<16, 1>(R, kmf, kon, fm)) return;
#pragma unroll
  for (int ks = 0; ks < 4; ++ks) { qf[ks] = ld128(R, qo + ks * 1024, 0); k0[ks] = ld128(R, k0o + ks * 1024, 0); k1[ks] = ld128(R, k1o + ks * 1024, 0); }
  __builtin_amdgcn_s_waitcnt(0x0F70);
  for (int s = 0; s < NSEG - 1; ++s) {
    u32x4 km[4], qc[4];
    f32x4 G = {0.f, 0.f, 0.f, 0.f}, T0 = G, T1 = G;
#pragma unroll
    for (int ks = 0; ks < 4; ++ks) { T0 = mfma(k0[ks], qf[ks], T0); T1 = mfma(k1[ks], qf[ks], T1); qc[ks] = qf[ks]; }
    float mx, sx;
    px_mask(T0, T1, qt, lane);
    px_seg(T0, T1, mx, sx);
    if (!wait_flag_c<PXS, 1>(R, kmf, s + kon, fm)) return;
#pragma unroll
    for (int ks = 0; ks < 4; ++ks) km[ks] = ld128(R, kmo + ks * 1024, (unsigned)s * 131072u);
    const unsigned so = (unsigned)clampseg(s + 1) * 262144u;
#pragma unroll
    for (int ks = 0; ks < 4; ++ks) {
      qf[ks] = ld128(R, qo + ks * 1024, so);
      k0[ks] = ld128(R, k0o + ks * 1024, so);
      k1[ks] = ld128(R, k1o + ks * 1024, so);
    }
    CBAR();
    fm = (int)lda(R, kmf);
    VMW(17);
    st32((unsigned)(s - 1), R, ppf, 0);
    CBAR();
#pragma unroll
    for (int ks = 0; ks < 4; ++ks) G = mfma(km[ks], qc[ks], G);
    u32x4 px; unsigned pw;
    px_fin(G, T0, T1, mx, sx, px, pw, lane);
    st128(px, R, OFF_PXF + (unsigned)((qt * 40 + h) * 1024 + lane * 16), (unsigned)s * 81920u);
    st32(pw, R, pmo, (unsigned)s * 81920u);
    CBAR();
  }
  VMW(0);
  st32((unsigned)(NSEG - 2), R, ppf, 0);
  RT(7);
}

__device__ __forceinline__ void role_vuw(const Args& a, rsrc_t R, const Go& go, lds_char* lds, int h, int tid, int wave, int lane) {
  const int l15 = lane & 15, g = lane >> 4;
  u32x4 wv[2][18], wo[9][4];
  u32x4 xr[9], xq[9];
  XL xl; x_init(xl, a.x, wave, lane);
  x_gload(xq, xl, 0);
  x_gload(xr, xl, 1);
  unsigned zo = OFF_ZF + (unsigned)(wave * 9 * 40960 + lane * 16) + h * 1024;
  const unsigned vpf = (lane == 0) ? (unsigned)(FL_UPROG + h * 128 + wave * 4) : OOB;
  f32x4 cc[2][2], av[2][2][2];
  {
    u32x4 sg[WDEP][9];
    const rsrc_t B0 = __builtin_amdgcn_make_buffer_rsrc((void*)(a.Wv + h * 128), 0, DM * HD * 2, 0x00020000);
    const rsrc_t B1 = __builtin_amdgcn_make_buffer_rsrc((void*)(a.Wo + (size_t)h * 128 * DM), 0, 128 * DM * 2, 0x00020000);
    const unsigned wof = (unsigned)((tid >> 4) * (HD * 2) + (tid & 15) * 16);
    auto gl = [&](int c, int k) {
      if (c < 5) { if (k < (c < 4 ? 8 : 4)) { sg[c % WDEP][k] = __builtin_amdgcn_raw_buffer_load_b128(B0, wof, (unsigned)(128 * c + 16 * k) * (HD * 2), (NTM & 8) ? 2 : 0); SB(); } }
      else if (c < 9) { sg[c % WDEP][k] = __builtin_amdgcn_raw_buffer_load_b128(B1, tid * 16, (unsigned)((c - 5) * 9 + k) * 4096, (NTM & 8) ? 2 : 0); SB(); }
    };
#pragma unroll
    for (int k = 0; k < 8 * WDEP; ++k) gl(k >> 3, k & 7);
    if (wave == 0) WALK(110); else if (wave == 1) WALK(111); else if (wave == 2) WALK(112); else WALK(113);
    RT(2);
    PADS(110);
    lds_char *x0p = lds + X0_OFF, *x1p = lds + X1_OFF;
    pin_v(x0p); pin_v(x1p);
    x_swrite(xq, x0p, xl);
    PAD();
    x_swrite(xr, x1p, xl);
    PAD();
    const XP x0r = x_at(x0p, xl), x1r = x_at(x1p, xl);
    lds_char *wp = lds + (tid >> 4) * 288 + (tid & 15) * 16, *rp = lds + (g * 8 + (l15 >> 2)) * 288 + wave * 64 + (l15 & 3) * 8;
    pin_v(wp); pin_v(rp);
    auto put = [&](int c, int k) {
      const int u = tid + 256 * k;
      lds_char* buf = lds + (c & 1) * STG_BYTES;
      if (c < 5) { if (k < (c < 4 ? 8 : 4)) { *(lds_u32x4*)(wp + (c & 1) * STG_BYTES + k * (16 * 288)) = sg[c % WDEP][k]; SB(); } }
      else if (c < 9) { *(lds_u32x4*)(buf + (u / 72) * 1184 + (u % 72) * 16) = sg[c % WDEP][k]; SB(); }
    };
#pragma unroll
    for (int k = 0; k < 8; ++k) { put(0, k); gl(WDEP, k); }
    PAD();
#pragma unroll
    for (int k = 0; k < 8; ++k) { av[k >> 2][(k >> 1) & 1][k & 1] = (f32x4){0.f, 0.f, 0.f, 0.f}; pin_v(av[k >> 2][(k >> 1) & 1][k & 1]); }
    auto chunk = [&](auto CC) {
      constexpr int c = decltype(CC)::value, nk = c < 4 ? 4 : 2;
      lds_char* buf = lds + (c & 1) * STG_BYTES;
      if (c == 4) { PADE(); PADS(111); } else PAD();
      BAR();
      if (c == 0) RT(3);
      if (c == 2) RT(4);
      if (c == 4) RT(5);
      if (c == 6) RT(6);
      if (c < 5) {
        u32x4 xb[2][4];
        u32x2 wh[2][4];
        auto rd = [&](int kk, int j) {
          if (kk >= nk) return;
          if (j & 1) xb[kk & 1][j >> 1] = XF((j & 4) ? x1r : x0r, (j >> 1) & 1, 4 * c + kk);
          else wh[kk & 1][j >> 1] = trd(rp + (c & 1) * STG_BYTES + (kk * 32 + (j & 2) * 2) * 288 + (j >> 2) * 32);
          SB();
        };
#pragma unroll
        for (int j = 0; j < 8; ++j) rd(0, j);
#pragma unroll
        for (int k = 0; k < 8 * nk; ++k) {
          const int kk = k >> 3;
          u32x4& f = wv[k & 1][(4 * c + kk) % 18];
          if ((k & 7) < 2) {
            f = (u32x4){wh[kk & 1][(k & 1) * 2][0], wh[kk & 1][(k & 1) * 2][1], wh[kk & 1][(k & 1) * 2 + 1][0], wh[kk & 1][(k & 1) * 2 + 1][1]};
            pin_a(f);
          }
          mfA(av[(k >> 2) & 1][k & 1][(k >> 1) & 1], f, xb[kk & 1][(k >> 1) & 3]);
          SB();
          rd(kk + 1, k & 7);
          if (k % nk == 0) put(c + 1, k / nk);
          if (k % nk == 1) gl(c + 1 + WDEP, k / nk);
          if (k % 12 == 11 || k == 8 * nk - 1) PAD();
        }
        put(c + 1, 8);
        gl(c + 1 + WDEP, 8);
      } else {
#pragma unroll
        for (int ct = 0; ct < 9; ++ct) {
          lds_char* p = buf + (4 * g + (l15 >> 2)) * 1184 + (wave * 9 + ct) * 32 + (l15 & 3) * 8;
          const u32x2 v0 = trd(p), v1 = trd(p + 16 * 1184);
          wo[ct][(c + 3) % 4] = (u32x4){v0[0], v0[1], v1[0], v1[1]};
        }
        PAD();
        SB();
        if (c == 5) {
#pragma unroll
          for (int tt = 0; tt < 2; ++tt)
            *(lds_u32x4*)(lds + X0_OFF + tt * 4096 + wave * 1024 + lane * 16) =
                (u32x4){pk2(av[0][0][tt][0], av[0][0][tt][1]), pk2(av[0][0][tt][2], av[0][0][tt][3]), pk2(av[0][1][tt][0], av[0][1][tt][1]), pk2(av[0][1][tt][2], av[0][1][tt][3])};
        }
#pragma unroll
        for (int k = 0; k < 9; ++k) { put(c + 1, k); gl(c + 1 + WDEP, k); }
        PAD();
#pragma unroll
        for (int ct = 0; ct < 9; ++ct) { if (ct < 7) pin_a(wo[ct][(c + 3) % 4]); else pin_v(wo[ct][(c + 3) % 4]); }
      }
      if (c == UPO) {
#pragma unroll
        for (int k = 0; k < 9; ++k) st128((u32x4){~0u, ~0u, ~0u, ~0u}, R, zo, k * 40960);
      }
      if (c == UGO) { go_wait(R, go, lane); st32(~1u, R, vpf, 0); }
      if (c == 5) x_gload(xq, xl, 2);
      if (c == 6) x_gload(xr, xl, 3);
      SB();
    };
#define CH(n) chunk(std::integral_constant<int, n>())
    CH(0); CH(1); CH(2); CH(3); CH(4); CH(5); CH(6); CH(7); CH(8);
#undef CH
    BAR();
  }
  RT(1);
  x_swrite(xq, lds, xl);
  XP rd0 = x_at(lds, xl), rd1 = x_at(lds + STG_BYTES, xl);
  unsigned b0 = 0, b1 = STG_BYTES, b2 = X1_OFF;
  const unsigned xw = (unsigned)(uintptr_t)lds + __builtin_amdgcn_readfirstlane(wave) * 9216;
  lds_char *vr = lds + 2 * STG_BYTES + lane * 16, *vw = vr + wave * 1024;
  pin_v(vr); pin_v(vw); pin_v(zo);
  u32x4 bb[3][2], vf[2][2][4];
#pragma unroll
  for (int k = 0; k < 4; ++k) cc[k >> 1][k & 1] = (f32x4){0.f, 0.f, 0.f, 0.f};
#pragma unroll
  for (int k = 0; k < 8; ++k) { vf[0][k >> 2][k & 3] = *(lds_u32x4*)(vr + k * 1024); vf[1][k >> 2][k & 3] = xq[0]; }
  BAR();
#pragma unroll
  for (int ks = 0; ks < 2; ++ks) { bb[ks][0] = XF(rd0, 0, ks); bb[ks][1] = XF(rd0, 1, ks); }
  __builtin_amdgcn_s_waitcnt(0x0F70);
  auto iter = [&](auto PC, int s) {
    constexpr int P = decltype(PC)::value & 1, W = 0, NOV = decltype(PC)::value >> 1;
    XP& buf = P ? rd1 : rd0;
    XP& nrd = P ? rd0 : rd1;
    f32x4 (&c)[2][2] = av[P];
    f32x4 (&e)[2][2] = av[P ^ 1];
    u32x4 o, q;
    unsigned s1, s2, sx, xm, xd, sf;
#define VM(ks, t, tt) do { if (NOV) break; if ((ks) == 0) c[t][tt] = mfA0(wv[t][ks], bb[(ks) % 3][tt]); else mfA(c[t][tt], wv[t][ks], bb[(ks) % 3][tt]); SB(); } while (0)
#define UM(k, tt) do { f32x4& d = cc[(gq + P) & 1][tt]; const u32x4& f = vf[gq < 2 ? P : P ^ 1][tt][k];                                      \
                       if (ct < 7) { if ((k) == 0) d = mfB0(f, wo[ct][k]); else mfB(d, f, wo[ct][k]); }                                       \
                       else { if ((k) == 0) d = mf0(f, wo[ct][k]); else mfa(d, f, wo[ct][k]); } SB(); } while (0)
#define BRD(ks, tt) do { if (NOV) break; bb[(ks) % 3][tt] = (ks) < 18 ? XF(buf, tt, (ks) % 18) : XF(nrd, tt, (ks) % 18); SB(); } while (0)
#define CV(j) do { if (NOV && (j) == 0) asm volatile("s_nop 3"); o[j] = pkv(cc[(gq + P + 1) & 1][(j) >> 1][((j) & 1) * 2], cc[(gq + P + 1) & 1][(j) >> 1][((j) & 1) * 2 + 1]); SB(); } while (0)
#define CE(j, tt) do { if (gq == 0) { q[j] = pkv(e[(j) >> 1][tt][((j) & 1) * 2], e[(j) >> 1][tt][((j) & 1) * 2 + 1]); SB(); } } while (0)
#define VR(j) do { if (gq == 1) { vf[P ^ 1][(j) & 1][(j) >> 1] = *(lds_u32x4*)(vr + P * 8192 + ((j) & 1) * 4096 + ((j) >> 1) * 1024); SB(); } } while (0)
#define LW(n) do { __builtin_amdgcn_s_waitcnt(0xC07F | ((n) << 8)); SB(); } while (0)
#pragma unroll
    for (int gq = NOV ? 2 : 0; gq < 9; ++gq) {
      const int ct = (gq + 7) % 9, k0 = 2 * gq, k1 = 2 * gq + 1, pt = (gq + 6) % 9;
      if (NOV) {} else if (gq == 0 || gq == 8) LW(2); else if (gq == 1) LW(6); else if (gq == 2) LW(8); else LW(2);
      if (gq == 8 && !NOV) { __builtin_amdgcn_s_barrier(); SB(); }
      VM(k0, 0, 0); BRD(k0 + 2, 0);
      if (gq == (NOV ? 2 : 0)) {
        s1 = (unsigned)(s - 1) < NSEG ? (unsigned)(s - 1) * ZS : OOB; s2 = s >= 2 ? (unsigned)(s - 2) * ZS : OOB; sx = (unsigned)clampseg(s + 2) * 36864u;
        s1 = __builtin_amdgcn_readfirstlane(s1); s2 = __builtin_amdgcn_readfirstlane(s2); sx = __builtin_amdgcn_readfirstlane(sx);
        asm volatile("" : "+s"(s1), "+s"(s2), "+s"(sx)); SB();
      }
      UM(0, 0); CV(0);
      unsigned su = (gq < 3 ? s2 : s1) + pt * 40960;
      asm volatile("" : "+s"(su)); SB();
      VM(k0, 0, 1); BRD(k0 + 2, 1);
      if (!NOV && gq == 0) { sf = FG >= 7 ? (unsigned)(s - 2) : (s == 2 ? ~1u : (unsigned)(s - 3)); pin_v(sf); SB(); }
      if (!NOV && gq == 2) {
        xm = xw + b2; xd = b2 - b0;
        const unsigned t = b0; b0 = b1; b1 = b2; b2 = t;
        asm volatile("" : "+s"(xm), "+s"(xd), "+s"(b0), "+s"(b1), "+s"(b2)); SB();
      }
      UM(0, 1); CV(1);
      if (gq == 1) { LW(2); __builtin_amdgcn_s_barrier(); SB(); }
      VM(k0, 1, 0); VR(0); CV(2);
      UM(1, 0); VR(1); CV(3);
#define ST(n) do { if (!NOST && STG0 + W * STGD == n) { st128(o, R, zo, su); SB(); } } while (0)
#define VW2(j, tt) do { if (gq == 0) { *(lds_u32x2*)(vw + P * 8192 + (tt) * 4096 + (j) * 4) = (u32x2){q[j], q[(j) + 1]}; SB(); } } while (0)
      ST(5);
      VM(k0, 1, 1); ST(6);
      UM(1, 1); CE(0, 0); CE(1, 0); VR(2); ST(7);
      if (NOV) {} else if (gq == 1) LW(5); else LW(2);
      VM(k1, 0, 0); BRD(k1 + 2, 0); CE(2, 0); ST(8);
      UM(2, 0); VW2(0, 0); VR(3); ST(9);
      if (!NOV && gq >= 2 && gq < 7) { x_m0(xm, gq - 2); SB(); }
      if (!NOV && gq == 8) { x_move(buf.p[0], xd); x_move(buf.p[1], xd); SB(); }
      VM(k1, 0, 1); BRD(k1 + 2, 1); CE(3, 0); ST(10);
      UM(2, 1); VW2(2, 0); VR(4); ST(11);
      if (!NOV && gq >= 2 && gq < 7) { x_dma(xl, sx, gq - 2); SB(); }
      VM(k1, 1, 0); VR(5); ST(12);
      CE(0, 1); CE(1, 1);
      if (!NOV && gq >= 3 && gq < 7) { x_m0(xm, gq + 2); SB(); }
      UM(3, 0); VW2(0, 1); VR(6); ST(13);
      if (!NOV && gq == 0) { PADE(); if (P) PADS(113); else PADS(112); } else PAD();
      SB();
      VM(k1, 1, 1); CE(2, 1); CE(3, 1); VR(7); ST(14);
      if (!NOV && gq >= 3 && gq < 7) { x_dma(xl, sx, gq + 2); SB(); }
      UM(3, 1); VW2(2, 1); ST(15);
#undef ST
#undef VW2
      if (!NOV && gq == FG && !NOFL) {
        asm volatile("s_waitcnt vmcnt(%0)" :: "n"(FG >= 7 ? FG + 7 : (FG < 2 ? FG + 16 : 20)) : "memory");
        st32(sf, R, vpf, 0); SB();
      }
      if (!NOV && gq == 7 && FG != 7) { VMW(20); SB(); }
      if (!NOV && gq == 3 && UNLIKELY(s == 3 || s == 4 || s >= NSEG)) { __builtin_amdgcn_s_waitcnt(0x0F70); st32((unsigned)(s - 2), R, vpf, 0); SB(); }
    }
#undef VM
#undef UM
#undef BRD
#undef CV
#undef CE
#undef VR
#undef LW
  };
  RT(8);
  iter(std::integral_constant<int, 3>(), 1);
  {
    f32x4 d[3][2];
#pragma unroll
    for (int k = 0; k < 16; ++k) {
      f32x4& e = d[1 + (k >> 3)][k & 1];
      if (((k >> 1) & 3) == 0) e = mf0(vf[0][k & 1][0], wo[7 + (k >> 3)][0]); else mfa(e, vf[0][k & 1][(k >> 1) & 3], wo[7 + (k >> 3)][(k >> 1) & 3]);
    }
    d[0][0] = cc[1][0]; d[0][1] = cc[1][1];
    mf_wait2(d[2][0], d[2][1]); SB();
#pragma unroll
    for (int t = 0; t < 3; ++t)
      st128((u32x4){pk2(d[t][0][0], d[t][0][1]), pk2(d[t][0][2], d[t][0][3]), pk2(d[t][1][0], d[t][1][1]), pk2(d[t][1][2], d[t][1][3])}, R, zo, (6 + t) * 40960);
    SB();
  }
  RT(9);
  x_swrite(xr, lds + STG_BYTES, xl);
  SB();
#pragma nounroll
  for (int s = 2; s < NSEG + 2; s += 2) {
    iter(std::integral_constant<int, 0>(), s);
    iter(std::integral_constant<int, 1>(), s + 1);
  }
  PADE();
  VMW(0);
  RT(7);
}

#define RING_OFF (2 * STG_BYTES)
#define RING_STR 456
#define OST_OFF (RING_OFF + 64 * RING_STR)
__device__ __forceinline__ float dot2(unsigned a, unsigned b, float c) {
  return __builtin_amdgcn_fdot2_f32_bf16(__builtin_bit_cast(bf16x2, a), __builtin_bit_cast(bf16x2, b), c, false);
}
__device__ __forceinline__ float swp1(float x) { return __uint_as_float(__builtin_amdgcn_update_dpp(0u, __float_as_uint(x), 0xB1, 0xf, 0xf, false)); }
__device__ __forceinline__ void role_aw(const Args& a, rsrc_t R, const Go& go, lds_char* lds, int ai, int tid, int wave, int lane) {
  const int l15 = lane & 15, g = lane >> 4;
  const int ga = ai & 7, rr = ai >> 3;
  for (int k = 0; k < AWD; ++k) __builtin_amdgcn_s_sleep(32);
  u32x4 w[4][18];
  {
    u32x4 sg[3][7];
    unsigned gv[9], wo[7], lo[7];
    const rsrc_t W = __builtin_amdgcn_make_buffer_rsrc((void*)a.wqkv, 0, DM * 1728 * 2, 0x00020000);
    const rsrc_t G = __builtin_amdgcn_make_buffer_rsrc((void*)a.gw, 0, DM * 2, 0x00020000);
#pragma unroll
    for (int k = 0; k < 7; ++k) {
      const int u = tid + 256 * k, row = u / 27, cu = u % 27;
      wo[k] = row < 64 ? (unsigned)(row * 3456 + (cu / 9) * 1152 + ga * 144 + (cu % 9) * 16) : OOB;
      lo[k] = row < 64 ? (unsigned)(row * 544 + cu * 16) : (unsigned)((tid & 63) * 544 + 448);
    }
    auto gl = [&](u32x4 (&st)[7], int c) {
#pragma unroll
      for (int k = 0; k < 7; ++k) st[k] = __builtin_amdgcn_raw_buffer_load_b128(W, wo[k], (unsigned)c * (64u * 3456u), (NTM & 16) ? 2 : 0);
    };
#pragma unroll
    for (int c = 0; c < 9; ++c) gv[c] = __builtin_amdgcn_raw_buffer_load_b16(G, (unsigned)(tid & 63) * 2, (unsigned)c * 128u, 0);
    gl(sg[0], 0); if (ASLP > 0) __builtin_amdgcn_s_sleep(ASLP);
    gl(sg[1], 1); if (ASLP > 0) __builtin_amdgcn_s_sleep(ASLP);
    gl(sg[2], 2);
#pragma unroll
    for (int c = 0; c < 9; ++c) {
      lds_char* buf = lds + (c & 1) * STG_BYTES;
#pragma unroll
      for (int k = 0; k < 7; ++k) *(lds_u32x4*)(buf + lo[k]) = sg[c % 3][k];
      *(lds_u32x4*)(buf + (tid & 63) * 544 + 432) = (u32x4){gv[c], 0u, 0u, 0u};
      if (ASLP > 0) __builtin_amdgcn_s_sleep(ASLP);
      if (c + 3 < 9) gl(sg[c % 3], c + 3);
      BAR();
#pragma unroll
      for (int kk = 0; kk < 2; ++kk)
#pragma unroll
        for (int t = 0; t < 4; ++t) {
          lds_char* p = buf + (kk * 32 + g * 8 + (l15 >> 2)) * 544 + (wave * 64 + t * 16) * 2 + (l15 & 3) * 8;
          u32x2 v0 = trd(p), v1 = trd(p + 4 * 544);
          w[t][2 * c + kk] = (u32x4){v0[0], v0[1], v1[0], v1[1]};
        }
      SB();
#pragma unroll
      for (int k = 0; k < 8; ++k) { if (c < 7) pin_a(w[k & 3][2 * c + (k >> 2)]); else pin_v(w[k & 3][2 * c + (k >> 2)]); }
    }
    BAR();
  }
  RT(1);
  go_wait(R, go, lane);
  auto sgm = [&](int it) { const int j = it / 5; return min(16 * j + 4 * rr + it - 5 * j - 1, NSEG - 1); };
  const int i0 = rr > 0 ? 0 : 1;
  u32x4 xr[9];
  XL xl; x_init(xl, a.x, wave, lane);
  x_gload(xr, xl, sgm(i0));
  x_swrite(xr, lds + i0 * STG_BYTES, xl);
  x_gload(xr, xl, sgm(i0 + 1));
  lds_char* ring = lds + RING_OFF;
  lds_char* ost = lds + OST_OFF;
  const int t = tid >> 3, hh = (tid >> 1) & 3, half = tid & 1;
  const float sc = 0.23570226039551584f * 1.4426950408889634f;
  auto done = [&](int s) { __builtin_amdgcn_raw_ptr_buffer_atomic_add_i32(1, R, tid == 0 && s >= 0 ? (unsigned)(FL_APROG + s * 128) : OOB, 0, SC1); };
  const unsigned gto = (ga == 0 && wave == 3 && g == 2) ? OFF_GATE + l15 * 4 : OOB;
  int sp = -1;
  for (int it = i0; it < 20; ++it) {
    const int s = sgm(it);
    BAR();
    {
      f32x4 acc[4][2];
      proj4(lds + (it & 1) * STG_BYTES, lds + ((it + 1) & 1) * STG_BYTES, xr, xl, sgm(it + 2), wave, lane, w[0], w[1], w[2], w[3], acc);
      VMW(9);
#pragma unroll
      for (int tl = 0; tl < 4; ++tl)
#pragma unroll
        for (int tt = 0; tt < 2; ++tt) {
          const int col = wave * 64 + tl * 16 + g * 4;
          u32x2 o = {pk2(acc[tl][tt][0], acc[tl][tt][1]), pk2(acc[tl][tt][2], acc[tl][tt][3])};
          if (col < 216) *(__attribute__((address_space(3))) u32x2*)(ring + (((s & 1) * 32 + tt * 16 + l15) * RING_STR) + col * 2) = o;
        }
#pragma unroll
      for (int tt = 0; tt < 2; ++tt) {
        float z = acc[1][tt][0];
        float gv = __builtin_amdgcn_rcpf(1.f + __builtin_amdgcn_exp2f(-1.4426950408889634f * z));
        st32(__float_as_uint(gv), R, gto + tt * 64, (unsigned)s * 128u);
      }
    }
    BAR();
    done(sp);
    sp = it % 5 != 0 ? s : -1;
    if (it % 5 != 0) {
      const int row = s * 32 + t;
      unsigned q[9];
      lds_char* qp = ring + (row & 63) * RING_STR + hh * 36;
#pragma unroll
      for (int j = 0; j < 9; ++j) q[j] = *(lds_u32*)(qp + j * 4);
      float lg[4];
      lds_char* vp[4];
#pragma unroll
      for (int wi = 0; wi < 4; ++wi) {
        const int r2 = row - 2 * (half * 4 + wi);
        const bool ok = r2 >= 0;
        lds_char* kp = ring + ((ok ? r2 : row) & 63) * RING_STR + hh * 36 + 144;
        vp[wi] = kp + 144;
        float d = 0.f;
#pragma unroll
        for (int j = 0; j < 9; ++j) d = dot2(q[j], *(lds_u32*)(kp + j * 4), d);
        lg[wi] = ok ? d : -__builtin_inff();
      }
      float m = fmaxf(fmaxf(lg[0], lg[1]), fmaxf(lg[2], lg[3]));
      m = fmaxf(m, swp1(m));
      const float ms = -m * sc;
      float sum = 0.f;
#pragma unroll
      for (int wi = 0; wi < 4; ++wi) { lg[wi] = __builtin_amdgcn_exp2f(fmaf(lg[wi], sc, ms)); sum += lg[wi]; }
      sum += swp1(sum);
      const float inv = __builtin_amdgcn_rcpf(sum);
      const unsigned p01 = pk2(lg[0] * inv, lg[1] * inv), p23 = pk2(lg[2] * inv, lg[3] * inv);
#pragma unroll
      for (int j = 0; j < 9; ++j) {
        const unsigned v0 = *(lds_u32*)(vp[0] + j * 4), v1 = *(lds_u32*)(vp[1] + j * 4), v2 = *(lds_u32*)(vp[2] + j * 4), v3 = *(lds_u32*)(vp[3] + j * 4);
        float lo = dot2(__builtin_amdgcn_perm(v1, v0, 0x05040100u), p01, 0.f);
        lo = dot2(__builtin_amdgcn_perm(v3, v2, 0x05040100u), p23, lo);
        float hi = dot2(__builtin_amdgcn_perm(v1, v0, 0x07060302u), p01, 0.f);
        hi = dot2(__builtin_amdgcn_perm(v3, v2, 0x07060302u), p23, hi);
        lo += swp1(lo); hi += swp1(hi);
        if ((j & 1) == half) *(lds_u32*)(ost + t * 144 + hh * 36 + j * 4) = pk2(lo, hi);
      }
      BAR();
#pragma unroll
      for (int k = 0; k < 2; ++k) {
        const int u = tid + 256 * k;
        const bool ok = u < 288;
        const int ur = u & 31, uc = ok ? u >> 5 : 0, cc = ga * 9 + uc;
        u32x4 o = *(lds_u32x4*)(ost + ur * 144 + uc * 16);
        st128(o, R, ok ? OFF_OA + (unsigned)(((ur >> 4) * 18 + (cc >> 2)) * 1024 + ((cc & 3) * 16 + (ur & 15)) * 16) : OOB, (unsigned)s * 36864u);
      }
      CBAR();
    }
  }
  VMW(0);
  BAR();
  done(sp);
  RT(7);
}

__device__ __forceinline__ void role_out(const Args& a, rsrc_t R, const Go& go, lds_char* lds, int o, int tid, int wave, int lane) {
  const int l15 = lane & 15, g = lane >> 4;
  const int cb = o % 9, grp = o < 36 ? o / 9 : 3, send = o / 9 == 3 ? NSEG - 1 : NSEG, sst = o < 36 ? 4 : NSEG - 4;
  u32x4 wf[4][5];
  {
    u32x4 st[18];
#pragma unroll
    for (int k = 0; k < 18; ++k) { int u = tid + 256 * k; st[k] = *(const u32x4*)(a.wo + (size_t)(u >> 3) * DM + cb * 64 + (u & 7) * 8); }
#pragma unroll
    for (int k = 0; k < 18; ++k) { int u = tid + 256 * k; *(lds_u32x4*)(lds + (u >> 3) * 144 + (u & 7) * 16) = st[k]; }
    BAR();
#pragma unroll
    for (int i = 0; i < 5; ++i)
#pragma unroll
      for (int ct = 0; ct < 4; ++ct) {
        const int ks = wave + 4 * i;
        lds_char* p = lds + ((ks < 18 ? ks : 17) * 32 + g * 8 + (l15 >> 2)) * 144 + ct * 32 + (l15 & 3) * 8;
        u32x2 v0 = trd(p), v1 = trd(p + 4 * 144);
        wf[ct][i] = (ks < 18) ? (u32x4){v0[0], v0[1], v1[0], v1[1]} : (u32x4){0u, 0u, 0u, 0u};
        pin_a(wf[ct][i]);
      }
    BAR();
  }
  const unsigned zb = OFF_ZF + (unsigned)(4 * cb) * 40960u + lane * 16;
  const unsigned flk = lane == 32 ? FL_KMPROG + ((o * 4 + wave) & 31) * 128 : OOB;
  const bool isu = (lane & 24) == 8 && lane < 48;
  auto faddr = [&](int s) -> unsigned {
    return lane < 8 ? (lane == 0 ? FL_APROG + s * 128 : OOB)
                    : (isu ? FL_UPROG + (wave * 8 + (lane & 7)) * 128 + ((cb * 64 + (lane < 16 ? 0 : 63)) / 144) * 4
                           : (lane < 32 ? (s == NSEG - 1 ? FL_PXLAST + (wave * 16 + lane - 16) * 128 : FL_PXPROG + (wave * 8 + (lane & 7)) * 128 + ((lane >> 3) & 1) * 4)
                                        : (s + 3 < NSEG ? flk : FL_YPROG + (wave * 8 + (lane & 7)) * 16 + ((cb * 64 + (lane < 48 ? 0 : 63)) / 144) * 4 + (lane < 56 ? 0 : OOB))));
  };
  const int t = tid >> 3, n8 = tid & 7;
  const rsrc_t O = __builtin_amdgcn_make_buffer_rsrc(a.out, 0, S_LEN * DM * 2, 0x00020000);
  const i32x4 D = mkdesc(a.wb);
  int fv = -1;
  RT(1);
  u32x4 zu[4][8];
  int fu = -1;
  const unsigned fua = isu ? faddr(0) : OOB;
  auto zuld = [&](int s) -> bool {
    unsigned spins = 0;
    while (ballot(isu && fu < s) != 0) {
      if (++spins > SPINCAP) return false;
      if (spins > 1) __builtin_amdgcn_s_sleep(16);
      if (s == NSEG - 1 && spins > 1) __builtin_amdgcn_s_sleep(100);
      CBAR();
      fu = (int)ld32(R, fua, 0);
    }
#pragma unroll
    for (int ct = 0; ct < 4; ++ct)
#pragma unroll
      for (int hh = 0; hh < 8; ++hh)
        asm volatile("buffer_load_dwordx4 %0, %1, %2, %3 offen offset:%4 sc1" : "=a"(zu[ct][hh])
                     : "v"(zb + ct * 40960 + wave * 8192 + (hh >> 2) * 4096), "s"(D), "s"((unsigned)(s & RMASK) * ZS), "n"((hh & 3) * 1024) : "memory");
    SB();
    return true;
  };
  if ((o != 44 && o != 40 && !go_wait(R, go, lane)) || !zuld(grp)) return;
  unsigned acc_t[8] = {0, 0, 0, 0, 0, 0, 0, 0}, t0 = 0, t1 = 0, t2 = 0, t3 = 0, t4 = 0, t5 = 0, t6 = 0;
  for (int s = grp; s < send; s += sst) {
    TS(t0);
    const unsigned fa = faddr(s);
    const int need = isu || fa >= OOB ? -1 : (lane == 0 ? 7 : ((lane >= 32 && s + 3 < NSEG) ? s + 2 : s));
    const bool far = lane >= 8 && lane < 48 && (isu || (lane < 32 && s < NSEG - 1));
    unsigned spins = 0;
    while ((ballot(fv < need) & 0xFFull) != 0) {
      if (++spins > SPINCAP) return;
      if (spins > 1) __builtin_amdgcn_s_sleep(32);
      CBAR();
      const int nv = (int)lda(R, fv < need && lane < 8 ? fa : OOB);
      fv = lane < 8 ? max(fv, nv) : fv;
    }
    TS(t1);
    u32x4 zy[4][2], pf[2][10], of[2][5];
    float gt[2];
#pragma unroll
    for (int qt = 0; qt < 2; ++qt) {
#pragma unroll
      for (int i = 0; i < 5; ++i) {
        const int ks = wave + 4 * i;
        of[qt][i] = ld128(R, OFF_OA + (unsigned)((qt * 18 + (ks < 18 ? ks : 17)) * 1024 + lane * 16), (unsigned)s * 36864u);
      }
      gt[qt] = __uint_as_float(ld32(R, OFF_GATE + (qt * 16 + l15) * 4, (unsigned)s * 128u));
    }
    SB();
    f32x4 aa[4][2];
#pragma unroll
    for (int ct = 0; ct < 4; ++ct)
#pragma unroll
      for (int qt = 0; qt < 2; ++qt) {
        f32x4 acc = {0.f, 0.f, 0.f, 0.f};
#pragma unroll
        for (int i = 0; i < 5; ++i) acc = mfma(wf[ct][i], of[qt][i], acc);
        aa[ct][qt] = acc * gt[qt];
        pin_v(aa[ct][qt]);
      }
    SB();
#define FWAIT(m) do { spins = 0;                                                                                          \
      while ((ballot(fv < need) & (m)) != 0) {                                                                            \
        if (++spins > SPINCAP) return;                                                                                    \
        if (spins > 1) { if (s < OFAST) __builtin_amdgcn_s_sleep(OSLP); else __builtin_amdgcn_s_sleep(OSL2); }            \
        CBAR();                                                                                                           \
        const int n0 = (int)lda(R, fv < need && !far ? fa : OOB), n1 = (int)ld32(R, fv < need && far ? fa : OOB, 0), nv = far ? n1 : n0;                                                             \
        fv = max(fv, nv);                                                                                                 \
      } } while (0)
#define ZYLD(zv) do { _Pragma("unroll") for (int hp = 0; hp < 2; ++hp) _Pragma("unroll") for (int ct = 0; ct < 4; ++ct) { \
                        zy[ct][hp] = ld128(R, (zv) + hp * 1024, (unsigned)(s & RMASK) * ZS + ct * 40960 + 32768 + wave * 2048); SB(); } } while (0)
#define MFS(h0, h1) do { _Pragma("unroll") for (int hh = h0; hh < h1; ++hh) _Pragma("unroll") for (int qt = 0; qt < 2; ++qt)                       \
                        _Pragma("unroll") for (int ct = 0; ct < 4; ++ct) {                                                                         \
                        ab[ct][qt] = mfma(hh < 8 ? zu[ct][hh & 7] : zy[ct][hh & 1], pf[qt][hh], hh ? ab[ct][qt] : (f32x4){0.f, 0.f, 0.f, 0.f}); SB(); } } while (0)
    TS(t2);
    FWAIT(s == NSEG - 1 ? 0xFFull << 32 : 0ull);
    const bool yr = (ballot(fv < need) >> 32) == 0;
    ZYLD(yr ? zb : OOB);
    FWAIT(0xFFFFFFFFull);
    TS(t3);
#pragma unroll
    for (int hh = 0; hh < 10; ++hh)
#pragma unroll
      for (int qt = 0; qt < 2; ++qt) {
        pf[qt][hh] = ld128(R, OFF_PXF + (unsigned)((qt * 40 + (hh < 8 ? wave * 8 + hh : 24 + wave * 2 + hh)) * 1024 + lane * 16), (unsigned)(s & RMASK) * 81920u);
        SB();
      }
    CBAR();
    f32x4 ab[4][2];
    MFS(0, 8);
    TS(t4);
    const int sn = s + sst < send ? s + sst : s;
    if (sn != s && !zuld(sn)) return;
    if (!yr) { FWAIT(~0ull); ZYLD(zb); }
    CBAR();
    {
      const bool fn = lane >= 8 && lane < 48 && (isu || (lane < 32 && sn < NSEG - 1));
      const int v0 = (int)lda(R, fn ? OOB : faddr(sn)), v1 = (int)ld32(R, fn ? faddr(sn) : OOB, 0);
      fv = fn ? v1 : v0;
    }
    SB();
    MFS(8, 10);
    fu = isu ? fv : fu;
    TS(t5);
#undef FWAIT
#undef ZYLD
#undef MFS
#pragma unroll
    for (int qt = 0; qt < 2; ++qt)
#pragma unroll
      for (int ct = 0; ct < 4; ++ct)
        *(lds_f32x4*)(lds + wave * 8704 + (qt * 16 + l15) * 272 + (ct * 16 + g * 4) * 4) = aa[ct][qt] + ab[ct][qt] * (1.f - gt[qt]);
    BAR();
    f32x4 b0 = *(lds_f32x4*)(lds + t * 272 + n8 * 32), b1 = *(lds_f32x4*)(lds + t * 272 + n8 * 32 + 16);
#pragma unroll
    for (int w = 1; w < 4; ++w) { b0 += *(lds_f32x4*)(lds + w * 8704 + t * 272 + n8 * 32); b1 += *(lds_f32x4*)(lds + w * 8704 + t * 272 + n8 * 32 + 16); }
    u32x4 ov = {pk2(b0[0], b0[1]), pk2(b0[2], b0[3]), pk2(b1[0], b1[1]), pk2(b1[2], b1[3])};
    __builtin_amdgcn_raw_buffer_store_b128(ov, O, (unsigned)((s * 32 + t) * DM + cb * 64 + n8 * 8) * 2, 0, OAUX);
    BAR();
    if (s < 4) RT(2);
    TS(t6); ACC(0, 4 * (t1 - t0)); ACC(1, 4 * (t2 - t1)); ACC(2, 4 * (t3 - t2)); ACC(3, 4 * (t4 - t3)); ACC(4, 4 * (t5 - t4)); ACC(5, 4 * (t6 - t5));
    STEPRT(0, 0);
  }
  for (int k = 0; k < 8; ++k) SV(16 + k, acc_t[k]);
  RT(7);
}

#define LDS_BYTES (153600 + 256)
__device__ __forceinline__ void code_touch(u64 pc, unsigned len, unsigned off, unsigned ld) {
  const i32x4 P = {(int)(unsigned)pc, (int)(unsigned)(pc >> 32), (int)len, 0x00020000};
  asm volatile("s_mov_b32 m0, %2\n\ts_nop 0\n\tbuffer_load_dword %0, %1, 0 offen lds" :: "v"(off), "s"(P), "s"(ld) : "memory");
}
__global__ void __launch_bounds__(256, 1) mega(Args a) {
  __shared__ __attribute__((aligned(16))) char smem[LDS_BYTES];
  lds_char* lds = (lds_char*)smem;
  const int b = blockIdx.x, tid = threadIdx.x, lane = tid & 63, wave = __builtin_amdgcn_readfirstlane(tid >> 6);
  const int x = b & 7, sl = b >> 3;
  const rsrc_t R = __builtin_amdgcn_make_buffer_rsrc(a.wb, 0, (int)OFF_END, 0x00020000);
  const Go go = go_look(R, b + wave * 4);
  if (b == 244) go_lead(R, tid, wave, lane);
  {
    u64 pc; unsigned len;
    asm volatile("s_getpc_b64 %0\n.Lcode_here:\n\ts_mov_b32 %1, zz_code_end-.Lcode_here" : "=s"(pc), "=s"(len));
    if (wave == CPF) code_touch(pc, len, (unsigned)(sl * 8192 + lane * 128), (unsigned)(uintptr_t)lds + 153600);
  }
  RT(0);
  if (wave == 3 && (sl == 24 || sl == 29)) {
    unsigned d[4];
#pragma unroll
    for (int k = 0; k < 3; ++k) d[k] = ld32(R, (unsigned)(k * 64 + lane) << 20, 0);
    d[3] = *(const unsigned*)(a.out + min(lane, 2) * (1 << 19));
    asm volatile("" :: "v"(d[0]), "v"(d[1]), "v"(d[2]), "v"(d[3]));
  }
  if (x < 4 && wave == 1 && sl >= 16 && sl < 24) calibrate(R, go, x + (sl & 4), sl & 3, lane);
  if (x < 4) {
    if (sl < 16) for (int k = 0; k < QDL; ++k) __builtin_amdgcn_s_sleep(32);
    if (sl < 8) role_qkc(a, R, go, lds, x * 8 + sl, tid, wave, lane);
    else if (sl < 16) role_vcu(a, R, go, lds, x * 8 + sl - 8, tid, wave, lane);
    else if (sl < 25) role_mix(a, R, go, x * 9 + sl - 16, wave, lane);
    else role_aw(a, R, go, lds, x * 8 + sl - 25, tid, wave, lane);
  } else {
    const int y = x - 4;
    if (sl < 8) {
      const int h = y * 8 + sl;
      role_prj(a, R, go, lds, a.Wk + h * 128, a.Wq + h * 128, OFF_KF + h * 8192u, OFF_QF + h * 8192u, FL_PROG + h * 128, tid, wave, lane);
    } else if (sl < 16) role_vuw(a, R, go, lds, y * 8 + sl - 8, tid, wave, lane);
    else if (sl < 20) role_px(a, R, go, y * 4 + sl - 16, wave, lane);
    else {
      if (sl == 31) role_aw(a, R, go, lds, y * 8 + 7, tid, wave, lane);
      for (int k = 0; k < XPD; ++k) __builtin_amdgcn_s_sleep(32);
      if (sl == 29) {
        const rsrc_t X = __builtin_amdgcn_make_buffer_rsrc((void*)a.x, 0, S_LEN * DM * 2, 0x00020000);
#pragma unroll 1
        for (int k = 0; k < 3; ++k) {
          unsigned t[12];
#pragma unroll
          for (int j = 0; j < 12; ++j) t[j] = __builtin_amdgcn_raw_buffer_load_b32(X, (unsigned)(((k * 12 + j) * 16 + y * 4 + wave) * 64 + lane) * 64, 0, 0);
#pragma unroll
          for (int j = 0; j < 12; ++j) asm volatile("" :: "v"(t[j]));
        }
      }
      if (sl < 31 || y == 0) { BAR(); role_out(a, R, go, lds, sl < 29 ? y * 9 + sl - 20 : (sl < 31 ? 36 + (sl - 29) * 4 + y : 44), tid, wave, lane); }
    }
  }
}

extern "C" __device__ __attribute__((noinline, used, visibility("hidden"))) void zz_code_end() {}
extern "C" void kernel_launch(void* const* d_in, const int* in_sizes, int n_in, void* d_out, int out_size, void* d_ws,
                              size_t ws_size, hipStream_t stream) {
  char* wb = (char*)(((uintptr_t)d_ws + 0x1FFFFF) & ~(uintptr_t)0x1FFFFF);
  Args a;
  a.x = (const u16*)d_in[0]; a.mem0 = (const u16*)d_in[4]; a.Wq = (const u16*)d_in[5]; a.Wk = (const u16*)d_in[6]; a.Wv = (const u16*)d_in[7];
  a.Wo = (const u16*)d_in[8]; a.wqkv = (const u16*)d_in[1]; a.wo = (const u16*)d_in[2]; a.gw = (const u16*)d_in[10]; a.out = (u16*)d_out; a.wb = wb;
  mega<<<256, 256, 0, stream>>>(a);
}
```

```cpp
#define S1 26
#define DEA 84
#define KA_ 68
#define S2 30
#define DEC 80
#define KC_ 64
#define DE 82
#define K2 66
#define T1D 140
#define MSL 2
#define XNT ""
#define UEF 1
#define OSW 28
#define HLP 1
#define HF0 9
#define HF1 4
#define HF2 0
#define HF3 4
#define AWT 700
#define XPT 760
#define HSH 0
#define NOCW 0
#define EFIRST
#define CSLP 1
#define RTOL 2
#define QDL 0
#define LTOL 16
#define GRID 48
#define XT 6
#define NOAT
#define AWD 8
#define XPD 8
#define NTM 15
#define FIXL
#define NSC 8
#define WRM 1
#define VED 0
#define NOYC 0
#define KEF 3
#define DE0 100
#define DELTA0 172
#include <hip/hip_runtime.h>
#include <stdint.h>
#include <type_traits>

typedef unsigned short u16;
typedef __attribute__((ext_vector_type(2))) __bf16 bf16x2;
typedef __attribute__((ext_vector_type(4))) __bf16 bf16x4;
typedef __attribute__((ext_vector_type(8))) __bf16 bf16x8;
typedef __attribute__((ext_vector_type(16))) __bf16 bf16x16;
typedef __attribute__((ext_vector_type(2))) float f32x2;
typedef __attribute__((ext_vector_type(4))) float f32x4;
typedef __attribute__((ext_vector_type(2))) unsigned u32x2;
typedef __attribute__((ext_vector_type(4))) unsigned u32x4;
typedef __attribute__((ext_vector_type(8))) unsigned u32x8;
typedef __attribute__((ext_vector_type(4))) short s16x4;
typedef __attribute__((address_space(3))) s16x4 lds_s16x4;
typedef __attribute__((address_space(3))) u32x4 lds_u32x4;
typedef __attribute__((address_space(3))) f32x4 lds_f32x4;
typedef __attribute__((address_space(3))) unsigned lds_u32;
typedef __attribute__((address_space(3))) u32x2 lds_u32x2;
typedef __attribute__((address_space(3))) char lds_char;

#define S_LEN 2048
#define DM 576
#define NSEG 64
#define HD 4096

#define SLOT_REAL  131072
#define NEAR_PM    (8192 + 1024)
#define NEAR_AL    (8192 + 5120)
#define NEAR_E     16384
#define OFF_FLAGS  0x100000
#define FL_FLIP    (OFF_FLAGS + 0)
#define FL_WP      (OFF_FLAGS + 16384)
#define FL_AN      (OFF_FLAGS + 18432)
#define FL_MRDY    (OFF_FLAGS + 2048)
#define FL_MPOI    (OFF_FLAGS + 2304)
#define FL_PROG    (OFF_FLAGS + 4096)
#define FL_UPROG   (OFF_FLAGS + 8192)
#define FL_KMPROG  (OFF_FLAGS + 12288)
#define FL_YPROG   (OFF_FLAGS + 20480)
#define FL_PXPROG  (OFF_FLAGS + 24576)
#define FL_APROG   (OFF_FLAGS + 40960)
#define FL_PXLAST  (OFF_FLAGS + 32768)
#define FL_ACK     (OFF_FLAGS + 28672)
#define FLAG_BYTES 0xC000
#define FL_GO      (OFF_FLAGS + FLAG_BYTES)
#define NACK       945
#define REAL_CAL   0x140000
#define OFF_YR     0x200000
#define YSLOT_REAL 0x100000
#define CPY1       16384
#define CPY2       0x80000
#define OFF_KF   (4u << 20)
#define OFF_QF   (OFF_KF + (16u << 20))
#define OFF_KMF  (OFF_QF + (16u << 20))
#define OFF_PXF  (OFF_KMF + (8u << 20))
#define OFF_ZF   (OFF_PXF + (6u << 20))
#define ZS       (36u * 40960u)
#define OFF_OA   (OFF_ZF + (91u << 20))
#define OFF_GATE (OFF_OA + (3u << 20))
#define OFF_END  (OFF_GATE + (1u << 20))

#define RMASK 63
#define SPINCAP (1u << 19)
#define SC1 16

#define BAR() do { asm volatile("s_waitcnt lgkmcnt(0)" ::: "memory"); __builtin_amdgcn_s_barrier(); asm volatile("" ::: "memory"); } while (0)
#define VMW(n) asm volatile("s_waitcnt vmcnt(" #n ")" ::: "memory")
#define CBAR() asm volatile("" ::: "memory")
#define PADA
#define PADN "s_nop 0\n\t"
#define PADM
#define PADZ
#define PAD()
#define PADS(id)
#define PADE()
#define WALK(id)

#define TKD
#define TK(i)
#define TKS
#define NOSTEP 0
#define OFF_STAMP (200u << 20)
#define TS(v)
#define RT(i)
#define SV(i, v)
#define ACC(k, d)
#define STEPRT(j, f)
#define EVASM
#define EV(v)
#define EVK(v)
#define EVPUT(j, v)
#define EVPUTI(j, v)
#define EVDECL
#define EVDUMP()
#define CKDECL
#define CKS()
#define CKDUMP()
typedef unsigned long long u64;
struct Args {
  const u16* x; const u16* mem0; const u16* Wq; const u16* Wk; const u16* Wv; const u16* Wo;
  const u16* wqkv; const u16* wo; const u16* gw; u16* out; char* wb;
};

__device__ __forceinline__ float bf2f(u16 v) { return __uint_as_float(((unsigned)v) << 16); }
__device__ __forceinline__ unsigned pk2(float a, float b) {
  f32x2 v = {a, b};
  bf16x2 r = __builtin_convertvector(v, bf16x2);
  return __builtin_bit_cast(unsigned, r);
}
__device__ __forceinline__ int nearoff(int o) { return ((o >> 13) << 15) + (o & 8191); }
__device__ __forceinline__ unsigned own_cpy(int cls, int x) { return ((cls >> (2 * x)) & 2) ? CPY1 : 0u; }
__device__ __forceinline__ unsigned ywin(int cls, int m) {
  const int x = m / 9, row = (x >> 1) * 9 + m - 9 * x;
  const int se = (cls >> (4 * (x >> 1) + 1)) & 1, so = (cls >> (4 * (x >> 1) + 3)) & 1, sd = se == so ? x & 1 : ((x & 1) ? so : se);
  return (unsigned)(OFF_YR + row * 32768 + ((cls >> 8) & 1) * 8192 + ((sd ^ (cls >> 1) ^ (cls >> 9) ^ (row >> 4)) & 1) * 16384);
}
__device__ __forceinline__ u32x2 trd(lds_char* p) {
  s16x4 a = __builtin_amdgcn_ds_read_tr16_b64_v4i16((lds_s16x4*)p);
  return __builtin_bit_cast(u32x2, a);
}
__device__ __forceinline__ f32x4 mfma(u32x4 a, u32x4 b, f32x4 c) {
  return __builtin_amdgcn_mfma_f32_16x16x32_bf16(__builtin_bit_cast(bf16x8, a), __builtin_bit_cast(bf16x8, b), c, 0, 0, 0);
}
__device__ __forceinline__ f32x4 mfv0(u32x4 a, u32x4 b) {
  f32x4 d; asm volatile("s_nop 1\n\tv_mfma_f32_16x16x32_bf16 %0, %1, %2, 0" : "=&v"(d) : "v"(a), "v"(b)); return d;
}
__device__ __forceinline__ f32x4 mfvc(u32x4 a, u32x4 b, f32x4 c) {
  f32x4 d; asm volatile("s_nop 1\n\tv_mfma_f32_16x16x32_bf16 %0, %1, %2, %3" : "=&v"(d) : "v"(a), "v"(b), "v"(c)); return d;
}
__device__ __forceinline__ void mfva(f32x4& d, u32x4 a, u32x4 b) {
  asm volatile("s_nop 1\n\tv_mfma_f32_16x16x32_bf16 %0, %1, %2, %0" : "+v"(d) : "v"(a), "v"(b));
}
__device__ __forceinline__ void mf_wait(f32x4& d) { asm volatile("s_nop 7" : "+v"(d)); }
__device__ __forceinline__ void mf_wait2(f32x4& d, f32x4& e) { asm volatile("s_nop 7" : "+v"(d), "+v"(e)); }
__device__ __forceinline__ f32x4 smf(u32x4 a, u32x8 b, f32x4 c, int idx) {
  return __builtin_amdgcn_smfmac_f32_16x16x64_bf16(__builtin_bit_cast(bf16x8, a), __builtin_bit_cast(bf16x16, b), c, idx, 0, 0);
}
__device__ __forceinline__ float xmax(float x) {
  auto s = __builtin_amdgcn_permlane16_swap(__float_as_uint(x), __float_as_uint(x), false, false);
  x = fmaxf(__uint_as_float(s[0]), __uint_as_float(s[1]));
  auto t = __builtin_amdgcn_permlane32_swap(__float_as_uint(x), __float_as_uint(x), false, false);
  return fmaxf(__uint_as_float(t[0]), __uint_as_float(t[1]));
}
__device__ __forceinline__ float xsum(float x) {
  auto s = __builtin_amdgcn_permlane16_swap(__float_as_uint(x), __float_as_uint(x), false, false);
  x = __uint_as_float(s[0]) + __uint_as_float(s[1]);
  auto t = __builtin_amdgcn_permlane32_swap(__float_as_uint(x), __float_as_uint(x), false, false);
  return __uint_as_float(t[0]) + __uint_as_float(t[1]);
}
__device__ __forceinline__ f32x4 halfadd(f32x4 a, f32x4 b) {
  f32x4 o;
#pragma unroll
  for (int r = 0; r < 4; ++r) {
    auto s = __builtin_amdgcn_permlane32_swap(__float_as_uint(a[r]), __float_as_uint(b[r]), false, false);
    o[r] = __uint_as_float(s[0]) + __uint_as_float(s[1]);
  }
  return o;
}
template <class T> __device__ __forceinline__ void pin_a(T& v) { asm volatile("" : "+a"(v)); }
typedef __amdgpu_buffer_rsrc_t rsrc_t;
__device__ __forceinline__ u32x4 ld128(rsrc_t R, unsigned vo, unsigned so) { return __builtin_amdgcn_raw_buffer_load_b128(R, vo, so, SC1); }
__device__ __forceinline__ unsigned ld32(rsrc_t R, unsigned vo, unsigned so) { return __builtin_amdgcn_raw_buffer_load_b32(R, vo, so, SC1); }
__device__ __forceinline__ void st128(u32x4 v, rsrc_t R, unsigned vo, unsigned so) { __builtin_amdgcn_raw_buffer_store_b128(v, R, vo, so, SC1); }
__device__ __forceinline__ void st32(unsigned v, rsrc_t R, unsigned vo, unsigned so) { __builtin_amdgcn_raw_buffer_store_b32(v, R, vo, so, SC1); }
#define OOB 0x7ff00000
__device__ __forceinline__ unsigned lda(rsrc_t R, unsigned vo) { return ld32(R, vo, 0); }

struct Go { unsigned vo, v0; };
__device__ __forceinline__ Go go_look(rsrc_t R, int b) {
  Go g; g.vo = FL_GO + (b & 15) * 128; g.v0 = ld32(R, g.vo, 0); return g;
}
__device__ __forceinline__ void go_ack(rsrc_t R, const Go& g, int lane) {
  __builtin_amdgcn_raw_ptr_buffer_atomic_add_i32(1, R, lane == 0 ? g.vo - FL_GO + FL_ACK : OOB, 0, SC1);
}
__device__ __forceinline__ bool go_wait(rsrc_t R, const Go& g, int lane) {
  for (unsigned spins = 0; spins < SPINCAP; ++spins) {
    CBAR();
    if (__all(ld32(R, g.vo, 0) != g.v0)) {
      go_ack(R, g, lane);
      return true;
    }
    __builtin_amdgcn_s_sleep(6);
  }
  return false;
}
__device__ __forceinline__ void go_lead(rsrc_t R, int tid, int wave, int lane) {
#pragma unroll
  for (int k = 0; k < FLAG_BYTES / 4096; ++k) st128((u32x4){~0u, ~0u, ~0u, ~0u}, R, OFF_FLAGS + k * 4096 + tid * 16, 0);
  VMW(0);
  BAR();
  if (wave != 0) return;
  for (unsigned k = 0; k < SPINCAP; ++k) {
    st32(k, R, lane < 16 ? FL_GO + lane * 128 : OOB, 0);
    int n = lane < 16 ? (int)ld32(R, FL_ACK + lane * 128, 0) + 1 : 0;
#pragma unroll
    for (int d = 1; d < 16; d <<= 1) n += __shfl_xor(n, d);
    if (__builtin_amdgcn_readfirstlane(n) >= NACK) break;
    __builtin_amdgcn_s_sleep(4);
  }
}
__device__ __forceinline__ bool wait_flag(rsrc_t R, unsigned vo, int need) {
  for (unsigned spins = 0; spins < SPINCAP; ++spins) {
    CBAR();
    int v = (int)ld32(R, vo, 0);
    if (__all(v >= need)) return true;
    __builtin_amdgcn_s_sleep(8);
  }
  return false;
}

template <int SLP, int AT = 0>
__device__ __forceinline__ bool wait_flag_c(rsrc_t R, unsigned vo, int need, int& cache) {
  if (__all(cache >= need)) return true;
  for (unsigned spins = 0; spins < SPINCAP; ++spins) {
    CBAR();
    cache = (int)(AT ? lda(R, vo) : ld32(R, vo, 0));
    if (__all(cache >= need)) return true;
    __builtin_amdgcn_s_sleep(SLP);
  }
  return false;
}
__device__ __forceinline__ unsigned umax3(unsigned a, unsigned b, unsigned c) { return max(max(a, b), c); }

template <int NTB> __device__ __forceinline__ u32x4 gld(const u16* p) {
  if (NTB) return __builtin_nontemporal_load((const u32x4*)p);
  return *(const u32x4*)p;
}
#define XSTR2(x) #x
#define XSTR(x) XSTR2(x)
#define LSLP 3
#define ESLP 0
#define MSLP 0
#define PXS 6
#define TSLP 2
#define XSLP 2
#define KSLP 10
#define OSLP 24
#define OSL2 4
#define UE 1
#define YL
#define CPF 3
#define PARK 58
#define OFAST 60
#define CPS 8
#define CSL 2
#define TSL 6
#define UGO 5
#define UPO 1
#define CALN 4
#define QDEP 4
#define PDEP 3
#define WDEP 3
#define VSLP 2
#define QLN 24
#define VLN 8
#define PLN 32
#define LMASK(n) ((n) >= 64 ? ~0ull : ((1ull << ((n) & 63)) - 1))
#define UP3 0
#define ORDT 0x73652410u
#define USLP 8
#define UT 0
#define UTD 1
#define EMF 0
#define EMV 0
#define MWPOS 1
#define DSLP "s_sleep 1\n\t"
typedef __attribute__((ext_vector_type(4))) int i32x4;
__device__ __forceinline__ u64 ballot(bool c) { return __builtin_amdgcn_ballot_w64(c); }
#define UNLIKELY(c) __builtin_expect(!!(c), 0)
#define DELTA 164
#define D1 98
#define DVQ 4
#define LEV0 1
#define LEV3 1
#define LEVMAX 7
#define CL0 8
#define HEAT 3
#define COOL0 4
#define PSLP 8
#define VK0 64
#define AWE 0
#define ASLP 0
#define LEVF 99
#define RX 0
#define VSL 6
#define D0 14
#define WLAST 41
#define UDL 8
#define MGAP 0
#define SMIN 8
#define K1 4
#define PAD0 4
#define PADD 4
#define PADMAX 16
#define OAUX 0
__device__ __forceinline__ unsigned rt_now() {
  u64 t; asm volatile("s_memrealtime %0\n\ts_waitcnt lgkmcnt(0)" : "=s"(t) :: "memory"); return (unsigned)t;
}
#define S0 3
#define PACE0 ((DE0 << 8) | DELTA0)
#define PACEA ((DEA << 8) | (DEA + KA_))
#define PACEB ((DE << 8) | (DE + K2))
#define PACEC ((DEC << 8) | (DEC + KC_))
__device__ __forceinline__ void next_time(unsigned& t, int s) {
  t += (s < S0 ? PACE0 : (s < S1 ? PACEA : (s < S2 ? PACEB : PACEC))) & 255u;
  t = __builtin_amdgcn_readfirstlane(t);
  asm volatile("" : "+s"(t));
}
#define SNOP 1
#define SPIN(T, a, b, c) a ":\n\ts_memrealtime vcc\n\ts_waitcnt lgkmcnt(0)\n\ts_sub_u32 %[r], %[" #T "], vcc_lo\n\ts_sub_u32 %[k], %[r], 4\n\t"   \
               "s_cmp_lt_u32 %[k], 2000\n\ts_cbranch_scc1 " a "b\n\t"                                                                       \
               "s_sub_u32 %[k], %[r], 1\n\ts_cmp_lt_u32 %[k], 3\n\ts_cbranch_scc0 " c "f\n\t"                                               \
               b ":\n\ts_nop " XSTR(SNOP) "\n\ts_sub_u32 %[k], %[k], 1\n\ts_cbranch_scc0 " b "b\n\t" c ":\n\t"
__device__ __forceinline__ int spin_until(unsigned T) {
  int r, k;
  asm volatile(SPIN(T, "1", "2", "3") : [r] "=&s"(r), [k] "=&s"(k) : [T] "s"(T) : "memory", "vcc", "scc");
  return r;
}
#define KA "v_mov_b32 v240, v240\n\ts_nop 2\n\t"
#define KAN KA KA
#define EVA(n)
#define EVC(k)
#define BUD(l) "s_sub_u32 %[bud], %[bud], 1\n\ts_cbranch_scc0 " l "\n\ts_mov_b32 %[bud], 0\n\t"
__device__ __forceinline__ i32x4 mkdesc(char* wb) {
  const u64 p = (u64)wb;
  return (i32x4){(int)(unsigned)p, (int)(unsigned)(p >> 32), (int)OFF_END, 0x00020000};
}
template <class T> __device__ __forceinline__ void pin_v(T& v) { asm volatile("" : "+v"(v)); }
#define SB() __builtin_amdgcn_sched_barrier(0)
__device__ __forceinline__ unsigned pkv(float a, float b) {
  unsigned d; asm volatile("v_cvt_pk_bf16_f32 %0, %1, %2" : "=v"(d) : "v"(a), "v"(b)); return d;
}
__device__ __forceinline__ f32x4 mfB0(u32x4 a, u32x4 b) {
  f32x4 d; asm volatile("v_mfma_f32_16x16x32_bf16 %0, %1, %2, 0" : "=&v"(d) : "v"(a), "a"(b)); return d;
}
__device__ __forceinline__ void mfB(f32x4& d, u32x4 a, u32x4 b) {
  asm volatile("v_mfma_f32_16x16x32_bf16 %0, %1, %2, %0" : "+v"(d) : "v"(a), "a"(b));
}

template <int NTB>
__device__ __forceinline__ void qk_gload(u32x4 (&st)[8], const Args& a, int h, int c, int tid) {
#pragma unroll
  for (int k = 0; k < 8; ++k) {
    int u = tid + 256 * k; int row = u >> 5, cu = u & 31;
    const u16* W = (cu < 16) ? a.Wq : a.Wk;
    st[k] = gld<NTB>(W + (((size_t)(64 * c + row) * HD + h * 128 + (cu & 15) * 8) & ((NOCW & 1) ? 2047 : ~0ull)));
  }
}
__device__ __forceinline__ void qk_swrite(const u32x4 (&st)[8], lds_char* buf, int tid) {
#pragma unroll
  for (int k = 0; k < 8; ++k) { int u = tid + 256 * k; int row = u >> 5, cu = u & 31; *(lds_u32x4*)(buf + row * 544 + cu * 16) = st[k]; }
}
#define STG_BYTES 38912
#define X0_OFF (2 * STG_BYTES)
#define X1_OFF (X0_OFF + 37888)

__device__ __forceinline__ int cls_look(rsrc_t R, int lane) { return (int)ld32(R, FL_FLIP + (lane & 31) * 4, 0); }
__device__ __forceinline__ int rowsum(int v) {
  v += __builtin_amdgcn_update_dpp(v, v, 0x128, 0xf, 0xf, false);
  v += __builtin_amdgcn_update_dpp(v, v, 0x124, 0xf, 0xf, false);
  v += __builtin_amdgcn_update_dpp(v, v, 0x122, 0xf, 0xf, false);
  return v + __builtin_amdgcn_update_dpp(v, v, 0x121, 0xf, 0xf, false);
}
__device__ __forceinline__ int get_cls(rsrc_t R, int lane, int v = -1) {
  for (unsigned spins = 0; spins < SPINCAP; ++spins) {
    if (__all(v >= 0)) {
      const int n = rowsum((lane & 1) ? -v : v) > 0;
      const int d = rowsum((lane & 1) != n ? 0 : (((lane >> 1) ^ (lane >> 2)) & 1 ? -v : v)) > 0;
      const int c = n | (d << 1);
      return (__builtin_amdgcn_readlane(c, 0) * 0x55 ^ 0x88) | (__builtin_amdgcn_readlane(c, 16) << 8);
    }
    if (spins) __builtin_amdgcn_s_sleep(CPS);
    CBAR();
    v = cls_look(R, lane);
  }
  return -1;
}
__device__ __forceinline__ void calibrate(rsrc_t R, const Go& go, int r, int k, int lane) {
  unsigned best = 0xFFFFFFu;
  for (int it = 0; it < CALN; ++it) {
    unsigned long long t0, t1;
    asm volatile("s_waitcnt vmcnt(0)\n\ts_memtime %0\n\ts_waitcnt lgkmcnt(0)" : "=s"(t0) :: "memory");
    st32((unsigned)it, R, (lane == 0) ? (unsigned)((r >> 2) * OFF_YR + REAL_CAL + (r & 3) * 256 + k * 8192 + (it & 1) * 4096) : OOB, 0);
    asm volatile("s_waitcnt vmcnt(0)\n\ts_memtime %0\n\ts_waitcnt lgkmcnt(0)" : "=s"(t1) :: "memory");
    best = min(best, (unsigned)(t1 - t0));
  }
  if (go_wait(R, go, lane)) st32(best, R, lane == 0 ? (unsigned)(FL_FLIP + (r * 4 + k) * 4) : OOB, 0);
}
__device__ __forceinline__ unsigned get_t1(rsrc_t R, unsigned vo, unsigned& bud) {
  unsigned v = 0;
#pragma nounroll
  for (; bud != 0; --bud) {
    CBAR();
    v = __builtin_amdgcn_readfirstlane(ld32(R, vo, 0));
    if (v != ~0u) break;
    __builtin_amdgcn_s_sleep(2);
  }
  return v;
}

#define F8_OFF 18432
#define QSTG 34816
#define ISOT()
#define ISOQ(n)
__device__ __forceinline__ float vmax(float a, float b) { float d; asm("v_max_f32 %0, %1, %2" : "=v"(d) : "v"(a), "v"(b)); return d; }
__device__ __forceinline__ float vmax3(float a, float b, float c) { float d; asm("v_max3_f32 %0, %1, %2, %3" : "=v"(d) : "v"(a), "v"(b), "v"(c)); return d; }
__device__ __forceinline__ float max32(float x) {
  auto t = __builtin_amdgcn_permlane32_swap(__float_as_uint(x), __float_as_uint(x), false, false);
  return vmax(__uint_as_float(t[0]), __uint_as_float(t[1]));
}
__device__ __forceinline__ float max16(float x) {
  auto t = __builtin_amdgcn_permlane16_swap(__float_as_uint(x), __float_as_uint(x), false, false);
  return vmax(__uint_as_float(t[0]), __uint_as_float(t[1]));
}
__device__ __forceinline__ float sum32(float x) {
  auto t = __builtin_amdgcn_permlane32_swap(__float_as_uint(x), __float_as_uint(x), false, false);
  return __uint_as_float(t[0]) + __uint_as_float(t[1]);
}
template <int r> __device__ __forceinline__ void swp32(f32x4& a, f32x4& b) {
  auto t = __builtin_amdgcn_permlane32_swap(__float_as_uint(a[r]), __float_as_uint(b[r]), false, false);
  a[r] = __uint_as_float(t[0]); b[r] = __uint_as_float(t[1]);
}
__device__ __forceinline__ float vadd(float a, float b) { float d; asm("v_add_f32 %0, %1, %2" : "=v"(d) : "v"(a), "v"(b)); return d; }
__device__ __forceinline__ float max8(float x) {
  float d; asm("s_nop 1\n\tv_max_f32_dpp %0, %1, %1 row_ror:8 row_mask:0xf bank_mask:0xf" : "=&v"(d) : "v"(x)); return d;
}
__device__ __forceinline__ float sum8(float x) {
  float d; asm("s_nop 1\n\tv_add_f32_dpp %0, %1, %1 row_ror:8 row_mask:0xf bank_mask:0xf" : "=&v"(d) : "v"(x)); return d;
}
__device__ __forceinline__ float sum16(float x) {
  auto t = __builtin_amdgcn_permlane16_swap(__float_as_uint(x), __float_as_uint(x), false, false);
  return __uint_as_float(t[0]) + __uint_as_float(t[1]);
}
__device__ __forceinline__ void st64(u32x2 v, rsrc_t R, unsigned vo, unsigned so) { __builtin_amdgcn_raw_buffer_store_b64(v, R, vo, so, SC1); }
template <int N>
__device__ __forceinline__ void mem_issue2(i32x4 D, unsigned m0, unsigned m1, unsigned m2, unsigned so, unsigned l0, unsigned l1, unsigned l2, u64& ew) {
  asm volatile("s_mov_b32 m0, %[l0]\n\ts_barrier\n\t" EVASM
               "buffer_load_dwordx4 %[m0], %[D], %[so] offen sc1 lds\n\t"
               "s_mov_b32 m0, %[l1]\n\ts_nop 0\n\t"
               "buffer_load_dwordx4 %[m1], %[D], %[so] offen sc1 lds\n\t"
               ".if %[n] == 3\n\ts_mov_b32 m0, %[l2]\n\ts_nop 0\n\t"
               "buffer_load_dwordx4 %[m2], %[D], %[so] offen sc1 lds\n\t.endif"
               : [ew] "=&s"(ew)
               : [m0] "v"(m0), [m1] "v"(m1), [m2] "v"(m2), [D] "s"(D), [so] "s"(so), [l0] "s"(l0), [l1] "s"(l1), [l2] "s"(l2), [n] "n"(N)
               : "memory");
}
#define TRYB ".if %[id] >= 0\n\ts_cbranch_vccnz .Ltry%[id]\n.Lend%[id]:\n\t.endif\n\t"
#define TRYC "vcc", "scc", "v236", "v237", "v238", "v239", "v240"
template <int ID>
__device__ __forceinline__ void try_top(unsigned& nt, unsigned& ms, unsigned& tm, unsigned& rcnt, unsigned ia) {
  asm volatile(".Ltry%[id]:\n\ts_waitcnt lgkmcnt(0)\n\ts_cmp_ge_i32 %[nt], " XSTR(NSC) "\n\ts_cbranch_scc1 1f\n"
               "2:\n\ts_nop 4\n\ts_xor_b32 %[ms], %[ms], %[fl]\n\ts_add_u32 %[tm], %[tm], " XSTR(GRID) "+" XSTR(XT) "\n\ts_add_u32 %[nt], %[nt], 1\n\t.subsection 1\n"
               "1:\n\tv_mov_b32 v240, 0\n\t"
               ".irp o, 0, 1024, 2048, 3072, 4096, 5120, 6144, 7168, " XSTR(F8_OFF) "\n\tds_read_b128 v[236:239], %[ia] offset:\\o\n\ts_waitcnt lgkmcnt(0)\n\t"
               "v_max3_u32 v240, v240, v236, v237\n\tv_max3_u32 v240, v240, v238, v239\n\t.endr\n\t"
               "v_cmp_eq_u32 vcc, -1, v240\n\ts_cbranch_vccz .Lend%[id]\n\ts_add_u32 %[rc], %[rc], 1\n\ts_cmp_lt_u32 %[rc], %[cap]\n\t"
               "s_cbranch_scc1 2b\n\ts_branch .Lend%[id]\n\t.subsection 0"
               : [nt] "+s"(nt), [ms] "+s"(ms), [tm] "+s"(tm), [rc] "+s"(rcnt)
               : [fl] "n"(CPY2 + CPY1), [id] "n"(ID), [ia] "v"(ia), [cap] "n"(SPINCAP / 2) : "memory", TRYC);
}
__device__ __forceinline__ void try_end(unsigned ia, unsigned nt, unsigned ms, unsigned tm, unsigned rcnt,
                                        unsigned m0, unsigned m1, unsigned m2, unsigned l0, unsigned l1, unsigned l2, i32x4 D) {
  asm volatile("" :: "s"(rcnt), "s"(nt), "s"(ms), "s"(tm), "v"(ia), "v"(m0), "v"(m1), "v"(m2), "s"(l0), "s"(l1), "s"(l2), "s"(D) : "memory");
}
__device__ __forceinline__ int spin_late(unsigned& t, unsigned d) {
  int r, k; unsigned T;
  asm volatile("s_add_u32 %[T], %[t], %[d]\n\t" SPIN(T, "1", "2", "3") : [r] "=&s"(r), [k] "=&s"(k), [T] "=&s"(T), [t] "+s"(t) : [d] "n"(d) : "memory", "vcc", "scc");
  return r;
}
__device__ __forceinline__ void move_on(unsigned& t, int r) {
  asm volatile("1:\n\ts_cmp_lt_i32 %1, -" XSTR(LTOL) "\n\ts_cbranch_scc0 2f\n\ts_add_u32 %0, %0, " XSTR(GRID) "+" XSTR(XT) "\n\ts_add_u32 %1, %1, " XSTR(GRID) "+" XSTR(XT) "\n\ts_branch 1b\n2:"
               : "+s"(t), "+s"(r) :: "scc");
}
__device__ __forceinline__ void gate_wait(rsrc_t R, unsigned vo, int need, int& cache, unsigned& bud) {
#pragma nounroll
  for (; bud != 0; --bud) {
    if (__all(cache >= need)) break;
    CBAR();
    cache = (int)ld32(R, vo, 0);
    __builtin_amdgcn_s_sleep(4);
  }
}
__device__ __forceinline__ unsigned um3v(unsigned a, unsigned b, unsigned c) {
  unsigned d; asm volatile("v_max3_u32 %0, %1, %2, %3" : "=v"(d) : "v"(a), "v"(b), "v"(c)); return d;
}
template <int OFF>
__device__ __forceinline__ void k_put(f32x4 k, unsigned ad) {
  unsigned t0, t1;
  asm volatile("v_cvt_pk_bf16_f32 %0, %2, %3\n\tv_cvt_pk_bf16_f32 %1, %4, %5\n\tds_write2_b32 %6, %0, %1 offset0:%7 offset1:%8"
               : "=&v"(t0), "=&v"(t1) : "v"(k[0]), "v"(k[1]), "v"(k[2]), "v"(k[3]), "v"(ad), "n"(OFF), "n"(OFF + 2) : "memory");
}
template <int OFF>
__device__ __forceinline__ void q_put(f32x4 k, unsigned ad) {
  unsigned t0, t1;
  asm volatile("v_cvt_pk_bf16_f32 %0, %2, %3\n\tv_cvt_pk_bf16_f32 %1, %4, %5\n\tds_write2_b32 %6, %0, %1 offset0:%7 offset1:%8"
               : "=&v"(t0), "=&v"(t1) : "v"(k[0]), "v"(k[1]), "v"(k[2]), "v"(k[3]), "v"(ad), "n"(OFF), "n"(OFF + 1) : "memory");
}
__device__ __forceinline__ f32x4 mf0(u32x4 a, u32x4 b) {
  f32x4 d; asm volatile("v_mfma_f32_16x16x32_bf16 %0, %1, %2, 0" : "=&v"(d) : "v"(a), "v"(b)); return d;
}
__device__ __forceinline__ void mfa(f32x4& d, u32x4 a, u32x4 b) {
  asm volatile("v_mfma_f32_16x16x32_bf16 %0, %1, %2, %0" : "+v"(d) : "v"(a), "v"(b));
}
__device__ __forceinline__ f32x4 mfA0(u32x4 a, u32x4 b) {
  f32x4 d; asm volatile("v_mfma_f32_16x16x32_bf16 %0, %1, %2, 0" : "=&v"(d) : "a"(a), "v"(b)); return d;
}
__device__ __forceinline__ void mfA(f32x4& d, u32x4 a, u32x4 b) {
  asm volatile("v_mfma_f32_16x16x32_bf16 %0, %1, %2, %0" : "+v"(d) : "a"(a), "v"(b));
}
#define DPPLO " row_ror:8 row_mask:0xf bank_mask:0x3\n\t"
#define DPPHI " row_ror:8 row_mask:0xf bank_mask:0xc\n\t"
#define DPPAL " row_ror:8 row_mask:0xf bank_mask:0xf\n\t"
#define SCK "0x3e0293ee"
#define SCN "0xbe0293ee"
__device__ __forceinline__ void px_mask(f32x4& T0, f32x4& T1, int qt, int lane) {
  const float ninf = -__builtin_inff();
#pragma unroll
  for (int r = 0; r < 4; ++r) {
    const bool diag = (4 * (lane >> 4) + r) <= (lane & 15);
    T0[r] = (qt == 1 || diag) ? T0[r] : ninf;
    T1[r] = (qt == 1 && diag) ? T1[r] : ninf;
  }
}
__device__ __forceinline__ void px_seg(f32x4& T0, f32x4& T1, float& mx, float& sx) {
  const float sc = 0.08838834764831845f * 1.4426950408889634f;
  mx = xmax(fmaxf(fmaxf(fmaxf(T0[0], T0[1]), fmaxf(T0[2], T0[3])), fmaxf(fmaxf(T1[0], T1[1]), fmaxf(T1[2], T1[3]))));
  const float ms = -mx * sc;
  sx = 0.f;
#pragma unroll
  for (int r = 0; r < 4; ++r) {
    T0[r] = __builtin_amdgcn_exp2f(fmaf(T0[r], sc, ms)); T1[r] = __builtin_amdgcn_exp2f(fmaf(T1[r], sc, ms));
    sx += T0[r] + T1[r];
  }
}
__device__ __forceinline__ void px_fin(f32x4 G, f32x4 T0, f32x4 T1, float mx, float sx, u32x4& px, unsigned& pw, int lane) {
  const float sc = 0.08838834764831845f * 1.4426950408889634f;
  float g0 = (lane & 16) ? G[2] : G[0], g1 = (lane & 16) ? G[3] : G[1];
  const float ms = -xmax(fmaxf(fmaxf(g0, g1), mx)) * sc;
  g0 = __builtin_amdgcn_exp2f(fmaf(g0, sc, ms)); g1 = __builtin_amdgcn_exp2f(fmaf(g1, sc, ms));
  const float f = __builtin_amdgcn_exp2f(fmaf(mx, sc, ms));
  asm volatile("" : "+v"(g0), "+v"(g1)); PAD();
  const float inv = __builtin_amdgcn_rcpf(xsum(fmaf(f, sx, g0 + g1))), fi = f * inv;
  px = (u32x4){pk2(T0[0] * fi, T0[1] * fi), pk2(T0[2] * fi, T0[3] * fi), pk2(T1[0] * fi, T1[1] * fi), pk2(T1[2] * fi, T1[3] * fi)};
  pw = pk2(g0 * inv, g1 * inv);
}
#define WPOS 4
#define QPOS 1
#define LPOS 1
#define QIM_OFF 8192
#define KIM_OFF 10240
#define FLG_OFF 17408
#define F8W 3
#define KX_OFF 36864
typedef __attribute__((ext_vector_type(16))) float f32x16;
typedef __attribute__((ext_vector_type(16))) unsigned u32x16;
__device__ __forceinline__ void ds_flag(unsigned ad, unsigned v) { asm volatile("ds_write_b32 %0, %1" :: "v"(ad), "v"(v) : "memory"); }
#define SMX(d, j, w) "v_smfmac_f32_16x16x64_bf16 v[" d "], %[a" #j "], %[" #w "], %[ix]\n\t"
#define CVX(d, x, y) "v_cvt_pk_bf16_f32 v" #d ", v" #x ", v" #y "\n\t"
#define PKA(d, b) "v_pk_add_f32 v[" #d ":" #d "+1], v[" #b ":" #b "+1], v[" #b "+2:" #b "+3]\n\t"
#define IMW(a, o) "ds_write_b32 " a ", v232 offset:" o "\n\t"
template <int T, int AG>
__device__ __forceinline__ void smo(f32x4& c, u32x4 a, u32x8 b, int ix) {
  if (T == 0 && AG) asm volatile(SMX("140:143", 0, w) : "+{v[140:143]}"(c) : [a0] "v"(a), [w] "a"(b), [ix] "v"(ix));
  if (T == 0 && !AG) asm volatile(SMX("140:143", 0, w) : "+{v[140:143]}"(c) : [a0] "v"(a), [w] "v"(b), [ix] "v"(ix));
  if (T == 1 && AG) asm volatile(SMX("144:147", 0, w) : "+{v[144:147]}"(c) : [a0] "v"(a), [w] "a"(b), [ix] "v"(ix));
  if (T == 1 && !AG) asm volatile(SMX("144:147", 0, w) : "+{v[144:147]}"(c) : [a0] "v"(a), [w] "v"(b), [ix] "v"(ix));
}
__device__ __forceinline__ void v_zero(f32x4& v0, f32x4& v1) {
  asm volatile("v_mov_b64 v[140:141], 0\n\tv_mov_b64 v[142:143], 0\n\tv_mov_b64 v[144:145], 0\n\tv_mov_b64 v[146:147], 0\n\ts_nop 1"
               : "={v[140:143]}"(v0), "={v[144:147]}"(v1));
}
__device__ __forceinline__ void k_zero(f32x4& k0, f32x4& k1, f32x4& k2) {
  asm volatile(".rept 6\n\tv_mov_b64 v[148+2*\\+:149+2*\\+], 0\n\t.endr" : "={v[148:151]}"(k0), "={v[152:155]}"(k1), "={v[156:159]}"(k2));
}
#define TZERO ".rept 6\n\tv_mov_b64 v[200+2*\\+:201+2*\\+], 0\n\t.endr\n\t"
__device__ __forceinline__ void t_zero(f32x4& k0, f32x16& T) {
  asm volatile("v_mov_b64 v[148:149], 0\n\tv_mov_b64 v[150:151], 0\n\t" TZERO : "={v[148:151]}"(k0), "={v[200:215]}"(T));
}
#define AD0(b) "v_add_f32 v230, v[" #b "], v[" #b "+2]\n\t"
#define AD1(b) "v_add_f32 v231, v[" #b "+1], v[" #b "+3]\n\t"
#define CVP CVX(232, 230, 231)
#define Q1(j) SMX("144:147", j, w##j)
template <int ID>
__device__ __forceinline__ void q_mid(const u32x4 (&af)[9], const u32x8 (&w)[9], int ix, unsigned qw, f32x4 q0, f32x4& q1) {
  asm volatile(Q1(3) Q1(4) AD0(140) Q1(5) AD1(140) "v_cmp_u_f32 vcc, v230, v231\n\t" Q1(6) CVP Q1(7) TRYB IMW("%[qw]", "0") Q1(8)
               : "+{v[144:147]}"(q1)
               : "{v[140:143]}"(q0), [a3] "v"(af[3]), [a4] "v"(af[4]), [a5] "v"(af[5]), [a6] "v"(af[6]), [a7] "v"(af[7]), [a8] "v"(af[8]),
                 [w3] "a"(w[3]), [w4] "a"(w[4]), [w5] "a"(w[5]), [w6] "a"(w[6]), [w7] "a"(w[7]), [w8] "v"(w[8]), [ix] "v"(ix), [qw] "v"(qw), [id] "n"(ID)
               : "memory", "v230", "v231", "v232", TRYC);
}
__device__ __forceinline__ void k_last(unsigned kw, f32x4 q1, unsigned fw, unsigned fv) {
  asm volatile("s_nop 7\n\t" PKA(230, 144) CVP IMW("%[kw]", "1024") "ds_write_b32 %[fw], %[fv]"
               :: "{v[144:147]}"(q1), [kw] "v"(kw), [fw] "v"(fw), [fv] "v"(fv) : "memory", "v230", "v231", "v232");
}
#define K0(j) SMX("148:151", j, u##j)
#define K1(j) SMX("152:155", j, u##j)
#define AF9 [a0] "v"(af[0]), [a1] "v"(af[1]), [a2] "v"(af[2]), [a3] "v"(af[3]), [a4] "v"(af[4]), [a5] "v"(af[5]), [a6] "v"(af[6]), [a7] "v"(af[7]), [a8] "v"(af[8])
#define WK9 [u0] "a"(wk[0]), [u1] "a"(wk[1]), [u2] "a"(wk[2]), [u3] "a"(wk[3]), [u4] "a"(wk[4]), [u5] "a"(wk[5]), [u6] "a"(wk[6]), [u7] "a"(wk[7]), [u8] "v"(wk[8])
__device__ __forceinline__ void k_one(const u32x4 (&af)[9], const u32x8 (&wk)[9], const u32x8 (&wp)[3], int ix, unsigned qw, f32x4 q1, f32x4& k0, f32x4& k2, float& t) {
  asm volatile(K0(8) K0(0) AD0(144) AD1(144) CVP K0(4) IMW("%[qw]", "1024") K0(1) K0(2) K0(3) K0(5) PADA K0(6) K0(7)
               SMX("156:159", 0, p0) SMX("156:159", 4, p1) AD0(148)
               : "+{v[148:151]}"(k0), "+{v[156:159]}"(k2), "=&{v230}"(t)
               : "{v[144:147]}"(q1), AF9, WK9, [p0] "v"(wp[0]), [p1] "v"(wp[1]), [ix] "v"(ix), [qw] "v"(qw) : "memory", "v231", "v232");
}
#define KB
template <int PO, int JC>
__device__ __forceinline__ void k_end(const u32x4 (&af)[9], const u32x8 (&wk)[9], u32x8 wp, int ix, f32x4 k0, f32x4 k1, f32x4 k2, float t, unsigned kw, unsigned fw, unsigned fv) {
  asm volatile(".if %[jc] == 3\n\t" SMX("156:159", 3, p2) ".endif\n\t.if %[jc] == 6\n\t" SMX("156:159", 6, p2) ".endif\n\t.if %[jc] == 8\n\t" SMX("156:159", 8, p2) ".endif\n\t"
               AD1(148) K1(8) CVP K1(0) IMW("%[kw]", "0") K1(4) AD0(156) K1(1) AD1(156) K1(2) CVP K1(3) IMW("%[kw]", "%[po]") K1(5) PADA K1(6) K1(7)
               "s_nop 7\n\t" PKA(230, 152) CVP IMW("%[kw]", "1024") KB "ds_write_b32 %[fw], %[fv]\n\t"
               :: "{v[148:151]}"(k0), "{v[152:155]}"(k1), "{v[156:159]}"(k2), "{v230}"(t), AF9, WK9, [p2] "v"(wp), [ix] "v"(ix), [kw] "v"(kw), [fw] "v"(fw),
                  [fv] "v"(fv), [po] "n"(PO), [jc] "n"(JC)
               : "memory", "v231", "v232");
}
__device__ __forceinline__ unsigned add3bf(unsigned a, unsigned b, unsigned c) {
  return pk2(__uint_as_float(a << 16) + __uint_as_float(b << 16) + __uint_as_float(c << 16),
             __uint_as_float(a & 0xffff0000u) + __uint_as_float(b & 0xffff0000u) + __uint_as_float(c & 0xffff0000u));
}
#define IS(n)
#define cKW "v112"
#define cKA "v113"
#define cQ0 "v114"
#define cQ1 "v115"
#define cQ3 "v116"
#define cFA "v117"
#define cEO "v118"
#define cLO "v119"
#define cC0 "v120"
#define cC1 "v121"
#define cC2 "v122"
#define cFW "v123"
#define cQW "v124"
#define cKX "v125"
#define TRD(r, a, o) "ds_read_b64_tr_b16 v[" #r ":" #r "+1], " a " offset:" #o "\n\t"
#define LGK(n) "s_waitcnt lgkmcnt(" #n ")\n\t"
#define SXM(t, c) "v_smfmac_f32_16x16x64_bf16 v[200+4*" #t ":203+4*" #t "], v[160+4*" #c ":163+4*" #c "], v[64+16*" #t "+8*" #c ":71+16*" #t "+8*" #c "], %[ix]\n\t"
#define SMM(c) "v_smfmac_f32_16x16x64_bf16 v[208:211], v[160+4*" #c ":163+4*" #c "], v[176+8*" #c ":183+8*" #c "], %[ix]\n\t"
#define QRE TRD(160, cQ0, 0) TRD(162, cQ1, 0)
#define QRL TRD(164, cQ0, 1024) TRD(166, cQ1, 1024)
#define CN(k)
#define QE 0
#define QL 1
#define KL -1
#define QP(n) ".if " XSTR(QE) " == " #n "\n\t" QRE ".endif\n\t"
#define QQ(n) ".if " XSTR(QL) " == " #n "\n\t" QRL ".endif\n\t"
template <int PH>
__device__ __forceinline__ void kx_get(u32x16 cst, u32x16& x0, u32x16& x1) {
#define KXR(k) "ds_read_b128 v[64+4*" #k ":67+4*" #k "], " cKX " offset:%[o]+1024*" #k "\n\tds_read_b128 v[80+4*" #k ":83+4*" #k "], " cKX " offset:%[o]+1024*" #k "+16\n\t"
  asm volatile(KXR(0) KXR(1) KXR(2) KXR(3)
               : "={v[64:79]}"(x0), "={v[80:95]}"(x1) : "{v[112:127]}"(cst), [o] "n"(KX_OFF + PH * 8192) : "memory");
}
#define NK 2
#define KIF(c, t) ".if " XSTR(NK) c "\n\t" t ".endif\n\t"
__device__ __forceinline__ void s_first(const u32x4 (&af)[9], const u32x8 (&wk)[9], int ix, u32x16 cst, u32x16 x0, u32x16 x1, f32x4 q1, f32x4& k0, f32x16& T, u32x8& Q) {
  asm volatile(K0(8) QP(0) K0(0) AD0(144) AD1(144) CVP K0(4) IMW(cQW, "1024") K0(1) QP(1) QQ(0) K0(2) QQ(1) K0(3) QQ(2) KIF(" == 0", K0(5))
               LGK(2) SXM(0, 0) SXM(1, 0) LGK(0) SXM(0, 1) SXM(1, 1)
               : "+{v[148:151]}"(k0), "+{v[200:215]}"(T), "=&{v[160:167]}"(Q)
               : "{v[144:147]}"(q1), [a0] "v"(af[0]), [a1] "v"(af[1]), [a2] "v"(af[2]), [a3] "v"(af[3]), [a4] "v"(af[4]), [a5] "v"(af[5]), [a8] "v"(af[8]),
                 [u0] "a"(wk[0]), [u1] "a"(wk[1]), [u2] "a"(wk[2]), [u3] "a"(wk[3]), [u4] "a"(wk[4]), [u5] "a"(wk[5]), [u8] "v"(wk[8]),
                 [ix] "v"(ix), "{v[112:127]}"(cst), "{v[64:79]}"(x0), "{v[80:95]}"(x1)
               : "memory", "v230", "v231", "v232");
}
#define DPPF " row_mask:0xf bank_mask:0xf\n\t"
#define DX1 " quad_perm:[1,0,3,2]" DPPF
#define DX2 " quad_perm:[2,3,0,1]" DPPF
#define DX4 " row_half_mirror" DPPF
__device__ __forceinline__ void el_phase(i32x4 D, const u32x4 (&af)[9], const u32x8 (&wk)[9], int ix, u32x16 cst, u32x16 x0, u32x16 x1, u32x8& Q, f32x4& k0, f32x16& T,
                                         unsigned& bud, u64& e1, u64& e4, unsigned& nr, u32x8 sc, unsigned fv) {
#define EST(s) "buffer_store_dwordx2 v[222:223], " cEO ", %[D], " s " offen sc1\n\t"
#define LST(s) "buffer_store_dword v227, " cLO ", %[D], " s " offen sc1\n\t"
#define EPART(KA, KB_)                                                                                                             \
               IS(4) KA "v_add_f32 v212, v200, v202\n\t" KB_ "v_add_f32 v213, v201, v203\n\tv_add_f32 v214, v204, v206\n\tv_add_f32 v215, v205, v207\n\t" \
               "v_max_f32 v216, v212, v214\n\tv_max_f32 v217, v213, v215\n\tv_mov_b32 v219, v212\n\t"                              \
               "v_max_f32_dpp v218, v216, v216" DPPLO "v_max_f32_dpp v218, v217, v217" DPPHI                                       \
               "v_mov_b32 v220, v214\n\ts_nop 0\n\t"                                                                             \
               "v_max_f32_dpp v218, v218, v218" DX1 "v_mov_b32_dpp v212, v213" DPPHI "v_mov_b32_dpp v214, v215" DPPHI              \
               "v_max_f32_dpp v218, v218, v218" DX2 "v_mov_b32_dpp v213, v219" DPPLO "v_mov_b32_dpp v215, v220" DPPLO              \
               "v_max_f32_dpp v218, v218, v218" DX4 IS(5)                                                                          \
               "v_mul_f32 v216, " SCN ", v218\n\t"                                                                                 \
               "v_pk_fma_f32 v[212:213], v[212:213], s[44:45], v[216:217] op_sel_hi:[1,0,0]\n\t"                                   \
               "v_pk_fma_f32 v[214:215], v[214:215], s[44:45], v[216:217] op_sel_hi:[1,0,0]\n\t"                                   \
               "v_exp_f32 v212, v212\n\tv_exp_f32 v214, v214\n\tv_exp_f32 v213, v213\n\tv_exp_f32 v215, v215\n\t"                  \
               CVX(222, 212, 214) CVX(223, 213, 215)
#define MPART(KA, KB_, KBW)                                                                                                        \
               KA EST("s40") TRD(168, cQ3, 0) TRD(192, cKA, 2048) EST("s41") EVA(4) IS(6) TRD(194, cKA, 2304) TRD(176, cKA, 0)      \
               KB_ TRD(178, cKA, 256) TRD(180, cKA, 512) TRD(182, cKA, 768)                                                        \
               "v_add_f32 v212, v212, v214\n\tv_add_f32 v213, v213, v215\n\tv_mov_b32 v225, " cC2 "\n\tv_add_f32 v212, v212, v213\n\t"  \
               LGK(4) PKA(230, 148) CVP "v_smfmac_f32_16x16x32_bf16 v[208:211], v[168:169], v[192:195], %[ix]\n\t"                 \
               IMW(cKW, "1024") TRD(190, cKA, 1792) KBW TRD(184, cKA, 1024) TRD(186, cKA, 1280) TRD(188, cKA, 1536) LGK(5) SMM(0)  \
               EST("s42") "ds_write_b32 " cFW ", %[fv]\n\t" LGK(1) SMM(1) IS(7)
#define GADD   "v_add_f32_dpp v220, v208, v210 quad_perm:[0,1,2,3] row_mask:0xf bank_mask:0x3\n\t"                                 \
               "v_add_f32_dpp v220, v209, v211 quad_perm:[0,1,2,3] row_mask:0xf bank_mask:0xc\n\t"
#define LEND   "v_add_f32_dpp v217, v217, v217" DX4 "v_rcp_f32 v218, v217\n\t" PADN                                                \
               "v_pk_mul_f32 v[224:225], v[224:225], v[218:219] op_sel_hi:[1,0]\n\t"                                               \
               CVX(227, 224, 225) IS(8) LST("s40") LST("s41") EVA(1) IS(9) LST("s42") EST("s43") LST("s43")
#define KBF
#define EVO
  asm volatile(EPART(K0(6) KIF(" == 2", "s_nop 1\n\t"), KIF(" < 2", K0(7)))
               MPART(KIF(" > 0", K0(5)), KIF(" == 2", K0(7)), KBF)
               "s_nop 1\n\tv_add_f32_dpp v212, v212, v212" DX1 "s_nop 4\n\t" GADD
               "v_fmamk_f32 v221, v220, " SCK ", v216\n14:\n\tv_exp_f32 v220, v221\n\tv_cmp_nge_f32 vcc, s45, v221\n\tv_add_f32 v217, v220, v212\n\t"
               "v_add_f32_dpp v217, v220, v217" DX1 "v_fma_f32 v224, v220, " cC0 ", " cC1 "\n\tv_fmac_f32_dpp v225, v220, " cC0 DX1
               "v_add_f32_dpp v217, v217, v217" DX2 "s_cmp_eq_u64 vcc, 0\n\ts_cselect_b64 exec, -1, 0\n\t" LEND "s_cbranch_scc0 19f\n"
               "13:\n\t.subsection 1\n19:\n\ts_mov_b64 exec, -1\n\t" CN(10000)
               "20:\n\tds_read_b32 v224, " cFA "\n\t" LGK(0) "v_cmp_gt_i32 vcc, %[fv], v224\n\ts_cbranch_vccz 21f\n\t" BUD("20b")
               "21:\n\t" QRE QRL TZERO LGK(0) SXM(0, 0) SXM(1, 0) SXM(0, 1) SXM(1, 1)
               EPART("s_nop 7\n\t", "") MPART("", "", "")
               "v_mov_b32 v213, v212\n\tv_add_f32_dpp v212, v212, v212" DX1 "s_nop 5\n\t" GADD
               "v_fmamk_f32 v221, v220, " SCK ", v216\n\tv_cmp_nge_f32 vcc, s45, v221\n\ts_cbranch_vccz 14b\n\t"
               "v_max_f32 v216, v220, v218\n\tv_mul_f32 v221, " SCK ", v218\n\ts_nop 0\n\t"
               "v_max_f32_dpp v216, v216, v216" DX1 "v_mul_f32 v220, " SCK ", v220\n\ts_nop 0\n\t"
               "v_max_f32_dpp v216, v216, v216" DX2 "s_nop 1\n\t"
               "v_max_f32_dpp v216, v216, v216" DX4
               "v_pk_fma_f32 v[220:221], v[216:217], s[44:45], v[220:221] op_sel_hi:[0,0,1] neg_lo:[1,0,0] neg_hi:[1,0,0]\n\t"
               "v_exp_f32 v221, v221\n\tv_exp_f32 v220, v220\n\t"
               "v_mul_f32 v224, v221, " cC1 "\n\tv_fma_f32 v217, v221, v213, v220\n\t"
               "v_mul_f32 v225, v221, " cC2 "\n\tv_fmac_f32 v224, v220, " cC0 "\n\t"
               "v_add_f32_dpp v217, v217, v217" DX1 "v_fmac_f32_dpp v225, v220, " cC0 DX1 "s_nop 0\n\t"
               "v_add_f32_dpp v217, v217, v217" DX2 "s_nop 1\n\t" LEND "s_branch 13b\n\t.subsection 0"
               : [bud] "+s"(bud), "+{v[160:167]}"(Q), "+{v[148:151]}"(k0), "+{v[200:215]}"(T) EVO
               : [a5] "v"(af[5]), [a6] "v"(af[6]), [a7] "v"(af[7]), [u5] "a"(wk[5]), [u6] "a"(wk[6]), [u7] "a"(wk[7]), [ix] "v"(ix),
                 "{v[112:127]}"(cst), "{v[64:79]}"(x0), "{v[80:95]}"(x1), [D] "s"(D), "{s[40:47]}"(sc), [fv] "v"(fv)
               : "memory", "vcc", "scc", "v168", "v169", "v176", "v177", "v178", "v179", "v180", "v181", "v182", "v183", "v184",
                 "v185", "v186", "v187", "v188", "v189", "v190", "v191", "v192", "v193", "v194", "v195",
                 "v216", "v217", "v218", "v219", "v220", "v221", "v222", "v223", "v224", "v225", "v227", "v230", "v231", "v232");
}
#define YMAX 0x42700000u
#define TWO 2
#define ARD(j, p) do { af[j] = *(lds_u32x4*)(p); SB(); } while (0)
#define SECOND(n, NXV) do { if (TWO == n) { asm volatile("s_waitcnt vmcnt(%0)" :: "n"(NXV) : "memory"); __builtin_amdgcn_s_barrier(); CBAR(); SB(); \
                         ARD(4, ard0 + 4096); ARD(5, ard1 + 4096); } if (TWO + 1 == n) { ARD(6, ard2 + 4096); ARD(7, ard3 + 4096); }               \
                         if (TWO + 2 == n) ARD(8, lds + F8_OFF + ar); } while (0)
#define HEAD(M, NXV, H7) do {                                                                                                                     \
    ARD(0, ard0); __builtin_amdgcn_s_barrier(); CBAR(); SB(); ARD(1, ard1); ARD(2, ard2); ARD(3, ard3);                                       \
    if (!TWO) { ARD(4, ard0 + 4096); ARD(5, ard1 + 4096); }                                                                                   \
    M(0, 0); SECOND(1, NXV); M(1, 0); SECOND(2, NXV); M(0, 1); SECOND(3, NXV); M(1, 1); SECOND(4, NXV); M(0, 2); SECOND(5, NXV);              \
    if (!(H7)) M(1, 2);                                                                                                                       \
    SECOND(6, NXV); M(0, 3); M(0, 4); M(0, 5); M(0, 6); M(0, 7); M(0, 8); } while (0)
__device__ __forceinline__ void role_qkc(const Args& a, rsrc_t R, const Go& go, lds_char* lds, int h, int tid, int hw, int lane) {
  const int wave = 3 - hw;
  const int l15 = lane & 15, g = lane >> 4;
  u32x8 wq[2][9], wk[2][9], wp[3];
  int cls = -1, kv0 = -1;
  const int wz = h >> 3, tq = (h >> 1) & 3, hp = h & 1, qi = 2 * g + (l15 >> 3), kq = l15 & 7;
  unsigned pxo = (unsigned)nearoff(NEAR_E + (h >> 1) * 1024 + ((kq >> 1) * 16 + 8 * (qi >> 2) + 4 * hp + (qi & 3)) * 16 + (kq & 1) * 8);
  unsigned plo = !(kq & 1) ? (unsigned)nearoff(NEAR_PM + wz * 1024 + ((2 * hp + (tq >> 1)) * 16 + 4 * (qi >> 1) + (kq & 2) + (qi & 1)) * 16 + (tq & 1) * 8 + (kq >> 2) * 4)
                           : (kq == 1 ? (unsigned)nearoff(NEAR_AL + wz * 256 + ((tq >> 1) * 8 + qi) * 16 + (2 * hp + (tq & 1)) * 4) : OOB);
  {
    u32x4 sg[QDEP][8];
#pragma unroll
    for (int c = 0; c < QDEP; ++c) qk_gload<NTM & 1>(sg[c], a, h, c, tid);
#define WK3(b) do { WALK(b + 2); WALK(b + 1); WALK(b); } while (0)
    if (wave == 0) WK3(200); else if (wave == 1) WK3(208); else if (wave == 2) WK3(216); else WK3(224);
    const int q = l15 >> 2;
    const int lrow = 16 * (g >> 1) + 4 * (g & 1) + 8 * (q >> 1) + (q & 1);
    auto wrd = [&](lds_char* p) {
      u32x2 v0 = trd(p), v1 = trd(p + 2 * 544), v2 = trd(p + 32 * 544), v3 = trd(p + 34 * 544);
      return (u32x8){v0[0], v0[1], v1[0], v1[1], v2[0], v2[1], v3[0], v3[1]};
    };
#pragma unroll
    for (int ph = 0; ph < 3; ++ph) {
#pragma unroll
      for (int i = 0; i < 4; ++i) {
        const int c = ph * 4 + i;
        if (c < 9) {
          qk_swrite(sg[c % QDEP], lds + i * QSTG, tid);
          if (c + QDEP < 9) qk_gload<NTM & 1>(sg[c % QDEP], a, h, c + QDEP, tid);
          CBAR();
        }
      }
      RT(8 + ph);
      if (ph == 2 && go_wait(R, go, lane)) {
        kv0 = (int)ld32(R, FL_PROG + h * 128 + (lane & 3) * 4, 0);
        cls = get_cls(R, lane, cls_look(R, lane));
        pxo += (unsigned)(cls & 1) * 8192u; plo += (unsigned)(cls & 1) * 8192u;
        if (wave < 3) {
#pragma unroll
          for (int k = 0; k < 4; ++k) {
            const unsigned so = wave * SLOT_REAL + (k & 1) * CPY1 + (k >> 1) * CPY2;
            st64((u32x2){~0u, ~0u}, R, pxo, so); st32(~0u, R, plo, so);
          }
        }
        CBAR();
        RT(11);
      }
      BAR();
#pragma unroll
      for (int i = 0; i < 4; ++i) {
        const int j = ph * 4 + i;
        if (j < 9) {
          lds_char* buf = lds + (ph < 2 ? ((i + wave) & 3) : 0) * QSTG + lrow * 544 + (l15 & 3) * 8;
#pragma unroll
          for (int t = 0; t < 4; ++t) {
            u32x8& w = t < 2 ? wq[t & 1][j] : wk[t & 1][j];
            w = wrd(buf + t * 128 + wave * 32);
          }
          if (j == 0 || j == 4) wp[j >> 2] = wrd(buf + 256 + 96);
        }
      }
      SB();
#pragma unroll
      for (int k = 0; k < 16; ++k) {
        const int j = ph * 4 + (k >> 2);
        if (j < 9) { u32x8& w = (k & 2) ? wk[k & 1][j] : wq[k & 1][j]; if (j < 8) pin_a(w); else pin_v(w); }
      }
      if (ph < 2) pin_v(wp[ph]);
      if (ph == 0 || wave == ph) wp[2] = wrd(lds + (ph < 2 ? 3 : 0) * QSTG + lrow * 544 + (l15 & 3) * 8 + 256 + 96);
      pin_v(wp[2]);
      BAR();
    }
  }
  RT(1);
  if (cls < 0) return;
  RT(2);
  auto run = [&](auto ROLEC) {
  constexpr int ROLE = decltype(ROLEC)::value;
  lds_char* img = lds;
  const unsigned fo = (unsigned)(cls & 1) * 8192u;
  const int r0 = ROLE * 1024 + lane * 16, r1 = r0 + 4096, r2 = 8192 + lane * 16;
  const unsigned m0 = nearoff(r0) + fo, m1 = nearoff(r1) + fo, m2 = nearoff(r2) + fo;
  const unsigned lb = (unsigned)(uintptr_t)lds, lw0 = lb + ROLE * 1024;
  const i32x4 D = mkdesc(a.wb);
  const int ar = g * 256 + ((l15 >> 1) & 1) * 128 + (2 * (l15 >> 2) + (l15 & 1)) * 16;
  lds_char* ard0 = img + ROLE * 1024 + ar;
  lds_char* ard1 = img + ((ROLE + 1) & 3) * 1024 + ar;
  lds_char* ard2 = img + ((ROLE + 2) & 3) * 1024 + ar;
  lds_char* ard3 = img + ((ROLE + 3) & 3) * 1024 + ar;
  const int gy = ((tq & 1) << 1) | (tq >> 1);
  const unsigned pcl = lane < 32 ? (unsigned)nearoff(NEAR_E + (h >> 1) * 1024 + ((lane >> 2) * 8 + 4 * hp + (lane & 3)) * 16) + fo
                     : (lane < 40 ? (unsigned)nearoff(NEAR_PM + wz * 1024 + ((2 * hp + (tq >> 1)) * 16 + (tq & 1) * 8 + (lane & 7)) * 16) + fo
                                  : (lane < 42 ? (unsigned)nearoff(NEAR_AL + wz * 256 + ((tq >> 1) * 8 + (2 * hp + (tq & 1)) * 2 + (lane & 1)) * 16) + fo : OOB));
  const unsigned kfo = OFF_KF + (unsigned)h * 8192u + lane * 16;
  constexpr int QT = ROLE >> 1;
  const unsigned kmo = OFF_KMF + (unsigned)h * 4096u + lane * 16;
  const unsigned pxs = OFF_PXF + (unsigned)((QT * 40 + h) * 1024 + lane * 16);
  const unsigned pws = OFF_PXF + (unsigned)((QT * 40 + 32 + wz * 2 + hp) * 1024 + (gy * 16 + l15) * 16 + g * 4);
  const unsigned kpf = FL_PROG + h * 128 + (lane & 3) * 4;
  const unsigned kmf = (lane == 0) ? (unsigned)(FL_KMPROG + h * 128) : OOB;
  const unsigned wpo = lane == 0 ? (unsigned)(FL_WP + h * 32) : OOB;
  const int idx = (lane & 2) ? 0xEEEE : 0x4444;
  const int jr = l15 >> 2;
  const unsigned qw = lb + QIM_OFF + (ROLE * 8 + ((2 * jr + (l15 & 1)) ^ ((ROLE & 1) << 1))) * 32 + g * 8 + (l15 & 2) * 2;
  const unsigned kw = lb + KIM_OFF + ROLE * 256 + l15 * 16 + g * 4;
  lds_char* krd = lds + KIM_OFF + (4 * g + jr) * 16 + (l15 & 2) * 4;
  unsigned fw = lb + FLG_OFF + ROLE * 4;
  pin_v(fw);
  lds_char* frd = lds + FLG_OFF + (lane & 3) * 4;
  const unsigned qa = lb + QIM_OFF + (16 * (g >> 1) + 4 * (g & 1) + (jr & 1) + 10 * (jr >> 1)) * 32 + (l15 & 3) * 8;
  u32x16 cst = {kw, lb + KIM_OFF + (4 * g + jr) * 16 + (l15 & 1) * 8, qa, qa ^ 64, lb + QIM_OFF + ((24 + 2 * g + (jr & 1)) ^ 2) * 32 + (l15 & 3) * 8,
                lb + FLG_OFF + min(lane & 3, 2) * 4, pxo, plo, (l15 & 1) ? 0u : 0x3f800000u, l15 == 1 ? 0x3f800000u : 0u, l15 == 9 ? 0x3f800000u : 0u, fw, qw,
                lb + (l15 >> 3) * 4096 + (g * 16 + 4 * (l15 & 3) + 2 * (jr & 1)) * 16, 0u, 0u};
  const unsigned own = own_cpy(cls, h >> 3);
  unsigned ima = lb + lane * 16;
  pin_v(ima);
  ds_flag(fw, 0u);
  ds_flag(fw + 16, 0u);
  auto poison = [&]() {
#pragma unroll
    for (int f = 0; f < 5; ++f) *(lds_u32x4*)(lds + QIM_OFF + f * 1024 + lane * 16) = (u32x4){~0u, ~0u, ~0u, ~0u};
    asm volatile("s_waitcnt lgkmcnt(0)" ::: "memory");
  };
  if (ROLE == 0) poison();

  int fl = kv0;
  u32x4 af0[3];
#pragma unroll
  for (int f = 0; f < 3; ++f) {
    const int u = (f == 2 ? 8 : f * 4 + ROLE) * 64 + lane;
    af0[f] = *(const u32x4*)(a.mem0 + (u & 7) * DM + (u >> 3) * 8);
  }
  auto k_dma = [&](int sg, int half) {
#pragma unroll
    for (int f = 0; f < 4; ++f)
      asm volatile("s_mov_b32 m0, %2\n\ts_nop 0\n\tbuffer_load_dwordx4 %0, %1, %3 offen offset:%4 sc1 lds"
                   :: "v"(kfo), "s"(D), "s"(lb + KX_OFF + (sg & 1) * 8192 + half * 4096), "s"((unsigned)(sg & RMASK) * 262144u + half * 4096), "n"(f * 1024) : "memory");
  };
  u32x4 qq[4], F[4];
#pragma unroll
  for (int f = 0; f < 4; ++f) { qq[f] = af0[0]; F[f] = af0[0]; }
  f32x4 T0 = {0.f, 0.f, 0.f, 0.f}, T1 = T0;
  float tmx = 0.f, tsx = 0.f;
  auto q_ld = [&](u32x4 (&qx)[4], int sg) {
#pragma unroll
    for (int f = 0; f < 4; ++f)
      asm volatile("buffer_load_dwordx4 %0, %1, %2, %3 offen offset:%4 sc1"
                   : "+v"(qx[f]) : "v"(kfo), "s"(D), "s"((unsigned)sg * 262144u + (OFF_QF - OFF_KF) + QT * 4096), "n"(f * 1024) : "memory");
  };
  auto t_part = [&](u32x4 (&qx)[4], int par, auto NC) {
    asm volatile("s_waitcnt vmcnt(%4)" : "+v"(qx[0]), "+v"(qx[1]), "+v"(qx[2]), "+v"(qx[3]) : "n"(decltype(NC)::value) : "memory");
    T0 = (f32x4){0.f, 0.f, 0.f, 0.f}; T1 = T0;
#pragma unroll
    for (int ks = 0; ks < 4; ++ks) {
      T0 = mfma(*(lds_u32x4*)(lds + KX_OFF + par * 8192 + ks * 1024 + lane * 16), qx[ks], T0);
      if (QT) T1 = mfma(*(lds_u32x4*)(lds + KX_OFF + par * 8192 + 4096 + ks * 1024 + lane * 16), qx[ks], T1);
    }
    pin_v(T0); pin_v(T1); PAD();
    px_mask(T0, T1, QT, lane);
    pin_v(T0); pin_v(T1); PAD();
    px_seg(T0, T1, tmx, tsx);
    pin_v(T0); pin_v(T1); pin_v(tmx); pin_v(tsx); PAD();
  };
  if (ROLE == 3) {
    const bool ok = wait_flag_c<8>(R, kpf, 0, fl);
    k_dma(0, 0); k_dma(0, 1);
    if (!ok) return;
  }
  VMW(0);

  unsigned iso_acc = 0, bud = SPINCAP, tm = 0, rcnt = 0;
  unsigned cv = 0;
  const unsigned cwo = FL_AN + (h & 15) * 128;
  EVDECL; CKDECL;
  auto k_get = [&](u32x4 (&F)[4], int s, bool parts) {
#pragma nounroll
    for (; bud != 0; --bud) {
      CBAR();
      if (__all((int)*(lds_u32*)frd > s)) break;
      __builtin_amdgcn_s_sleep(1);
    }
    CBAR();
#pragma unroll
    for (int f = 0; f < 4; ++f) { u32x2 lo = trd(krd + f * 512), hi = trd(krd + f * 512 + 256); F[f] = (u32x4){lo[0], lo[1], hi[0], hi[1]}; }
    if (!parts) return;
    const u32x2 p0 = trd(krd + 2048), p1 = trd(krd + 2304);
    F[1][2] = add3bf(F[1][2], p0[0], p1[0]); F[1][3] = add3bf(F[1][3], p0[1], p1[1]);
  };
  auto step = [&](auto PHC, int s) {
    constexpr int PH = decltype(PHC)::value & 1, FIRST = (decltype(PHC)::value >> 1) & 1, LAST = decltype(PHC)::value >> 2;
    constexpr int JC = ROLE == 0 ? 3 : (ROLE == 1 ? 6 : 8);
    u64 e0 = 0, e1 = 0, e3 = 0, e4 = 0; unsigned nr0 = 0;
    u32x4 af[9];
    f32x4 q0, q1, k0, k1, k2;
    PADS(200 + ROLE * 8 + decltype(PHC)::value);
    unsigned f3 = s + 1;
    pin_v(f3);
    constexpr int RD = ROLE == 3 && !LAST;
    f32x16 T;
    u32x16 x0, x1;
    if (RD) t_zero(k0, T); else if (!LAST) k_zero(k0, k1, k2);
    const u32x8 (&wf)[2][9] = LAST ? wk : wq;
#define MQ(t, j) do { smo<t, (j) < 8>(q##t, af[j], wf[t][j], idx); SB(); } while (0)
    lds_char* f8 = lds + F8_OFF + lane * 16;
    bud = __builtin_amdgcn_readfirstlane(bud);
    asm volatile("" : "+s"(bud));
    unsigned nt = 0; int rq = 0;
    tm = __builtin_amdgcn_readfirstlane(tm); rcnt = __builtin_amdgcn_readfirstlane(rcnt);
    asm volatile("" : "+s"(tm), "+s"(rcnt));
    if (RD) asm volatile("" : "+{v[112:127]}"(cst));
    unsigned p0 = (s & 7) * SLOT_REAL, p1 = p0 + CPY1, p2 = p0 ^ CPY2, p3 = p2 + CPY1;
    unsigned ms = (p2 + own) ^ ((s & 4) ? 0 : CPY1);
    u32x8 sc = {p0, p1, p2, p3, 0x3e0293eeu, YMAX, 0u, 0u};
    if (RD) asm volatile("" : "+{s[40:47]}"(sc));
    constexpr int NX = ROLE == 3 || LAST ? 0 : (ROLE == 0 ? 8 : 4);
    constexpr int ID = FIRST ? -1 : ROLE * 8 + decltype(PHC)::value;
    auto head = [&]() {
    v_zero(q0, q1);
    if (!FIRST) {
      int r = 0;
      if (ROLE == 1) r = spin_late(tm, 0);
      rq = r;
      mem_issue2<ROLE == F8W ? 3 : 2>(D, m0, m1, m2, ms, lw0, lw0 + 4096, lb + F8_OFF, e3);
      if (ROLE == 1) { move_on(tm, r); SB(); }
      PAD();
      if (RD) { kx_get<PH>(cst, x0, x1); asm volatile("s_waitcnt lgkmcnt(0)" ::: "memory"); }
      if (ROLE == 3) asm volatile("s_waitcnt vmcnt(%0)" :: "n"(TWO ? 1 + (F8W == 3) : 0) : "memory");
      else if (nt != 0) VMW(0);
      else {
      __builtin_amdgcn_s_sleep(XSLP);
      if (!LAST && (ROLE == 1 || ROLE == 2)) k_dma(s + 1, ROLE - 1);
      if (ROLE == 0 && !LAST) {
#pragma unroll
        for (int ks = 0; ks < 4; ++ks) st128(F[ks], R, kmo + ks * 1024, (unsigned)(s - 1) * 131072u);
      }
      if (ROLE == 0 && !LAST) {
        st128((u32x4){~0u, ~0u, ~0u, ~0u}, R, pcl, (unsigned)((s + 2) & 3) * SLOT_REAL);
        st128((u32x4){~0u, ~0u, ~0u, ~0u}, R, pcl, (unsigned)((s + 2) & 3) * SLOT_REAL + CPY1);
        st128((u32x4){~0u, ~0u, ~0u, ~0u}, R, pcl, (unsigned)((s + 2) & 3) * SLOT_REAL + CPY2);
        st128((u32x4){~0u, ~0u, ~0u, ~0u}, R, pcl, (unsigned)((s + 2) & 3) * SLOT_REAL + CPY2 + CPY1);
      }
      PAD();
      CBAR();
      asm volatile("s_waitcnt vmcnt(%0)" :: "n"(NX + (TWO ? 1 + (ROLE == F8W) : 0)) : "memory");
      }
    } else {
      *(lds_u32x4*)(img + r0) = af0[0]; *(lds_u32x4*)(img + r1) = af0[1]; *(lds_u32x4*)f8 = af0[2];
      asm volatile("s_waitcnt lgkmcnt(0)" ::: "memory");
    }
    SB();
    HEAD(MQ, NX, 0);
    if (s == 0) RT(3);
    if (s == 1) RT(4);
    if (s == 8) RT(5);
    if (s == 60) RT(6);
    ISOQ(1);
    q_mid<ID>(af, wf[1], idx, LAST ? kw : qw, q0, q1); SB();
    };
    if (!FIRST) {
      tm -= GRID + XT; nt = ~0u;
      try_top<ID>(nt, ms, tm, rcnt, ima); SB();
      head();
      try_end(ima, nt, ms, tm, rcnt, m0, m1, m2, lw0, lw0 + 4096, lb + F8_OFF, D); SB();
    } else
    {
      if (FIRST && RD) { kx_get<PH>(cst, x0, x1); asm volatile("s_waitcnt lgkmcnt(0)" ::: "memory"); }
      head();
    }
    ISOQ(2);
    EV(e0);
    if (LAST) k_last(kw, q1, fw, f3);
    if (ROLE < 3) {
      if (!LAST) {
        float t;
        k_one(af, wk[0], wp, idx, qw, q1, k0, k2, t); SB();
        ISOQ(4);
        if (ROLE == 1 && FIRST) { st32(f3, R, wpo, 0); SB(); }
        k_end<ROLE == 2 ? 256 : 2048, JC>(af, wk[1], wp[2], idx, k0, k1, k2, t, kw, fw, f3); SB();
      }
      ISOQ(6);
      PAD();
      if (ROLE == 1 && !LAST) {
        nr0 = nt != 0;
        if (!FIRST) next_time(tm, s);
      }
      if (ROLE == 0 && !LAST) {
        __builtin_amdgcn_s_sleep(TSLP);
        k_get(F, s, true);
        ds_flag(lb + FLG_OFF + 20, f3);
        pin_v(F[0]); pin_v(F[1]); pin_v(F[2]); pin_v(F[3]);
        VMW(0);
        st32((unsigned)(s - 1), R, kmf, 0);
        if (!PH && !FIRST && UNLIKELY(s == NSEG - 2)) {
#pragma unroll
          for (int ks = 0; ks < 4; ++ks) st128(F[ks], R, kmo + ks * 1024, (unsigned)s * 131072u);
          VMW(0);
          st32((unsigned)s, R, kmf, 0);
        }
      }
      if (ROLE == 2 && !LAST) {
        if (FIRST) { k_dma(1, 0); k_dma(1, 1); }
        gate_wait(R, kpf, min(2 * s + 4 + (s >= NSEG - 4), 2 * NSEG - 1), fl, bud);
        fl = (int)ld32(R, kpf, 0);
        VMW(0);
        pin_v(fl);
      }
      if ((ROLE == 0 || ROLE == 2) && !PH && !FIRST && UNLIKELY(s == NSEG - 2)) {
        k_dma(s + 1, 0);
        q_ld(qq, s + 1);
        t_part(qq, 1, std::integral_constant<int, 0>());
      }
      if ((ROLE == 0 || ROLE == 2) && LAST) {
        f32x4 G = {0.f, 0.f, 0.f, 0.f};
        u32x4 px; unsigned pw;
        k_get(F, s, false);
#pragma unroll
        for (int ks = 0; ks < 4; ++ks) G = mfma(F[ks], qq[ks], G);
        px_fin(G, T0, T1, tmx, tsx, px, pw, lane);
        st128(px, R, pxs, (unsigned)s * 81920u); st32(pw, R, pws, (unsigned)s * 81920u);
        VMW(0);
        st32((unsigned)s, R, lane == 0 ? (unsigned)(FL_PXLAST + h * 256 + QT * 128) : OOB, 0);
      }
      CBAR();
      if (ROLE == 0) CKS();
    } else if (!LAST) {
      u32x8 Q;
      s_first(af, wk[1], idx, cst, x0, x1, q1, k0, T, Q); SB();
      el_phase(D, af, wk[1], idx, cst, x0, x1, Q, k0, T, bud, e1, e4, nr0, sc, f3);
      ds_flag(fw + 4, f3);
      EVPUT(2, e1); EVPUT(4, e4);
    }
    EVPUT(0, e0);
    EVPUTI(1, nr0); if (!FIRST) EVPUT(3, e3);
    if (ROLE == 1 && !LAST) {
#pragma nounroll
      for (; bud != 0; --bud) {
        CBAR();
        if (__all((int)*(lds_u32*)(lds + FLG_OFF + 16 + (lane & 1) * 4) > s)) break;
        __builtin_amdgcn_s_sleep(1);
      }
      poison();
      VMW(0);
      if (FIRST) { tm = get_t1(R, cwo, bud); asm volatile("" : "+s"(tm)); }
    }
    if (PH && !LAST && UNLIKELY(s == WLAST)) WALK(200 + ROLE * 8 + 5);
    PADE();
#undef MQ
  };
  step(std::integral_constant<int, 2>(), 0);
#pragma nounroll
  for (int s = 1; s < NSEG - 1; s += 2) {
    step(std::integral_constant<int, 1>(), s);
    step(std::integral_constant<int, 0>(), s + 1);
  }
  step(std::integral_constant<int, 5>(), NSEG - 1);
  EVDUMP();
  if (ROLE == 0) CKDUMP();
  };
  if (hw == 0) run(std::integral_constant<int, 3>());
  else if (hw == 1) run(std::integral_constant<int, 2>());
  else if (hw == 2) run(std::integral_constant<int, 1>());
  else run(std::integral_constant<int, 0>());
  RT(7);
  SV(15, cls);
}

#define VIM_OFF 8192
#define NOP(n) do { asm volatile("s_nop " #n); SB(); } while (0)
#define cVW "v112"
#define cRA "v113"
#define cRB "v114"
#define cHA "v115"
#define cHB "v116"
#define cVF "v117"
#define cVR "v118"
#define cIY "v119"
#define YP(p, c) "v_smfmac_f32_16x16x64_bf16 v[148+4*" #p ":151+4*" #p "], v[168+4*" #c "+4*(" #c ">>1):171+4*" #c "+4*(" #c ">>1)], %[w" #p #c "], " cIY "\n\t"
#define Y8(k) "v_smfmac_f32_16x16x64_bf16 v[164:167], v[176+12*" #k ":179+12*" #k "], %[w8" #k "], " cIY "\n\t"
#define HA(d, b) "v_add_f32 v" #d ", v[" b "], v[" b "+2]\n\t"
#define HB(d, b) "v_add_f32 v" #d ", v[" b "+1], v[" b "+3]\n\t"
#define YC(p, e) "v_cvt_pk_bf16_f32 v[192+2*" #p "+" #e "], v[148+4*" #p "+2*" #e "], v[149+4*" #p "+2*" #e "]\n\t"
#define YS(p) ".if %[st]\n\tbuffer_store_dwordx2 v[192+2*" #p ":193+2*" #p "], v[120+" #p "], %[D], %[ys] offen sc1\n\t.endif\n\t"
#define S16(a, b) "v_permlane16_swap_b32 v" #a ", v" #b "\n\t"
#define Z2(r) "v_mov_b64 v[" #r ":" #r "+1], 0\n\tv_mov_b64 v[" #r "+2:" #r "+3], 0\n\t"
#define YZERO Z2(148) Z2(152) Z2(156) Z2(160) Z2(164)
#define VR0 TRD(168, cRA, 0) TRD(170, cRB, 0)
#define VR1 TRD(172, cRA, 512) TRD(174, cRB, 512) TRD(176, cHA, 0) TRD(178, cHB, 0)
__device__ __forceinline__ void y_zero(f32x16& ya, f32x4& yc) {
  asm volatile(YZERO : "={v[148:163]}"(ya), "={v[164:167]}"(yc));
}
#define HV 0
#define VO 1
#define VR 1
#define YEO
#define YEN
template <int ID>
__device__ __forceinline__ void v_end(const u32x4 (&af)[9], const u32x8 (&w)[9], int ix, u32x16 cst, f32x4 v0, f32x4& v1, u32x16& A,
                                      const u32x8 (&wo)[4][4], f32x16& ya, u64& e1) {
#define VH0 HA(201, "140") HB(202, "140") CVX(203, 201, 202)
#define VH1 HA(201, "144") HB(202, "144") CVX(203, 201, 202)
#define VW0 "ds_write_b32 " cVW ", v203\n\t"
#define VW1 "ds_write_b32 " cVW ", v203 offset:1024\n\t"
#define VCM "v_cmp_u_f32 vcc, v201, v202\n\t"
#define TRYL TRYB
  asm volatile(
               Q1(3) Q1(4) VH0 Q1(5) VW0 Q1(6) "s_barrier\n\t" VR0 Q1(7) TRD(172, cRA, 512) TRD(174, cRB, 512) Q1(8) TRD(176, cHA, 0) TRD(178, cHB, 0) VCM
               LGK(4) YP(0, 0) IS(16) VH1 YP(1, 0) VW1 TRYL
               : "+{v[144:147]}"(v1), "=&{v[168:183]}"(A), "+{v[148:163]}"(ya) YEO
               : "{v[140:143]}"(v0), [a2] "v"(af[2]), [a3] "v"(af[3]), [a4] "v"(af[4]), [a5] "v"(af[5]), [a6] "v"(af[6]), [a7] "v"(af[7]), [a8] "v"(af[8]),
                 [w2] "a"(w[2]), [w3] "a"(w[3]), [w4] "a"(w[4]), [w5] "a"(w[5]), [w6] "a"(w[6]), [w7] "a"(w[7]), [w8] "v"(w[8]), [ix] "v"(ix), "{v[112:127]}"(cst), [id] "n"(ID),
                 [w00] "a"(wo[0][0]), [w10] "a"(wo[1][0])
               : "memory", "v201", "v202", "v203", TRYC);
}
template <int ST>
__device__ __forceinline__ void y_all(const u32x8 (&wo)[4][4], const u32x8 (&w8)[2], u32x16 cst, f32x16 ya, f32x4 yc, u32x16 A, i32x4 D,
                                      unsigned ys, unsigned fv, u32x8& o, unsigned& o8, u64& e1, unsigned& nr) {
#define RH1 TRD(188, cHA, 1024) TRD(190, cHB, 1024)
#define RQ2 TRD(180, cRA, 1024) TRD(182, cRB, 1024)
#define RQ3 TRD(184, cRA, 1536) TRD(186, cRB, 1536)
#define VFL "ds_write_b32 " cVF ", %[fv]\n\t"
#define YK0 YP(2, 0) YP(3, 0) RH1 LGK(3) YP(0, 1) RQ2 Y8(0) RQ3 YP(1, 1) VFL IS(17)
#define YK1    LGK(5) Y8(1) IS(18) YP(2, 1) YP(3, 1) S16(164, 166) LGK(3) YP(0, 2) S16(165, 167) LGK(1) YP(0, 3) HA(201, "164") YP(1, 2) HB(202, "164") CVX(200, 201, 202) \
               YP(1, 3) ".if %[st]\n\tbuffer_store_dword v200, v124, %[D], %[ys] offen sc1\n\t.endif\n\t" YC(0, 0) YP(2, 2) YC(0, 1) YP(2, 3) YS(0) YC(1, 0)   \
               YP(3, 2) YC(1, 1) YP(3, 3) IS(19) YS(1) YC(2, 0) YC(2, 1) YS(2) "s_nop 5-2*%[st]\n\t" YC(3, 0) YC(3, 1) YS(3) IS(20) EVA(1)
  asm volatile(YK0 YK1
               "v_cmp_u_f32 vcc, v148, v150\n\tv_cmp_u_f32 s[46:47], v201, v202\n\ts_or_b64 vcc, vcc, s[46:47]\n\ts_cbranch_scc1 19f\n"
               "13:\n\t.subsection 1\n"
               "19:\n\tds_read_b32 v204, " cVR "\n\t" LGK(0) "v_cmp_gt_i32 vcc, %[fv], v204\n\ts_cbranch_vccnz 19b\n\t"
               CN(100) VR0 VR1 RH1 RQ2 RQ3 YZERO LGK(0) YP(0, 0) YP(1, 0) YP(2, 0) YP(3, 0) YP(0, 1) Y8(0) YP(1, 1) YK1 "s_branch 13b\n\t.subsection 0"
               : "=&{v[192:199]}"(o), "=&{v200}"(o8) YEO YEN
               : [w00] "a"(wo[0][0]), [w01] "a"(wo[0][1]), [w02] "a"(wo[0][2]), [w03] "a"(wo[0][3]), [w10] "a"(wo[1][0]), [w11] "a"(wo[1][1]),
                 [w12] "a"(wo[1][2]), [w13] "a"(wo[1][3]), [w20] "a"(wo[2][0]), [w21] "a"(wo[2][1]), [w22] "a"(wo[2][2]), [w23] "a"(wo[2][3]),
                 [w30] "a"(wo[3][0]), [w31] "a"(wo[3][1]), [w32] "a"(wo[3][2]), [w33] "a"(wo[3][3]), [w80] "v"(w8[0]), [w81] "v"(w8[1]),
                 "{v[112:127]}"(cst), "{v[148:163]}"(ya), "{v[164:167]}"(yc), "{v[168:183]}"(A),
                 [D] "s"(D), [ys] "s"(ys), [fv] "v"(fv), [st] "n"(ST)
               : "memory", "vcc", "scc", "s46", "s47", "v184", "v185", "v186", "v187", "v188", "v189", "v190", "v191", "v201", "v202", "v204");
}
__device__ __forceinline__ void role_vcu(const Args& a, rsrc_t R, const Go& go, lds_char* lds, int h, int tid, int wave, int lane) {
  const int l15 = lane & 15, g = lane >> 4;
  u32x8 wv[2][9], wo[4][4], w8[2];
  int cv0 = -1;
  {
    u32x4 st[36], so[36];
#pragma unroll
    for (int k = 0; k < 36; ++k) so[k] = gld<NTM & 2>(a.Wo + (((size_t)h * 128 * DM + (tid + 256 * k) * 8) & ((NOCW & 2) ? 2047 : ~0ull)));
    if (wave == 0) WK3(232); else if (wave == 1) WK3(240); else if (wave == 2) WK3(248); else WK3(256);
    const int q = l15 >> 2;
    const int lrow = 16 * (g >> 1) + 4 * (g & 1) + 8 * (q >> 1) + (q & 1), lrow2 = 8 * (g >> 1) + 4 * (g & 1) + (q & 1), up2 = (g & 1) ? 12 : 20;
    auto wrd = [&](lds_char* p, int rs, int up) {
      u32x2 v0 = trd(p), v1 = trd(p + 2 * rs), v2 = trd(p + up * rs), v3 = trd(p + (up + 2) * rs);
      return (u32x8){v0[0], v0[1], v1[0], v1[1], v2[0], v2[1], v3[0], v3[1]};
    };
#pragma unroll
    for (int ph = 0; ph < 4; ++ph) {
      if (ph < 2) {
#pragma unroll
        for (int k = 0; k < 18; ++k) { const int u = tid + 256 * k; *(lds_u32x4*)(lds + (u / 72) * 1184 + (u % 72) * 16) = so[ph * 18 + k]; }
#pragma unroll
        for (int k = ph * 18; k < ph * 18 + 18; ++k) { const int u = tid + 256 * k; st[k] = gld<NTM & 2>(a.Wv + (((size_t)(u >> 4) * HD + h * 128 + (u & 15) * 8) & ((NOCW & 2) ? 2047 : ~0ull))); }
      } else {
#pragma unroll
        for (int k = (ph - 2) * 16; k < (ph == 2 ? 16 : 36); ++k) { const int u = tid + 256 * k; *(lds_u32x4*)(lds + ((u >> 4) - (ph - 2) * 256) * 288 + (u & 15) * 16) = st[k]; }
      }
      if (ph == 3 && go_wait(R, go, lane)) { cv0 = cls_look(R, lane); CBAR(); }
      BAR();
      if (ph < 2) {
#pragma unroll
        for (int k = 0; k < 8; ++k) wo[k >> 1][2 * ph + (k & 1)] = wrd(lds + (lrow2 + 32 * (k & 1)) * 1184 + (wave * 9 + (k & 6) + (q >> 1)) * 32 + (l15 & 3) * 8, 1184, up2);
        w8[ph] = wrd(lds + (lrow2 + 32 * (q >> 1)) * 1184 + (wave * 9 + 8) * 32 + (l15 & 3) * 8, 1184, up2);
        SB();
#pragma unroll
        for (int k = 0; k < 8; ++k) pin_a(wo[k >> 1][2 * ph + (k & 1)]);
        pin_v(w8[ph]);
      } else {
#pragma unroll
        for (int k = 0; k < (ph == 2 ? 8 : 10); ++k) {
          const int i = k >> 1, t = k & 1;
          wv[t][(ph - 2) * 4 + i] = wrd(lds + ((i < 4 ? (i + wave) & 3 : 4) * 64 + lrow) * 288 + (64 * t + 16 * wave) * 2 + (l15 & 3) * 8, 288, 32);
        }
        SB();
#pragma unroll
        for (int k = 0; k < (ph == 2 ? 8 : 10); ++k) { if (k < 8) pin_a(wv[k & 1][(ph - 2) * 4 + (k >> 1)]); else pin_v(wv[k & 1][8]); }
      }
      BAR();
    }
  }
  RT(1);
  const int cls = get_cls(R, lane, cv0);
  if (cls < 0) return;
  RT(2);
  auto run = [&](auto ROLEC) {
  constexpr int ROLE = decltype(ROLEC)::value;
  lds_char* img = lds;
  const unsigned fo = (unsigned)(cls & 1) * 8192u;
  const int r0 = ROLE * 1024 + lane * 16, r1 = r0 + 4096, r2 = 8192 + lane * 16;
  const unsigned m0 = nearoff(r0) + fo, m1 = nearoff(r1) + fo, m2 = nearoff(r2) + fo;
  const unsigned lb = (unsigned)(uintptr_t)lds, lw0 = lb + ROLE * 1024;
  const i32x4 D = mkdesc(a.wb);
  const int ar = g * 256 + ((l15 >> 1) & 1) * 128 + (2 * (l15 >> 2) + (l15 & 1)) * 16;
  lds_char* ard0 = img + ROLE * 1024 + ar;
  lds_char* ard1 = img + ((ROLE + 1) & 3) * 1024 + ar;
  lds_char* ard2 = img + ((ROLE + 2) & 3) * 1024 + ar;
  lds_char* ard3 = img + ((ROLE + 3) & 3) * 1024 + ar;
  const int wz = h >> 3, tq = (h >> 1) & 3, hp = h & 1;
  const int gh = ((tq & 1) << 1) | (tq >> 1);
  unsigned yr[5], yz[5];
#pragma unroll
  for (int q = 0; q < 5; ++q) {
    const int m = ROLE * 9 + q * 2 + (q < 4 ? g & 1 : 0), o = l15 * 16 + (q < 4 ? (g >> 1) * 8 : g * 4);
    yr[q] = ywin(cls, m) + (unsigned)((wz * 2 + hp) * 1024 + tq * 256 + o);
    yz[q] = NOYC ? OOB : OFF_ZF + (unsigned)(m * 40960 + (32 + wz * 2 + hp) * 1024 + gh * 256 + o);
  }
  lds_char* vw = lds + VIM_OFF + (ROLE * 16 + l15) * 16 + g * 4;
  const unsigned ra = lb + VIM_OFF + (8 * g + 4 * (g >> 1) + (l15 >> 2)) * 16 + (l15 & 2) * 4, rh = ra + (l15 & 1) * 512;
  u32x16 cst = {(unsigned)(uintptr_t)vw, ra, ra ^ 64, rh, rh ^ 64, lb + FLG_OFF + ROLE * 4, lb + FLG_OFF + (lane & 3) * 4, (lane & 4) ? 0xEEEEu : 0x4444u,
                yr[0], yr[1], yr[2], yr[3], yr[4], 0u, 0u, 0u};
  ds_flag(lb + FLG_OFF + ROLE * 4, 0u);
  const unsigned own = own_cpy(cls, h >> 3);
  unsigned ima = lb + lane * 16;
  pin_v(ima);
  const int idx = (lane & 2) ? 0xEEEE : 0x4444;
  u32x4 af0[3];
#pragma unroll
  for (int f = 0; f < 3; ++f) {
    const int u = (f == 2 ? 8 : f * 4 + ROLE) * 64 + lane;
    af0[f] = *(const u32x4*)(a.mem0 + (u & 7) * DM + (u >> 3) * 8);
  }
  unsigned tm = 0, rcnt = 0, cv = 0, iso_acc = 0, o8 = 0, bud = SPINCAP;
  u32x8 o = {0u, 0u, 0u, 0u, 0u, 0u, 0u, 0u};
  auto y_copy = [&](int s) {
#pragma unroll
    for (int q = 0; q < 4; ++q) st64((u32x2){o[2 * q], o[2 * q + 1]}, R, yz[q], (unsigned)s * ZS);
    st32(o8, R, yz[4], (unsigned)s * ZS);
  };
  auto poison = [&]() { asm volatile("v_mov_b32 v203, -1\n\tds_write2st64_b32 " cVW ", v203, v203 offset1:4" :: "{v[112:127]}"(cst) : "memory", "v203"); };
  const unsigned cwo = FL_AN + ((h + 8) & 15) * 128;
  EVDECL;
  auto step = [&](auto PHC, int s) {
    constexpr int PH = decltype(PHC)::value & 1, FIRST = (decltype(PHC)::value >> 1) & 1, LAST = decltype(PHC)::value >> 2;
    u64 e0 = 0, e1 = 0, e3 = 0, e4 = 0, e5 = 0, e6 = 0;
    u32x4 af[9];
    f32x4 v0, v1, yc;
    f32x16 ya;
    u32x16 A;
    unsigned nsl = 0;
    PADS(232 + ROLE * 8 + decltype(PHC)::value);
    unsigned f3 = s + 1;
    pin_v(f3);
    asm volatile("" : "+{v[112:127]}"(cst));
    PAD();
    unsigned nt = 0;
    tm = __builtin_amdgcn_readfirstlane(tm); rcnt = __builtin_amdgcn_readfirstlane(rcnt);
    asm volatile("" : "+s"(tm), "+s"(rcnt));
#define MV(t, j) do { smo<t, (j) < 8>(v##t, af[j], wv[t][j], idx); SB(); } while (0)
    unsigned ms = ((((s & 7) * SLOT_REAL) ^ CPY2) + own) ^ ((s & 4) ? 0 : CPY1);
    constexpr int ID = FIRST ? -1 : 32 + ROLE * 8 + decltype(PHC)::value;
    auto head = [&]() {
      y_zero(ya, yc);
      v_zero(v0, v1);
      if (!FIRST) {
        int r = 0;
        if (ROLE == 1) r = spin_late(tm, DVQ);
        mem_issue2<ROLE == F8W ? 3 : 2>(D, m0, m1, m2, ms, lw0, lw0 + 4096, lb + F8_OFF, e3);
        if (ROLE == 1) { move_on(tm, r); SB(); }
        poison();
        PAD();
        if (LAST || nt != 0) VMW(0);
        else {
          __builtin_amdgcn_s_sleep(XSLP);
          y_copy(s - 1);
          CBAR();
          asm volatile("s_waitcnt vmcnt(%0)" :: "n"(5 + (TWO ? 1 + (ROLE == F8W) : 0)) : "memory");
        }
      } else {
        *(lds_u32x4*)(img + r0) = af0[0]; *(lds_u32x4*)(img + r1) = af0[1]; *(lds_u32x4*)(lds + F8_OFF + lane * 16) = af0[2];
        poison();
        asm volatile("s_waitcnt lgkmcnt(0)" ::: "memory");
      }
      SB();
      EV(e6);
      HEAD(MV, 5, HV);
      ISOQ(11);
      v_end<ID>(af, wv[1], idx, cst, v0, v1, A, wo, ya, e0); SB();
    };
    if (!FIRST) {
      tm -= GRID + XT; nt = ~0u;
      try_top<ID>(nt, ms, tm, rcnt, ima); SB();
      head();
      try_end(ima, nt, ms, tm, rcnt, m0, m1, m2, lw0, lw0 + 4096, lb + F8_OFF, D); SB();
    } else
    head();
    if (LAST) { st32((unsigned)(s - 1), R, lane == 0 ? (unsigned)(FL_YPROG + h * 16 + ROLE * 4) : OOB, 0); SB(); }
    EV(e4); ISOQ(13);
    y_all<!LAST>(wo, w8, cst, ya, yc, A, D, (unsigned)(s & 1) * YSLOT_REAL, f3, o, o8, e5, nsl); SB();
    ISOQ(15);
    PAD();
#undef MV
    if (FIRST) {
      VMW(0); BAR();
      st32(0u, R, tid == 0 ? (unsigned)(FL_WP + (32 + h) * 32) : OOB, 0);
    }
    if (LAST || (!PH && !FIRST && UNLIKELY(s == NSEG - 2))) y_copy(s);
    CBAR();
    if (ROLE == 1 && !LAST) {
      if (FIRST) { tm = get_t1(R, cwo, bud); asm volatile("" : "+s"(tm)); }
      else
      next_time(tm, s);
    }
    if (PH && !LAST && UNLIKELY(s == WLAST)) WALK(232 + ROLE * 8 + 5);
    PADE();
    EVPUT(0, e0); EVPUT(6, e6); EVPUT(4, e4);
    EVPUTI(1, nt); EVPUT(2, e1); if (!FIRST) EVPUT(3, e3);
  };
  step(std::integral_constant<int, 2>(), 0);
#pragma nounroll
  for (int s = 1; s < NSEG - 1; s += 2) {
    step(std::integral_constant<int, 1>(), s);
    step(std::integral_constant<int, 0>(), s + 1);
  }
  step(std::integral_constant<int, 5>(), NSEG - 1);
  VMW(0);
  st32((unsigned)(NSEG - 1), R, lane == 0 ? (unsigned)(FL_YPROG + h * 16 + ROLE * 4) : OOB, 0);
  EVDUMP();
  };
  if (wave == 0) run(std::integral_constant<int, 0>());
  else if (wave == 1) run(std::integral_constant<int, 1>());
  else if (wave == 2) run(std::integral_constant<int, 2>());
  else run(std::integral_constant<int, 3>());
  RT(7);
}

#define NG 4
#define UL(k, o) "buffer_load_dwordx4 v[128+4*" #k ":128+4*" #k "+3], %[u" #o ", %[D], %[su] offen offset:(" #k "&3)*1024 sc1\n\t"
#define UL4(c, k0, k1, k2, k3, o) ".if " #k0 c "4*" XSTR(NG) "\n\t" UL(k0, o) UL(k1, o) UL(k2, o) UL(k3, o) ".endif\n\t"
#define ULD(c) UL4(c, 0, 1, 2, 3, 0]) UL4(c, 4, 5, 6, 7, 1]) UL4(c, 8, 9, 10, 11, 2]) UL4(c, 12, 13, 14, 15, 3])   \
               UL4(c, 16, 17, 18, 19, 4]) UL4(c, 20, 21, 22, 23, 5]) UL4(c, 24, 25, 26, 27, 6]) UL4(c, 28, 29, 30, 31, 7])
#define UG(G, k0, k1, k2, k3, o) "s_bfe_u32 %[k], %[r], 0x40000+4*" #G "\n\ts_cmp_lg_u32 %[k], 15\n\ts_cbranch_scc1 23f\n\t" \
                                 UL(k0, o) UL(k1, o) UL(k2, o) UL(k3, o) "s_or_b32 %[sa], %[sa], 15<<(4*" #G ")\n23:\n\t"
#define USL 2
#define ULD0 ULD(" < ")
#define ULD1 ULD(" >= ")
#define TB 12
#define UM(T, lo, hi, n) ".if " #T " >= " lo " && " #T " < " hi "\n\t.if !(" #T " & 1)\n\ts_waitcnt vmcnt(" n "+28-2*" #T ")\n\t.endif\n\t"   \
                         "v_swap_b32 v[129+8*" #T "], v[132+8*" #T "]\n\tv_swap_b32 v[131+8*" #T "], v[134+8*" #T "]\n\t.endif\n\t"
#define UMS(...) UM(0, __VA_ARGS__) UM(1, __VA_ARGS__) UM(2, __VA_ARGS__) UM(3, __VA_ARGS__) UM(4, __VA_ARGS__) UM(5, __VA_ARGS__) UM(6, __VA_ARGS__) UM(7, __VA_ARGS__) \
                 UM(8, __VA_ARGS__) UM(9, __VA_ARGS__) UM(10, __VA_ARGS__) UM(11, __VA_ARGS__) UM(12, __VA_ARGS__) UM(13, __VA_ARGS__) UM(14, __VA_ARGS__) UM(15, __VA_ARGS__)
#define TIMES "s_and_b32 %[tt], %[lev], 255\n\ts_add_u32 %[tn], %[tq], %[tt]\n\ts_add_u32 %[tn], %[tn], %[t0]\n\t" \
              "s_lshr_b32 %[tt], %[lev], 8\n\ts_add_u32 %[tt], %[tt], %[tq]\n\t"
#define DZERO ".rept 32\n\tv_mov_b64 v[64+2*\\+:65+2*\\+], 0\n\t.endr\n\tv_mov_b64 v[60:61], 0\n\tv_mov_b64 v[62:63], 0\n\t"
#define WP(a, b) #a ":\n\ts_cbranch_execz " #b "\n\t"
#define DZ6(k) ".rept 6\n\tv_mov_b64 v[60+12*" #k "+2*\\+:61+12*" #k "+2*\\+], 0\n\t.endr\n\t"
#define YLD(k, o) "buffer_load_dwordx4 a[64+4*" #k ":64+4*" #k "+3], %[y" #o ", %[D], %[sy] offen offset:(" #k "&3)*1024 sc1\n\t"
#define YPO(k, o) "buffer_store_dwordx4 %[one], %[y" #o ", %[D], %[sy] offen offset:(" #k "&3)*1024 sc1\n\t"
#define ELD(T, o) "buffer_load_dwordx4 a[4*" #T ":4*" #T "+3], %[e" #o ", %[D], %[sq] offen offset:(" #T "&3)*1024 sc1\n\t"
#define PLD(G) "buffer_load_dwordx4 a[96+4*" #G ":96+4*" #G "+3], %[pm], %[D], %[sq] offen offset:" #G "*1024 sc1\n\t"
#define ALD(G) "buffer_load_dwordx4 a[112+4*" #G ":112+4*" #G "+3], %[al], %[D], %[sq] offen offset:" #G "*256 sc1\n\t"
#define VW(X, n) "s_waitcnt vmcnt(1+" n ")\n\t"
#define EM(T) "v_smfmac_f32_16x16x64_bf16 v[64+4*" T ":64+4*" T "+3], a[4*" T ":4*" T "+3], v[128+8*" T ":128+8*" T "+7], %[ie]\n\t"
#define WC0(T) "v_cvt_pk_bf16_f32 v[64+2*" #T "], v[64+4*" #T "], v[64+4*" #T "+1]\n\t"
#define WC1(T) "v_cvt_pk_bf16_f32 v[65+2*" #T "], v[64+4*" #T "+2], v[64+4*" #T "+3]\n\t"
#define WCV(T) WC0(T) WC1(T)
#define EM2_0(T, C, o) VW("0", "30-" #T) EM(#T) WC0(C) EM("(" #T "+1)") WC1(C)

#define PMM(X, G) VW(X, "7-" #G) "v_smfmac_f32_16x16x64_bf16 v[60:63], a[96+4*" #G ":96+4*" #G "+3], a[64+8*" #G ":64+8*" #G "+7], %[ip]\n\t"
#define AMM(X, G) VW(X, "3-" #G) "v_smfmac_f32_16x16x64_bf16 v[60:63], a[112+4*" #G ":112+4*" #G "+3], v[64+8*" #G ":64+8*" #G "+7], %[ia]\n\t"
#define TRY(X, EM2, a, b, c, d)                                                                                                       \
      SPIN(tt, a, b, c) MEV(1) MCY(0)                                                                                                \
      "buffer_load_dword v59, %[es], %[D], %[sq] offen sc1\n\t" ELD(0, 0]) ELD(1, 0]) ELD(2, 0]) ELD(3, 0]) ELD(4, 1]) ELD(5, 1]) ELD(6, 1]) ELD(7, 1])                                        \
      ELD(8, 2]) ELD(9, 2]) ELD(10, 2]) ELD(11, 2]) ELD(12, 3]) ELD(13, 3]) ELD(14, 3]) ELD(15, 3])                                  \
      YLD(0, 0]) YLD(1, 0]) YLD(2, 0]) YLD(3, 0]) YLD(4, 1]) YLD(5, 1]) YLD(6, 1]) YLD(7, 1])                                        \
      PLD(0) PLD(1) PLD(2) PLD(3) ALD(0) ALD(1) ALD(2) ALD(3) MCY(1) "buffer_load_dword %[ub], %[uq], %[D], 0 offen sc1\n\ts_cmp_lg_u32 %[sx], 0\n\ts_cbranch_scc1 78f\n\t" UMS(XSTR(TB), "16", "43") "s_mov_b32 %[sx], 1\n77:\n\t"                                                                 \
      VW("0", "32") "v_cmp_eq_u32 vcc, -1, v59\n\t" VW("0", "30") "s_cbranch_vccnz 61f\n62:\n\t" EM("0") MCY(2) EM("1")                                                                                           \
      EM2(2, 0, 0]) EM2(4, 1, 0]) EM2(6, 2, 0]) EM2(8, 3, 0]) EM2(10, 4, 1]) EM2(12, 5, 1]) EM2(14, 6, 1]) MCY(3)                           \
      "s_cmp_lt_i32 %[r], -" XSTR(LTOL) "\n\ts_cbranch_scc0 " d "f\n\t"                              \
      "63:\n\ts_add_u32 %[tq], %[tq], " XSTR(GRID) "\n\ts_add_u32 %[t0], %[t0], " XSTR(XT) "\n\ts_add_u32 %[tn], %[tn], " XSTR(GRID) "+" XSTR(XT) "\n\ts_add_u32 %[r], %[r], " XSTR(GRID) "\n\t"   \
      "s_cmp_lt_i32 %[r], -" XSTR(LTOL) "\n\ts_cbranch_scc1 63b\n\t" MCA(1000)                                                        \
      d ":\n\t" WCV(7) WCV(8) WCV(9) WCV(10) WCV(11) WCV(12) WCV(13) WCV(14) WCV(15)                                                 \
      PMM(X, 0) MCY(4) PMM(X, 1) PMM(X, 2) PMM(X, 3) AMM(X, 0) AMM(X, 1) AMM(X, 2) AMM(X, 3) MCY(5)                                  \
      PADN "s_nop 6\n\t"                                                                                                             \
      "v_pk_add_f32 v[60:61], v[60:61], v[62:63]\n\t"                                                                                \
      "v_cvt_pk_bf16_f32 v62, v60, v61\n\tv_cmp_u_f32 vcc, v60, v61\n\ts_nop 0\n\t"                                                  \
      "v_mov_b32_dpp v63, v62 quad_perm:[1,0,3,2] row_mask:0xf bank_mask:0xf\n\t"                                                    \
      "v_perm_b32 v61, v63, v62, %[sel]\n\ts_cbranch_vccnz 50f\n\t"
#define MEV(n)
#define MEVPUT
#define MCY(n)
#define MCYPUT
#define MCYO
#define MCN
#define MCA(k)
#define FME
#define FML
#define FMZ
#define VR8(p, b) p #b "0", p #b "1", p #b "2", p #b "3", p #b "4", p #b "5", p #b "6", p #b "7", p #b "8", p #b "9"
__device__ __forceinline__ void role_mix(const Args& a, rsrc_t R, const Go& go, int m, int wave, int lane) {
  if (wave >= 2) {
    unsigned t[4];
#pragma unroll
    for (int k = 0; k < 4; ++k) {
      const int u = ((m * 2 + wave - 2) * 4 + k) * 64 + lane;
      t[k] = ld32(R, u < 8192 ? (unsigned)u * 128u : (u < 12800 ? OFF_YR + (unsigned)(u - 8192) * 128u : (u < 17408 ? OFF_YR + YSLOT_REAL + (unsigned)(u - 12800) * 128u : OOB)), 0);
    }
    asm volatile("" :: "v"(t[0]), "v"(t[1]), "v"(t[2]), "v"(t[3]));
  }
  if (wave != 0 && (wave != 2 || m != 0)) return;
  const int l15 = lane & 15, g = lane >> 4;
  if (wave == 0) {
#pragma unroll
    for (int k = 0; k < 32; ++k) st128((u32x4){~0u, ~0u, ~0u, ~0u}, R, OFF_ZF + (unsigned)(m * 40960 + lane * 16), k * 1024);
  }
  if (!go_wait(R, go, lane)) return;
  VMW(0);
  st32(0u, R, lane == 0 && wave == 0 ? (unsigned)(FL_MPOI + m * 4) : OOB, 0);
  RT(3);
  if (wave == 2) {
    for (int k = 0; k < TSL; ++k) __builtin_amdgcn_s_sleep(32);
    for (unsigned spins = 0; spins < SPINCAP; ++spins) {
      CBAR();
      if (__all((int)(ld32(R, FL_MRDY + min(lane, 35) * 4, 0) | ld32(R, FL_WP + lane * 32, 0)) >= 0)) break;
      __builtin_amdgcn_s_sleep(1);
    }
    st32((rt_now() + T1D) & ~1u, R, lane < 16 ? (unsigned)(FL_AN + lane * 128) : OOB, 0);
    return;
  }
  for (int k = 0; k < CSL; ++k) __builtin_amdgcn_s_sleep(32);
  const int cls = get_cls(R, lane);
  if (cls < 0) return;
  RT(4);
  const unsigned fo = (unsigned)(cls & 1) * 8192u, y0 = ywin(cls, m) + lane * 16;
  const u32x4 one = {~0u, ~0u, ~0u, ~0u};
  const unsigned mc = (unsigned)nearoff(m * 256 + l15 * 16) + fo + (g & 1) * CPY1 + (g >> 1) * CPY2;
#pragma unroll
  for (int k = 0; k < 8; ++k) st128(one, R, y0 + k * 1024, YSLOT_REAL);
  st128(one, R, mc, SLOT_REAL);
  RT(1);
  unsigned uo[8], eo[4];
#pragma unroll
  for (int k = 0; k < 8; ++k) uo[k] = OFF_ZF + (unsigned)(m * 40960 + k * 4096 + lane * 16);
#pragma unroll
  for (int k = 0; k < 4; ++k) {
    eo[k] = (unsigned)nearoff(NEAR_E + k * 4096 + lane * 16) + fo;
  }
  const unsigned pm = (unsigned)nearoff(NEAR_PM + lane * 16) + fo;
  const int qi = 2 * (l15 >> 2) + (l15 & 1);
  const unsigned al = !(l15 & 2) && (g & 1) == (qi >> 2) ? (unsigned)nearoff(NEAR_AL + ((g >> 1) * 8 + qi) * 16) + fo : OOB;
  const unsigned mo = (unsigned)nearoff((2 * m + (l15 >> 3)) * 128 + (2 * g + (l15 & 1)) * 16 + ((l15 & 7) >> 1) * 4) + fo;
  const unsigned sm = lane < 36 ? (unsigned)nearoff(lane * 256) + fo : OOB, own = own_cpy(cls, m / 9);
  const unsigned an = FL_AN + (m & 15) * 128, wp = FL_WP + lane * 32;
  const unsigned ie = (lane & 4) ? 0xEEEE : 0x4444, ip = (lane & 2) ? 0xEEEE : 0x4444, ia = ie;
  const unsigned sel = (lane & 1) ? 0x03020706u : 0x05040100u;
  const i32x4 D = mkdesc(a.wb);
  const unsigned uf = lane < 32 ? (unsigned)(FL_UPROG + lane * 128 + (m / 9) * 4) : OOB;
  for (int k = 0; k < MSL; ++k) __builtin_amdgcn_s_sleep(16);
  RT(2);
  unsigned tq = 0, lev = PACE0, bud = SPINCAP, s = 0, su = 0, sy = 0, sp = 0, tt, sa, sb, sc, sn, tn, ln, ue, t0, sx, sq, cw, sv, ua = ~0u, ub, uq, nr = 0, cv = 0;
  int ufl = lane < 32 ? -1 : 0x7fffffff;
  unsigned ox = lane < 32 ? OOB : an;
  unsigned ev0 = 0, ev1 = 0, ev2 = 0, ev3 = 0, ec[7] = {0, 0, 0, 0, 0, 0, 0};
  int r, k;
  asm volatile(
      "s_mov_b64 exec, 0\n\ts_branch 200f\n299:\n\ts_mov_b64 exec, -1\n\t"
      "s_mov_b32 %[sa], 0\n\ts_mov_b32 %[sb], " XSTR(D0) "\n"
      "27:\n\tbuffer_load_dword v59, %[up], %[D], 0 offen sc1\n\tbuffer_load_dword %[cw], %[wp], %[D], 0 offen sc1\n\t"
      "s_mov_b32 %[tq], %[sb]\n\ts_waitcnt vmcnt(0)\n\tv_cmp_ne_u32 vcc, -1, v59\n\ts_andn2_b32 %[r], vcc_lo, %[sa]\n\ts_cbranch_scc0 26f\n\t"
      UG(0, 0, 1, 2, 3, 0]) UG(1, 4, 5, 6, 7, 1]) UG(2, 8, 9, 10, 11, 2]) UG(3, 12, 13, 14, 15, 3])
      UG(4, 16, 17, 18, 19, 4]) UG(5, 20, 21, 22, 23, 5]) UG(6, 24, 25, 26, 27, 6]) UG(7, 28, 29, 30, 31, 7])
      "s_cmp_lg_u32 %[sa], -1\n\ts_cbranch_scc1 26f\n\tv_mov_b32 v59, 0\n\tbuffer_store_dword v59, %[rdy], %[D], 0 offen sc1\n"
      "26:\n\tv_cmp_gt_i32 vcc, 0, %[cw]\n\ts_cbranch_vccnz 25f\n\ts_mov_b32 %[sb], 0\n\ts_cmp_lg_u32 %[sa], -1\n\ts_cbranch_scc0 24f\n"
      "25:\n\ts_sleep " XSTR(USL) "\n\t" BUD("27b")
      "24:\n\ts_memrealtime vcc\n\ts_waitcnt lgkmcnt(0)\n\ts_add_u32 %[tq], %[tq], vcc_lo\n\ts_sub_u32 %[tq], %[tq], " XSTR(DE0) "\n\ts_branch 28f\n"
      "29:\n\t" ULD0 ULD1 "28:\n\ts_mov_b32 %[t0], 0\n\ts_mov_b32 %[sb], 0\n\ts_mov_b32 %[ue], 2*" XSTR(SLOT_REAL) "\n\ts_waitcnt vmcnt(0)\n\t"
      "30:\n\t" FMZ DZERO
      "s_add_u32 %[su], %[s], 1\n\ts_and_b32 %[su], %[su], " XSTR(RMASK) "\n\ts_mul_i32 %[su], %[su], 36*40960\n\t"
      "s_add_u32 %[sn], %[sp], " XSTR(SLOT_REAL) "\n\ts_and_b32 %[sn], %[sn], 7*" XSTR(SLOT_REAL) "\n\t"
      "s_lshr_b32 %[sq], %[sp], 5\n\ts_and_b32 %[sq], %[sq], " XSTR(CPY1) "\n\ts_xor_b32 %[sq], %[sq], %[own]\n\ts_add_u32 %[sq], %[sq], %[sp]\n\t"
      "s_movk_i32 %[ln], " XSTR(PACEC) "\n\ts_cmp_lt_u32 %[s], " XSTR(S2) "-1\n\ts_cmovk_i32 %[ln], " XSTR(PACEB) "\n\t"
      "s_cmp_lt_u32 %[s], " XSTR(S1) "-1\n\ts_cmovk_i32 %[ln], " XSTR(PACEA) "\n\ts_cmp_lt_u32 %[s], " XSTR(S0) "-1\n\ts_cmovk_i32 %[ln], " XSTR(PACE0) "\n\t"
      "s_add_u32 %[sc], %[sn], " XSTR(CPY1) "\n\ts_xor_b32 %[sb], %[sn], " XSTR(CPY2) "\n\ts_mov_b32 %[sx], 0\n\t" TIMES
      UMS("0", "2*" XSTR(NG), "8") "s_cmp_lg_u32 %[s], 0\n\t"
      "buffer_store_dwordx4 %[one], %[mc], %[D], %[ue] offen sc1\n\tv_max_i32 %[ufl], %[ufl], %[ua]\n\ts_cbranch_scc0 33f\n\t"
      "v_cmp_eq_u32 vcc, -1, %[sv]\n\ts_cbranch_vccnz 35f\n"
      "33:\n\tv_cmp_ge_i32 vcc, %[s], %[ufl]\n\ts_nop 0\n\tv_cndmask_b32 %[uq], %[ox], %[uf], vcc\n\t"
      UMS("2*" XSTR(NG), XSTR(TB), "9") MEV(3)
      "75:\n\t" TRY("0", EM2_0, "40", "41", "42", "43")
      "51:\n\tbuffer_store_dword v61, %[mo], %[D], %[sn] offen sc1\n\tbuffer_store_dword v61, %[mo], %[D], %[sc] offen sc1\n\ts_memrealtime vcc\n\t" MEV(2) MCY(6)
      "buffer_store_dword v61, %[mo], %[D], %[sb] offen sc1\n\ts_add_u32 %[sb], %[sb], " XSTR(CPY1) "\n\tbuffer_store_dword v61, %[mo], %[D], %[sb] offen sc1\n\t"
      "s_waitcnt vmcnt(4) lgkmcnt(0)\n\ts_add_u32 %[k], vcc_lo, " XSTR(MGAP) "\n"
      "97:\n\ts_sub_u32 %[r], %[tn], %[k]\n\ts_cmp_lt_i32 %[r], 0\n\ts_cbranch_scc0 98f\n\ts_add_u32 %[tn], %[tn], " XSTR(GRID) "+" XSTR(XT) "\n\ts_branch 97b\n"
      "98:\n\t" MEVPUT MCYPUT MCN
      "s_add_u32 %[s], %[s], 1\n\ts_cmp_ge_u32 %[s], 63\n\ts_cbranch_scc1 60f\n\t"
      "v_max_i32 %[ufl], %[ufl], %[ub]\n\tv_cmp_gt_i32 vcc, %[s], %[ufl]\n\ts_mov_b32 %[sx], 1\n\ts_cbranch_vccnz 100f\n\t"
      "s_mov_b32 %[sx], 0\n\t" ULD0 "100:\n\t"
      "s_cmp_lg_u32 %[s], 1\n\ts_cbranch_scc1 67f\n\t"
      "v_readlane_b32 %[tn], %[ub], 32\n\tv_mov_b32 %[ox], " XSTR(OOB) "\n\ts_cmp_lg_u32 %[tn], -1\n\ts_cbranch_scc1 67f\n"
      "66:\n\tbuffer_load_dword %[cw], %[an], %[D], 0 offen sc1\n\ts_waitcnt vmcnt(0)\n\tv_readfirstlane_b32 %[tn], %[cw]\n\t"
      "s_cmp_lg_u32 %[tn], -1\n\ts_cbranch_scc1 67f\n\t" BUD("66b")
      "67:\n\t"
      "s_xor_b32 %[ue], %[sn], 2*" XSTR(SLOT_REAL) "\n\ts_and_b32 %[ue], %[ue], 3*" XSTR(SLOT_REAL) "\n\ts_mov_b32 %[sp], %[sn]\n\t"
      "s_mov_b32 %[tq], %[tn]\n\ts_mov_b32 %[t0], 0\n\ts_mov_b32 %[lev], %[ln]\n\t"
      "v_cmp_ge_i32 vcc, %[s], %[ufl]\n\tv_mov_b32 %[ua], " XSTR(OOB) "\n\tv_cndmask_b32 %[ua], %[ua], %[uf], vcc\n\t"
      "s_lshr_b32 %[sa], %[sn], 5\n\ts_and_b32 %[sa], %[sa], " XSTR(CPY1) "\n\ts_xor_b32 %[sa], %[sa], %[own]\n\ts_add_u32 %[sa], %[sa], %[sn]\n\t"
      SPIN(tq, "52", "53", "54") "buffer_load_dword %[sv], %[sm], %[D], %[sa] offen sc1\n\t"
      "buffer_load_dword %[ua], %[ua], %[D], 0 offen sc1\n\t"
      "s_cmp_eq_u32 %[sx], 0\n\ts_cbranch_scc1 101f\n\t"
      "55:\n\ts_waitcnt vmcnt(0)\n\tv_max_i32 %[ufl], %[ufl], %[ua]\n\tv_cmp_gt_i32 vcc, %[s], %[ufl]\n\ts_cbranch_vccz 57f\n\t" MCA(100000)
      "buffer_load_dword %[ua], %[uf], %[D], 0 offen sc1\n\t" BUD("55b")
      "57:\n\t" ULD0 "101:\n\t" ULD1 YPO(0, 0]) YPO(1, 0]) YPO(2, 0]) YPO(3, 0]) YPO(4, 1]) YPO(5, 1]) YPO(6, 1]) YPO(7, 1])
      "s_xor_b32 %[sy], %[sy], " XSTR(YSLOT_REAL) "\n\ts_branch 30b\n\t"
      WP(200, 201f) "35:\n\t" MCA(100) BUD("36f") "s_branch 32f\n\t"
      "36:\n\ts_add_u32 %[tq], %[tq], " XSTR(GRID) "+" XSTR(XT) "\n\ts_xor_b32 %[sa], %[sa], %[flp]\n\t" WP(201, 202f)
      SPIN(tq, "37", "38", "39") "buffer_load_dword %[sv], %[sm], %[D], %[sa] offen sc1\n\t" WP(202, 203f) "s_waitcnt vmcnt(0)\n\t"
      "v_cmp_eq_u32 vcc, -1, %[sv]\n\ts_cbranch_vccnz 35b\n"
      "32:\n\t" TIMES "s_branch 33b\n" WP(203, 204f)
      "59:\n\t" WP(204, 205f) "s_waitcnt lgkmcnt(0)\n\ts_lshr_b32 %[tt], %[lev], 8\n\ts_sub_u32 %[k], vcc_lo, %[tt]\n\ts_sub_u32 %[k], %[k], " XSTR(RTOL) "\n\t"
      "34:\n\ts_add_u32 %[tq], %[tq], " XSTR(GRID) "\n\ts_add_u32 %[t0], %[t0], " XSTR(XT) "\n\ts_sub_u32 %[r], %[tq], %[k]\n\ts_cmp_lt_i32 %[r], 0\n\ts_cbranch_scc1 34b\n\t"
      WP(205, 206f) TIMES "s_branch 75b\n\t" WP(206, 207f)
      "58:\n\ts_cmp_lg_u32 %[s], 0\n\ts_cbranch_scc1 102f\n\ts_mov_b32 %[su], 0\n\ts_branch 29b\n102:\n\ts_memrealtime vcc\n\ts_xor_b32 %[sq], %[sq], %[flp]\n\ts_branch 59b\n\t"
      WP(207, 208f) "78:\n\t" DZ6(0) WP(208, 209f) DZ6(1) WP(209, 210f) DZ6(2) WP(210, 211f) DZ6(3) WP(211, 212f) DZ6(4) WP(212, 213f)
      ".rept 4\n\tv_mov_b64 v[120+2*\\+:121+2*\\+], 0\n\t.endr\n\ts_branch 77b\n\t" WP(213, 214f)
      "50:\n\t" FML MCA(1) BUD("58b") "v_min_u32 v61, -2, v61\n\ts_branch 51b\n\t" WP(214, 215f)
      "99:\n\ts_memrealtime vcc\n\ts_xor_b32 %[sq], %[sq], %[flp]\n\ts_branch 59b\n\t"
      "61:\n\t" FME MCA(1) BUD("99b") "s_branch 62b\n\t" WP(215, 299b)
      "60:\n\ts_waitcnt vmcnt(0)"
      : [tq] "+&s"(tq), [lev] "+&s"(lev), [bud] "+&s"(bud), [s] "+&s"(s), [su] "+&s"(su), [sy] "+&s"(sy), [sp] "+&s"(sp), [tt] "=&s"(tt), [sa] "=&s"(sa),
        [sn] "=&s"(sn), [sb] "=&s"(sb), [sc] "=&s"(sc), [tn] "=&s"(tn), [ln] "=&s"(ln), [ue] "=&s"(ue), [t0] "=&s"(t0), [sx] "=&s"(sx), [sq] "=&s"(sq), [cw] "=&v"(cw), [sv] "=&v"(sv), [ua] "+&v"(ua), [ub] "=&v"(ub), [uq] "=&v"(uq), [ufl] "+&v"(ufl), [ox] "+&v"(ox), [r] "=&s"(r), [k] "=&s"(k), [nr] "+&s"(nr), [cv] "+&v"(cv),
        [ev0] "+&v"(ev0), [ev1] "+&v"(ev1), [ev2] "+&v"(ev2), [ev3] "+&v"(ev3) MCYO
      : [D] "s"(D), [u0] "v"(uo[0]), [u1] "v"(uo[1]), [u2] "v"(uo[2]), [u3] "v"(uo[3]), [u4] "v"(uo[4]), [u5] "v"(uo[5]), [u6] "v"(uo[6]), [u7] "v"(uo[7]), [e0] "v"(eo[0]), [e1] "v"(eo[1]), [e2] "v"(eo[2]),
        [e3] "v"(eo[3]), [y0] "v"(y0), [y1] "v"(y0 + 4096), [pm] "v"(pm), [al] "v"(al), [mo] "v"(mo), [mc] "v"(mc), [an] "v"(an), [wp] "v"(wp),
        [ie] "v"(ie), [ip] "v"(ip), [ia] "v"(ia), [sel] "v"(sel), [one] "v"(one), [sm] "v"(sm), [uf] "v"(uf), [es] "v"(lane < 32 ? (unsigned)nearoff(NEAR_E + lane * 512 + (lane & 1) * (64 - 512)) + fo : OOB), [rdy] "v"(lane == 0 ? (unsigned)(FL_MRDY + m * 4) : OOB), [own] "s"(own), [flp] "s"(CPY2 + CPY1),
        [up] "v"(OFF_ZF + (unsigned)(m * 40960 + (lane & 31) * 1024 + 1020))
      : "memory", "vcc", "scc", "s68", "s69", "s70", "s71", "s72", "s73", "s74", "s75", "s76", "s77", "s78", "s79", "s80", "s81", "s84", "s85", "s86", "s87", "s88", "s89", "s90", "s91",
        "v59", VR8("v", 6), VR8("v", 7), VR8("v", 8), VR8("v", 9), VR8("v", 10), VR8("v", 11), VR8("v", 12), VR8("v", 13), VR8("v", 14), VR8("v", 15),
        VR8("v", 16), VR8("v", 17), VR8("v", 18), VR8("v", 19), VR8("v", 20), VR8("v", 21), VR8("v", 22), VR8("v", 23), VR8("v", 24),
        "v250", "v251", "v252", "v253", "v254", "v255",
        "a0", "a1", "a2", "a3", "a4", "a5", "a6", "a7", "a8", "a9", VR8("a", 1), VR8("a", 2), VR8("a", 3), VR8("a", 4), VR8("a", 5), VR8("a", 6),
        VR8("a", 7), VR8("a", 8), VR8("a", 9), VR8("a", 10), VR8("a", 11), VR8("a", 12), VR8("a", 13), VR8("a", 14), VR8("a", 15),
        VR8("a", 16), VR8("a", 17), VR8("a", 18), VR8("a", 19), VR8("a", 20), VR8("a", 21), VR8("a", 22), VR8("a", 23), VR8("a", 24),
        "a250", "a251", "a252", "a253", "a254", "a255");
  RT(7);
}

struct XL { rsrc_t X; i32x4 D; unsigned vo; int wr, rd; };
struct XP { lds_char* p[2]; };
__device__ __forceinline__ void x_init(XL& xl, const u16* x, int wave, int lane) {
  xl.X = __builtin_amdgcn_make_buffer_rsrc((void*)x, 0, S_LEN * DM * 2, 0x00020000);
  xl.D = (i32x4){(int)(unsigned)(u64)x, (int)(unsigned)((u64)x >> 32), S_LEN * DM * 2, 0x00020000};
  xl.vo = (unsigned)((wave * 8 + (lane >> 3)) * 1152 + ((lane ^ (lane >> 3)) & 7) * 16);
  xl.wr = wave * 9216 + lane * 16;
  xl.rd = ((lane >> 3) & 1) * 9216 + (lane & 7) * 128 + ((lane >> 4) ^ (lane & 7)) * 16;
}
__device__ __forceinline__ XP x_at(lds_char* buf, const XL& xl) {
  XP r = {{buf + xl.rd, buf + (xl.rd ^ 64)}};
  pin_v(r.p[0]); pin_v(r.p[1]);
  return r;
}
__device__ __forceinline__ void x_m0(unsigned b, int k) { asm volatile("s_add_u32 m0, %0, %1" :: "s"(b), "n"(k * 896) : "scc"); }
__device__ __forceinline__ void x_dma(const XL& xl, unsigned so, int k) {
  asm volatile("buffer_load_dwordx4 %0, %1, %2 offen offset:%3 lds" :: "v"(xl.vo), "s"(xl.D), "s"(so), "n"(k * 128) : "memory");
}
__device__ __forceinline__ void x_dma0(const XL& xl, unsigned so, int k) {
  asm volatile("s_nop 0\n\tbuffer_load_dwordx4 %0, %1, %2 offen offset:%3 " XNT " lds" :: "v"(xl.vo), "s"(xl.D), "s"(so), "n"(k * 128) : "memory");
}
__device__ __forceinline__ i32x4 wdesc(const u16* p) { return (i32x4){(int)(unsigned)(u64)p, (int)(unsigned)((u64)p >> 32), DM * HD * 2, 0x00020000}; }
__device__ __forceinline__ void w_dma(i32x4 D, unsigned vo, unsigned so, unsigned b, int o) {
  asm volatile("s_add_u32 m0, %3, %4\n\ts_nop 0\n\tbuffer_load_dwordx4 %0, %1, %2 offen nt lds" :: "v"(vo), "s"(D), "s"(so), "s"(b), "n"(o) : "memory", "scc");
}
#define VB_OFF 73728
#define VX_OFF (VB_OFF + 73728)
constexpr int vbuf(int j) { return j % 5 == 3 ? VX_OFF : VB_OFF + ((j + 1) % 5) * 18432; }
__device__ __forceinline__ void x_move(lds_char*& p, unsigned d) { asm volatile("v_add_u32 %0, %1, %0" : "+v"(p) : "s"(d)); }
__device__ __forceinline__ u32x4 x_ld1(const XL& xl, int s, int k) { return __builtin_amdgcn_raw_buffer_load_b128(xl.X, xl.vo + k * 128, (unsigned)s * 36864u, 0); }
__device__ __forceinline__ void x_gload(u32x4 (&xr)[9], const XL& xl, int s) {
#pragma unroll
  for (int k = 0; k < 9; ++k) xr[k] = x_ld1(xl, s, k);
}
__device__ __forceinline__ void x_swrite(const u32x4 (&xr)[9], lds_char* buf, const XL& xl) {
#pragma unroll
  for (int k = 0; k < 9; ++k) *(lds_u32x4*)(buf + xl.wr + k * 1024) = xr[k];
}
#define XF(buf, tt, ks) (*(lds_u32x4*)((buf).p[(ks) & 1] + (tt) * 18432 + ((ks) >> 1) * 1024))
__device__ __forceinline__ void proj4(lds_char* cbuf, lds_char* nbuf, u32x4 (&xr)[9], const XL& xl, int snext, int wave, int lane,
                                      const u32x4 (&w0)[18], const u32x4 (&w1)[18], const u32x4 (&w2)[18], const u32x4 (&w3)[18],
                                      f32x4 (&acc)[4][2]) {
  u32x4 bb[3][2];
  const XP buf = x_at(cbuf, xl);
  nbuf += xl.wr;
#pragma unroll
  for (int ks = 0; ks < 2; ++ks) { bb[ks][0] = XF(buf, 0, ks); bb[ks][1] = XF(buf, 1, ks); }
#pragma unroll
  for (int t = 0; t < 4; ++t) { acc[t][0] = (f32x4){0.f, 0.f, 0.f, 0.f}; acc[t][1] = acc[t][0]; }
#pragma unroll
  for (int ks = 0; ks < 18; ++ks) {
    const u32x4 b0 = bb[ks % 3][0], b1 = bb[ks % 3][1];
    SB();
    acc[0][0] = mfma(w0[ks], b0, acc[0][0]); SB();
    if (ks + 2 < 18) bb[(ks + 2) % 3][0] = XF(buf, 0, ks + 2);
    SB();
    acc[0][1] = mfma(w0[ks], b1, acc[0][1]); SB();
    if (ks + 2 < 18) bb[(ks + 2) % 3][1] = XF(buf, 1, ks + 2);
    SB();
    acc[1][0] = mfma(w1[ks], b0, acc[1][0]); SB();
    if ((ks & 1) == 0) *(lds_u32x4*)(nbuf + (ks >> 1) * 1024) = xr[ks >> 1];
    SB();
    acc[1][1] = mfma(w1[ks], b1, acc[1][1]);
    acc[2][0] = mfma(w2[ks], b0, acc[2][0]); SB();
    if ((ks & 1) == 0) xr[ks >> 1] = x_ld1(xl, snext, ks >> 1);
    SB();
    acc[2][1] = mfma(w2[ks], b1, acc[2][1]);
    acc[3][0] = mfma(w3[ks], b0, acc[3][0]); acc[3][1] = mfma(w3[ks], b1, acc[3][1]);
    SB();
  }
}
#define PFL 13
#define STG0 6
#define STGD 0
#define NOST 0
#define NOFL 0
#define FG 7
#define UFL 5
__device__ __forceinline__ int clampseg(int s) { return s < NSEG ? s : NSEG - 1; }

__device__ __forceinline__ void role_prj(const Args& a, rsrc_t R, const Go& go, lds_char* lds, const u16* W0, const u16* W1, unsigned o0, unsigned o1, unsigned flag,
                                         int tid, int wave, int lane) {
  const int l15 = lane & 15, g = lane >> 4;
  u32x4 w[2][2][18];
  u32x4 xr[9], xq[9];
  XL xl; x_init(xl, a.x, wave, lane);
  x_gload(xq, xl, 0);
  x_gload(xr, xl, 1);
  const unsigned fo = wave * 1024 + lane * 16;
  const unsigned pf = lane == 0 ? flag + wave * 4 : OOB;
  f32x4 acc[2][2][2];
  {
    u32x4 sg[PDEP][8];
    const rsrc_t B0 = __builtin_amdgcn_make_buffer_rsrc((void*)W0, 0, DM * HD * 2, 0x00020000), B1 = __builtin_amdgcn_make_buffer_rsrc((void*)W1, 0, DM * HD * 2, 0x00020000);
    const unsigned wof = (unsigned)((tid >> 4) * (HD * 2) + (tid & 15) * 16);
    auto gl = [&](int i, int k) {
      if (i < 10 && (i % 5 < 4 || k < 4)) { sg[i % PDEP][k] = __builtin_amdgcn_raw_buffer_load_b128(i < 5 ? B0 : B1, wof, (NOCW & 4) ? 0u : (unsigned)(128 * (i % 5) + 16 * k) * (HD * 2), (NTM & 4) ? 2 : 0); SB(); }
    };
#pragma unroll
    for (int k = 0; k < 8 * PDEP; ++k) gl(k >> 3, k & 7);
    if (wave == 0) WALK(100); else if (wave == 1) WALK(101); else if (wave == 2) WALK(102); else WALK(103);
    RT(2);
    PADS(100);
    lds_char *x0p = lds + X0_OFF, *x1p = lds + X1_OFF;
    pin_v(x0p); pin_v(x1p);
    x_swrite(xq, x0p, xl);
    PAD();
    x_swrite(xr, x1p, xl);
    PAD();
    const XP x0r = x_at(x0p, xl), x1r = x_at(x1p, xl);
    lds_char *wp = lds + (tid >> 4) * 288 + (tid & 15) * 16, *rp = lds + (g * 8 + (l15 >> 2)) * 288 + wave * 64 + (l15 & 3) * 8;
    pin_v(wp); pin_v(rp);
    auto put = [&](int i, int k) {
      if (i < 10 && (i % 5 < 4 || k < 4)) { *(lds_u32x4*)(wp + (i & 1) * STG_BYTES + k * (16 * 288)) = sg[i % PDEP][k]; SB(); }
    };
#pragma unroll
    for (int k = 0; k < 8; ++k) { put(0, k); gl(PDEP, k); }
    auto chunk = [&](auto IC) {
      constexpr int i = decltype(IC)::value, m = i / 5, c = i % 5, nk = c < 4 ? 4 : 2;
      if (c == 0) {
#pragma unroll
        for (int k = 0; k < 8; ++k) { acc[k >> 2][(k >> 1) & 1][k & 1] = (f32x4){0.f, 0.f, 0.f, 0.f}; pin_v(acc[k >> 2][(k >> 1) & 1][k & 1]); }
      }
      if (i == 4) { PADE(); PADS(101); } else if (i == 8) { PADE(); PADS(102); } else PAD();
      BAR();
      if (i == 0) RT(3);
      if (i == 2) RT(4);
      if (i == 4) RT(5);
      if (i == 6) RT(6);
      if (i == 7) RT(8);
      if (i == 8) RT(9);
      u32x4 xb[2][4];
      u32x2 wh[2][4];
      auto rd = [&](int kk, int j) {
        if (kk >= nk) return;
        if (j & 1) xb[kk & 1][j >> 1] = XF((j & 4) ? x1r : x0r, (j >> 1) & 1, 4 * c + kk);
        else wh[kk & 1][j >> 1] = trd(rp + (i & 1) * STG_BYTES + (kk * 32 + (j & 2) * 2) * 288 + (j >> 2) * 32);
        SB();
      };
#pragma unroll
      for (int j = 0; j < 8; ++j) rd(0, j);
#pragma unroll
      for (int k = 0; k < 8 * nk; ++k) {
        const int kk = k >> 3;
        f32x4& d = acc[(k >> 2) & 1][k & 1][(k >> 1) & 1];
        u32x4& f = w[m][k & 1][4 * c + kk];
        if ((k & 7) < 2) {
          f = (u32x4){wh[kk & 1][(k & 1) * 2][0], wh[kk & 1][(k & 1) * 2][1], wh[kk & 1][(k & 1) * 2 + 1][0], wh[kk & 1][(k & 1) * 2 + 1][1]};
          if (c < 4) pin_a(f); else pin_v(f);
        }
        if (c < 4) mfA(d, f, xb[kk & 1][(k >> 1) & 3]); else mfa(d, f, xb[kk & 1][(k >> 1) & 3]);
        SB();
        rd(kk + 1, k & 7);
        if (k % nk == 0) put(i + 1, k / nk);
        if (k % nk == 1) gl(i + 1 + PDEP, k / nk);
        if (k % 12 == 11 || k == 8 * nk - 1) PAD();
      }
      if (i == 6) x_gload(xq, xl, 2);
      if (i == 7) x_gload(xr, xl, 3);
      if (i == 6) {
        go_wait(R, go, lane);
        st32(0u, R, pf, 0);
      }
      SB();
      if (c == 4) {
        asm volatile("s_nop 7"); SB();
#pragma unroll
        for (int k = 0; k < 4; ++k) {
          PAD();
          const f32x4 (&e)[2][2] = acc[k >> 1];
          const int tt = k & 1;
          st128((u32x4){pk2(e[0][tt][0], e[0][tt][1]), pk2(e[0][tt][2], e[0][tt][3]), pk2(e[1][tt][0], e[1][tt][1]), pk2(e[1][tt][2], e[1][tt][3])}, R,
                (m ? o1 : o0) + fo + tt * 4096, (unsigned)(k >> 1) * 262144u);
        }
        SB();
      }
    };
#define CH(n) chunk(std::integral_constant<int, n>())
    CH(0); CH(1); CH(2); CH(3); CH(4); CH(5); CH(6); CH(7); CH(8); CH(9);
#undef CH
    BAR();
  }
  RT(12);
  PAD();
  x_swrite(xq, lds, xl);
  x_swrite(xr, lds + STG_BYTES, xl);
  PAD();
  u32x4 bb[3][2];
  XP rd0 = x_at(lds, xl), rd1 = x_at(lds + STG_BYTES, xl);
  unsigned b0 = 0, b1 = STG_BYTES, b2 = X1_OFF;
  const unsigned xw = (unsigned)(uintptr_t)lds + __builtin_amdgcn_readfirstlane(wave) * 9216;
  BAR();
#pragma unroll
  for (int ks = 0; ks < 2; ++ks) { bb[ks][0] = XF(rd0, 0, ks); bb[ks][1] = XF(rd0, 1, ks); }
  unsigned oa = o0 + fo, ob = o1 + fo, tc0 = 0, tr0 = 0;
  pin_v(oa); pin_v(ob);
  auto half = [&](auto HC, int s) {
    constexpr int H = decltype(HC)::value & 1, P = decltype(HC)::value >> 1;
    XP& buf = P ? rd1 : rd0;
    XP& nrd = H ? (P ? rd0 : rd1) : buf;
    const unsigned oo = H ? oa : ob;
    unsigned so, sx, xm, xd;
    f32x4 (&c)[2][2] = acc[H];
    f32x4 (&e)[2][2] = acc[H ^ 1];
    u32x4 o;
#define PM(t, tt) do { if (ks == 0) c[t][tt] = mfA0(w[H][t][0], bb[0][tt]); else if (ks < 16) mfA(c[t][tt], w[H][t][ks], bb[ks % 3][tt]); \
                       else mfa(c[t][tt], w[H][t][ks], bb[ks % 3][tt]); SB(); } while (0)
#define PR(tt) do { bb[(ks + 2) % 3][tt] = ks < 16 ? XF(buf, tt, ks + 2) : XF(nrd, tt, ks - 16); SB(); } while (0)
#pragma unroll
    for (int ks = 0; ks < 18; ++ks) {
      const int xi = H * 5 + ((ks - 7) >> 1);
      const bool xs = ks >= 7 && ks < 17 && xi < 9 && (H == 0 || ks < 15);
      if (H == 1 && ks == 16) { __builtin_amdgcn_s_waitcnt(0xC27F); __builtin_amdgcn_s_barrier(); SB(); }
      else { __builtin_amdgcn_s_waitcnt(0xC27F); SB(); }
      PM(0, 0); PR(0);
      if (ks == 0) {
        so = (unsigned)(s + H - 1) * 262144u;
        sx = (unsigned)clampseg(s + 2) * 36864u;
        asm volatile("" : "+s"(so), "+s"(sx)); SB();
      }
      if (ks == 1) {
        xm = xw + b2; xd = b2 - b0;
        if (H) { const unsigned t = b0; b0 = b1; b1 = b2; b2 = t; }
        asm volatile("" : "+s"(xm), "+s"(xd), "+s"(b0), "+s"(b1), "+s"(b2)); SB();
      }
      PM(0, 1); PR(1);
      PM(1, 0);
      if (ks == 1 || ks == 2) { o[(ks - 1) * 2] = pkv(e[ks - 1][0][0], e[ks - 1][0][1]); SB(); }
      if (ks == 4 || ks == 5) { o[(ks - 4) * 2] = pkv(e[ks - 4][1][0], e[ks - 4][1][1]); SB(); }
      if (ks == 3 && NOST < 2) { st128(o, R, oo, so); SB(); }
      if (ks == 6 && NOST < 2) { st128(o, R, oo + 4096, so); SB(); }
      if (xs && (ks & 1)) { x_m0(xm, xi); SB(); }
      if (xs && !(ks & 1)) { x_dma(xl, sx, xi); SB(); }
      if (H && ks >= 16) { x_move(buf.p[ks & 1], xd); SB(); }
      PM(1, 1);
      if (ks % 3 == 0) { if (H == 1 && P == 0 && ks == 0) { PADE(); PADS(103); } else PAD(); SB(); }
      if (ks == 1 || ks == 2) { o[(ks - 1) * 2 + 1] = pkv(e[ks - 1][0][2], e[ks - 1][0][3]); SB(); }
      if (ks == 4 || ks == 5) { o[(ks - 4) * 2 + 1] = pkv(e[ks - 4][1][2], e[ks - 4][1][3]); SB(); }
      if (ks == PFL) {
        asm volatile("s_waitcnt vmcnt(%0)" :: "n"((H ? 8 : 7) + (PFL - 6) / 2) : "memory");
        st32((unsigned)(H || P || s > 2 ? 2 * s + H - 2 : 0), R, pf, 0); SB();
      }
      if (ks == 17 && H == 1 && UNLIKELY(s < KEF)) { RT(10); VMW(5); st32((unsigned)(2 * s), R, pf, 0); RT(11); SB(); }
    }
#undef PM
#undef PR
  };
#pragma nounroll
  for (int s = 2; s < NSEG; ++s) {
    half(std::integral_constant<int, 0>(), s);
    half(std::integral_constant<int, 1>(), s);
    STEPRT(0, 0);
    ++s;
    half(std::integral_constant<int, 2>(), s);
    half(std::integral_constant<int, 3>(), s);
    STEPRT(0, 0);
  }
  PADE();
  mf_wait2(acc[1][0][0], acc[1][0][1]); mf_wait2(acc[1][1][0], acc[1][1][1]);
#pragma unroll
  for (int tt = 0; tt < 2; ++tt) {
    const u32x4 o = {pk2(acc[1][0][tt][0], acc[1][0][tt][1]), pk2(acc[1][0][tt][2], acc[1][0][tt][3]), pk2(acc[1][1][tt][0], acc[1][1][tt][1]), pk2(acc[1][1][tt][2], acc[1][1][tt][3])};
    st128(o, R, o1 + fo + tt * 4096, (unsigned)((NSEG - 1) & RMASK) * 262144u);
  }
  VMW(0);
  st32((unsigned)(2 * NSEG - 1), R, pf, 0);
  RT(7);
}

__device__ __forceinline__ void role_px(const Args& a, rsrc_t R, const Go& go, int pi, int wave, int lane) {
  const int l15 = lane & 15, g = lane >> 4;
  const int h = pi * 2 + (wave >> 1), par = wave & 1;
  const int wz = h >> 3, tq = (h >> 1) & 3, hp = h & 1;
  const int gy = ((tq & 1) << 1) | (tq >> 1);
  const unsigned kmo = OFF_KMF + (unsigned)h * 4096u + lane * 16;
  const unsigned pmo = OFF_PXF + (unsigned)((32 + wz * 2 + hp) * 1024 + (gy * 16 + l15) * 16 + g * 4), pxo = OFF_PXF + (unsigned)(h * 1024 + lane * 16);
  const unsigned ppf = (lane == 0) ? (unsigned)(FL_PXPROG + h * 128 + par * 4) : OOB;
  const unsigned qo = OFF_QF + (unsigned)h * 8192u + lane * 16, ko = OFF_KF + (unsigned)h * 8192u + lane * 16;
  const unsigned kmf = lane == 4 ? FL_KMPROG + h * 128 : (lane < 4 ? FL_PROG + h * 128 + lane * 4 : OOB);
  int fm = -1;
  RT(1);
  u32x4 qf[2][4], kf[2][4];
  auto qk = [&](int s) {
#pragma unroll
    for (int k = 0; k < 8; ++k) { kf[k >> 2][k & 3] = ld128(R, ko + k * 1024, (unsigned)s * 262144u); qf[k >> 2][k & 3] = ld128(R, qo + k * 1024, (unsigned)s * 262144u); }
  };
  if (!go_wait(R, go, lane) || !wait_flag_c<16>(R, lane == 4 ? kmf : OOB, lane == 4 ? 0 : -1, fm)) return;
  qk(par);
  for (int s = par; s < NSEG - 1; s += 2) {
    u32x4 km[4], qc[2][4];
    f32x4 T[2][2];
    float mx[2], sx[2];
#pragma unroll
    for (int qt = 0; qt < 2; ++qt) {
      T[qt][0] = T[qt][1] = (f32x4){0.f, 0.f, 0.f, 0.f};
#pragma unroll
      for (int ks = 0; ks < 4; ++ks) {
        T[qt][0] = mfma(kf[0][ks], qf[qt][ks], T[qt][0]);
        if (qt) T[1][1] = mfma(kf[1][ks], qf[1][ks], T[1][1]);
        qc[qt][ks] = qf[qt][ks];
      }
      px_mask(T[qt][0], T[qt][1], qt, lane);
      px_seg(T[qt][0], T[qt][1], mx[qt], sx[qt]);
      pin_v(T[qt][0]); pin_v(T[qt][1]); pin_v(mx[qt]); pin_v(sx[qt]);
    }
    VMW(0);
    st32((unsigned)(s - 2), R, ppf, 0);
    STEPRT(2, 0);
    const int need = lane < 4 ? min(2 * s + 5, 2 * NSEG - 1) : (lane == 4 ? s : -1);
    for (unsigned spins = 0; !__all(fm >= need); ++spins) {
      if (spins > SPINCAP) return;
      if (spins) __builtin_amdgcn_s_sleep(PXS);
      CBAR();
      fm = max(fm, (int)ld32(R, fm < need && (lane == 4 || __builtin_amdgcn_readlane(fm, 4) >= s) ? kmf : OOB, 0));
    }
    CBAR();
#pragma unroll
    for (int ks = 0; ks < 4; ++ks) km[ks] = ld128(R, kmo + ks * 1024, (unsigned)s * 131072u);
    SB();
    qk(clampseg(s + 2));
    CBAR();
#pragma unroll
    for (int qt = 0; qt < 2; ++qt) {
      f32x4 G = {0.f, 0.f, 0.f, 0.f};
#pragma unroll
      for (int ks = 0; ks < 4; ++ks) G = mfma(km[ks], qc[qt][ks], G);
      u32x4 px; unsigned pw;
      px_fin(G, T[qt][0], T[qt][1], mx[qt], sx[qt], px, pw, lane);
      st128(px, R, pxo, (unsigned)s * 81920u + qt * 40960);
      st32(pw, R, pmo, (unsigned)s * 81920u + qt * 40960);
    }
    CBAR();
    fm = (int)ld32(R, kmf, 0);
    CBAR();
  }
  VMW(0);
  st32((unsigned)(NSEG - 2 - par), R, ppf, 0);
  RT(7);
}

__device__ __forceinline__ void role_vuw(const Args& a, rsrc_t R, const Go& go, lds_char* lds, int h, int tid, int wave, int lane) {
  const int l15 = lane & 15, g = lane >> 4;
  u32x4 wv[2][18], wo[9][4];
  XL xl; x_init(xl, a.x, wave, lane);
  unsigned zo = OFF_ZF + (unsigned)(wave * 9 * 40960 + lane * 16) + h * 1024;
  const unsigned vpf = (lane == 0) ? (unsigned)(FL_UPROG + h * 128 + wave * 4) : OOB;
  const unsigned lb = (unsigned)(uintptr_t)lds, xw = lb + wave * 9216;
  lds_char *vr = lds + VX_OFF + lane * 16, *vw = vr + wave * 1024;
  f32x4 cc[2][2], av[2][2][2];
  TKD;
  {
    Go g0 = go;
    const i32x4 DV = wdesc(a.Wv + h * 128), DO = wdesc(a.Wo + (size_t)h * 128 * DM), D = mkdesc(a.wb);
    const unsigned wa = (unsigned)((16 * wave + g) * (HD * 2) + (l15 ^ (2 * g)) * 16), wb = wa ^ 128, ww = lb + wave * 4096, w1 = lb + wave * 1024;
    auto stage = [&](int j) {
      x_m0(xw, j); x_dma0(xl, 0, j);
      x_m0(xw + 36864, j); x_dma0(xl, 36864, j);
#pragma unroll
      for (int k = 0; k < 4; ++k) w_dma(DV, k < 2 ? wa : wb, (64 * j + 4 * k) * (HD * 2), ww, vbuf(j) + k * 1024);
      SB();
    };
#pragma unroll
    for (int j = 0; j < 3; ++j) stage(j);
    TK(0);
    unsigned oo[9], g1, g2;
#pragma unroll
    for (int i = 0; i < 9; ++i) { const int u = (4 * i + wave) * 64 + lane, r = u / 72; oo[i] = r * 1152 + ((u % 72) ^ (r & 6)) * 16; pin_v(oo[i]); }
    auto o_dma = [&](int k, int i0, int i1) {
#pragma unroll
      for (int i = i0; i < i1; ++i) w_dma(DO, oo[i], k * 36864, w1, ((k + 2) & 3) * 36864 + i * 4096);
      SB();
    };
    const XP x0r = x_at(lds, xl), x1r = x_at(lds + 36864, xl);
    lds_char* rp[2][2];
#pragma unroll
    for (int k = 0; k < 4; ++k) {
      rp[k >> 1][k & 1] = lds + ((k >> 1) ? VX_OFF : VB_OFF) + (g * 8 + (l15 >> 2)) * 256 + ((4 * wave + 2 * (k & 1)) ^ (2 * (l15 >> 2) + 8 * (g & 1))) * 16 + (l15 & 3) * 8;
      pin_v(rp[k >> 1][k & 1]);
    }
    asm volatile("s_load_dword %0, %1, %2\n\ts_waitcnt lgkmcnt(0)" : "=s"(g0.v0) : "s"(a.wb), "s"(__builtin_amdgcn_readfirstlane(go.vo)) : "memory");
    const unsigned pf = lane < 9 ? (unsigned)(FL_MPOI + (wave * 9 + lane) * 4) : OOB;
    TK(1);
    stage(3); stage(4);
    TK(2);
    auto vst = [&](auto JC) {
      constexpr int j = decltype(JC)::value, n = j == 0 ? 24 : (j < 4 ? 18 : (j < 6 ? 19 : (j < 7 ? 17 : 15))), hi = vbuf(j) == VX_OFF, bo = hi ? 0 : vbuf(j) - VB_OFF;
      asm volatile("s_waitcnt vmcnt(%0)" :: "n"(n) : "memory");
      __builtin_amdgcn_s_barrier(); CBAR(); SB();
      if (!(j & 1)) TK(3 + j / 2);
      u32x4 xb[2][4];
      u32x2 wh[2][4];
#pragma unroll
      for (int k = 0; k < 16; ++k) {
        const int kk = k >> 3, q = (k >> 1) & 3;
        if (k & 1) xb[kk][q] = XF((q & 2) ? x1r : x0r, q & 1, 2 * j + kk);
        else wh[kk][q] = trd(rp[hi][q >> 1] + bo + (kk * 32 + (q & 1) * 4) * 256);
        SB();
      }
      if (j == 3) { asm volatile("buffer_load_dword %0, %1, %2, 0 offen sc1" : "=v"(g1) : "v"(go.vo), "s"(D) : "memory"); SB(); }
      if (j == 7) {
        asm volatile("" : "+v"(g1));
        if (__all(g1 != g0.v0)) go_ack(R, g0, lane); else go_wait(R, g0, lane);
        asm volatile("buffer_load_dword %0, %1, %2, 0 offen sc1" : "=v"(g2) : "v"(pf), "s"(D) : "memory"); SB();
      }
      if (j == 0) {} else if (j < 5) stage(j + 4); else o_dma((j - 5) >> 1, (j & 1) ? 0 : 4, (j & 1) ? 4 : 9);
#pragma unroll
      for (int k = 0; k < 16; ++k) {
        const int kk = k >> 3;
        u32x4& f = wv[k & 1][2 * j + kk];
        if ((k & 7) < 2) { f = (u32x4){wh[kk][(k & 1) * 2][0], wh[kk][(k & 1) * 2][1], wh[kk][(k & 1) * 2 + 1][0], wh[kk][(k & 1) * 2 + 1][1]}; pin_a(f); }
        f32x4& d = av[(k >> 2) & 1][k & 1][(k >> 1) & 1];
        if (j == 0 && k < 8) d = mfA0(f, xb[kk][(k >> 1) & 3]); else mfA(d, f, xb[kk][(k >> 1) & 3]);
        SB();
      }
    };
#define CH(n) vst(std::integral_constant<int, n>())
    CH(0); CH(1); CH(2); CH(3); CH(4); CH(5); CH(6); CH(7); CH(8);
#undef CH
    BAR();
    TK(8);
    o_dma(2, 0, 9); o_dma(3, 0, 9);
    TK(9);
#pragma unroll
    for (int k = 0; k < 4; ++k) {
      const f32x4 (&e)[2][2] = av[k >> 1];
      const int tt = k & 1;
      *(lds_u32x4*)(vw + k * 4096) = (u32x4){pkv(e[0][tt][0], e[0][tt][1]), pkv(e[0][tt][2], e[0][tt][3]), pkv(e[1][tt][0], e[1][tt][1]), pkv(e[1][tt][2], e[1][tt][3])};
    }
    SB();
    lds_char* op[4];
#pragma unroll
    for (int k = 0; k < 4; ++k) {
      const int T = 9 * wave + k;
      op[k] = lds + VB_OFF + (4 * g + (l15 >> 2)) * 1152 + (T ^ ((2 * g + (l15 >> 3)) & 3)) * 32 + (l15 & 3) * 8;
      pin_v(op[k]);
    }
    f32x4 ua[18][2];
    auto ust = [&](auto KC) {
      constexpr int k = decltype(KC)::value;
      asm volatile("s_waitcnt vmcnt(%0) lgkmcnt(0)" :: "n"(k == 0 ? 29 : (k < 3 ? 18 : 9)) : "memory");
      __builtin_amdgcn_s_barrier(); CBAR(); SB();
      TK(10 + k);
      if (k == 2) {
#pragma unroll
        for (int i = 0; i < 4; ++i) x_move(op[i], -VB_OFF);
      }
      u32x4 f[4];
      u32x2 wh[9][2];
#pragma unroll
      for (int i = 0; i < 2; ++i) { f[i] = *(lds_u32x4*)(vr + i * 4096 + k * 1024); SB(); }
#pragma unroll
      for (int i = 0; i < 18; ++i) { wh[i >> 1][i & 1] = trd(op[(i >> 1) & 3] + (k & 1) * 36864 + (i & 1) * 18432 + (i >> 3) * 128); SB(); }
#pragma unroll
      for (int i = 2; i < 4; ++i) { f[i] = *(lds_u32x4*)(vr + i * 4096 + k * 1024); SB(); }
      if (k == 1) {
#pragma unroll
        for (int i = 0; i < 9; ++i) { x_m0(xw + VB_OFF, i); x_dma0(xl, 2 * 36864, i); }
        SB();
      }
      if (k == 2) {
        asm volatile("" : "+v"(g2));
        int c = (int)g2;
        wait_flag_c<4>(R, pf, 0, c); SB();
      }
#pragma unroll
      for (int t = 0; t < 19; ++t) {
        const int ct = t % 9;
        if (t < 9) { wo[ct][k] = (u32x4){wh[ct][0][0], wh[ct][0][1], wh[ct][1][0], wh[ct][1][1]}; if (ct < 7) pin_a(wo[ct][k]); else pin_v(wo[ct][k]); }
#pragma unroll
        for (int tt = 0; tt < 2; ++tt) {
          if (t == 18) break;
          f32x4& d = ua[t][tt];
          const u32x4& v = f[(t / 9) * 2 + tt];
          if (ct < 7) { if (k == 0) d = mfB0(v, wo[ct][k]); else mfB(d, v, wo[ct][k]); }
          else { if (k == 0) d = mf0(v, wo[ct][k]); else mfa(d, v, wo[ct][k]); }
          SB();
        }
        if (k == 3 && t > 0) {
          f32x4 (&e)[2] = ua[t - 1];
          if (t == 18) mf_wait2(e[0], e[1]);
          st128((u32x4){pkv(e[0][0], e[0][1]), pkv(e[0][2], e[0][3]), pkv(e[1][0], e[1][1]), pkv(e[1][2], e[1][3])}, R, zo, ((t - 1) / 9) * ZS + ((t - 1) % 9) * 40960);
          SB();
        }
      }
    };
    ust(std::integral_constant<int, 0>()); ust(std::integral_constant<int, 1>()); ust(std::integral_constant<int, 2>()); ust(std::integral_constant<int, 3>());
    asm volatile("" :: "v"(go.v0));
#pragma unroll
    for (int i = 0; i < 9; ++i) { x_m0(xw + VB_OFF + 36864, i); x_dma0(xl, 3 * 36864, i); }
    SB();
  }
  TK(14);
  XP rd0 = x_at(lds + VB_OFF, xl), rd1 = x_at(lds + VB_OFF + 36864, xl);
  unsigned b0 = VB_OFF, b1 = VB_OFF + 36864, b2 = 0;
  pin_v(vr); pin_v(vw); pin_v(zo);
  u32x4 bb[3][2], vf[2][2][4];
#pragma unroll
  for (int k = 0; k < 4; ++k) cc[k >> 1][k & 1] = (f32x4){0.f, 0.f, 0.f, 0.f};
#pragma unroll
  for (int k = 0; k < 16; ++k) asm volatile("" : "=v"(vf[k >> 3][(k >> 2) & 1][k & 3]));
  VMW(27);
  BAR();
#pragma unroll
  for (int ks = 0; ks < 2; ++ks) { bb[ks][0] = XF(rd0, 0, ks); bb[ks][1] = XF(rd0, 1, ks); }
  auto iter = [&](auto PC, int s) {
    constexpr int P = decltype(PC)::value & 1, W = 0, NOV = 0, NOU = decltype(PC)::value >> 1;
    XP& buf = P ? rd1 : rd0;
    XP& nrd = P ? rd0 : rd1;
    f32x4 (&c)[2][2] = av[P];
    f32x4 (&e)[2][2] = av[P ^ 1];
    u32x4 o, q;
    unsigned s1, s2, sx, xm, xd, sf;
#define VM(ks, t, tt) do { if (NOV) break; if ((ks) == 0) c[t][tt] = mfA0(wv[t][ks], bb[(ks) % 3][tt]); else mfA(c[t][tt], wv[t][ks], bb[(ks) % 3][tt]); SB(); } while (0)
#define UM(k, tt) do { if (NOU) break; f32x4& d = cc[(gq + P) & 1][tt]; const u32x4& f = vf[gq < 2 ? P : P ^ 1][tt][k];                                      \
                       if (ct < 7) { if ((k) == 0) d = mfB0(f, wo[ct][k]); else mfB(d, f, wo[ct][k]); }                                       \
                       else { if ((k) == 0) d = mf0(f, wo[ct][k]); else mfa(d, f, wo[ct][k]); } SB(); } while (0)
#define BRD(ks, tt) do { if (NOV) break; bb[(ks) % 3][tt] = (ks) < 18 ? XF(buf, tt, (ks) % 18) : XF(nrd, tt, (ks) % 18); SB(); } while (0)
#define CV(j) do { if (NOU) break; o[j] = pkv(cc[(gq + P + 1) & 1][(j) >> 1][((j) & 1) * 2], cc[(gq + P + 1) & 1][(j) >> 1][((j) & 1) * 2 + 1]); SB(); } while (0)
#define CE(j, tt) do { if (gq == 0 && !NOU) { q[j] = pkv(e[(j) >> 1][tt][((j) & 1) * 2], e[(j) >> 1][tt][((j) & 1) * 2 + 1]); SB(); } } while (0)
#define VR(j) do { if (gq == 1 && !NOU) { vf[P ^ 1][(j) & 1][(j) >> 1] = *(lds_u32x4*)(vr + P * 8192 + ((j) & 1) * 4096 + ((j) >> 1) * 1024); SB(); } } while (0)
#define LW(n) do { __builtin_amdgcn_s_waitcnt(0xC07F | ((n) << 8)); SB(); } while (0)
#pragma unroll
    for (int gq = NOV ? 2 : 0; gq < 9; ++gq) {
      const int ct = (gq + 7) % 9, k0 = 2 * gq, k1 = 2 * gq + 1, pt = (gq + 6) % 9;
      if (NOU || gq == 0 || gq > 2) LW(2); else if (gq == 1) LW(6); else LW(8);
      if (gq == 8 && !NOV) { __builtin_amdgcn_s_barrier(); SB(); }
      VM(k0, 0, 0); BRD(k0 + 2, 0);
      if (gq == (NOV ? 2 : 0)) {
        s1 = (unsigned)(s - 1) < NSEG ? (unsigned)(s - 1) * ZS : OOB; s2 = s >= 4 ? (unsigned)(s - 2) * ZS : OOB; sx = (unsigned)clampseg(s + 2) * 36864u;
        s1 = __builtin_amdgcn_readfirstlane(s1); s2 = __builtin_amdgcn_readfirstlane(s2); sx = __builtin_amdgcn_readfirstlane(sx);
        asm volatile("" : "+s"(s1), "+s"(s2), "+s"(sx)); SB();
      }
      UM(0, 0); CV(0);
      unsigned su = (gq < 3 ? s2 : s1) + pt * 40960;
      asm volatile("" : "+s"(su)); SB();
      VM(k0, 0, 1); BRD(k0 + 2, 1);
      if (!NOV && gq == 0) { sf = FG >= 7 ? (unsigned)(s - 2) : (s == 2 ? ~1u : (unsigned)(s - 3)); pin_v(sf); SB(); }
      if (!NOV && gq == 2) {
        xm = xw + b2; xd = b2 - b0;
        const unsigned t = b0; b0 = b1; b1 = b2; b2 = t;
        asm volatile("" : "+s"(xm), "+s"(xd), "+s"(b0), "+s"(b1), "+s"(b2)); SB();
      }
      UM(0, 1); CV(1);
      if (gq == 1 && !NOU) { LW(2); __builtin_amdgcn_s_barrier(); SB(); }
      VM(k0, 1, 0); VR(0); CV(2);
      UM(1, 0); VR(1); CV(3);
#define ST(n) do { if (!NOU && !NOST && STG0 + W * STGD == n) { st128(o, R, zo, su); SB(); } } while (0)
#define VW2(j, tt) do { if (gq == 0 && !NOU) { *(lds_u32x2*)(vw + P * 8192 + (tt) * 4096 + (j) * 4) = (u32x2){q[j], q[(j) + 1]}; SB(); } } while (0)
      ST(5);
      VM(k0, 1, 1); ST(6);
      UM(1, 1); CE(0, 0); CE(1, 0); VR(2); ST(7);
      if (gq == 1 && !NOU) LW(5); else LW(2);
      VM(k1, 0, 0); BRD(k1 + 2, 0); CE(2, 0); ST(8);
      UM(2, 0); VW2(0, 0); VR(3); ST(9);
      if (!NOV && gq >= 2 && gq < 7) { x_m0(xm, gq - 2); SB(); }
      if (!NOV && gq == 8) { x_move(buf.p[0], xd); x_move(buf.p[1], xd); SB(); }
      VM(k1, 0, 1); BRD(k1 + 2, 1); CE(3, 0); ST(10);
      UM(2, 1); VW2(2, 0); VR(4); ST(11);
      if (!NOV && gq >= 2 && gq < 7) { x_dma(xl, sx, gq - 2); SB(); }
      VM(k1, 1, 0); VR(5); ST(12);
      CE(0, 1); CE(1, 1);
      if (!NOV && gq >= 3 && gq < 7) { x_m0(xm, gq + 2); SB(); }
      UM(3, 0); VW2(0, 1); VR(6); ST(13);
      if (!NOV && gq == 0) { PADE(); if (P) PADS(113); else PADS(112); } else PAD();
      SB();
      VM(k1, 1, 1); CE(2, 1); CE(3, 1); VR(7); ST(14);
      if (!NOV && gq >= 3 && gq < 7) { x_dma(xl, sx, gq + 2); SB(); }
      UM(3, 1); VW2(2, 1); ST(15);
#undef ST
#undef VW2
      if (NOU && gq == 7) { VMW(9); st32(1u, R, vpf, 0); SB(); }
      if (!NOU && gq == FG && !NOFL) {
        asm volatile("s_waitcnt vmcnt(%0)" :: "n"(FG >= 7 ? FG + 7 : (FG < 2 ? FG + 16 : 20)) : "memory");
        st32(sf, R, vpf, 0); SB();
      }
      if (!NOU && gq == 7 && FG != 7) { VMW(20); SB(); }
      if (!NOU && gq == 3 && UNLIKELY((unsigned)(s - 4) < UEF || s >= NSEG)) { __builtin_amdgcn_s_waitcnt(0x0F70); st32((unsigned)(s - 2), R, vpf, 0); SB(); }
    }
#undef VM
#undef UM
#undef BRD
#undef CV
#undef CE
#undef VR
#undef LW
  };
  iter(std::integral_constant<int, 2>(), 2);
  RT(8);
  TKS;
#pragma nounroll
  for (int s = 3;; s += 2) {
    iter(std::integral_constant<int, 1>(), s);
    if (s == NSEG + 1) break;
    iter(std::integral_constant<int, 0>(), s + 1);
  }
  PADE();
  VMW(0);
  RT(7);
}

#define RING_OFF (2 * STG_BYTES)
#define RING_STR 456
#define OST_OFF (RING_OFF + 64 * RING_STR)
__device__ __forceinline__ float dot2(unsigned a, unsigned b, float c) {
  return __builtin_amdgcn_fdot2_f32_bf16(__builtin_bit_cast(bf16x2, a), __builtin_bit_cast(bf16x2, b), c, false);
}
__device__ __forceinline__ float swp1(float x) { return __uint_as_float(__builtin_amdgcn_update_dpp(0u, __float_as_uint(x), 0xB1, 0xf, 0xf, false)); }
__device__ __forceinline__ void role_aw(const Args& a, rsrc_t R, const Go& go, lds_char* lds, int ai, int tid, int wave, int lane) {
  const int l15 = lane & 15, g = lane >> 4;
  const int ga = ai & 7, rr = ai >> 3;
  u32x4 w[4][18];
  {
    u32x4 sg[3][7];
    unsigned gv[9], wo[7], lo[7];
    const rsrc_t W = __builtin_amdgcn_make_buffer_rsrc((void*)a.wqkv, 0, DM * 1728 * 2, 0x00020000);
    const rsrc_t G = __builtin_amdgcn_make_buffer_rsrc((void*)a.gw, 0, DM * 2, 0x00020000);
#pragma unroll
    for (int k = 0; k < 7; ++k) {
      const int u = tid + 256 * k, row = u / 27, cu = u % 27;
      wo[k] = row < 64 ? (unsigned)(row * 3456 + (cu / 9) * 1152 + ga * 144 + (cu % 9) * 16) : OOB;
      lo[k] = row < 64 ? (unsigned)(row * 544 + cu * 16) : (unsigned)((tid & 63) * 544 + 448);
    }
    auto gl = [&](u32x4 (&st)[7], int c) {
#pragma unroll
      for (int k = 0; k < 7; ++k) st[k] = __builtin_amdgcn_raw_buffer_load_b128(W, wo[k], (unsigned)c * (64u * 3456u), (NTM & 16) ? 2 : 0);
    };
#pragma unroll
    for (int c = 0; c < 9; ++c) gv[c] = __builtin_amdgcn_raw_buffer_load_b16(G, (unsigned)(tid & 63) * 2, (unsigned)c * 128u, 0);
    gl(sg[0], 0); if (ASLP > 0) __builtin_amdgcn_s_sleep(ASLP);
    gl(sg[1], 1); if (ASLP > 0) __builtin_amdgcn_s_sleep(ASLP);
    gl(sg[2], 2);
#pragma unroll
    for (int c = 0; c < 9; ++c) {
      lds_char* buf = lds + (c & 1) * STG_BYTES;
#pragma unroll
      for (int k = 0; k < 7; ++k) *(lds_u32x4*)(buf + lo[k]) = sg[c % 3][k];
      *(lds_u32x4*)(buf + (tid & 63) * 544 + 432) = (u32x4){gv[c], 0u, 0u, 0u};
      if (ASLP > 0) __builtin_amdgcn_s_sleep(ASLP);
      if (c + 3 < 9) gl(sg[c % 3], c + 3);
      BAR();
#pragma unroll
      for (int kk = 0; kk < 2; ++kk)
#pragma unroll
        for (int t = 0; t < 4; ++t) {
          lds_char* p = buf + (kk * 32 + g * 8 + (l15 >> 2)) * 544 + (wave * 64 + t * 16) * 2 + (l15 & 3) * 8;
          u32x2 v0 = trd(p), v1 = trd(p + 4 * 544);
          w[t][2 * c + kk] = (u32x4){v0[0], v0[1], v1[0], v1[1]};
        }
      SB();
#pragma unroll
      for (int k = 0; k < 8; ++k) { if (c < 7) pin_a(w[k & 3][2 * c + (k >> 2)]); else pin_v(w[k & 3][2 * c + (k >> 2)]); }
    }
    BAR();
  }
  RT(1);
  go_wait(R, go, lane);
  auto sgm = [&](int it) { const int j = it / 5; return min(16 * j + 4 * rr + it - 5 * j - 1, NSEG - 1); };
  const int i0 = rr > 0 ? 0 : 1;
  u32x4 xr[9];
  XL xl; x_init(xl, a.x, wave, lane);
  x_gload(xr, xl, sgm(i0));
  x_swrite(xr, lds + i0 * STG_BYTES, xl);
  x_gload(xr, xl, sgm(i0 + 1));
  lds_char* ring = lds + RING_OFF;
  lds_char* ost = lds + OST_OFF;
  const int t = tid >> 3, hh = (tid >> 1) & 3, half = tid & 1;
  const float sc = 0.23570226039551584f * 1.4426950408889634f;
  auto done = [&](int s) { __builtin_amdgcn_raw_ptr_buffer_atomic_add_i32(1, R, tid == 0 && s >= 0 ? (unsigned)(FL_APROG + s * 128) : OOB, 0, SC1); };
  const unsigned gto = (ga == 0 && wave == 3 && g == 2) ? OFF_GATE + l15 * 4 : OOB;
  int sp = -1;
  for (int it = i0; it < 20; ++it) {
    const int s = sgm(it);
    BAR();
    {
      f32x4 acc[4][2];
      proj4(lds + (it & 1) * STG_BYTES, lds + ((it + 1) & 1) * STG_BYTES, xr, xl, sgm(it + 2), wave, lane, w[0], w[1], w[2], w[3], acc);
      VMW(9);
#pragma unroll
      for (int tl = 0; tl < 4; ++tl)
#pragma unroll
        for (int tt = 0; tt < 2; ++tt) {
          const int col = wave * 64 + tl * 16 + g * 4;
          u32x2 o = {pk2(acc[tl][tt][0], acc[tl][tt][1]), pk2(acc[tl][tt][2], acc[tl][tt][3])};
          if (col < 216) *(__attribute__((address_space(3))) u32x2*)(ring + (((s & 1) * 32 + tt * 16 + l15) * RING_STR) + col * 2) = o;
        }
#pragma unroll
      for (int tt = 0; tt < 2; ++tt) {
        float z = acc[1][tt][0];
        float gv = __builtin_amdgcn_rcpf(1.f + __builtin_amdgcn_exp2f(-1.4426950408889634f * z));
        st32(__float_as_uint(gv), R, gto + tt * 64, (unsigned)s * 128u);
      }
    }
    BAR();
    done(sp);
    sp = it % 5 != 0 ? s : -1;
    { const int s = it; STEPRT(2, 0); }
    if (it % 5 != 0) {
      const int row = s * 32 + t;
      unsigned q[9];
      lds_char* qp = ring + (row & 63) * RING_STR + hh * 36;
#pragma unroll
      for (int j = 0; j < 9; ++j) q[j] = *(lds_u32*)(qp + j * 4);
      float lg[4];
      lds_char* vp[4];
#pragma unroll
      for (int wi = 0; wi < 4; ++wi) {
        const int r2 = row - 2 * (half * 4 + wi);
        const bool ok = r2 >= 0;
        lds_char* kp = ring + ((ok ? r2 : row) & 63) * RING_STR + hh * 36 + 144;
        vp[wi] = kp + 144;
        float d = 0.f;
#pragma unroll
        for (int j = 0; j < 9; ++j) d = dot2(q[j], *(lds_u32*)(kp + j * 4), d);
        lg[wi] = ok ? d : -__builtin_inff();
      }
      float m = fmaxf(fmaxf(lg[0], lg[1]), fmaxf(lg[2], lg[3]));
      m = fmaxf(m, swp1(m));
      const float ms = -m * sc;
      float sum = 0.f;
#pragma unroll
      for (int wi = 0; wi < 4; ++wi) { lg[wi] = __builtin_amdgcn_exp2f(fmaf(lg[wi], sc, ms)); sum += lg[wi]; }
      sum += swp1(sum);
      const float inv = __builtin_amdgcn_rcpf(sum);
      const unsigned p01 = pk2(lg[0] * inv, lg[1] * inv), p23 = pk2(lg[2] * inv, lg[3] * inv);
#pragma unroll
      for (int j = 0; j < 9; ++j) {
        const unsigned v0 = *(lds_u32*)(vp[0] + j * 4), v1 = *(lds_u32*)(vp[1] + j * 4), v2 = *(lds_u32*)(vp[2] + j * 4), v3 = *(lds_u32*)(vp[3] + j * 4);
        float lo = dot2(__builtin_amdgcn_perm(v1, v0, 0x05040100u), p01, 0.f);
        lo = dot2(__builtin_amdgcn_perm(v3, v2, 0x05040100u), p23, lo);
        float hi = dot2(__builtin_amdgcn_perm(v1, v0, 0x07060302u), p01, 0.f);
        hi = dot2(__builtin_amdgcn_perm(v3, v2, 0x07060302u), p23, hi);
        lo += swp1(lo); hi += swp1(hi);
        if ((j & 1) == half) *(lds_u32*)(ost + t * 144 + hh * 36 + j * 4) = pk2(lo, hi);
      }
      BAR();
#pragma unroll
      for (int k = 0; k < 2; ++k) {
        const int u = tid + 256 * k;
        const bool ok = u < 288;
        const int ur = u & 31, uc = ok ? u >> 5 : 0, cc = ga * 9 + uc;
        u32x4 o = *(lds_u32x4*)(ost + ur * 144 + uc * 16);
        st128(o, R, ok ? OFF_OA + (unsigned)(((ur >> 4) * 18 + (cc >> 2)) * 1024 + ((cc & 3) * 16 + (ur & 15)) * 16) : OOB, (unsigned)s * 36864u);
      }
      CBAR();
    }
  }
  VMW(0);
  BAR();
  done(sp);
  RT(7);
}

__device__ __forceinline__ void role_out(const Args& a, rsrc_t R, const Go& go, lds_char* lds, int o, int tid, int wave, int lane) {
  const int l15 = lane & 15, g = lane >> 4;
  const int cb = o % 9, oj = o / 9, sw = cb == 8 ? OSW : 0, grp = oj < 4 ? oj : (oj == 4 ? sw + 4 : NSEG - 16 + oj);
  auto nxt = [&](int s) { return s < sw ? s + 4 : (s < NSEG - 16 ? s + 5 : s + 8); };
  u32x4 wf[4][5];
  {
    u32x4 st[18];
#pragma unroll
    for (int k = 0; k < 18; ++k) { int u = tid + 256 * k; st[k] = *(const u32x4*)(a.wo + (size_t)(u >> 3) * DM + cb * 64 + (u & 7) * 8); }
#pragma unroll
    for (int k = 0; k < 18; ++k) { int u = tid + 256 * k; *(lds_u32x4*)(lds + (u >> 3) * 144 + (u & 7) * 16) = st[k]; }
    BAR();
#pragma unroll
    for (int i = 0; i < 5; ++i)
#pragma unroll
      for (int ct = 0; ct < 4; ++ct) {
        const int ks = wave + 4 * i;
        lds_char* p = lds + ((ks < 18 ? ks : 17) * 32 + g * 8 + (l15 >> 2)) * 144 + ct * 32 + (l15 & 3) * 8;
        u32x2 v0 = trd(p), v1 = trd(p + 4 * 144);
        wf[ct][i] = (ks < 18) ? (u32x4){v0[0], v0[1], v1[0], v1[1]} : (u32x4){0u, 0u, 0u, 0u};
        pin_a(wf[ct][i]);
      }
    BAR();
  }
  const unsigned zb = OFF_ZF + (unsigned)(4 * cb) * 40960u + lane * 16;
  const unsigned flk = lane == 32 ? FL_KMPROG + ((o * 4 + wave) & 31) * 128 : OOB;
  const bool isu = (lane & 24) == 8 && lane < 48;
  auto faddr = [&](int s) -> unsigned {
    return lane < 8 ? (lane == 0 ? FL_APROG + s * 128 : OOB)
                    : (isu ? FL_UPROG + (wave * 8 + (lane & 7)) * 128 + ((cb * 64 + (lane < 16 ? 0 : 63)) / 144) * 4
                           : (lane < 32 ? (s == NSEG - 1 ? FL_PXLAST + (wave * 16 + lane - 16) * 128 : FL_PXPROG + (wave * 8 + (lane & 7)) * 128 + (s & 1) * 4)
                                        : (s + 3 < NSEG ? flk : FL_YPROG + (wave * 8 + (lane & 7)) * 16 + ((cb * 64 + (lane < 48 ? 0 : 63)) / 144) * 4 + (lane < 56 ? 0 : OOB))));
  };
  const int t = tid >> 3, n8 = tid & 7;
  const rsrc_t O = __builtin_amdgcn_make_buffer_rsrc(a.out, 0, S_LEN * DM * 2, 0x00020000);
  const i32x4 D = mkdesc(a.wb);
  int fv = -1;
  RT(1);
  const unsigned ofo = OFF_OA + (unsigned)(wave * 1024 + lane * 16), ofl = wave < 2 ? ofo : OOB, gto = OFF_GATE + l15 * 4;
  const unsigned pfo = OFF_PXF + (unsigned)(wave * 8192 + lane * 16), pfy = OFF_PXF + (unsigned)((24 + wave * 2) * 1024 + lane * 16);
  u32x4 zu[4][8];
  int fu = -1;
  const unsigned fua = isu ? faddr(0) : OOB;
  const unsigned zw = zb + wave * 8192;
#pragma unroll
  for (int k = 0; k < 32; ++k) asm volatile("" : "=a"(zu[k >> 3][k & 7]));
  auto zu4 = [&](unsigned vo, int s, int hh) {
#pragma unroll
    for (int ct = 0; ct < 4; ++ct)
      asm volatile("buffer_load_dwordx4 %0, %1, %2, %3 offen offset:%4 sc1" : "+a"(zu[ct][hh])
                   : "v"(vo), "s"(D), "s"((unsigned)(s & RMASK) * ZS + ct * 40960 + (hh >> 2) * 4096), "n"((hh & 3) * 1024) : "memory");
  };
  auto zuld = [&](int s) -> bool {
    unsigned spins = 0;
    while (ballot(isu && fu < s) != 0) {
      if (++spins > SPINCAP) return false;
      if (spins > 1) __builtin_amdgcn_s_sleep(16);
      if (s == NSEG - 1 && spins > 1) __builtin_amdgcn_s_sleep(100);
      CBAR();
      fu = (int)ld32(R, fua, 0);
    }
#pragma unroll
    for (int hh = 0; hh < 8; ++hh) zu4(zw, s, hh);
    SB();
    return true;
  };
  if ((o < 44 && o != 40 && !go_wait(R, go, lane)) || !zuld(grp)) return;
  unsigned acc_t[8] = {0, 0, 0, 0, 0, 0, 0, 0}, t0 = 0, t1 = 0, t2 = 0, t3 = 0, t4 = 0, t5 = 0, t6 = 0;
  for (int s = grp; s < NSEG; s = nxt(s)) {
    TS(t0);
    const unsigned fa = faddr(s);
    const int need = isu || fa >= OOB ? -1 : (lane == 0 ? 7 : ((lane >= 32 && s + 3 < NSEG) ? s + 2 : s));
    const bool far = lane >= 8 && lane < 48 && (isu || (lane < 32 && s < NSEG - 1));
    unsigned spins = 0;
    while ((ballot(fv < need) & 0xFFull) != 0) {
      if (++spins > SPINCAP) return;
      if (spins > 1) __builtin_amdgcn_s_sleep(32);
      CBAR();
      const int nv = (int)lda(R, fv < need && lane < 8 ? fa : OOB);
      fv = lane < 8 ? max(fv, nv) : fv;
    }
    TS(t1);
#define OLD(d, vo, so, o) asm volatile("buffer_load_dwordx4 %0, %1, %2, %3 offen offset:%4 sc1" : "=v"(d) : "v"(vo), "s"(D), "s"(so), "n"(o) : "memory")
    u32x4 zy[4][2], pf[2][10], of[2][5];
    float gt[2];
#pragma unroll
    for (int qt = 0; qt < 2; ++qt) {
#pragma unroll
      for (int i = 0; i < 5; ++i) OLD(of[qt][i], i < 4 ? ofo : ofl, (unsigned)s * 36864u + qt * 18432 + i * 4096, 0);
      asm volatile("buffer_load_dword %0, %1, %2, %3 offen offset:%4 sc1" : "=v"(gt[qt]) : "v"(gto), "s"(D), "s"((unsigned)s * 128u), "n"(qt * 64) : "memory");
    }
    SB();
#define FWAIT(m) do { spins = 0;                                                                                          \
      while ((ballot(fv < need) & (m)) != 0) {                                                                            \
        if (++spins > SPINCAP) return;                                                                                    \
        if (spins > 1) { if (s < OFAST) __builtin_amdgcn_s_sleep(OSLP); else __builtin_amdgcn_s_sleep(OSL2); }            \
        CBAR();                                                                                                           \
        const int n0 = (int)lda(R, fv < need && !far ? fa : OOB), n1 = (int)ld32(R, fv < need && far ? fa : OOB, 0), nv = far ? n1 : n0;                                                             \
        fv = max(fv, nv);                                                                                                 \
      } } while (0)
#define ZYLD(zv) do { _Pragma("unroll") for (int hp = 0; hp < 2; ++hp) _Pragma("unroll") for (int ct = 0; ct < 4; ++ct)                        \
                        asm volatile("buffer_load_dwordx4 %0, %1, %2, %3 offen offset:%4 sc1" : "+v"(zy[ct][hp])                                   \
                                     : "v"(zv), "s"(D), "s"((unsigned)(s & RMASK) * ZS + ct * 40960 + 32768 + wave * 2048), "n"(hp * 1024) : "memory"); } while (0)
#define MFS(hh) do { _Pragma("unroll") for (int qt = 0; qt < 2; ++qt) _Pragma("unroll") for (int ct = 0; ct < 4; ++ct) {                           \
                        ab[ct][qt] = mfma(hh < 8 ? zu[ct][hh & 7] : zy[ct][hh & 1], pf[qt][hh], hh ? ab[ct][qt] : (f32x4){0.f, 0.f, 0.f, 0.f}); SB(); } } while (0)
    TS(t2);
#pragma unroll
    for (int k = 0; k < 8; ++k) asm volatile("" : "=v"(zy[k >> 1][k & 1]));
    bool yr = false;
#define ZYW() asm volatile("s_waitcnt vmcnt(0)" : "+v"(zy[0][0]), "+v"(zy[0][1]), "+v"(zy[1][0]), "+v"(zy[1][1]), "+v"(zy[2][0]), "+v"(zy[2][1]), "+v"(zy[3][0]), "+v"(zy[3][1]) :: "memory")
#define ZYTRY(w) do { if (!yr && (ballot(fv < need) >> 32) == 0) { ZYLD(zb); if (w) ZYW(); yr = true; } } while (0)
    spins = 0;
#pragma nounroll
    while ((unsigned)ballot(fv < need) != 0) {
      if (++spins > SPINCAP) return;
      if (spins > 1) { if (s < OFAST) __builtin_amdgcn_s_sleep(OSLP); else __builtin_amdgcn_s_sleep(OSL2); }
      CBAR();
      const int nv = (int)ld32(R, fv < need ? fa : OOB, 0);
      CBAR();
      ZYTRY(1);
      fv = max(fv, nv);
    }
    TS(t3);
#pragma unroll
    for (int hh = 0; hh < 10; ++hh)
#pragma unroll
      for (int qt = 0; qt < 2; ++qt) OLD(pf[qt][hh], hh < 8 ? pfo : pfy, (unsigned)(s & RMASK) * 81920u + qt * 40960 + (hh >> 2) * 4096, (hh & 3) * 1024);
    ZYTRY(0);
    const int sn = nxt(s) < NSEG ? nxt(s) : s;
    int n1;
    asm volatile("buffer_load_dword %0, %1, %2, 0 offen sc1" : "=v"(n1) : "v"(faddr(sn)), "s"(D) : "memory");
    const bool uok = sn != s && ballot(isu && fu < sn) == 0;
    unsigned zn = uok ? zw : OOB;
    pin_v(zn);
    asm volatile("s_waitcnt vmcnt(21)" : "+v"(of[0][0]), "+v"(of[0][1]), "+v"(of[0][2]), "+v"(of[0][3]), "+v"(of[0][4]), "+v"(of[1][0]), "+v"(of[1][1]),
                 "+v"(of[1][2]), "+v"(of[1][3]), "+v"(of[1][4]), "+v"(gt[0]), "+v"(gt[1]) :: "memory");
    SB();
    f32x4 aa[4][2];
#pragma unroll
    for (int ct = 0; ct < 4; ++ct)
#pragma unroll
      for (int qt = 0; qt < 2; ++qt) {
        f32x4 acc = {0.f, 0.f, 0.f, 0.f};
#pragma unroll
        for (int i = 0; i < 5; ++i) acc = mfma(wf[ct][i], of[qt][i], acc);
        aa[ct][qt] = acc * gt[qt];
        pin_v(aa[ct][qt]);
      }
    SB();
    f32x4 ab[4][2];
#pragma unroll
    for (int hh = 0; hh < 8; ++hh) {
      asm volatile("s_waitcnt vmcnt(%2)" : "+v"(pf[0][hh]), "+v"(pf[1][hh]) : "n"(19 + 2 * hh) : "memory");
      SB();
      MFS(hh);
      zu4(zn, sn, hh);
      SB();
    }
    TS(t4);
    asm volatile("s_waitcnt vmcnt(32)" : "+v"(n1), "+v"(pf[0][8]), "+v"(pf[1][8]), "+v"(pf[0][9]), "+v"(pf[1][9]) :: "memory");
    fu = isu ? n1 : fu;
    if (sn != s && !uok && !zuld(sn)) return;
    if (!yr) { FWAIT(~0ull); ZYLD(zb); ZYW(); }
#pragma unroll
    for (int k = 0; k < 8; ++k) pin_v(zy[k >> 1][k & 1]);
    fv = n1;
    SB();
    MFS(8); MFS(9);
    TS(t5);
#undef OLD
#undef ZYTRY
#undef ZYW
#undef FWAIT
#undef ZYLD
#undef MFS
#pragma unroll
    for (int qt = 0; qt < 2; ++qt)
#pragma unroll
      for (int ct = 0; ct < 4; ++ct)
        *(lds_f32x4*)(lds + wave * 8704 + (qt * 16 + l15) * 272 + (ct * 16 + g * 4) * 4) = aa[ct][qt] + ab[ct][qt] * (1.f - gt[qt]);
    BAR();
    f32x4 b0 = *(lds_f32x4*)(lds + t * 272 + n8 * 32), b1 = *(lds_f32x4*)(lds + t * 272 + n8 * 32 + 16);
#pragma unroll
    for (int w = 1; w < 4; ++w) { b0 += *(lds_f32x4*)(lds + w * 8704 + t * 272 + n8 * 32); b1 += *(lds_f32x4*)(lds + w * 8704 + t * 272 + n8 * 32 + 16); }
    u32x4 ov = {pk2(b0[0], b0[1]), pk2(b0[2], b0[3]), pk2(b1[0], b1[1]), pk2(b1[2], b1[3])};
    __builtin_amdgcn_raw_buffer_store_b128(ov, O, (unsigned)((s * 32 + t) * DM + cb * 64 + n8 * 8) * 2, 0, OAUX);
    BAR();
    if (s < 4) RT(2);
    TS(t6); ACC(0, 4 * (t1 - t0)); ACC(1, 4 * (t2 - t1)); ACC(2, 4 * (t3 - t2)); ACC(3, 4 * (t4 - t3)); ACC(4, 4 * (t5 - t4)); ACC(5, 4 * (t6 - t5));
    STEPRT(0, 0);
  }
  for (int k = 0; k < 8; ++k) SV(16 + k, acc_t[k]);
  RT(7);
}

#define LDS_BYTES 163840
__device__ __forceinline__ void code_touch(u64 pc, unsigned len, unsigned off, unsigned ld) {
  const i32x4 P = {(int)(unsigned)pc, (int)(unsigned)(pc >> 32), (int)len, 0x00020000};
  asm volatile("s_mov_b32 m0, %2\n\ts_nop 0\n\tbuffer_load_dword %0, %1, 0 offen lds" :: "v"(off), "s"(P), "s"(ld) : "memory");
}
__global__ void __launch_bounds__(256, 1) mega(Args a) {
  __shared__ __attribute__((aligned(16))) char smem[LDS_BYTES];
  lds_char* lds = (lds_char*)smem;
  const int b = blockIdx.x, tid = threadIdx.x, lane = tid & 63, wave = __builtin_amdgcn_readfirstlane(tid >> 6);
  const int x = b & 7, sl = b >> 3;
  const rsrc_t R = __builtin_amdgcn_make_buffer_rsrc(a.wb, 0, (int)OFF_END, 0x00020000);
  const Go go = go_look(R, b + wave * 4);
  if (b == 244) go_lead(R, tid, wave, lane);
  {
    u64 pc; unsigned len;
    asm volatile("s_getpc_b64 %0\n.Lcode_here:\n\ts_mov_b32 %1, zz_code_end-.Lcode_here" : "=s"(pc), "=s"(len));
    if (wave == CPF) code_touch(pc, len, (unsigned)(sl * 8192 + lane * 128), (unsigned)(uintptr_t)lds + (x >= 4 && sl >= 8 && sl < 16 ? VB_OFF + 16384 : 153600));
  }
  RT(0);
  if (wave == 3 && (sl == 24 || sl == 29)) {
    unsigned d[4];
#pragma unroll
    for (int k = 0; k < 3; ++k) d[k] = ld32(R, (unsigned)(k * 64 + lane) << 20, 0);
    d[3] = *(const unsigned*)(a.out + min(lane, 2) * (1 << 19));
    asm volatile("" :: "v"(d[0]), "v"(d[1]), "v"(d[2]), "v"(d[3]));
  }
  if (x < 4 ? sl >= 25 : sl >= 16) {
    const unsigned t0 = rt_now();
    const int nw = x < 4 ? 28 : 56, id = __builtin_amdgcn_readfirstlane((sl - (x < 4 ? 25 : 16)) * 4 + wave);
    const int fa = x < 4 ? HF1 : HF3, fb = x < 4 ? HF0 : HF2, na = 6 * (18 - fa);
    unsigned d = 0;
    for (int e = id < nw ? id : 999; e < na + 3 * (18 - fb); e += nw) {
      const int j = e % 3, q = e / 3, blk = e < na ? fa + (q >> 1) : q - 2 * (18 - fa) + fb;
      const u16* W = e < na && (q & 1) ? a.Wk : ((e < na) == (x < 4) ? a.Wq : a.Wv);
      const int c = (int)((u64)W >> 8), hh = j < 2 ? (x & 3) * 8 + ((1 - c) & 3) + 4 * j : (31 - c) & 31;
      if ((hh >> 3) != (x & 3) || !HLP) continue;
      const u16* p = W + (size_t)(blk * 32 + (lane >> 1)) * HD + hh * 128 + (lane & 1) * 64;
      asm volatile("global_load_dword %0, %1, off nt" : "+v"(d) : "v"(p) : "memory");
    }
    asm volatile("s_waitcnt vmcnt(0)" : "+v"(d) :: "memory");
    if (sl >= 20) for (int k = 0; k < 64 && rt_now() - t0 < (x < 4 || sl == 31 ? AWT : XPT); ++k) __builtin_amdgcn_s_sleep(8);
  }
  if (x < 4 && wave == 1 && sl >= 16 && sl < 24) calibrate(R, go, x + (sl & 4), sl & 3, lane);
  if (x < 4) {
    if (sl < 16) for (int k = 0; k < QDL; ++k) __builtin_amdgcn_s_sleep(32);
    if (sl < 8) role_qkc(a, R, go, lds, x * 8 + sl, tid, wave, lane);
    else if (sl < 16) role_vcu(a, R, go, lds, x * 8 + sl - 8, tid, wave, lane);
    else if (sl < 25) role_mix(a, R, go, x * 9 + sl - 16, wave, lane);
    else role_aw(a, R, go, lds, x * 8 + sl - 25, tid, wave, lane);
  } else {
    const int y = x - 4;
    if (sl < 8) {
      const int h = y * 8 + ((sl + HSH) & 7);
      role_prj(a, R, go, lds, a.Wk + h * 128, a.Wq + h * 128, OFF_KF + h * 8192u, OFF_QF + h * 8192u, FL_PROG + h * 128, tid, wave, lane);
    } else if (sl < 16) role_vuw(a, R, go, lds, y * 8 + sl - 8, tid, wave, lane);
    else if (sl < 20) { role_px(a, R, go, y * 4 + sl - 16, wave, lane); __builtin_amdgcn_s_waitcnt(0x0F70); }
    else {
      if (sl == 31) { role_aw(a, R, go, lds, y * 8 + 7, tid, wave, lane); __builtin_amdgcn_s_waitcnt(0x0F70); }
      if (sl == 29) {
        const rsrc_t X = __builtin_amdgcn_make_buffer_rsrc((void*)a.x, 0, S_LEN * DM * 2, 0x00020000);
#pragma unroll 1
        for (int k = 0; k < 3; ++k) {
          unsigned t[12];
#pragma unroll
          for (int j = 0; j < 12; ++j) t[j] = __builtin_amdgcn_raw_buffer_load_b32(X, (unsigned)(((k * 12 + j) * 16 + y * 4 + wave) * 64 + lane) * 64, 0, 0);
#pragma unroll
          for (int j = 0; j < 12; ++j) asm volatile("" :: "v"(t[j]));
        }
      }
    }
    const int o = sl < 8 ? 45 + y * 8 + sl : (sl < 20 ? 99 : (sl < 29 ? y * 9 + sl - 20 : (sl < 31 ? 36 + (sl - 29) * 4 + y : 44 + y * 99)));
    if (o < 72) { BAR(); role_out(a, R, go, lds, o, tid, wave, lane); }
  }
}

extern "C" __device__ __attribute__((noinline, used, visibility("hidden"))) void zz_code_end() {}
extern "C" void kernel_launch(void* const* d_in, const int* in_sizes, int n_in, void* d_out, int out_size, void* d_ws,
                              size_t ws_size, hipStream_t stream) {
  char* wb = (char*)(((uintptr_t)d_ws + 0x1FFFFF) & ~(uintptr_t)0x1FFFFF);
  Args a;
  a.x = (const u16*)d_in[0]; a.mem0 = (const u16*)d_in[4]; a.Wq = (const u16*)d_in[5]; a.Wk = (const u16*)d_in[6]; a.Wv = (const u16*)d_in[7];
  a.Wo = (const u16*)d_in[8]; a.wqkv = (const u16*)d_in[1]; a.wo = (const u16*)d_in[2]; a.gw = (const u16*)d_in[10]; a.out = (u16*)d_out; a.wb = wb;
  mega<<<256, 256, 0, stream>>>(a);
}
```

```cpp
#define S1 26
#define KC_ 64
#define T1D 140
#define XPF 800
#define DEA 84
#define KA_ 68
#define S2 30
#define DEC 80
#define DE 82
#define K2 66
#define G1 10
#define GSL 3
#define CT0 1100
#define CT1 950
#define EG 5
#define APF 600
#define AWT 900
#define AL2 1
#define MSL 2
#define XNT ""
#define UEF 1
#define OSW 28
#define HLP 1
#define HF0 9
#define HF1 4
#define HF2 0
#define HF3 4
#define XPT 760
#define HSH 0
#define NOCW 0
#define EFIRST
#define CSLP 1
#define RTOL 2
#define QDL 0
#define LTOL 16
#define GRID 48
#define XT 6
#define NOAT
#define AWD 8
#define XPD 8
#define NTM 15
#define FIXL
#define NSC 8
#define WRM 3
#define VED 0
#define NOYC 0
#define G0 36
#define GM 15
#define KFI 6
#define EG1 0
#define XP0 18
#define KEF 3
#define DE0 100
#define DELTA0 172
#include <hip/hip_runtime.h>
#include <stdint.h>
#include <type_traits>

typedef unsigned short u16;
typedef __attribute__((ext_vector_type(2))) __bf16 bf16x2;
typedef __attribute__((ext_vector_type(4))) __bf16 bf16x4;
typedef __attribute__((ext_vector_type(8))) __bf16 bf16x8;
typedef __attribute__((ext_vector_type(16))) __bf16 bf16x16;
typedef __attribute__((ext_vector_type(2))) float f32x2;
typedef __attribute__((ext_vector_type(4))) float f32x4;
typedef __attribute__((ext_vector_type(2))) unsigned u32x2;
typedef __attribute__((ext_vector_type(4))) unsigned u32x4;
typedef __attribute__((ext_vector_type(8))) unsigned u32x8;
typedef __attribute__((ext_vector_type(4))) short s16x4;
typedef __attribute__((address_space(3))) s16x4 lds_s16x4;
typedef __attribute__((address_space(3))) u32x4 lds_u32x4;
typedef __attribute__((address_space(3))) f32x4 lds_f32x4;
typedef __attribute__((address_space(3))) unsigned lds_u32;
typedef __attribute__((address_space(3))) u32x2 lds_u32x2;
typedef __attribute__((address_space(3))) char lds_char;

#define S_LEN 2048
#define DM 576
#define NSEG 64
#define HD 4096

#define SLOT_REAL  131072
#define NEAR_PM    (8192 + 1024)
#define NEAR_AL    (8192 + 5120)
#define NEAR_E     16384
#define OFF_FLAGS  0x100000
#define FL_FLIP    (OFF_FLAGS + 0)
#define FL_WP      (OFF_FLAGS + 16384)
#define FL_AN      (OFF_FLAGS + 18432)
#define FL_MRDY    (OFF_FLAGS + 2048)
#define FL_MPOI    (OFF_FLAGS + 2304)
#define FL_PROG    (OFF_FLAGS + 4096)
#define FL_UPROG   (OFF_FLAGS + 8192)
#define FL_KMPROG  (OFF_FLAGS + 12288)
#define FL_YPROG   (OFF_FLAGS + 20480)
#define FL_PXPROG  (OFF_FLAGS + 24576)
#define FL_APROG   (OFF_FLAGS + 40960)
#define FL_PXLAST  (OFF_FLAGS + 32768)
#define FL_ACK     (OFF_FLAGS + 28672)
#define FLAG_BYTES 0xC000
#define FL_GO      (OFF_FLAGS + FLAG_BYTES)
#define NACK       945
#define REAL_CAL   0x140000
#define OFF_YR     0x200000
#define YSLOT_REAL 0x100000
#define CPY1       16384
#define CPY2       0x80000
#define OFF_KF   (4u << 20)
#define OFF_QF   (OFF_KF + (16u << 20))
#define OFF_KMF  (OFF_QF + (16u << 20))
#define OFF_PXF  (OFF_KMF + (8u << 20))
#define OFF_ZF   (OFF_PXF + (6u << 20))
#define ZS       (36u * 40960u)
#define OFF_OA   (OFF_ZF + (91u << 20))
#define OFF_GATE (OFF_OA + (3u << 20))
#define OFF_END  (OFF_GATE + (1u << 20))

#define RMASK 63
#define SPINCAP (1u << 19)
#define SC1 16

#define BAR() do { asm volatile("s_waitcnt lgkmcnt(0)" ::: "memory"); __builtin_amdgcn_s_barrier(); asm volatile("" ::: "memory"); } while (0)
#define VMW(n) asm volatile("s_waitcnt vmcnt(" #n ")" ::: "memory")
#define CBAR() asm volatile("" ::: "memory")
#define PADA
#define PADN "s_nop 0\n\t"
#define PADM
#define PADZ
#define PAD()
#define PADS(id)
#define PADE()
#define WALK(id)

#define TKD
#define TK(i)
#define TKS
#define NOSTEP 0
#define OFF_STAMP (200u << 20)
#define TS(v)
#define RT(i)
#define SV(i, v)
#define ACC(k, d)
#define STEPRT(j, f)
#define EVASM
#define EV(v)
#define EVK(v)
#define EVPUT(j, v)
#define EVPUTI(j, v)
#define EVDECL
#define EVDUMP()
#define CKDECL
#define CKS()
#define CKDUMP()
typedef unsigned long long u64;
struct Args {
  const u16* x; const u16* mem0; const u16* Wq; const u16* Wk; const u16* Wv; const u16* Wo;
  const u16* wqkv; const u16* wo; const u16* gw; u16* out; char* wb;
};

__device__ __forceinline__ float bf2f(u16 v) { return __uint_as_float(((unsigned)v) << 16); }
__device__ __forceinline__ unsigned pk2(float a, float b) {
  f32x2 v = {a, b};
  bf16x2 r = __builtin_convertvector(v, bf16x2);
  return __builtin_bit_cast(unsigned, r);
}
__device__ __forceinline__ int nearoff(int o) { return ((o >> 13) << 15) + (o & 8191); }
__device__ __forceinline__ unsigned own_cpy(int cls, int x) { return ((cls >> (2 * x)) & 2) ? CPY1 : 0u; }
__device__ __forceinline__ unsigned ywin(int cls, int m) {
  const int x = m / 9, row = (x >> 1) * 9 + m - 9 * x;
  const int se = (cls >> (4 * (x >> 1) + 1)) & 1, so = (cls >> (4 * (x >> 1) + 3)) & 1, sd = se == so ? x & 1 : ((x & 1) ? so : se);
  return (unsigned)(OFF_YR + row * 32768 + ((cls >> 8) & 1) * 8192 + ((sd ^ (cls >> 1) ^ (cls >> 9) ^ (row >> 4)) & 1) * 16384);
}
__device__ __forceinline__ u32x2 trd(lds_char* p) {
  s16x4 a = __builtin_amdgcn_ds_read_tr16_b64_v4i16((lds_s16x4*)p);
  return __builtin_bit_cast(u32x2, a);
}
__device__ __forceinline__ f32x4 mfma(u32x4 a, u32x4 b, f32x4 c) {
  return __builtin_amdgcn_mfma_f32_16x16x32_bf16(__builtin_bit_cast(bf16x8, a), __builtin_bit_cast(bf16x8, b), c, 0, 0, 0);
}
__device__ __forceinline__ f32x4 mfv0(u32x4 a, u32x4 b) {
  f32x4 d; asm volatile("s_nop 1\n\tv_mfma_f32_16x16x32_bf16 %0, %1, %2, 0" : "=&v"(d) : "v"(a), "v"(b)); return d;
}
__device__ __forceinline__ f32x4 mfvc(u32x4 a, u32x4 b, f32x4 c) {
  f32x4 d; asm volatile("s_nop 1\n\tv_mfma_f32_16x16x32_bf16 %0, %1, %2, %3" : "=&v"(d) : "v"(a), "v"(b), "v"(c)); return d;
}
__device__ __forceinline__ void mfva(f32x4& d, u32x4 a, u32x4 b) {
  asm volatile("s_nop 1\n\tv_mfma_f32_16x16x32_bf16 %0, %1, %2, %0" : "+v"(d) : "v"(a), "v"(b));
}
__device__ __forceinline__ void mf_wait(f32x4& d) { asm volatile("s_nop 7" : "+v"(d)); }
__device__ __forceinline__ void mf_wait2(f32x4& d, f32x4& e) { asm volatile("s_nop 7" : "+v"(d), "+v"(e)); }
__device__ __forceinline__ f32x4 smf(u32x4 a, u32x8 b, f32x4 c, int idx) {
  return __builtin_amdgcn_smfmac_f32_16x16x64_bf16(__builtin_bit_cast(bf16x8, a), __builtin_bit_cast(bf16x16, b), c, idx, 0, 0);
}
__device__ __forceinline__ float xmax(float x) {
  auto s = __builtin_amdgcn_permlane16_swap(__float_as_uint(x), __float_as_uint(x), false, false);
  x = fmaxf(__uint_as_float(s[0]), __uint_as_float(s[1]));
  auto t = __builtin_amdgcn_permlane32_swap(__float_as_uint(x), __float_as_uint(x), false, false);
  return fmaxf(__uint_as_float(t[0]), __uint_as_float(t[1]));
}
__device__ __forceinline__ float xsum(float x) {
  auto s = __builtin_amdgcn_permlane16_swap(__float_as_uint(x), __float_as_uint(x), false, false);
  x = __uint_as_float(s[0]) + __uint_as_float(s[1]);
  auto t = __builtin_amdgcn_permlane32_swap(__float_as_uint(x), __float_as_uint(x), false, false);
  return __uint_as_float(t[0]) + __uint_as_float(t[1]);
}
__device__ __forceinline__ f32x4 halfadd(f32x4 a, f32x4 b) {
  f32x4 o;
#pragma unroll
  for (int r = 0; r < 4; ++r) {
    auto s = __builtin_amdgcn_permlane32_swap(__float_as_uint(a[r]), __float_as_uint(b[r]), false, false);
    o[r] = __uint_as_float(s[0]) + __uint_as_float(s[1]);
  }
  return o;
}
template <class T> __device__ __forceinline__ void pin_a(T& v) { asm volatile("" : "+a"(v)); }
typedef __amdgpu_buffer_rsrc_t rsrc_t;
__device__ __forceinline__ u32x4 ld128(rsrc_t R, unsigned vo, unsigned so) { return __builtin_amdgcn_raw_buffer_load_b128(R, vo, so, SC1); }
__device__ __forceinline__ unsigned ld32(rsrc_t R, unsigned vo, unsigned so) { return __builtin_amdgcn_raw_buffer_load_b32(R, vo, so, SC1); }
__device__ __forceinline__ void st128(u32x4 v, rsrc_t R, unsigned vo, unsigned so) { __builtin_amdgcn_raw_buffer_store_b128(v, R, vo, so, SC1); }
__device__ __forceinline__ void st32(unsigned v, rsrc_t R, unsigned vo, unsigned so) { __builtin_amdgcn_raw_buffer_store_b32(v, R, vo, so, SC1); }
#define OOB 0x7ff00000
__device__ __forceinline__ unsigned lda(rsrc_t R, unsigned vo) { return ld32(R, vo, 0); }

struct Go { unsigned vo, v0; };
__device__ __forceinline__ Go go_look(rsrc_t R, int b) {
  Go g; g.vo = FL_GO + (b & 15) * 128; g.v0 = ld32(R, g.vo, 0); return g;
}
__device__ __forceinline__ void go_ack(rsrc_t R, const Go& g, int lane) {
  __builtin_amdgcn_raw_ptr_buffer_atomic_add_i32(1, R, lane == 0 ? g.vo - FL_GO + FL_ACK : OOB, 0, SC1);
}
__device__ __forceinline__ bool go_wait(rsrc_t R, const Go& g, int lane) {
  for (unsigned spins = 0; spins < SPINCAP; ++spins) {
    CBAR();
    if (__all(ld32(R, g.vo, 0) != g.v0)) {
      go_ack(R, g, lane);
      return true;
    }
    __builtin_amdgcn_s_sleep(6);
  }
  return false;
}
__device__ __forceinline__ void go_lead(rsrc_t R, int tid, int wave, int lane) {
#pragma unroll
  for (int k = 0; k < FLAG_BYTES / 4096; ++k) st128((u32x4){~0u, ~0u, ~0u, ~0u}, R, OFF_FLAGS + k * 4096 + tid * 16, 0);
  VMW(0);
  BAR();
  if (wave != 0) return;
  for (unsigned k = 0; k < SPINCAP; ++k) {
    st32(k, R, lane < 16 ? FL_GO + lane * 128 : OOB, 0);
    int n = lane < 16 ? (int)ld32(R, FL_ACK + lane * 128, 0) + 1 : 0;
#pragma unroll
    for (int d = 1; d < 16; d <<= 1) n += __shfl_xor(n, d);
    if (__builtin_amdgcn_readfirstlane(n) >= NACK) break;
    __builtin_amdgcn_s_sleep(4);
  }
}
__device__ __forceinline__ bool wait_flag(rsrc_t R, unsigned vo, int need) {
  for (unsigned spins = 0; spins < SPINCAP; ++spins) {
    CBAR();
    int v = (int)ld32(R, vo, 0);
    if (__all(v >= need)) return true;
    __builtin_amdgcn_s_sleep(8);
  }
  return false;
}

template <int SLP, int AT = 0>
__device__ __forceinline__ bool wait_flag_c(rsrc_t R, unsigned vo, int need, int& cache) {
  if (__all(cache >= need)) return true;
  for (unsigned spins = 0; spins < SPINCAP; ++spins) {
    CBAR();
    cache = (int)(AT ? lda(R, vo) : ld32(R, vo, 0));
    if (__all(cache >= need)) return true;
    __builtin_amdgcn_s_sleep(SLP);
  }
  return false;
}
__device__ __forceinline__ unsigned umax3(unsigned a, unsigned b, unsigned c) { return max(max(a, b), c); }

template <int NTB> __device__ __forceinline__ u32x4 gld(const u16* p) {
  if (NTB) return __builtin_nontemporal_load((const u32x4*)p);
  return *(const u32x4*)p;
}
#define XSTR2(x) #x
#define XSTR(x) XSTR2(x)
#define LSLP 3
#define ESLP 0
#define MSLP 0
#define PXS 6
#define TSLP 2
#define XSLP 2
#define KSLP 10
#define OSLP 24
#define OSL2 4
#define UE 1
#define YL
#define CPF 3
#define PARK 58
#define OFAST 60
#define CPS 8
#define CSL 2
#define TSL 6
#define UGO 5
#define UPO 1
#define CALN 4
#define QDEP 4
#define PDEP 3
#define WDEP 3
#define VSLP 2
#define QLN 24
#define VLN 8
#define PLN 32
#define LMASK(n) ((n) >= 64 ? ~0ull : ((1ull << ((n) & 63)) - 1))
#define UP3 0
#define ORDT 0x73652410u
#define USLP 8
#define UT 0
#define UTD 1
#define EMF 0
#define EMV 0
#define MWPOS 1
#define DSLP "s_sleep 1\n\t"
typedef __attribute__((ext_vector_type(4))) int i32x4;
__device__ __forceinline__ u64 ballot(bool c) { return __builtin_amdgcn_ballot_w64(c); }
#define UNLIKELY(c) __builtin_expect(!!(c), 0)
#define DELTA 164
#define D1 98
#define DVQ 4
#define LEV0 1
#define LEV3 1
#define LEVMAX 7
#define CL0 8
#define HEAT 3
#define COOL0 4
#define PSLP 8
#define VK0 64
#define AWE 0
#define ASLP 0
#define LEVF 99
#define RX 0
#define VSL 6
#define D0 14
#define WLAST 41
#define UDL 8
#define MGAP 0
#define SMIN 8
#define K1 4
#define PAD0 4
#define PADD 4
#define PADMAX 16
#define OAUX 0
__device__ __forceinline__ unsigned rt_now() {
  u64 t; asm volatile("s_memrealtime %0\n\ts_waitcnt lgkmcnt(0)" : "=s"(t) :: "memory"); return (unsigned)t;
}
#define S0 3
#define PACE0 ((DE0 << 8) | DELTA0)
#define PACEA ((DEA << 8) | (DEA + KA_))
#define PACEB ((DE << 8) | (DE + K2))
#define PACEC ((DEC << 8) | (DEC + KC_))
__device__ __forceinline__ void next_time(unsigned& t, int s) {
  t += (s < S0 ? PACE0 : (s < S1 ? PACEA : (s < S2 ? PACEB : PACEC))) & 255u;
  t = __builtin_amdgcn_readfirstlane(t);
  asm volatile("" : "+s"(t));
}
#define SNOP 1
#define SPIN(T, a, b, c) a ":\n\ts_memrealtime vcc\n\ts_waitcnt lgkmcnt(0)\n\ts_sub_u32 %[r], %[" #T "], vcc_lo\n\ts_sub_u32 %[k], %[r], 4\n\t"   \
               "s_cmp_lt_u32 %[k], 2000\n\ts_cbranch_scc1 " a "b\n\t"                                                                       \
               "s_sub_u32 %[k], %[r], 1\n\ts_cmp_lt_u32 %[k], 3\n\ts_cbranch_scc0 " c "f\n\t"                                               \
               b ":\n\ts_nop " XSTR(SNOP) "\n\ts_sub_u32 %[k], %[k], 1\n\ts_cbranch_scc0 " b "b\n\t" c ":\n\t"
__device__ __forceinline__ int spin_until(unsigned T) {
  int r, k;
  asm volatile(SPIN(T, "1", "2", "3") : [r] "=&s"(r), [k] "=&s"(k) : [T] "s"(T) : "memory", "vcc", "scc");
  return r;
}
#define KA "v_mov_b32 v240, v240\n\ts_nop 2\n\t"
#define KAN KA KA
#define EVA(n)
#define EVC(k)
#define BUD(l) "s_sub_u32 %[bud], %[bud], 1\n\ts_cbranch_scc0 " l "\n\ts_mov_b32 %[bud], 0\n\t"
__device__ __forceinline__ i32x4 mkdesc(char* wb) {
  const u64 p = (u64)wb;
  return (i32x4){(int)(unsigned)p, (int)(unsigned)(p >> 32), (int)OFF_END, 0x00020000};
}
template <class T> __device__ __forceinline__ void pin_v(T& v) { asm volatile("" : "+v"(v)); }
#define SB() __builtin_amdgcn_sched_barrier(0)
__device__ __forceinline__ unsigned pkv(float a, float b) {
  unsigned d; asm volatile("v_cvt_pk_bf16_f32 %0, %1, %2" : "=v"(d) : "v"(a), "v"(b)); return d;
}
__device__ __forceinline__ f32x4 mfB0(u32x4 a, u32x4 b) {
  f32x4 d; asm volatile("v_mfma_f32_16x16x32_bf16 %0, %1, %2, 0" : "=&v"(d) : "v"(a), "a"(b)); return d;
}
__device__ __forceinline__ void mfB(f32x4& d, u32x4 a, u32x4 b) {
  asm volatile("v_mfma_f32_16x16x32_bf16 %0, %1, %2, %0" : "+v"(d) : "v"(a), "a"(b));
}

template <int NTB>
__device__ __forceinline__ void qk_gload(u32x4 (&st)[8], const Args& a, int h, int c, int tid) {
#pragma unroll
  for (int k = 0; k < 8; ++k) {
    int u = tid + 256 * k; int row = u >> 5, cu = u & 31;
    const u16* W = (cu < 16) ? a.Wq : a.Wk;
    st[k] = gld<NTB>(W + (((size_t)(64 * c + row) * HD + h * 128 + (cu & 15) * 8) & ((NOCW & 1) ? 2047 : ~0ull)));
  }
}
__device__ __forceinline__ void qk_swrite(const u32x4 (&st)[8], lds_char* buf, int tid) {
#pragma unroll
  for (int k = 0; k < 8; ++k) { int u = tid + 256 * k; int row = u >> 5, cu = u & 31; *(lds_u32x4*)(buf + row * 544 + cu * 16) = st[k]; }
}
#define STG_BYTES 38912
#define X0_OFF (2 * STG_BYTES)
#define X1_OFF (X0_OFF + 37888)

__device__ __forceinline__ int cls_look(rsrc_t R, int lane) { return (int)ld32(R, FL_FLIP + (lane & 31) * 4, 0); }
__device__ __forceinline__ int rowsum(int v) {
  v += __builtin_amdgcn_update_dpp(v, v, 0x128, 0xf, 0xf, false);
  v += __builtin_amdgcn_update_dpp(v, v, 0x124, 0xf, 0xf, false);
  v += __builtin_amdgcn_update_dpp(v, v, 0x122, 0xf, 0xf, false);
  return v + __builtin_amdgcn_update_dpp(v, v, 0x121, 0xf, 0xf, false);
}
__device__ __forceinline__ int get_cls(rsrc_t R, int lane, int v = -1) {
  for (unsigned spins = 0; spins < SPINCAP; ++spins) {
    if (__all(v >= 0)) {
      const int n = rowsum((lane & 1) ? -v : v) > 0;
      const int d = rowsum((lane & 1) != n ? 0 : (((lane >> 1) ^ (lane >> 2)) & 1 ? -v : v)) > 0;
      const int c = n | (d << 1);
      return (__builtin_amdgcn_readlane(c, 0) * 0x55 ^ 0x88) | (__builtin_amdgcn_readlane(c, 16) << 8);
    }
    if (spins) __builtin_amdgcn_s_sleep(CPS);
    CBAR();
    v = cls_look(R, lane);
  }
  return -1;
}
__device__ __forceinline__ void calibrate(rsrc_t R, const Go& go, int r, int k, int lane) {
  unsigned best = 0xFFFFFFu;
  for (int it = 0; it < CALN; ++it) {
    unsigned long long t0, t1;
    asm volatile("s_waitcnt vmcnt(0)\n\ts_memtime %0\n\ts_waitcnt lgkmcnt(0)" : "=s"(t0) :: "memory");
    st32((unsigned)it, R, (lane == 0) ? (unsigned)((r >> 2) * OFF_YR + REAL_CAL + (r & 3) * 256 + k * 8192 + (it & 1) * 4096) : OOB, 0);
    asm volatile("s_waitcnt vmcnt(0)\n\ts_memtime %0\n\ts_waitcnt lgkmcnt(0)" : "=s"(t1) :: "memory");
    best = min(best, (unsigned)(t1 - t0));
  }
  if (go_wait(R, go, lane)) st32(best, R, lane == 0 ? (unsigned)(FL_FLIP + (r * 4 + k) * 4) : OOB, 0);
}
__device__ __forceinline__ unsigned get_t1(rsrc_t R, unsigned vo, unsigned& bud) {
  unsigned v = 0;
#pragma nounroll
  for (; bud != 0; --bud) {
    CBAR();
    v = __builtin_amdgcn_readfirstlane(ld32(R, vo, 0));
    if (v != ~0u) break;
    __builtin_amdgcn_s_sleep(2);
  }
  return v;
}

#define F8_OFF 18432
#define QSTG 34816
#define ISOT()
#define ISOQ(n)
__device__ __forceinline__ float vmax(float a, float b) { float d; asm("v_max_f32 %0, %1, %2" : "=v"(d) : "v"(a), "v"(b)); return d; }
__device__ __forceinline__ float vmax3(float a, float b, float c) { float d; asm("v_max3_f32 %0, %1, %2, %3" : "=v"(d) : "v"(a), "v"(b), "v"(c)); return d; }
__device__ __forceinline__ float max32(float x) {
  auto t = __builtin_amdgcn_permlane32_swap(__float_as_uint(x), __float_as_uint(x), false, false);
  return vmax(__uint_as_float(t[0]), __uint_as_float(t[1]));
}
__device__ __forceinline__ float max16(float x) {
  auto t = __builtin_amdgcn_permlane16_swap(__float_as_uint(x), __float_as_uint(x), false, false);
  return vmax(__uint_as_float(t[0]), __uint_as_float(t[1]));
}
__device__ __forceinline__ float sum32(float x) {
  auto t = __builtin_amdgcn_permlane32_swap(__float_as_uint(x), __float_as_uint(x), false, false);
  return __uint_as_float(t[0]) + __uint_as_float(t[1]);
}
template <int r> __device__ __forceinline__ void swp32(f32x4& a, f32x4& b) {
  auto t = __builtin_amdgcn_permlane32_swap(__float_as_uint(a[r]), __float_as_uint(b[r]), false, false);
  a[r] = __uint_as_float(t[0]); b[r] = __uint_as_float(t[1]);
}
__device__ __forceinline__ float vadd(float a, float b) { float d; asm("v_add_f32 %0, %1, %2" : "=v"(d) : "v"(a), "v"(b)); return d; }
__device__ __forceinline__ float max8(float x) {
  float d; asm("s_nop 1\n\tv_max_f32_dpp %0, %1, %1 row_ror:8 row_mask:0xf bank_mask:0xf" : "=&v"(d) : "v"(x)); return d;
}
__device__ __forceinline__ float sum8(float x) {
  float d; asm("s_nop 1\n\tv_add_f32_dpp %0, %1, %1 row_ror:8 row_mask:0xf bank_mask:0xf" : "=&v"(d) : "v"(x)); return d;
}
__device__ __forceinline__ float sum16(float x) {
  auto t = __builtin_amdgcn_permlane16_swap(__float_as_uint(x), __float_as_uint(x), false, false);
  return __uint_as_float(t[0]) + __uint_as_float(t[1]);
}
__device__ __forceinline__ void st64(u32x2 v, rsrc_t R, unsigned vo, unsigned so) { __builtin_amdgcn_raw_buffer_store_b64(v, R, vo, so, SC1); }
template <int N>
__device__ __forceinline__ void mem_issue2(i32x4 D, unsigned m0, unsigned m1, unsigned m2, unsigned so, unsigned l0, unsigned l1, unsigned l2, u64& ew) {
  asm volatile("s_mov_b32 m0, %[l0]\n\ts_barrier\n\t" EVASM
               "buffer_load_dwordx4 %[m0], %[D], %[so] offen sc1 lds\n\t"
               "s_mov_b32 m0, %[l1]\n\ts_nop 0\n\t"
               "buffer_load_dwordx4 %[m1], %[D], %[so] offen sc1 lds\n\t"
               ".if %[n] == 3\n\ts_mov_b32 m0, %[l2]\n\ts_nop 0\n\t"
               "buffer_load_dwordx4 %[m2], %[D], %[so] offen sc1 lds\n\t.endif"
               : [ew] "=&s"(ew)
               : [m0] "v"(m0), [m1] "v"(m1), [m2] "v"(m2), [D] "s"(D), [so] "s"(so), [l0] "s"(l0), [l1] "s"(l1), [l2] "s"(l2), [n] "n"(N)
               : "memory");
}
#define TRYB ".if %[id] >= 0\n\ts_cbranch_vccnz .Ltry%[id]\n.Lend%[id]:\n\t.endif\n\t"
#define TRYC "vcc", "scc", "v236", "v237", "v238", "v239", "v240"
template <int ID>
__device__ __forceinline__ void try_top(unsigned& nt, unsigned& ms, unsigned& tm, unsigned& rcnt, unsigned ia) {
  asm volatile(".Ltry%[id]:\n\ts_waitcnt lgkmcnt(0)\n\ts_cmp_ge_i32 %[nt], " XSTR(NSC) "\n\ts_cbranch_scc1 1f\n"
               "2:\n\ts_nop 4\n\ts_xor_b32 %[ms], %[ms], %[fl]\n\ts_add_u32 %[tm], %[tm], " XSTR(GRID) "+" XSTR(XT) "\n\ts_add_u32 %[nt], %[nt], 1\n\t.subsection 1\n"
               "1:\n\tv_mov_b32 v240, 0\n\t"
               ".irp o, 0, 1024, 2048, 3072, 4096, 5120, 6144, 7168, " XSTR(F8_OFF) "\n\tds_read_b128 v[236:239], %[ia] offset:\\o\n\ts_waitcnt lgkmcnt(0)\n\t"
               "v_max3_u32 v240, v240, v236, v237\n\tv_max3_u32 v240, v240, v238, v239\n\t.endr\n\t"
               "v_cmp_eq_u32 vcc, -1, v240\n\ts_cbranch_vccz .Lend%[id]\n\ts_add_u32 %[rc], %[rc], 1\n\ts_cmp_lt_u32 %[rc], %[cap]\n\t"
               "s_cbranch_scc1 2b\n\ts_branch .Lend%[id]\n\t.subsection 0"
               : [nt] "+s"(nt), [ms] "+s"(ms), [tm] "+s"(tm), [rc] "+s"(rcnt)
               : [fl] "n"(CPY2 + CPY1), [id] "n"(ID), [ia] "v"(ia), [cap] "n"(SPINCAP / 2) : "memory", TRYC);
}
__device__ __forceinline__ void try_end(unsigned ia, unsigned nt, unsigned ms, unsigned tm, unsigned rcnt,
                                        unsigned m0, unsigned m1, unsigned m2, unsigned l0, unsigned l1, unsigned l2, i32x4 D) {
  asm volatile("" :: "s"(rcnt), "s"(nt), "s"(ms), "s"(tm), "v"(ia), "v"(m0), "v"(m1), "v"(m2), "s"(l0), "s"(l1), "s"(l2), "s"(D) : "memory");
}
__device__ __forceinline__ int spin_late(unsigned& t, unsigned d) {
  int r, k; unsigned T;
  asm volatile("s_add_u32 %[T], %[t], %[d]\n\t" SPIN(T, "1", "2", "3") : [r] "=&s"(r), [k] "=&s"(k), [T] "=&s"(T), [t] "+s"(t) : [d] "n"(d) : "memory", "vcc", "scc");
  return r;
}
__device__ __forceinline__ void move_on(unsigned& t, int r) {
  asm volatile("1:\n\ts_cmp_lt_i32 %1, -" XSTR(LTOL) "\n\ts_cbranch_scc0 2f\n\ts_add_u32 %0, %0, " XSTR(GRID) "+" XSTR(XT) "\n\ts_add_u32 %1, %1, " XSTR(GRID) "+" XSTR(XT) "\n\ts_branch 1b\n2:"
               : "+s"(t), "+s"(r) :: "scc");
}
__device__ __forceinline__ void gate_wait(rsrc_t R, unsigned vo, int need, int& cache, unsigned& bud) {
#pragma nounroll
  for (; bud != 0; --bud) {
    if (__all(cache >= need)) break;
    CBAR();
    cache = (int)ld32(R, vo, 0);
    __builtin_amdgcn_s_sleep(4);
  }
}
__device__ __forceinline__ unsigned um3v(unsigned a, unsigned b, unsigned c) {
  unsigned d; asm volatile("v_max3_u32 %0, %1, %2, %3" : "=v"(d) : "v"(a), "v"(b), "v"(c)); return d;
}
template <int OFF>
__device__ __forceinline__ void k_put(f32x4 k, unsigned ad) {
  unsigned t0, t1;
  asm volatile("v_cvt_pk_bf16_f32 %0, %2, %3\n\tv_cvt_pk_bf16_f32 %1, %4, %5\n\tds_write2_b32 %6, %0, %1 offset0:%7 offset1:%8"
               : "=&v"(t0), "=&v"(t1) : "v"(k[0]), "v"(k[1]), "v"(k[2]), "v"(k[3]), "v"(ad), "n"(OFF), "n"(OFF + 2) : "memory");
}
template <int OFF>
__device__ __forceinline__ void q_put(f32x4 k, unsigned ad) {
  unsigned t0, t1;
  asm volatile("v_cvt_pk_bf16_f32 %0, %2, %3\n\tv_cvt_pk_bf16_f32 %1, %4, %5\n\tds_write2_b32 %6, %0, %1 offset0:%7 offset1:%8"
               : "=&v"(t0), "=&v"(t1) : "v"(k[0]), "v"(k[1]), "v"(k[2]), "v"(k[3]), "v"(ad), "n"(OFF), "n"(OFF + 1) : "memory");
}
__device__ __forceinline__ f32x4 mf0(u32x4 a, u32x4 b) {
  f32x4 d; asm volatile("v_mfma_f32_16x16x32_bf16 %0, %1, %2, 0" : "=&v"(d) : "v"(a), "v"(b)); return d;
}
__device__ __forceinline__ void mfa(f32x4& d, u32x4 a, u32x4 b) {
  asm volatile("v_mfma_f32_16x16x32_bf16 %0, %1, %2, %0" : "+v"(d) : "v"(a), "v"(b));
}
__device__ __forceinline__ f32x4 mfA0(u32x4 a, u32x4 b) {
  f32x4 d; asm volatile("v_mfma_f32_16x16x32_bf16 %0, %1, %2, 0" : "=&v"(d) : "a"(a), "v"(b)); return d;
}
__device__ __forceinline__ void mfA(f32x4& d, u32x4 a, u32x4 b) {
  asm volatile("v_mfma_f32_16x16x32_bf16 %0, %1, %2, %0" : "+v"(d) : "a"(a), "v"(b));
}
#define DPPLO " row_ror:8 row_mask:0xf bank_mask:0x3\n\t"
#define DPPHI " row_ror:8 row_mask:0xf bank_mask:0xc\n\t"
#define DPPAL " row_ror:8 row_mask:0xf bank_mask:0xf\n\t"
#define SCK "0x3e0293ee"
#define SCN "0xbe0293ee"
__device__ __forceinline__ void px_mask(f32x4& T0, f32x4& T1, int qt, int lane) {
  const float ninf = -__builtin_inff();
#pragma unroll
  for (int r = 0; r < 4; ++r) {
    const bool diag = (4 * (lane >> 4) + r) <= (lane & 15);
    T0[r] = (qt == 1 || diag) ? T0[r] : ninf;
    T1[r] = (qt == 1 && diag) ? T1[r] : ninf;
  }
}
__device__ __forceinline__ void px_seg(f32x4& T0, f32x4& T1, float& mx, float& sx) {
  const float sc = 0.08838834764831845f * 1.4426950408889634f;
  mx = xmax(fmaxf(fmaxf(fmaxf(T0[0], T0[1]), fmaxf(T0[2], T0[3])), fmaxf(fmaxf(T1[0], T1[1]), fmaxf(T1[2], T1[3]))));
  const float ms = -mx * sc;
  sx = 0.f;
#pragma unroll
  for (int r = 0; r < 4; ++r) {
    T0[r] = __builtin_amdgcn_exp2f(fmaf(T0[r], sc, ms)); T1[r] = __builtin_amdgcn_exp2f(fmaf(T1[r], sc, ms));
    sx += T0[r] + T1[r];
  }
}
__device__ __forceinline__ void px_fin(f32x4 G, f32x4 T0, f32x4 T1, float mx, float sx, u32x4& px, unsigned& pw, int lane) {
  const float sc = 0.08838834764831845f * 1.4426950408889634f;
  float g0 = (lane & 16) ? G[2] : G[0], g1 = (lane & 16) ? G[3] : G[1];
  const float ms = -xmax(fmaxf(fmaxf(g0, g1), mx)) * sc;
  g0 = __builtin_amdgcn_exp2f(fmaf(g0, sc, ms)); g1 = __builtin_amdgcn_exp2f(fmaf(g1, sc, ms));
  const float f = __builtin_amdgcn_exp2f(fmaf(mx, sc, ms));
  asm volatile("" : "+v"(g0), "+v"(g1)); PAD();
  const float inv = __builtin_amdgcn_rcpf(xsum(fmaf(f, sx, g0 + g1))), fi = f * inv;
  px = (u32x4){pk2(T0[0] * fi, T0[1] * fi), pk2(T0[2] * fi, T0[3] * fi), pk2(T1[0] * fi, T1[1] * fi), pk2(T1[2] * fi, T1[3] * fi)};
  pw = pk2(g0 * inv, g1 * inv);
}
#define WPOS 4
#define QPOS 1
#define LPOS 1
#define QIM_OFF 8192
#define KIM_OFF 10240
#define FLG_OFF 17408
#define F8W 3
#define KX_OFF 36864
typedef __attribute__((ext_vector_type(16))) float f32x16;
typedef __attribute__((ext_vector_type(16))) unsigned u32x16;
__device__ __forceinline__ void ds_flag(unsigned ad, unsigned v) { asm volatile("ds_write_b32 %0, %1" :: "v"(ad), "v"(v) : "memory"); }
#define SMX(d, j, w) "v_smfmac_f32_16x16x64_bf16 v[" d "], %[a" #j "], %[" #w "], %[ix]\n\t"
#define CVX(d, x, y) "v_cvt_pk_bf16_f32 v" #d ", v" #x ", v" #y "\n\t"
#define PKA(d, b) "v_pk_add_f32 v[" #d ":" #d "+1], v[" #b ":" #b "+1], v[" #b "+2:" #b "+3]\n\t"
#define IMW(a, o) "ds_write_b32 " a ", v232 offset:" o "\n\t"
template <int T, int AG>
__device__ __forceinline__ void smo(f32x4& c, u32x4 a, u32x8 b, int ix) {
  if (T == 0 && AG) asm volatile(SMX("140:143", 0, w) : "+{v[140:143]}"(c) : [a0] "v"(a), [w] "a"(b), [ix] "v"(ix));
  if (T == 0 && !AG) asm volatile(SMX("140:143", 0, w) : "+{v[140:143]}"(c) : [a0] "v"(a), [w] "v"(b), [ix] "v"(ix));
  if (T == 1 && AG) asm volatile(SMX("144:147", 0, w) : "+{v[144:147]}"(c) : [a0] "v"(a), [w] "a"(b), [ix] "v"(ix));
  if (T == 1 && !AG) asm volatile(SMX("144:147", 0, w) : "+{v[144:147]}"(c) : [a0] "v"(a), [w] "v"(b), [ix] "v"(ix));
}
__device__ __forceinline__ void v_zero(f32x4& v0, f32x4& v1) {
  asm volatile("v_mov_b64 v[140:141], 0\n\tv_mov_b64 v[142:143], 0\n\tv_mov_b64 v[144:145], 0\n\tv_mov_b64 v[146:147], 0\n\ts_nop 1"
               : "={v[140:143]}"(v0), "={v[144:147]}"(v1));
}
__device__ __forceinline__ void k_zero(f32x4& k0, f32x4& k1, f32x4& k2) {
  asm volatile(".rept 6\n\tv_mov_b64 v[148+2*\\+:149+2*\\+], 0\n\t.endr" : "={v[148:151]}"(k0), "={v[152:155]}"(k1), "={v[156:159]}"(k2));
}
#define TZERO ".rept 6\n\tv_mov_b64 v[200+2*\\+:201+2*\\+], 0\n\t.endr\n\t"
__device__ __forceinline__ void t_zero(f32x4& k0, f32x16& T) {
  asm volatile("v_mov_b64 v[148:149], 0\n\tv_mov_b64 v[150:151], 0\n\t" TZERO : "={v[148:151]}"(k0), "={v[200:215]}"(T));
}
#define AD0(b) "v_add_f32 v230, v[" #b "], v[" #b "+2]\n\t"
#define AD1(b) "v_add_f32 v231, v[" #b "+1], v[" #b "+3]\n\t"
#define CVP CVX(232, 230, 231)
#define Q1(j) SMX("144:147", j, w##j)
template <int ID>
__device__ __forceinline__ void q_mid(const u32x4 (&af)[9], const u32x8 (&w)[9], int ix, unsigned qw, f32x4 q0, f32x4& q1) {
  asm volatile(Q1(3) Q1(4) AD0(140) Q1(5) AD1(140) "v_cmp_u_f32 vcc, v230, v231\n\t" Q1(6) CVP Q1(7) TRYB IMW("%[qw]", "0") Q1(8)
               : "+{v[144:147]}"(q1)
               : "{v[140:143]}"(q0), [a3] "v"(af[3]), [a4] "v"(af[4]), [a5] "v"(af[5]), [a6] "v"(af[6]), [a7] "v"(af[7]), [a8] "v"(af[8]),
                 [w3] "a"(w[3]), [w4] "a"(w[4]), [w5] "a"(w[5]), [w6] "a"(w[6]), [w7] "a"(w[7]), [w8] "v"(w[8]), [ix] "v"(ix), [qw] "v"(qw), [id] "n"(ID)
               : "memory", "v230", "v231", "v232", TRYC);
}
__device__ __forceinline__ void k_last(unsigned kw, f32x4 q1, unsigned fw, unsigned fv) {
  asm volatile("s_nop 7\n\t" PKA(230, 144) CVP IMW("%[kw]", "1024") "ds_write_b32 %[fw], %[fv]"
               :: "{v[144:147]}"(q1), [kw] "v"(kw), [fw] "v"(fw), [fv] "v"(fv) : "memory", "v230", "v231", "v232");
}
#define K0(j) SMX("148:151", j, u##j)
#define K1(j) SMX("152:155", j, u##j)
#define AF9 [a0] "v"(af[0]), [a1] "v"(af[1]), [a2] "v"(af[2]), [a3] "v"(af[3]), [a4] "v"(af[4]), [a5] "v"(af[5]), [a6] "v"(af[6]), [a7] "v"(af[7]), [a8] "v"(af[8])
#define WK9 [u0] "a"(wk[0]), [u1] "a"(wk[1]), [u2] "a"(wk[2]), [u3] "a"(wk[3]), [u4] "a"(wk[4]), [u5] "a"(wk[5]), [u6] "a"(wk[6]), [u7] "a"(wk[7]), [u8] "v"(wk[8])
__device__ __forceinline__ void k_one(const u32x4 (&af)[9], const u32x8 (&wk)[9], const u32x8 (&wp)[3], int ix, unsigned qw, f32x4 q1, f32x4& k0, f32x4& k2, float& t) {
  asm volatile(K0(8) K0(0) AD0(144) AD1(144) CVP K0(4) IMW("%[qw]", "1024") K0(1) K0(2) K0(3) K0(5) PADA K0(6) K0(7)
               SMX("156:159", 0, p0) SMX("156:159", 4, p1) AD0(148)
               : "+{v[148:151]}"(k0), "+{v[156:159]}"(k2), "=&{v230}"(t)
               : "{v[144:147]}"(q1), AF9, WK9, [p0] "v"(wp[0]), [p1] "v"(wp[1]), [ix] "v"(ix), [qw] "v"(qw) : "memory", "v231", "v232");
}
#define KB
template <int PO, int JC>
__device__ __forceinline__ void k_end(const u32x4 (&af)[9], const u32x8 (&wk)[9], u32x8 wp, int ix, f32x4 k0, f32x4 k1, f32x4 k2, float t, unsigned kw, unsigned fw, unsigned fv) {
  asm volatile(".if %[jc] == 3\n\t" SMX("156:159", 3, p2) ".endif\n\t.if %[jc] == 6\n\t" SMX("156:159", 6, p2) ".endif\n\t.if %[jc] == 8\n\t" SMX("156:159", 8, p2) ".endif\n\t"
               AD1(148) K1(8) CVP K1(0) IMW("%[kw]", "0") K1(4) AD0(156) K1(1) AD1(156) K1(2) CVP K1(3) IMW("%[kw]", "%[po]") K1(5) PADA K1(6) K1(7)
               "s_nop 7\n\t" PKA(230, 152) CVP IMW("%[kw]", "1024") KB "ds_write_b32 %[fw], %[fv]\n\t"
               :: "{v[148:151]}"(k0), "{v[152:155]}"(k1), "{v[156:159]}"(k2), "{v230}"(t), AF9, WK9, [p2] "v"(wp), [ix] "v"(ix), [kw] "v"(kw), [fw] "v"(fw),
                  [fv] "v"(fv), [po] "n"(PO), [jc] "n"(JC)
               : "memory", "v231", "v232");
}
__device__ __forceinline__ unsigned add3bf(unsigned a, unsigned b, unsigned c) {
  return pk2(__uint_as_float(a << 16) + __uint_as_float(b << 16) + __uint_as_float(c << 16),
             __uint_as_float(a & 0xffff0000u) + __uint_as_float(b & 0xffff0000u) + __uint_as_float(c & 0xffff0000u));
}
#define IS(n)
#define cKW "v112"
#define cKA "v113"
#define cQ0 "v114"
#define cQ1 "v115"
#define cQ3 "v116"
#define cFA "v117"
#define cEO "v118"
#define cLO "v119"
#define cC0 "v120"
#define cC1 "v121"
#define cC2 "v122"
#define cFW "v123"
#define cQW "v124"
#define cKX "v125"
#define TRD(r, a, o) "ds_read_b64_tr_b16 v[" #r ":" #r "+1], " a " offset:" #o "\n\t"
#define LGK(n) "s_waitcnt lgkmcnt(" #n ")\n\t"
#define SXM(t, c) "v_smfmac_f32_16x16x64_bf16 v[200+4*" #t ":203+4*" #t "], v[160+4*" #c ":163+4*" #c "], v[64+16*" #t "+8*" #c ":71+16*" #t "+8*" #c "], %[ix]\n\t"
#define SMM(c) "v_smfmac_f32_16x16x64_bf16 v[208:211], v[160+4*" #c ":163+4*" #c "], v[176+8*" #c ":183+8*" #c "], %[ix]\n\t"
#define PQ ".if pq\n97:\n\ts_cbranch_execz 97f\n\t.endif\n\t"
#define PQ0 ".set pq, 0\n\t"
#define PQA ".set pq, %[wid] >= 0\n\t.if pq\n.Lwa%[wid]:\n\t.endif\n\t" PQ
#define PQB ".if pq\n97:\n\ts_cbranch_execz .Lwb%[wid]\n\t.endif\n\t" PQ0
template <int A> __device__ __forceinline__ void pq_walk() {
  asm volatile(".if %1\n\ts_mov_b64 exec, 0\n\ts_branch .Lwa%0\n\t.endif\n.Lwb%0:\n\ts_mov_b64 exec, -1" :: "n"(A), "n"(WRM >> 1 & 1) : "memory");
}
#define QRE TRD(160, cQ0, 0) TRD(162, cQ1, 0)
#define QRL TRD(164, cQ0, 1024) TRD(166, cQ1, 1024)
#define CN(k)
#define QE 0
#define QL 1
#define KL -1
#define QP(n) ".if " XSTR(QE) " == " #n "\n\t" QRE ".endif\n\t"
#define QQ(n) ".if " XSTR(QL) " == " #n "\n\t" QRL ".endif\n\t"
template <int PH>
__device__ __forceinline__ void kx_get(u32x16 cst, u32x16& x0, u32x16& x1) {
#define KXR(k) "ds_read_b128 v[64+4*" #k ":67+4*" #k "], " cKX " offset:%[o]+1024*" #k "\n\tds_read_b128 v[80+4*" #k ":83+4*" #k "], " cKX " offset:%[o]+1024*" #k "+16\n\t"
  asm volatile(KXR(0) KXR(1) KXR(2) KXR(3)
               : "={v[64:79]}"(x0), "={v[80:95]}"(x1) : "{v[112:127]}"(cst), [o] "n"(KX_OFF + PH * 8192) : "memory");
}
#define NK 2
#define KIF(c, t) ".if " XSTR(NK) c "\n\t" t ".endif\n\t"
__device__ __forceinline__ void s_first(const u32x4 (&af)[9], const u32x8 (&wk)[9], int ix, u32x16 cst, u32x16 x0, u32x16 x1, f32x4 q1, f32x4& k0, f32x16& T, u32x8& Q) {
  asm volatile(K0(8) QP(0) K0(0) AD0(144) AD1(144) CVP K0(4) IMW(cQW, "1024") K0(1) QP(1) QQ(0) K0(2) QQ(1) K0(3) QQ(2) KIF(" == 0", K0(5))
               LGK(2) SXM(0, 0) SXM(1, 0) LGK(0) SXM(0, 1) SXM(1, 1)
               : "+{v[148:151]}"(k0), "+{v[200:215]}"(T), "=&{v[160:167]}"(Q)
               : "{v[144:147]}"(q1), [a0] "v"(af[0]), [a1] "v"(af[1]), [a2] "v"(af[2]), [a3] "v"(af[3]), [a4] "v"(af[4]), [a5] "v"(af[5]), [a8] "v"(af[8]),
                 [u0] "a"(wk[0]), [u1] "a"(wk[1]), [u2] "a"(wk[2]), [u3] "a"(wk[3]), [u4] "a"(wk[4]), [u5] "a"(wk[5]), [u8] "v"(wk[8]),
                 [ix] "v"(ix), "{v[112:127]}"(cst), "{v[64:79]}"(x0), "{v[80:95]}"(x1)
               : "memory", "v230", "v231", "v232");
}
#define DPPF " row_mask:0xf bank_mask:0xf\n\t"
#define DX1 " quad_perm:[1,0,3,2]" DPPF
#define DX2 " quad_perm:[2,3,0,1]" DPPF
#define DX4 " row_half_mirror" DPPF
template <int WID>
__device__ __forceinline__ void el_phase(i32x4 D, const u32x4 (&af)[9], const u32x8 (&wk)[9], int ix, u32x16 cst, u32x16 x0, u32x16 x1, u32x8& Q, f32x4& k0, f32x16& T,
                                         unsigned& bud, u64& e1, u64& e4, unsigned& nr, u32x8 sc, unsigned fv) {
#define EST(s) "buffer_store_dwordx2 v[222:223], " cEO ", %[D], " s " offen sc1\n\t"
#define LST(s) "buffer_store_dword v227, " cLO ", %[D], " s " offen sc1\n\t"
#define EPART(KA, KB_)                                                                                                             \
               IS(4) KA "v_add_f32 v212, v200, v202\n\t" KB_ "v_add_f32 v213, v201, v203\n\tv_add_f32 v214, v204, v206\n\tv_add_f32 v215, v205, v207\n\t" \
               "v_max_f32 v216, v212, v214\n\tv_max_f32 v217, v213, v215\n\tv_mov_b32 v219, v212\n\t"                              \
               "v_max_f32_dpp v218, v216, v216" DPPLO "v_max_f32_dpp v218, v217, v217" DPPHI PQ                                    \
               "v_mov_b32 v220, v214\n\ts_nop 0\n\t"                                                                             \
               "v_max_f32_dpp v218, v218, v218" DX1 "v_mov_b32_dpp v212, v213" DPPHI "v_mov_b32_dpp v214, v215" DPPHI              \
               "v_max_f32_dpp v218, v218, v218" DX2 "v_mov_b32_dpp v213, v219" DPPLO "v_mov_b32_dpp v215, v220" DPPLO              \
               PQ "v_max_f32_dpp v218, v218, v218" DX4 IS(5)                                                                       \
               "v_mul_f32 v216, " SCN ", v218\n\t"                                                                                 \
               "v_pk_fma_f32 v[212:213], v[212:213], s[44:45], v[216:217] op_sel_hi:[1,0,0]\n\t"                                   \
               "v_pk_fma_f32 v[214:215], v[214:215], s[44:45], v[216:217] op_sel_hi:[1,0,0]\n\t"                                   \
               "v_exp_f32 v212, v212\n\tv_exp_f32 v214, v214\n\tv_exp_f32 v213, v213\n\tv_exp_f32 v215, v215\n\t"                  \
               PQ CVX(222, 212, 214) CVX(223, 213, 215)
#define MPART(KA, KB_, KBW)                                                                                                        \
               KA EST("s40") TRD(168, cQ3, 0) TRD(192, cKA, 2048) EST("s41") EVA(4) IS(6) PQ TRD(194, cKA, 2304) TRD(176, cKA, 0)   \
               KB_ TRD(178, cKA, 256) TRD(180, cKA, 512) TRD(182, cKA, 768)                                                        \
               "v_add_f32 v212, v212, v214\n\tv_add_f32 v213, v213, v215\n\tv_mov_b32 v225, " cC2 "\n\tv_add_f32 v212, v212, v213\n\t"  \
               PQ LGK(4) PKA(230, 148) CVP "v_smfmac_f32_16x16x32_bf16 v[208:211], v[168:169], v[192:195], %[ix]\n\t"                 \
               IMW(cKW, "1024") TRD(190, cKA, 1792) KBW TRD(184, cKA, 1024) PQ TRD(186, cKA, 1280) TRD(188, cKA, 1536) LGK(5) SMM(0)  \
               EST("s42") "ds_write_b32 " cFW ", %[fv]\n\t" LGK(1) SMM(1) IS(7)
#define GADD   "v_add_f32_dpp v220, v208, v210 quad_perm:[0,1,2,3] row_mask:0xf bank_mask:0x3\n\t"                                 \
               "v_add_f32_dpp v220, v209, v211 quad_perm:[0,1,2,3] row_mask:0xf bank_mask:0xc\n\t"
#define LEND   "v_add_f32_dpp v217, v217, v217" DX4 "v_rcp_f32 v218, v217\n\t" PADN                                                \
               "v_pk_mul_f32 v[224:225], v[224:225], v[218:219] op_sel_hi:[1,0]\n\t"                                               \
               CVX(227, 224, 225) IS(8) LST("s40") LST("s41") EVA(1) IS(9) LST("s42") EST("s43") LST("s43")
#define KBF
#define EVO
  asm volatile(PQ0 EPART(K0(6) KIF(" == 2", "s_nop 1\n\t"), KIF(" < 2", K0(7)))
               MPART(KIF(" > 0", K0(5)), KIF(" == 2", K0(7)), KBF)
               "s_nop 1\n\tv_add_f32_dpp v212, v212, v212" DX1 "s_nop 4\n\t" GADD
               "v_fmamk_f32 v221, v220, " SCK ", v216\n14:\n\tv_exp_f32 v220, v221\n\tv_cmp_nge_f32 vcc, s45, v221\n\tv_add_f32 v217, v220, v212\n\t"
               "v_add_f32_dpp v217, v220, v217" DX1 "v_fma_f32 v224, v220, " cC0 ", " cC1 "\n\tv_fmac_f32_dpp v225, v220, " cC0 DX1
               "v_add_f32_dpp v217, v217, v217" DX2 "s_cmp_eq_u64 vcc, 0\n\ts_cselect_b64 exec, -1, 0\n\t" LEND "s_cbranch_scc0 19f\n"
               "13:\n\t.subsection 1\n19:\n\ts_mov_b64 exec, -1\n\t" PQA CN(10000)
               "20:\n\tds_read_b32 v224, " cFA "\n\t" LGK(0) "v_cmp_gt_i32 vcc, %[fv], v224\n\ts_cbranch_vccz 21f\n\t" BUD("20b")
               "21:\n\t" QRE PQ QRL PQ TZERO PQ LGK(0) SXM(0, 0) SXM(1, 0) SXM(0, 1) SXM(1, 1) PQ
               EPART("s_nop 7\n\t", "") MPART("", "", "") PQ
               "v_mov_b32 v213, v212\n\tv_add_f32_dpp v212, v212, v212" DX1 "s_nop 5\n\t" GADD
               "v_fmamk_f32 v221, v220, " SCK ", v216\n\tv_cmp_nge_f32 vcc, s45, v221\n\ts_cbranch_vccz 14b\n\t" PQB
               "v_max_f32 v216, v220, v218\n\tv_mul_f32 v221, " SCK ", v218\n\ts_nop 0\n\t"
               "v_max_f32_dpp v216, v216, v216" DX1 "v_mul_f32 v220, " SCK ", v220\n\ts_nop 0\n\t"
               "v_max_f32_dpp v216, v216, v216" DX2 "s_nop 1\n\t"
               "v_max_f32_dpp v216, v216, v216" DX4
               "v_pk_fma_f32 v[220:221], v[216:217], s[44:45], v[220:221] op_sel_hi:[0,0,1] neg_lo:[1,0,0] neg_hi:[1,0,0]\n\t"
               "v_exp_f32 v221, v221\n\tv_exp_f32 v220, v220\n\t"
               "v_mul_f32 v224, v221, " cC1 "\n\tv_fma_f32 v217, v221, v213, v220\n\t"
               "v_mul_f32 v225, v221, " cC2 "\n\tv_fmac_f32 v224, v220, " cC0 "\n\t"
               "v_add_f32_dpp v217, v217, v217" DX1 "v_fmac_f32_dpp v225, v220, " cC0 DX1 "s_nop 0\n\t"
               "v_add_f32_dpp v217, v217, v217" DX2 "s_nop 1\n\t" LEND "s_branch 13b\n\t.subsection 0"
               : [bud] "+s"(bud), "+{v[160:167]}"(Q), "+{v[148:151]}"(k0), "+{v[200:215]}"(T) EVO
               : [a5] "v"(af[5]), [a6] "v"(af[6]), [a7] "v"(af[7]), [u5] "a"(wk[5]), [u6] "a"(wk[6]), [u7] "a"(wk[7]), [ix] "v"(ix),
                 "{v[112:127]}"(cst), "{v[64:79]}"(x0), "{v[80:95]}"(x1), [D] "s"(D), "{s[40:47]}"(sc), [fv] "v"(fv), [wid] "n"(WID)
               : "memory", "vcc", "scc", "v168", "v169", "v176", "v177", "v178", "v179", "v180", "v181", "v182", "v183", "v184",
                 "v185", "v186", "v187", "v188", "v189", "v190", "v191", "v192", "v193", "v194", "v195",
                 "v216", "v217", "v218", "v219", "v220", "v221", "v222", "v223", "v224", "v225", "v227", "v230", "v231", "v232");
}
#define YMAX 0x42700000u
#define TWO 2
#define ARD(j, p) do { af[j] = *(lds_u32x4*)(p); SB(); } while (0)
#define SECOND(n, NXV) do { if (TWO == n) { asm volatile("s_waitcnt vmcnt(%0)" :: "n"(NXV) : "memory"); __builtin_amdgcn_s_barrier(); CBAR(); SB(); \
                         ARD(4, ard0 + 4096); ARD(5, ard1 + 4096); } if (TWO + 1 == n) { ARD(6, ard2 + 4096); ARD(7, ard3 + 4096); }               \
                         if (TWO + 2 == n) ARD(8, lds + F8_OFF + ar); } while (0)
#define HEAD(M, NXV, H7) do {                                                                                                                     \
    ARD(0, ard0); __builtin_amdgcn_s_barrier(); CBAR(); SB(); ARD(1, ard1); ARD(2, ard2); ARD(3, ard3);                                       \
    if (!TWO) { ARD(4, ard0 + 4096); ARD(5, ard1 + 4096); }                                                                                   \
    M(0, 0); SECOND(1, NXV); M(1, 0); SECOND(2, NXV); M(0, 1); SECOND(3, NXV); M(1, 1); SECOND(4, NXV); M(0, 2); SECOND(5, NXV);              \
    if (!(H7)) M(1, 2);                                                                                                                       \
    SECOND(6, NXV); M(0, 3); M(0, 4); M(0, 5); M(0, 6); M(0, 7); M(0, 8); } while (0)
__device__ __forceinline__ void role_qkc(const Args& a, rsrc_t R, const Go& go, lds_char* lds, int h, int tid, int hw, int lane) {
  const int wave = 3 - hw;
  const int l15 = lane & 15, g = lane >> 4;
  u32x8 wq[2][9], wk[2][9], wp[3];
  int cls = -1, kv0 = -1;
  unsigned g1;
  u32x4 af0[3];
  const int wz = h >> 3, tq = (h >> 1) & 3, hp = h & 1, qi = 2 * g + (l15 >> 3), kq = l15 & 7;
  unsigned pxo = (unsigned)nearoff(NEAR_E + (h >> 1) * 1024 + ((kq >> 1) * 16 + 8 * (qi >> 2) + 4 * hp + (qi & 3)) * 16 + (kq & 1) * 8);
  unsigned plo = !(kq & 1) ? (unsigned)nearoff(NEAR_PM + wz * 1024 + ((2 * hp + (tq >> 1)) * 16 + 4 * (qi >> 1) + (kq & 2) + (qi & 1)) * 16 + (tq & 1) * 8 + (kq >> 2) * 4)
                           : (kq == 1 ? (unsigned)nearoff(NEAR_AL + wz * 256 + ((tq >> 1) * 8 + qi) * 16 + (2 * hp + (tq & 1)) * 4) : OOB);
  {
    u32x4 sg[QDEP][8];
#pragma unroll
    for (int c = 0; c < QDEP; ++c) qk_gload<NTM & 1>(sg[c], a, h, c, tid);
#define WK3(b) do { WALK(b + 2); WALK(b + 1); WALK(b); } while (0)
    if (wave == 0) WK3(200); else if (wave == 1) WK3(208); else if (wave == 2) WK3(216); else WK3(224);
    if (wave == 0) pq_walk<24>(); else if (wave == 1) pq_walk<25>();
    const int q = l15 >> 2;
    const int lrow = 16 * (g >> 1) + 4 * (g & 1) + 8 * (q >> 1) + (q & 1);
    auto wrd = [&](lds_char* p) {
      u32x2 v0 = trd(p), v1 = trd(p + 2 * 544), v2 = trd(p + 32 * 544), v3 = trd(p + 34 * 544);
      return (u32x8){v0[0], v0[1], v1[0], v1[1], v2[0], v2[1], v3[0], v3[1]};
    };
#pragma unroll
    for (int ph = 0; ph < 3; ++ph) {
      if (ph == 2) {
        cls = get_cls(R, lane, cls);
        pxo += (unsigned)(cls & 1) * 8192u; plo += (unsigned)(cls & 1) * 8192u;
        CBAR();
        kv0 = (int)ld32(R, FL_PROG + h * 128 + (lane & 3) * 4, 0);
#pragma unroll
        for (int f = 0; f < 3; ++f) {
          const int u = (f == 2 ? 8 : f * 4 + wave) * 64 + lane;
          af0[f] = *(const u32x4*)(a.mem0 + (u & 7) * DM + (u >> 3) * 8);
        }
        CBAR();
      }
#pragma unroll
      for (int i = 0; i < 4; ++i) {
        const int c = ph * 4 + i;
        if (c < 9) {
          qk_swrite(sg[c % QDEP], lds + i * QSTG, tid);
          if (c + QDEP < 9) qk_gload<NTM & 1>(sg[c % QDEP], a, h, c + QDEP, tid);
          CBAR();
        }
      }
      RT(8 + ph);
      if (ph == 0) { g1 = ld32(R, go.vo, 0); CBAR(); }
      if (ph == 1) {
        if (__all(g1 != go.v0)) go_ack(R, go, lane); else go_wait(R, go, lane);
        cls = cls_look(R, lane);
        CBAR();
      }
      if (ph == 2) {
        if (wave < 3) {
#pragma unroll
          for (int k = 0; k < 4; ++k) {
            const unsigned so = wave * SLOT_REAL + (k & 1) * CPY1 + (k >> 1) * CPY2;
            st64((u32x2){~0u, ~0u}, R, pxo, so); st32(~0u, R, plo, so);
          }
        }
        CBAR();
        RT(11);
      }
      BAR();
#pragma unroll
      for (int i = 0; i < 4; ++i) {
        const int j = ph * 4 + i;
        if (j < 9) {
          lds_char* buf = lds + (ph < 2 ? ((i + wave) & 3) : 0) * QSTG + lrow * 544 + (l15 & 3) * 8;
#pragma unroll
          for (int t = 0; t < 4; ++t) {
            u32x8& w = t < 2 ? wq[t & 1][j] : wk[t & 1][j];
            w = wrd(buf + t * 128 + wave * 32);
          }
          if (j == 0 || j == 4) wp[j >> 2] = wrd(buf + 256 + 96);
        }
      }
      SB();
#pragma unroll
      for (int k = 0; k < 16; ++k) {
        const int j = ph * 4 + (k >> 2);
        if (j < 9) { u32x8& w = (k & 2) ? wk[k & 1][j] : wq[k & 1][j]; if (j < 8) pin_a(w); else pin_v(w); }
      }
      if (ph < 2) pin_v(wp[ph]);
      if (ph == 0 || wave == ph) wp[2] = wrd(lds + (ph < 2 ? 3 : 0) * QSTG + lrow * 544 + (l15 & 3) * 8 + 256 + 96);
      pin_v(wp[2]);
      BAR();
    }
  }
  RT(1);
  if (cls < 0) return;
  RT(2);
  auto run = [&](auto ROLEC) {
  constexpr int ROLE = decltype(ROLEC)::value;
  lds_char* img = lds;
  const unsigned fo = (unsigned)(cls & 1) * 8192u;
  const int r0 = ROLE * 1024 + lane * 16, r1 = r0 + 4096, r2 = 8192 + lane * 16;
  const unsigned m0 = nearoff(r0) + fo, m1 = nearoff(r1) + fo, m2 = nearoff(r2) + fo;
  const unsigned lb = (unsigned)(uintptr_t)lds, lw0 = lb + ROLE * 1024;
  const i32x4 D = mkdesc(a.wb);
  const int ar = g * 256 + ((l15 >> 1) & 1) * 128 + (2 * (l15 >> 2) + (l15 & 1)) * 16;
  lds_char* ard0 = img + ROLE * 1024 + ar;
  lds_char* ard1 = img + ((ROLE + 1) & 3) * 1024 + ar;
  lds_char* ard2 = img + ((ROLE + 2) & 3) * 1024 + ar;
  lds_char* ard3 = img + ((ROLE + 3) & 3) * 1024 + ar;
  const int gy = ((tq & 1) << 1) | (tq >> 1);
  const unsigned pcl = lane < 32 ? (unsigned)nearoff(NEAR_E + (h >> 1) * 1024 + ((lane >> 2) * 8 + 4 * hp + (lane & 3)) * 16) + fo
                     : (lane < 40 ? (unsigned)nearoff(NEAR_PM + wz * 1024 + ((2 * hp + (tq >> 1)) * 16 + (tq & 1) * 8 + (lane & 7)) * 16) + fo
                                  : (lane < 42 ? (unsigned)nearoff(NEAR_AL + wz * 256 + ((tq >> 1) * 8 + (2 * hp + (tq & 1)) * 2 + (lane & 1)) * 16) + fo : OOB));
  const unsigned kfo = OFF_KF + (unsigned)h * 8192u + lane * 16;
  constexpr int QT = ROLE >> 1;
  const unsigned kmo = OFF_KMF + (unsigned)h * 4096u + lane * 16;
  const unsigned pxs = OFF_PXF + (unsigned)((QT * 40 + h) * 1024 + lane * 16);
  const unsigned pws = OFF_PXF + (unsigned)((QT * 40 + 32 + wz * 2 + hp) * 1024 + (gy * 16 + l15) * 16 + g * 4);
  const unsigned kpf = FL_PROG + h * 128 + (lane & 3) * 4;
  const unsigned kmf = (lane == 0) ? (unsigned)(FL_KMPROG + h * 128) : OOB;
  const unsigned wpo = lane == 0 ? (unsigned)(FL_WP + h * 32) : OOB;
  const int idx = (lane & 2) ? 0xEEEE : 0x4444;
  const int jr = l15 >> 2;
  const unsigned qw = lb + QIM_OFF + (ROLE * 8 + ((2 * jr + (l15 & 1)) ^ ((ROLE & 1) << 1))) * 32 + g * 8 + (l15 & 2) * 2;
  const unsigned kw = lb + KIM_OFF + ROLE * 256 + l15 * 16 + g * 4;
  lds_char* krd = lds + KIM_OFF + (4 * g + jr) * 16 + (l15 & 2) * 4;
  unsigned fw = lb + FLG_OFF + ROLE * 4;
  pin_v(fw);
  lds_char* frd = lds + FLG_OFF + (lane & 3) * 4;
  const unsigned qa = lb + QIM_OFF + (16 * (g >> 1) + 4 * (g & 1) + (jr & 1) + 10 * (jr >> 1)) * 32 + (l15 & 3) * 8;
  u32x16 cst = {kw, lb + KIM_OFF + (4 * g + jr) * 16 + (l15 & 1) * 8, qa, qa ^ 64, lb + QIM_OFF + ((24 + 2 * g + (jr & 1)) ^ 2) * 32 + (l15 & 3) * 8,
                lb + FLG_OFF + min(lane & 3, 2) * 4, pxo, plo, (l15 & 1) ? 0u : 0x3f800000u, l15 == 1 ? 0x3f800000u : 0u, l15 == 9 ? 0x3f800000u : 0u, fw, qw,
                lb + (l15 >> 3) * 4096 + (g * 16 + 4 * (l15 & 3) + 2 * (jr & 1)) * 16, 0u, 0u};
  const unsigned own = own_cpy(cls, h >> 3);
  unsigned ima = lb + lane * 16;
  pin_v(ima);
  ds_flag(fw, 0u);
  ds_flag(fw + 16, 0u);
  auto poison = [&]() {
#pragma unroll
    for (int f = 0; f < 5; ++f) *(lds_u32x4*)(lds + QIM_OFF + f * 1024 + lane * 16) = (u32x4){~0u, ~0u, ~0u, ~0u};
    asm volatile("s_waitcnt lgkmcnt(0)" ::: "memory");
  };
  if (ROLE == 0) poison();

  int fl = kv0;
  auto k_dma = [&](int sg, int half) {
#pragma unroll
    for (int f = 0; f < 4; ++f)
      asm volatile("s_mov_b32 m0, %2\n\ts_nop 0\n\tbuffer_load_dwordx4 %0, %1, %3 offen offset:%4 sc1 lds"
                   :: "v"(kfo), "s"(D), "s"(lb + KX_OFF + (sg & 1) * 8192 + half * 4096), "s"((unsigned)(sg & RMASK) * 262144u + half * 4096), "n"(f * 1024) : "memory");
  };
  u32x4 qq[4], F[4];
#pragma unroll
  for (int f = 0; f < 4; ++f) { qq[f] = af0[0]; F[f] = af0[0]; }
  f32x4 T0 = {0.f, 0.f, 0.f, 0.f}, T1 = T0;
  float tmx = 0.f, tsx = 0.f;
  auto q_ld = [&](u32x4 (&qx)[4], int sg) {
#pragma unroll
    for (int f = 0; f < 4; ++f)
      asm volatile("buffer_load_dwordx4 %0, %1, %2, %3 offen offset:%4 sc1"
                   : "+v"(qx[f]) : "v"(kfo), "s"(D), "s"((unsigned)sg * 262144u + (OFF_QF - OFF_KF) + QT * 4096), "n"(f * 1024) : "memory");
  };
  auto t_part = [&](u32x4 (&qx)[4], int par, auto NC) {
    asm volatile("s_waitcnt vmcnt(%4)" : "+v"(qx[0]), "+v"(qx[1]), "+v"(qx[2]), "+v"(qx[3]) : "n"(decltype(NC)::value) : "memory");
    T0 = (f32x4){0.f, 0.f, 0.f, 0.f}; T1 = T0;
#pragma unroll
    for (int ks = 0; ks < 4; ++ks) {
      T0 = mfma(*(lds_u32x4*)(lds + KX_OFF + par * 8192 + ks * 1024 + lane * 16), qx[ks], T0);
      if (QT) T1 = mfma(*(lds_u32x4*)(lds + KX_OFF + par * 8192 + 4096 + ks * 1024 + lane * 16), qx[ks], T1);
    }
    pin_v(T0); pin_v(T1); PAD();
    px_mask(T0, T1, QT, lane);
    pin_v(T0); pin_v(T1); PAD();
    px_seg(T0, T1, tmx, tsx);
    pin_v(T0); pin_v(T1); pin_v(tmx); pin_v(tsx); PAD();
  };
  if (ROLE == 3) {
    const bool ok = wait_flag_c<8>(R, kpf, 0, fl);
    k_dma(0, 0); k_dma(0, 1);
    if (!ok) return;
  }
  VMW(0);

  unsigned iso_acc = 0, bud = SPINCAP, tm = 0, rcnt = 0;
  unsigned cv = 0;
  const unsigned cwo = FL_AN + (h & 15) * 128;
  EVDECL; CKDECL;
  auto k_get = [&](u32x4 (&F)[4], int s, bool parts) {
#pragma nounroll
    for (; bud != 0; --bud) {
      CBAR();
      if (__all((int)*(lds_u32*)frd > s)) break;
      __builtin_amdgcn_s_sleep(1);
    }
    CBAR();
#pragma unroll
    for (int f = 0; f < 4; ++f) { u32x2 lo = trd(krd + f * 512), hi = trd(krd + f * 512 + 256); F[f] = (u32x4){lo[0], lo[1], hi[0], hi[1]}; }
    if (!parts) return;
    const u32x2 p0 = trd(krd + 2048), p1 = trd(krd + 2304);
    F[1][2] = add3bf(F[1][2], p0[0], p1[0]); F[1][3] = add3bf(F[1][3], p0[1], p1[1]);
  };
  auto step = [&](auto PHC, int s) {
    constexpr int PH = decltype(PHC)::value & 1, FIRST = (decltype(PHC)::value >> 1) & 1, LAST = decltype(PHC)::value >> 2;
    constexpr int JC = ROLE == 0 ? 3 : (ROLE == 1 ? 6 : 8);
    u64 e0 = 0, e1 = 0, e3 = 0, e4 = 0; unsigned nr0 = 0;
    u32x4 af[9];
    f32x4 q0, q1, k0, k1, k2;
    PADS(200 + ROLE * 8 + decltype(PHC)::value);
    unsigned f3 = s + 1;
    pin_v(f3);
    constexpr int RD = ROLE == 3 && !LAST;
    f32x16 T;
    u32x16 x0, x1;
    if (RD) t_zero(k0, T); else if (!LAST) k_zero(k0, k1, k2);
    const u32x8 (&wf)[2][9] = LAST ? wk : wq;
#define MQ(t, j) do { smo<t, (j) < 8>(q##t, af[j], wf[t][j], idx); SB(); } while (0)
    lds_char* f8 = lds + F8_OFF + lane * 16;
    bud = __builtin_amdgcn_readfirstlane(bud);
    asm volatile("" : "+s"(bud));
    unsigned nt = 0; int rq = 0;
    tm = __builtin_amdgcn_readfirstlane(tm); rcnt = __builtin_amdgcn_readfirstlane(rcnt);
    asm volatile("" : "+s"(tm), "+s"(rcnt));
    if (RD) asm volatile("" : "+{v[112:127]}"(cst));
    unsigned p0 = (s & 7) * SLOT_REAL, p1 = p0 + CPY1, p2 = p0 ^ CPY2, p3 = p2 + CPY1;
    unsigned ms = (p2 + own) ^ ((s & 4) ? 0 : CPY1);
    u32x8 sc = {p0, p1, p2, p3, 0x3e0293eeu, YMAX, 0u, 0u};
    if (RD) asm volatile("" : "+{s[40:47]}"(sc));
    constexpr int NX = ROLE == 3 || LAST ? 0 : (ROLE == 0 ? 8 : 4);
    constexpr int ID = FIRST ? -1 : ROLE * 8 + decltype(PHC)::value;
    auto head = [&]() {
    v_zero(q0, q1);
    if (!FIRST) {
      int r = 0;
      if (ROLE == 1) r = spin_late(tm, 0);
      rq = r;
      mem_issue2<ROLE == F8W ? 3 : 2>(D, m0, m1, m2, ms, lw0, lw0 + 4096, lb + F8_OFF, e3);
      if (ROLE == 1) { move_on(tm, r); SB(); }
      PAD();
      if (RD) { kx_get<PH>(cst, x0, x1); asm volatile("s_waitcnt lgkmcnt(0)" ::: "memory"); }
      if (ROLE == 3) asm volatile("s_waitcnt vmcnt(%0)" :: "n"(TWO ? 1 + (F8W == 3) : 0) : "memory");
      else if (nt != 0) VMW(0);
      else {
      __builtin_amdgcn_s_sleep(XSLP);
      if (!LAST && (ROLE == 1 || ROLE == 2)) k_dma(s + 1, ROLE - 1);
      if (ROLE == 0 && !LAST) {
#pragma unroll
        for (int ks = 0; ks < 4; ++ks) st128(F[ks], R, kmo + ks * 1024, (unsigned)(s - 1) * 131072u);
      }
      if (ROLE == 0 && !LAST) {
        st128((u32x4){~0u, ~0u, ~0u, ~0u}, R, pcl, (unsigned)((s + 2) & 3) * SLOT_REAL);
        st128((u32x4){~0u, ~0u, ~0u, ~0u}, R, pcl, (unsigned)((s + 2) & 3) * SLOT_REAL + CPY1);
        st128((u32x4){~0u, ~0u, ~0u, ~0u}, R, pcl, (unsigned)((s + 2) & 3) * SLOT_REAL + CPY2);
        st128((u32x4){~0u, ~0u, ~0u, ~0u}, R, pcl, (unsigned)((s + 2) & 3) * SLOT_REAL + CPY2 + CPY1);
      }
      PAD();
      CBAR();
      asm volatile("s_waitcnt vmcnt(%0)" :: "n"(NX + (TWO ? 1 + (ROLE == F8W) : 0)) : "memory");
      }
    } else {
      *(lds_u32x4*)(img + r0) = af0[0]; *(lds_u32x4*)(img + r1) = af0[1]; *(lds_u32x4*)f8 = af0[2];
      asm volatile("s_waitcnt lgkmcnt(0)" ::: "memory");
    }
    SB();
    HEAD(MQ, NX, 0);
    if (s == 0) RT(3);
    if (s == 1) RT(4);
    if (s == 8) RT(5);
    if (s == 60) RT(6);
    ISOQ(1);
    q_mid<ID>(af, wf[1], idx, LAST ? kw : qw, q0, q1); SB();
    };
    if (!FIRST) {
      tm -= GRID + XT; nt = ~0u;
      try_top<ID>(nt, ms, tm, rcnt, ima); SB();
      head();
      try_end(ima, nt, ms, tm, rcnt, m0, m1, m2, lw0, lw0 + 4096, lb + F8_OFF, D); SB();
    } else
    {
      if (FIRST && RD) { kx_get<PH>(cst, x0, x1); asm volatile("s_waitcnt lgkmcnt(0)" ::: "memory"); }
      head();
    }
    ISOQ(2);
    EV(e0);
    if (LAST) k_last(kw, q1, fw, f3);
    if (ROLE < 3) {
      if (!LAST) {
        float t;
        k_one(af, wk[0], wp, idx, qw, q1, k0, k2, t); SB();
        ISOQ(4);
        if (ROLE == 1 && FIRST) { st32(f3, R, wpo, 0); SB(); }
        k_end<ROLE == 2 ? 256 : 2048, JC>(af, wk[1], wp[2], idx, k0, k1, k2, t, kw, fw, f3); SB();
      }
      ISOQ(6);
      PAD();
      if (ROLE == 1 && !LAST) {
        nr0 = nt != 0;
        if (!FIRST) next_time(tm, s);
      }
      if (ROLE == 0 && !LAST) {
        __builtin_amdgcn_s_sleep(TSLP);
        k_get(F, s, true);
        ds_flag(lb + FLG_OFF + 20, f3);
        pin_v(F[0]); pin_v(F[1]); pin_v(F[2]); pin_v(F[3]);
        VMW(0);
        st32((unsigned)(s - 1), R, kmf, 0);
        if (!PH && !FIRST && UNLIKELY(s == NSEG - 2)) {
#pragma unroll
          for (int ks = 0; ks < 4; ++ks) st128(F[ks], R, kmo + ks * 1024, (unsigned)s * 131072u);
          VMW(0);
          st32((unsigned)s, R, kmf, 0);
        }
      }
      if (ROLE == 2 && !LAST) {
        if (FIRST) { k_dma(1, 0); k_dma(1, 1); }
        gate_wait(R, kpf, min(2 * s + 4 + (s >= NSEG - 4), 2 * NSEG - 1), fl, bud);
        fl = (int)ld32(R, kpf, 0);
        VMW(0);
        pin_v(fl);
      }
      if ((ROLE == 0 || ROLE == 2) && !PH && !FIRST && UNLIKELY(s == NSEG - 2)) {
        k_dma(s + 1, 0);
        q_ld(qq, s + 1);
        t_part(qq, 1, std::integral_constant<int, 0>());
      }
      if ((ROLE == 0 || ROLE == 2) && LAST) {
        f32x4 G = {0.f, 0.f, 0.f, 0.f};
        u32x4 px; unsigned pw;
        k_get(F, s, false);
#pragma unroll
        for (int ks = 0; ks < 4; ++ks) G = mfma(F[ks], qq[ks], G);
        px_fin(G, T0, T1, tmx, tsx, px, pw, lane);
        st128(px, R, pxs, (unsigned)s * 81920u); st32(pw, R, pws, (unsigned)s * 81920u);
        VMW(0);
        st32((unsigned)s, R, lane == 0 ? (unsigned)(FL_PXLAST + h * 256 + QT * 128) : OOB, 0);
      }
      CBAR();
      if (ROLE == 0) CKS();
    } else if (!LAST) {
      u32x8 Q;
      s_first(af, wk[1], idx, cst, x0, x1, q1, k0, T, Q); SB();
      el_phase<ID>(D, af, wk[1], idx, cst, x0, x1, Q, k0, T, bud, e1, e4, nr0, sc, f3);
      ds_flag(fw + 4, f3);
      EVPUT(2, e1); EVPUT(4, e4);
    }
    EVPUT(0, e0);
    EVPUTI(1, nr0); if (!FIRST) EVPUT(3, e3);
    if (ROLE == 1 && !LAST) {
#pragma nounroll
      for (; bud != 0; --bud) {
        CBAR();
        if (__all((int)*(lds_u32*)(lds + FLG_OFF + 16 + (lane & 1) * 4) > s)) break;
        __builtin_amdgcn_s_sleep(1);
      }
      poison();
      VMW(0);
      if (FIRST) { tm = get_t1(R, cwo, bud); asm volatile("" : "+s"(tm)); }
    }
    if (PH && !LAST && UNLIKELY(s == WLAST)) WALK(200 + ROLE * 8 + 5);
    PADE();
#undef MQ
  };
  step(std::integral_constant<int, 2>(), 0);
#pragma nounroll
  for (int s = 1; s < NSEG - 1; s += 2) {
    step(std::integral_constant<int, 1>(), s);
    step(std::integral_constant<int, 0>(), s + 1);
  }
  step(std::integral_constant<int, 5>(), NSEG - 1);
  EVDUMP();
  if (ROLE == 0) CKDUMP();
  };
  if (hw == 0) run(std::integral_constant<int, 3>());
  else if (hw == 1) run(std::integral_constant<int, 2>());
  else if (hw == 2) run(std::integral_constant<int, 1>());
  else run(std::integral_constant<int, 0>());
  RT(7);
  SV(15, cls);
}

#define VIM_OFF 8192
#define NOP(n) do { asm volatile("s_nop " #n); SB(); } while (0)
#define cVW "v112"
#define cRA "v113"
#define cRB "v114"
#define cHA "v115"
#define cHB "v116"
#define cVF "v117"
#define cVR "v118"
#define cIY "v119"
#define YP(p, c) "v_smfmac_f32_16x16x64_bf16 v[148+4*" #p ":151+4*" #p "], v[168+4*" #c "+4*(" #c ">>1):171+4*" #c "+4*(" #c ">>1)], %[w" #p #c "], " cIY "\n\t"
#define Y8(k) "v_smfmac_f32_16x16x64_bf16 v[164:167], v[176+12*" #k ":179+12*" #k "], %[w8" #k "], " cIY "\n\t"
#define HA(d, b) "v_add_f32 v" #d ", v[" b "], v[" b "+2]\n\t"
#define HB(d, b) "v_add_f32 v" #d ", v[" b "+1], v[" b "+3]\n\t"
#define YC(p, e) "v_cvt_pk_bf16_f32 v[192+2*" #p "+" #e "], v[148+4*" #p "+2*" #e "], v[149+4*" #p "+2*" #e "]\n\t"
#define YS(p) ".if %[st]\n\tbuffer_store_dwordx2 v[192+2*" #p ":193+2*" #p "], v[120+" #p "], %[D], %[ys] offen sc1\n\t.endif\n\t"
#define S16(a, b) "v_permlane16_swap_b32 v" #a ", v" #b "\n\t"
#define Z2(r) "v_mov_b64 v[" #r ":" #r "+1], 0\n\tv_mov_b64 v[" #r "+2:" #r "+3], 0\n\t"
#define YZERO Z2(148) Z2(152) Z2(156) Z2(160) Z2(164)
#define VR0 TRD(168, cRA, 0) TRD(170, cRB, 0)
#define VR1 TRD(172, cRA, 512) TRD(174, cRB, 512) TRD(176, cHA, 0) TRD(178, cHB, 0)
__device__ __forceinline__ void y_zero(f32x16& ya, f32x4& yc) {
  asm volatile(YZERO : "={v[148:163]}"(ya), "={v[164:167]}"(yc));
}
#define HV 0
#define VO 1
#define VR 1
#define YEO
#define YEN
template <int ID>
__device__ __forceinline__ void v_end(const u32x4 (&af)[9], const u32x8 (&w)[9], int ix, u32x16 cst, f32x4 v0, f32x4& v1, u32x16& A,
                                      const u32x8 (&wo)[4][4], f32x16& ya, u64& e1) {
#define VH0 HA(201, "140") HB(202, "140") CVX(203, 201, 202)
#define VH1 HA(201, "144") HB(202, "144") CVX(203, 201, 202)
#define VW0 "ds_write_b32 " cVW ", v203\n\t"
#define VW1 "ds_write_b32 " cVW ", v203 offset:1024\n\t"
#define VCM "v_cmp_u_f32 vcc, v201, v202\n\t"
#define TRYL TRYB
  asm volatile(
               Q1(3) Q1(4) VH0 Q1(5) VW0 Q1(6) "s_barrier\n\t" VR0 Q1(7) TRD(172, cRA, 512) TRD(174, cRB, 512) Q1(8) TRD(176, cHA, 0) TRD(178, cHB, 0) VCM
               LGK(4) YP(0, 0) IS(16) VH1 YP(1, 0) VW1 TRYL
               : "+{v[144:147]}"(v1), "=&{v[168:183]}"(A), "+{v[148:163]}"(ya) YEO
               : "{v[140:143]}"(v0), [a2] "v"(af[2]), [a3] "v"(af[3]), [a4] "v"(af[4]), [a5] "v"(af[5]), [a6] "v"(af[6]), [a7] "v"(af[7]), [a8] "v"(af[8]),
                 [w2] "a"(w[2]), [w3] "a"(w[3]), [w4] "a"(w[4]), [w5] "a"(w[5]), [w6] "a"(w[6]), [w7] "a"(w[7]), [w8] "v"(w[8]), [ix] "v"(ix), "{v[112:127]}"(cst), [id] "n"(ID),
                 [w00] "a"(wo[0][0]), [w10] "a"(wo[1][0])
               : "memory", "v201", "v202", "v203", TRYC);
}
template <int ST, int WID>
__device__ __forceinline__ void y_all(const u32x8 (&wo)[4][4], const u32x8 (&w8)[2], u32x16 cst, f32x16 ya, f32x4 yc, u32x16 A, i32x4 D,
                                      unsigned ys, unsigned fv, u32x8& o, unsigned& o8, u64& e1, unsigned& nr) {
#define RH1 TRD(188, cHA, 1024) TRD(190, cHB, 1024)
#define RQ2 TRD(180, cRA, 1024) TRD(182, cRB, 1024)
#define RQ3 TRD(184, cRA, 1536) TRD(186, cRB, 1536)
#define VFL "ds_write_b32 " cVF ", %[fv]\n\t"
#define YK0 YP(2, 0) YP(3, 0) RH1 LGK(3) YP(0, 1) RQ2 Y8(0) RQ3 YP(1, 1) VFL IS(17)
#define YK1    LGK(5) Y8(1) IS(18) YP(2, 1) YP(3, 1) S16(164, 166) LGK(3) YP(0, 2) PQ S16(165, 167) LGK(1) YP(0, 3) HA(201, "164") YP(1, 2) HB(202, "164") CVX(200, 201, 202) PQ \
               YP(1, 3) ".if %[st]\n\tbuffer_store_dword v200, v124, %[D], %[ys] offen sc1\n\t.endif\n\t" YC(0, 0) YP(2, 2) YC(0, 1) YP(2, 3) PQ YS(0) YC(1, 0)\
               YP(3, 2) YC(1, 1) YP(3, 3) IS(19) YS(1) PQ YC(2, 0) YC(2, 1) YS(2) "s_nop 5-2*%[st]\n\t" YC(3, 0) YC(3, 1) YS(3) IS(20) EVA(1)
  asm volatile(PQ0 YK0 YK1
               "v_cmp_u_f32 vcc, v148, v150\n\tv_cmp_u_f32 s[46:47], v201, v202\n\ts_or_b64 vcc, vcc, s[46:47]\n\ts_cbranch_scc1 19f\n"
               "13:\n\t.subsection 1\n"
               PQA "19:\n\tds_read_b32 v204, " cVR "\n\t" LGK(0) "v_cmp_gt_i32 vcc, %[fv], v204\n\ts_cbranch_vccnz 19b\n\t"
               CN(100) VR0 PQ VR1 RH1 PQ RQ2 RQ3 Z2(148) PQ Z2(152) Z2(156) Z2(160) PQ Z2(164) LGK(0) YP(0, 0) YP(1, 0) YP(2, 0) YP(3, 0) PQ YP(0, 1) Y8(0) YP(1, 1) PQ YK1
               "s_branch 13b\n\t" PQB ".subsection 0"
               : "=&{v[192:199]}"(o), "=&{v200}"(o8) YEO YEN
               : [w00] "a"(wo[0][0]), [w01] "a"(wo[0][1]), [w02] "a"(wo[0][2]), [w03] "a"(wo[0][3]), [w10] "a"(wo[1][0]), [w11] "a"(wo[1][1]),
                 [w12] "a"(wo[1][2]), [w13] "a"(wo[1][3]), [w20] "a"(wo[2][0]), [w21] "a"(wo[2][1]), [w22] "a"(wo[2][2]), [w23] "a"(wo[2][3]),
                 [w30] "a"(wo[3][0]), [w31] "a"(wo[3][1]), [w32] "a"(wo[3][2]), [w33] "a"(wo[3][3]), [w80] "v"(w8[0]), [w81] "v"(w8[1]),
                 "{v[112:127]}"(cst), "{v[148:163]}"(ya), "{v[164:167]}"(yc), "{v[168:183]}"(A),
                 [D] "s"(D), [ys] "s"(ys), [fv] "v"(fv), [st] "n"(ST), [wid] "n"(WID)
               : "memory", "vcc", "scc", "s46", "s47", "v184", "v185", "v186", "v187", "v188", "v189", "v190", "v191", "v201", "v202", "v204");
}
__device__ __forceinline__ void role_vcu(const Args& a, rsrc_t R, const Go& go, lds_char* lds, int h, int tid, int wave, int lane) {
  const int l15 = lane & 15, g = lane >> 4;
  u32x8 wv[2][9], wo[4][4], w8[2];
  int cv0 = -1;
  unsigned g1;
  u32x4 af0[3];
  {
    u32x4 st[36], so[36];
#pragma unroll
    for (int k = 0; k < 36; ++k) so[k] = gld<NTM & 2>(a.Wo + (((size_t)h * 128 * DM + (tid + 256 * k) * 8) & ((NOCW & 2) ? 2047 : ~0ull)));
    if (wave == 0) WK3(232); else if (wave == 1) WK3(240); else if (wave == 2) WK3(248); else WK3(256);
    if (wave == 0) { pq_walk<32>(); pq_walk<33>(); } else if (wave == 1) { pq_walk<40>(); pq_walk<41>(); }
    else if (wave == 2) { pq_walk<48>(); pq_walk<49>(); } else { pq_walk<56>(); pq_walk<57>(); }
    const int q = l15 >> 2;
    const int lrow = 16 * (g >> 1) + 4 * (g & 1) + 8 * (q >> 1) + (q & 1), lrow2 = 8 * (g >> 1) + 4 * (g & 1) + (q & 1), up2 = (g & 1) ? 12 : 20;
    auto wrd = [&](lds_char* p, int rs, int up) {
      u32x2 v0 = trd(p), v1 = trd(p + 2 * rs), v2 = trd(p + up * rs), v3 = trd(p + (up + 2) * rs);
      return (u32x8){v0[0], v0[1], v1[0], v1[1], v2[0], v2[1], v3[0], v3[1]};
    };
#pragma unroll
    for (int ph = 0; ph < 4; ++ph) {
      if (ph < 2) {
#pragma unroll
        for (int k = 0; k < 18; ++k) { const int u = tid + 256 * k; *(lds_u32x4*)(lds + (u / 72) * 1184 + (u % 72) * 16) = so[ph * 18 + k]; }
#pragma unroll
        for (int k = ph * 18; k < ph * 18 + 18; ++k) { const int u = tid + 256 * k; st[k] = gld<NTM & 2>(a.Wv + (((size_t)(u >> 4) * HD + h * 128 + (u & 15) * 8) & ((NOCW & 2) ? 2047 : ~0ull))); }
      } else {
#pragma unroll
        for (int k = (ph - 2) * 16; k < (ph == 2 ? 16 : 36); ++k) { const int u = tid + 256 * k; *(lds_u32x4*)(lds + ((u >> 4) - (ph - 2) * 256) * 288 + (u & 15) * 16) = st[k]; }
      }
      if (ph == 0) { CBAR(); g1 = ld32(R, go.vo, 0); CBAR(); }
      if (ph == 2) {
        if (__all(g1 != go.v0)) go_ack(R, go, lane); else go_wait(R, go, lane);
        cv0 = cls_look(R, lane);
#pragma unroll
        for (int f = 0; f < 3; ++f) {
          const int u = (f == 2 ? 8 : f * 4 + wave) * 64 + lane;
          af0[f] = *(const u32x4*)(a.mem0 + (u & 7) * DM + (u >> 3) * 8);
        }
        CBAR();
      }
      BAR();
      if (ph < 2) {
#pragma unroll
        for (int k = 0; k < 8; ++k) wo[k >> 1][2 * ph + (k & 1)] = wrd(lds + (lrow2 + 32 * (k & 1)) * 1184 + (wave * 9 + (k & 6) + (q >> 1)) * 32 + (l15 & 3) * 8, 1184, up2);
        w8[ph] = wrd(lds + (lrow2 + 32 * (q >> 1)) * 1184 + (wave * 9 + 8) * 32 + (l15 & 3) * 8, 1184, up2);
        SB();
#pragma unroll
        for (int k = 0; k < 8; ++k) pin_a(wo[k >> 1][2 * ph + (k & 1)]);
        pin_v(w8[ph]);
      } else {
#pragma unroll
        for (int k = 0; k < (ph == 2 ? 8 : 10); ++k) {
          const int i = k >> 1, t = k & 1;
          wv[t][(ph - 2) * 4 + i] = wrd(lds + ((i < 4 ? (i + wave) & 3 : 4) * 64 + lrow) * 288 + (64 * t + 16 * wave) * 2 + (l15 & 3) * 8, 288, 32);
        }
        SB();
#pragma unroll
        for (int k = 0; k < (ph == 2 ? 8 : 10); ++k) { if (k < 8) pin_a(wv[k & 1][(ph - 2) * 4 + (k >> 1)]); else pin_v(wv[k & 1][8]); }
      }
      BAR();
    }
  }
  RT(1);
  const int cls = get_cls(R, lane, cv0);
  if (cls < 0) return;
  RT(2);
  auto run = [&](auto ROLEC) {
  constexpr int ROLE = decltype(ROLEC)::value;
  lds_char* img = lds;
  const unsigned fo = (unsigned)(cls & 1) * 8192u;
  const int r0 = ROLE * 1024 + lane * 16, r1 = r0 + 4096, r2 = 8192 + lane * 16;
  const unsigned m0 = nearoff(r0) + fo, m1 = nearoff(r1) + fo, m2 = nearoff(r2) + fo;
  const unsigned lb = (unsigned)(uintptr_t)lds, lw0 = lb + ROLE * 1024;
  const i32x4 D = mkdesc(a.wb);
  const int ar = g * 256 + ((l15 >> 1) & 1) * 128 + (2 * (l15 >> 2) + (l15 & 1)) * 16;
  lds_char* ard0 = img + ROLE * 1024 + ar;
  lds_char* ard1 = img + ((ROLE + 1) & 3) * 1024 + ar;
  lds_char* ard2 = img + ((ROLE + 2) & 3) * 1024 + ar;
  lds_char* ard3 = img + ((ROLE + 3) & 3) * 1024 + ar;
  const int wz = h >> 3, tq = (h >> 1) & 3, hp = h & 1;
  const int gh = ((tq & 1) << 1) | (tq >> 1);
  unsigned yr[5], yz[5];
#pragma unroll
  for (int q = 0; q < 5; ++q) {
    const int m = ROLE * 9 + q * 2 + (q < 4 ? g & 1 : 0), o = l15 * 16 + (q < 4 ? (g >> 1) * 8 : g * 4);
    yr[q] = ywin(cls, m) + (unsigned)((wz * 2 + hp) * 1024 + tq * 256 + o);
    yz[q] = NOYC ? OOB : OFF_ZF + (unsigned)(m * 40960 + (32 + wz * 2 + hp) * 1024 + gh * 256 + o);
  }
  lds_char* vw = lds + VIM_OFF + (ROLE * 16 + l15) * 16 + g * 4;
  const unsigned ra = lb + VIM_OFF + (8 * g + 4 * (g >> 1) + (l15 >> 2)) * 16 + (l15 & 2) * 4, rh = ra + (l15 & 1) * 512;
  u32x16 cst = {(unsigned)(uintptr_t)vw, ra, ra ^ 64, rh, rh ^ 64, lb + FLG_OFF + ROLE * 4, lb + FLG_OFF + (lane & 3) * 4, (lane & 4) ? 0xEEEEu : 0x4444u,
                yr[0], yr[1], yr[2], yr[3], yr[4], 0u, 0u, 0u};
  ds_flag(lb + FLG_OFF + ROLE * 4, 0u);
  const unsigned own = own_cpy(cls, h >> 3);
  unsigned ima = lb + lane * 16;
  pin_v(ima);
  const int idx = (lane & 2) ? 0xEEEE : 0x4444;
  unsigned tm = 0, rcnt = 0, cv = 0, iso_acc = 0, o8 = 0, bud = SPINCAP;
  u32x8 o = {0u, 0u, 0u, 0u, 0u, 0u, 0u, 0u};
  auto y_copy = [&](int s) {
#pragma unroll
    for (int q = 0; q < 4; ++q) st64((u32x2){o[2 * q], o[2 * q + 1]}, R, yz[q], (unsigned)s * ZS);
    st32(o8, R, yz[4], (unsigned)s * ZS);
  };
  auto poison = [&]() { asm volatile("v_mov_b32 v203, -1\n\tds_write2st64_b32 " cVW ", v203, v203 offset1:4" :: "{v[112:127]}"(cst) : "memory", "v203"); };
  const unsigned cwo = FL_AN + ((h + 8) & 15) * 128;
  EVDECL;
  auto step = [&](auto PHC, int s) {
    constexpr int PH = decltype(PHC)::value & 1, FIRST = (decltype(PHC)::value >> 1) & 1, LAST = decltype(PHC)::value >> 2;
    u64 e0 = 0, e1 = 0, e3 = 0, e4 = 0, e5 = 0, e6 = 0;
    u32x4 af[9];
    f32x4 v0, v1, yc;
    f32x16 ya;
    u32x16 A;
    unsigned nsl = 0;
    PADS(232 + ROLE * 8 + decltype(PHC)::value);
    unsigned f3 = s + 1;
    pin_v(f3);
    asm volatile("" : "+{v[112:127]}"(cst));
    PAD();
    unsigned nt = 0;
    tm = __builtin_amdgcn_readfirstlane(tm); rcnt = __builtin_amdgcn_readfirstlane(rcnt);
    asm volatile("" : "+s"(tm), "+s"(rcnt));
#define MV(t, j) do { smo<t, (j) < 8>(v##t, af[j], wv[t][j], idx); SB(); } while (0)
    unsigned ms = ((((s & 7) * SLOT_REAL) ^ CPY2) + own) ^ ((s & 4) ? 0 : CPY1);
    constexpr int ID = FIRST ? -1 : 32 + ROLE * 8 + decltype(PHC)::value;
    auto head = [&]() {
      y_zero(ya, yc);
      v_zero(v0, v1);
      if (!FIRST) {
        int r = 0;
        if (ROLE == 1) r = spin_late(tm, DVQ);
        mem_issue2<ROLE == F8W ? 3 : 2>(D, m0, m1, m2, ms, lw0, lw0 + 4096, lb + F8_OFF, e3);
        if (ROLE == 1) { move_on(tm, r); SB(); }
        poison();
        PAD();
        if (LAST || nt != 0) VMW(0);
        else {
          __builtin_amdgcn_s_sleep(XSLP);
          y_copy(s - 1);
          CBAR();
          asm volatile("s_waitcnt vmcnt(%0)" :: "n"(5 + (TWO ? 1 + (ROLE == F8W) : 0)) : "memory");
        }
      } else {
        *(lds_u32x4*)(img + r0) = af0[0]; *(lds_u32x4*)(img + r1) = af0[1]; *(lds_u32x4*)(lds + F8_OFF + lane * 16) = af0[2];
        poison();
        asm volatile("s_waitcnt lgkmcnt(0)" ::: "memory");
      }
      SB();
      EV(e6);
      HEAD(MV, 5, HV);
      ISOQ(11);
      v_end<ID>(af, wv[1], idx, cst, v0, v1, A, wo, ya, e0); SB();
    };
    if (!FIRST) {
      tm -= GRID + XT; nt = ~0u;
      try_top<ID>(nt, ms, tm, rcnt, ima); SB();
      head();
      try_end(ima, nt, ms, tm, rcnt, m0, m1, m2, lw0, lw0 + 4096, lb + F8_OFF, D); SB();
    } else
    head();
    if (LAST) { st32((unsigned)(s - 1), R, lane == 0 ? (unsigned)(FL_YPROG + h * 16 + ROLE * 4) : OOB, 0); SB(); }
    EV(e4); ISOQ(13);
    y_all<!LAST, LAST ? -1 : ID>(wo, w8, cst, ya, yc, A, D, (unsigned)(s & 1) * YSLOT_REAL, f3, o, o8, e5, nsl); SB();
    ISOQ(15);
    PAD();
#undef MV
    if (FIRST) {
      VMW(0); BAR();
      st32(0u, R, tid == 0 ? (unsigned)(FL_WP + (32 + h) * 32) : OOB, 0);
    }
    if (LAST || (!PH && !FIRST && UNLIKELY(s == NSEG - 2))) y_copy(s);
    CBAR();
    if (ROLE == 1 && !LAST) {
      if (FIRST) { tm = get_t1(R, cwo, bud); asm volatile("" : "+s"(tm)); }
      else
      next_time(tm, s);
    }
    if (PH && !LAST && UNLIKELY(s == WLAST)) WALK(232 + ROLE * 8 + 5);
    PADE();
    EVPUT(0, e0); EVPUT(6, e6); EVPUT(4, e4);
    EVPUTI(1, nt); EVPUT(2, e1); if (!FIRST) EVPUT(3, e3);
  };
  step(std::integral_constant<int, 2>(), 0);
#pragma nounroll
  for (int s = 1; s < NSEG - 1; s += 2) {
    step(std::integral_constant<int, 1>(), s);
    step(std::integral_constant<int, 0>(), s + 1);
  }
  step(std::integral_constant<int, 5>(), NSEG - 1);
  VMW(0);
  st32((unsigned)(NSEG - 1), R, lane == 0 ? (unsigned)(FL_YPROG + h * 16 + ROLE * 4) : OOB, 0);
  EVDUMP();
  };
  if (wave == 0) run(std::integral_constant<int, 0>());
  else if (wave == 1) run(std::integral_constant<int, 1>());
  else if (wave == 2) run(std::integral_constant<int, 2>());
  else run(std::integral_constant<int, 3>());
  RT(7);
}

#define NG 4
#define UL(k, o) "buffer_load_dwordx4 v[128+4*" #k ":128+4*" #k "+3], %[u" #o ", %[D], %[su] offen offset:(" #k "&3)*1024 sc1\n\t"
#define UL4(c, k0, k1, k2, k3, o) ".if " #k0 c "4*" XSTR(NG) "\n\t" UL(k0, o) UL(k1, o) UL(k2, o) UL(k3, o) ".endif\n\t"
#define ULD(c) UL4(c, 0, 1, 2, 3, 0]) UL4(c, 4, 5, 6, 7, 1]) UL4(c, 8, 9, 10, 11, 2]) UL4(c, 12, 13, 14, 15, 3])   \
               UL4(c, 16, 17, 18, 19, 4]) UL4(c, 20, 21, 22, 23, 5]) UL4(c, 24, 25, 26, 27, 6]) UL4(c, 28, 29, 30, 31, 7])
#define UG(G, k0, k1, k2, k3, o) "s_bfe_u32 %[k], %[r], 0x40000+4*" #G "\n\ts_cmp_lg_u32 %[k], 15\n\ts_cbranch_scc1 23f\n\t" \
                                 UL(k0, o) UL(k1, o) UL(k2, o) UL(k3, o) "s_or_b32 %[sa], %[sa], 15<<(4*" #G ")\n23:\n\t"
#define USL 2
#define ULD0 ULD(" < ")
#define ULD1 ULD(" >= ")
#define TB 12
#define UM(T, lo, hi, n) ".if " #T " >= " lo " && " #T " < " hi "\n\t.if !(" #T " & 1)\n\ts_waitcnt vmcnt(" n "+28-2*" #T ")\n\t.endif\n\t"   \
                         "v_swap_b32 v[129+8*" #T "], v[132+8*" #T "]\n\tv_swap_b32 v[131+8*" #T "], v[134+8*" #T "]\n\t.endif\n\t"
#define UMS(...) UM(0, __VA_ARGS__) UM(1, __VA_ARGS__) UM(2, __VA_ARGS__) UM(3, __VA_ARGS__) UM(4, __VA_ARGS__) UM(5, __VA_ARGS__) UM(6, __VA_ARGS__) UM(7, __VA_ARGS__) \
                 UM(8, __VA_ARGS__) UM(9, __VA_ARGS__) UM(10, __VA_ARGS__) UM(11, __VA_ARGS__) UM(12, __VA_ARGS__) UM(13, __VA_ARGS__) UM(14, __VA_ARGS__) UM(15, __VA_ARGS__)
#define TIMES "s_and_b32 %[tt], %[lev], 255\n\ts_add_u32 %[tn], %[tq], %[tt]\n\ts_add_u32 %[tn], %[tn], %[t0]\n\t" \
              "s_lshr_b32 %[tt], %[lev], 8\n\ts_add_u32 %[tt], %[tt], %[tq]\n\t"
#define DZERO ".rept 32\n\tv_mov_b64 v[64+2*\\+:65+2*\\+], 0\n\t.endr\n\tv_mov_b64 v[60:61], 0\n\tv_mov_b64 v[62:63], 0\n\t"
#define WP(a, b) #a ":\n\ts_cbranch_execz " #b "\n\t"
#define DZ6(k) ".rept 6\n\tv_mov_b64 v[60+12*" #k "+2*\\+:61+12*" #k "+2*\\+], 0\n\t.endr\n\t"
#define YLD(k, o) "buffer_load_dwordx4 a[64+4*" #k ":64+4*" #k "+3], %[y" #o ", %[D], %[sy] offen offset:(" #k "&3)*1024 sc1\n\t"
#define YPO(k, o) "buffer_store_dwordx4 %[one], %[y" #o ", %[D], %[sy] offen offset:(" #k "&3)*1024 sc1\n\t"
#define ELD(T, o) "buffer_load_dwordx4 a[4*" #T ":4*" #T "+3], %[e" #o ", %[D], %[sq] offen offset:(" #T "&3)*1024 sc1\n\t"
#define PLD(G) "buffer_load_dwordx4 a[96+4*" #G ":96+4*" #G "+3], %[pm], %[D], %[sq] offen offset:" #G "*1024 sc1\n\t"
#define ALD(G) "buffer_load_dwordx4 a[112+4*" #G ":112+4*" #G "+3], %[al], %[D], %[sq] offen offset:" #G "*256 sc1\n\t"
#define VW(X, n) "s_waitcnt vmcnt(1+" n ")\n\t"
#define EM(T) "v_smfmac_f32_16x16x64_bf16 v[64+4*" T ":64+4*" T "+3], a[4*" T ":4*" T "+3], v[128+8*" T ":128+8*" T "+7], %[ie]\n\t"
#define WC0(T) "v_cvt_pk_bf16_f32 v[64+2*" #T "], v[64+4*" #T "], v[64+4*" #T "+1]\n\t"
#define WC1(T) "v_cvt_pk_bf16_f32 v[65+2*" #T "], v[64+4*" #T "+2], v[64+4*" #T "+3]\n\t"
#define WCV(T) WC0(T) WC1(T)
#define EM2_0(T, C, o) VW("0", "30-" #T) EM(#T) WC0(C) EM("(" #T "+1)") WC1(C)

#define PMM(X, G) VW(X, "7-" #G) "v_smfmac_f32_16x16x64_bf16 v[60:63], a[96+4*" #G ":96+4*" #G "+3], a[64+8*" #G ":64+8*" #G "+7], %[ip]\n\t"
#define AMM(X, G) VW(X, "3-" #G) "v_smfmac_f32_16x16x64_bf16 v[60:63], a[112+4*" #G ":112+4*" #G "+3], v[64+8*" #G ":64+8*" #G "+7], %[ia]\n\t"
#define TRY(X, EM2, a, b, c, d)                                                                                                       \
      SPIN(tt, a, b, c) MEV(1) MCY(0)                                                                                                \
      "buffer_load_dword v59, %[es], %[D], %[sq] offen sc1\n\t" ELD(0, 0]) ELD(1, 0]) ELD(2, 0]) ELD(3, 0]) ELD(4, 1]) ELD(5, 1]) ELD(6, 1]) ELD(7, 1])                                        \
      ELD(8, 2]) ELD(9, 2]) ELD(10, 2]) ELD(11, 2]) ELD(12, 3]) ELD(13, 3]) ELD(14, 3]) ELD(15, 3])                                  \
      YLD(0, 0]) YLD(1, 0]) YLD(2, 0]) YLD(3, 0]) YLD(4, 1]) YLD(5, 1]) YLD(6, 1]) YLD(7, 1])                                        \
      PLD(0) PLD(1) PLD(2) PLD(3) ALD(0) ALD(1) ALD(2) ALD(3) MCY(1) "buffer_load_dword %[ub], %[uq], %[D], 0 offen sc1\n\ts_cmp_lg_u32 %[sx], 0\n\ts_cbranch_scc1 77f\n\t" UMS(XSTR(TB), "16", "43") "s_mov_b32 %[sx], 1\n77:\n\t"                                                                 \
      VW("0", "32") "v_cmp_eq_u32 vcc, -1, v59\n\t" VW("0", "30") "s_cbranch_vccnz 61f\n62:\n\t" EM("0") MCY(2) EM("1")                                                                                           \
      EM2(2, 0, 0]) EM2(4, 1, 0]) EM2(6, 2, 0]) EM2(8, 3, 0]) EM2(10, 4, 1]) EM2(12, 5, 1]) EM2(14, 6, 1]) MCY(3)                           \
      "s_cmp_lt_i32 %[r], -" XSTR(LTOL) "\n\ts_cbranch_scc0 " d "f\n\t"                              \
      "63:\n\ts_add_u32 %[tq], %[tq], " XSTR(GRID) "\n\ts_add_u32 %[t0], %[t0], " XSTR(XT) "\n\ts_add_u32 %[tn], %[tn], " XSTR(GRID) "+" XSTR(XT) "\n\ts_add_u32 %[r], %[r], " XSTR(GRID) "\n\t"   \
      "s_cmp_lt_i32 %[r], -" XSTR(LTOL) "\n\ts_cbranch_scc1 63b\n\t" MCA(1000)                                                        \
      d ":\n\t" WCV(7) WCV(8) WCV(9) WCV(10) WCV(11) WCV(12) WCV(13) WCV(14) WCV(15)                                                 \
      PMM(X, 0) MCY(4) PMM(X, 1) PMM(X, 2) PMM(X, 3) AMM(X, 0) AMM(X, 1) AMM(X, 2) AMM(X, 3) MCY(5)                                  \
      PADN "s_nop 6\n\t"                                                                                                             \
      "v_pk_add_f32 v[60:61], v[60:61], v[62:63]\n\t"                                                                                \
      "v_cvt_pk_bf16_f32 v62, v60, v61\n\tv_cmp_u_f32 vcc, v60, v61\n\ts_nop 0\n\t"                                                  \
      "v_mov_b32_dpp v63, v62 quad_perm:[1,0,3,2] row_mask:0xf bank_mask:0xf\n\t"                                                    \
      "v_perm_b32 v61, v63, v62, %[sel]\n\ts_cbranch_vccnz 50f\n\t"
#define MEV(n)
#define MEVPUT
#define MCY(n)
#define MCYPUT
#define MCYO
#define MCN
#define MCA(k)
#define FME
#define FML
#define FMZ
#define VR8(p, b) p #b "0", p #b "1", p #b "2", p #b "3", p #b "4", p #b "5", p #b "6", p #b "7", p #b "8", p #b "9"
__device__ __forceinline__ void role_mix(const Args& a, rsrc_t R, const Go& go, int m, int wave, int lane) {
  if (wave >= 2) {
    unsigned t[4];
#pragma unroll
    for (int k = 0; k < 4; ++k) {
      const int u = ((m * 2 + wave - 2) * 4 + k) * 64 + lane;
      t[k] = ld32(R, u < 8192 ? (unsigned)u * 128u : (u < 12800 ? OFF_YR + (unsigned)(u - 8192) * 128u : (u < 17408 ? OFF_YR + YSLOT_REAL + (unsigned)(u - 12800) * 128u : OOB)), 0);
    }
    asm volatile("" :: "v"(t[0]), "v"(t[1]), "v"(t[2]), "v"(t[3]));
  }
  if (wave != 0 && (wave != 2 || m != 0)) return;
  const int l15 = lane & 15, g = lane >> 4;
  if (wave == 0) {
#pragma unroll
    for (int k = 0; k < 32; ++k) st128((u32x4){~0u, ~0u, ~0u, ~0u}, R, OFF_ZF + (unsigned)(m * 40960 + lane * 16), k * 1024);
  }
  if (!go_wait(R, go, lane)) return;
  VMW(0);
  st32(0u, R, lane == 0 && wave == 0 ? (unsigned)(FL_MPOI + m * 4) : OOB, 0);
  RT(3);
  if (wave == 2) {
    for (int k = 0; k < TSL; ++k) __builtin_amdgcn_s_sleep(32);
    for (unsigned spins = 0; spins < SPINCAP; ++spins) {
      CBAR();
      if (__all((int)(ld32(R, FL_MRDY + min(lane, 35) * 4, 0) | ld32(R, FL_WP + lane * 32, 0)) >= 0)) break;
      __builtin_amdgcn_s_sleep(1);
    }
    st32((rt_now() + T1D) & ~1u, R, lane < 16 ? (unsigned)(FL_AN + lane * 128) : OOB, 0);
    return;
  }
  for (int k = 0; k < CSL; ++k) __builtin_amdgcn_s_sleep(32);
  const int cls = get_cls(R, lane);
  if (cls < 0) return;
  RT(4);
  const unsigned fo = (unsigned)(cls & 1) * 8192u, y0 = ywin(cls, m) + lane * 16;
  const u32x4 one = {~0u, ~0u, ~0u, ~0u};
  const unsigned mc = (unsigned)nearoff(m * 256 + l15 * 16) + fo + (g & 1) * CPY1 + (g >> 1) * CPY2;
#pragma unroll
  for (int k = 0; k < 8; ++k) st128(one, R, y0 + k * 1024, YSLOT_REAL);
  st128(one, R, mc, SLOT_REAL);
  RT(1);
  unsigned uo[8], eo[4];
#pragma unroll
  for (int k = 0; k < 8; ++k) uo[k] = OFF_ZF + (unsigned)(m * 40960 + k * 4096 + lane * 16);
#pragma unroll
  for (int k = 0; k < 4; ++k) {
    eo[k] = (unsigned)nearoff(NEAR_E + k * 4096 + lane * 16) + fo;
  }
  const unsigned pm = (unsigned)nearoff(NEAR_PM + lane * 16) + fo;
  const int qi = 2 * (l15 >> 2) + (l15 & 1);
  const unsigned al = !(l15 & 2) && (g & 1) == (qi >> 2) ? (unsigned)nearoff(NEAR_AL + ((g >> 1) * 8 + qi) * 16) + fo : OOB;
  const unsigned mo = (unsigned)nearoff((2 * m + (l15 >> 3)) * 128 + (2 * g + (l15 & 1)) * 16 + ((l15 & 7) >> 1) * 4) + fo;
  const unsigned sm = lane < 36 ? (unsigned)nearoff(lane * 256) + fo : OOB, own = own_cpy(cls, m / 9);
  const unsigned an = FL_AN + (m & 15) * 128, wp = FL_WP + lane * 32;
  const unsigned ie = (lane & 4) ? 0xEEEE : 0x4444, ip = (lane & 2) ? 0xEEEE : 0x4444, ia = ie;
  const unsigned sel = (lane & 1) ? 0x03020706u : 0x05040100u;
  const i32x4 D = mkdesc(a.wb);
  const unsigned uf = lane < 32 ? (unsigned)(FL_UPROG + lane * 128 + (m / 9) * 4) : OOB;
  for (int k = 0; k < MSL; ++k) __builtin_amdgcn_s_sleep(16);
  RT(2);
  unsigned tq = 0, lev = PACE0, bud = SPINCAP, s = 0, su = 0, sy = 0, sp = 0, tt, sa, sb, sc, sn, tn, ln, ue, t0, sx, sq, cw, sv, ua = ~0u, ub, uq, nr = 0, cv = 0;
  int ufl = lane < 32 ? -1 : 0x7fffffff;
  unsigned ox = lane < 32 ? OOB : an;
  unsigned ev0 = 0, ev1 = 0, ev2 = 0, ev3 = 0, ec[7] = {0, 0, 0, 0, 0, 0, 0};
  int r, k;
  asm volatile(
      "s_mov_b64 exec, 0\n\ts_branch 200f\n299:\n\ts_mov_b64 exec, -1\n\t"
      "s_mov_b32 %[sa], 0\n\ts_mov_b32 %[sb], " XSTR(D0) "\n"
      "27:\n\tbuffer_load_dword v59, %[up], %[D], 0 offen sc1\n\tbuffer_load_dword %[cw], %[wp], %[D], 0 offen sc1\n\t"
      "s_mov_b32 %[tq], %[sb]\n\ts_waitcnt vmcnt(0)\n\tv_cmp_ne_u32 vcc, -1, v59\n\ts_andn2_b32 %[r], vcc_lo, %[sa]\n\ts_cbranch_scc0 26f\n\t"
      UG(0, 0, 1, 2, 3, 0]) UG(1, 4, 5, 6, 7, 1]) UG(2, 8, 9, 10, 11, 2]) UG(3, 12, 13, 14, 15, 3])
      UG(4, 16, 17, 18, 19, 4]) UG(5, 20, 21, 22, 23, 5]) UG(6, 24, 25, 26, 27, 6]) UG(7, 28, 29, 30, 31, 7])
      "s_cmp_lg_u32 %[sa], -1\n\ts_cbranch_scc1 26f\n\tv_mov_b32 v59, 0\n\tbuffer_store_dword v59, %[rdy], %[D], 0 offen sc1\n"
      "26:\n\tv_cmp_gt_i32 vcc, 0, %[cw]\n\ts_cbranch_vccnz 25f\n\ts_mov_b32 %[sb], 0\n\ts_cmp_lg_u32 %[sa], -1\n\ts_cbranch_scc0 24f\n"
      "25:\n\ts_sleep " XSTR(USL) "\n\t" BUD("27b")
      "24:\n\ts_memrealtime vcc\n\ts_waitcnt lgkmcnt(0)\n\ts_add_u32 %[tq], %[tq], vcc_lo\n\ts_sub_u32 %[tq], %[tq], " XSTR(DE0) "\n\ts_branch 28f\n"
      "29:\n\t" ULD0 ULD1 "28:\n\ts_mov_b32 %[t0], 0\n\ts_mov_b32 %[sb], 0\n\ts_mov_b32 %[ue], 2*" XSTR(SLOT_REAL) "\n\ts_waitcnt vmcnt(0)\n\t"
      "30:\n\t" FMZ DZERO
      "s_add_u32 %[su], %[s], 1\n\ts_and_b32 %[su], %[su], " XSTR(RMASK) "\n\ts_mul_i32 %[su], %[su], 36*40960\n\t"
      "s_add_u32 %[sn], %[sp], " XSTR(SLOT_REAL) "\n\ts_and_b32 %[sn], %[sn], 7*" XSTR(SLOT_REAL) "\n\t"
      "s_lshr_b32 %[sq], %[sp], 5\n\ts_and_b32 %[sq], %[sq], " XSTR(CPY1) "\n\ts_xor_b32 %[sq], %[sq], %[own]\n\ts_add_u32 %[sq], %[sq], %[sp]\n\t"
      "s_movk_i32 %[ln], " XSTR(PACEC) "\n\ts_cmp_lt_u32 %[s], " XSTR(S2) "-1\n\ts_cmovk_i32 %[ln], " XSTR(PACEB) "\n\t"
      "s_cmp_lt_u32 %[s], " XSTR(S1) "-1\n\ts_cmovk_i32 %[ln], " XSTR(PACEA) "\n\ts_cmp_lt_u32 %[s], " XSTR(S0) "-1\n\ts_cmovk_i32 %[ln], " XSTR(PACE0) "\n\t"
      "s_add_u32 %[sc], %[sn], " XSTR(CPY1) "\n\ts_xor_b32 %[sb], %[sn], " XSTR(CPY2) "\n\ts_mov_b32 %[sx], 0\n\t" TIMES
      UMS("0", "2*" XSTR(NG), "8") "s_cmp_lg_u32 %[s], 0\n\t"
      "buffer_store_dwordx4 %[one], %[mc], %[D], %[ue] offen sc1\n\tv_max_i32 %[ufl], %[ufl], %[ua]\n\ts_cbranch_scc0 33f\n\t"
      "v_cmp_eq_u32 vcc, -1, %[sv]\n\ts_cbranch_vccnz 35f\n"
      "33:\n\tv_cmp_ge_i32 vcc, %[s], %[ufl]\n\ts_nop 0\n\tv_cndmask_b32 %[uq], %[ox], %[uf], vcc\n\t"
      UMS("2*" XSTR(NG), XSTR(TB), "9") MEV(3)
      "75:\n\t" TRY("0", EM2_0, "40", "41", "42", "43")
      "51:\n\tbuffer_store_dword v61, %[mo], %[D], %[sn] offen sc1\n\tbuffer_store_dword v61, %[mo], %[D], %[sc] offen sc1\n\ts_memrealtime vcc\n\t" MEV(2) MCY(6)
      "buffer_store_dword v61, %[mo], %[D], %[sb] offen sc1\n\ts_add_u32 %[sb], %[sb], " XSTR(CPY1) "\n\tbuffer_store_dword v61, %[mo], %[D], %[sb] offen sc1\n\t"
      "s_waitcnt vmcnt(4) lgkmcnt(0)\n\ts_add_u32 %[k], vcc_lo, " XSTR(MGAP) "\n"
      "97:\n\ts_sub_u32 %[r], %[tn], %[k]\n\ts_cmp_lt_i32 %[r], 0\n\ts_cbranch_scc0 98f\n\ts_add_u32 %[tn], %[tn], " XSTR(GRID) "+" XSTR(XT) "\n\ts_branch 97b\n"
      "98:\n\t" MEVPUT MCYPUT MCN
      "s_add_u32 %[s], %[s], 1\n\ts_cmp_ge_u32 %[s], 63\n\ts_cbranch_scc1 60f\n\t"
      "v_max_i32 %[ufl], %[ufl], %[ub]\n\tv_cmp_gt_i32 vcc, %[s], %[ufl]\n\ts_mov_b32 %[sx], 1\n\ts_cbranch_vccnz 100f\n\t"
      "s_mov_b32 %[sx], 0\n\t" ULD0 "100:\n\t"
      "s_cmp_lg_u32 %[s], 1\n\ts_cbranch_scc1 67f\n\t"
      "v_readlane_b32 %[tn], %[ub], 32\n\tv_mov_b32 %[ox], " XSTR(OOB) "\n\ts_cmp_lg_u32 %[tn], -1\n\ts_cbranch_scc1 67f\n"
      "66:\n\tbuffer_load_dword %[cw], %[an], %[D], 0 offen sc1\n\ts_waitcnt vmcnt(0)\n\tv_readfirstlane_b32 %[tn], %[cw]\n\t"
      "s_cmp_lg_u32 %[tn], -1\n\ts_cbranch_scc1 67f\n\t" BUD("66b")
      "67:\n\t"
      "s_xor_b32 %[ue], %[sn], 2*" XSTR(SLOT_REAL) "\n\ts_and_b32 %[ue], %[ue], 3*" XSTR(SLOT_REAL) "\n\ts_mov_b32 %[sp], %[sn]\n\t"
      "s_mov_b32 %[tq], %[tn]\n\ts_mov_b32 %[t0], 0\n\ts_mov_b32 %[lev], %[ln]\n\t"
      "v_cmp_ge_i32 vcc, %[s], %[ufl]\n\tv_mov_b32 %[ua], " XSTR(OOB) "\n\tv_cndmask_b32 %[ua], %[ua], %[uf], vcc\n\t"
      "s_lshr_b32 %[sa], %[sn], 5\n\ts_and_b32 %[sa], %[sa], " XSTR(CPY1) "\n\ts_xor_b32 %[sa], %[sa], %[own]\n\ts_add_u32 %[sa], %[sa], %[sn]\n\t"
      SPIN(tq, "52", "53", "54") "buffer_load_dword %[sv], %[sm], %[D], %[sa] offen sc1\n\t"
      "buffer_load_dword %[ua], %[ua], %[D], 0 offen sc1\n\t"
      "s_cmp_eq_u32 %[sx], 0\n\ts_cbranch_scc1 101f\n\t"
      "55:\n\ts_waitcnt vmcnt(0)\n\tv_max_i32 %[ufl], %[ufl], %[ua]\n\tv_cmp_gt_i32 vcc, %[s], %[ufl]\n\ts_cbranch_vccz 57f\n\t" MCA(100000)
      "buffer_load_dword %[ua], %[uf], %[D], 0 offen sc1\n\t" BUD("55b")
      "57:\n\t" ULD0 "101:\n\t" ULD1 YPO(0, 0]) YPO(1, 0]) YPO(2, 0]) YPO(3, 0]) YPO(4, 1]) YPO(5, 1]) YPO(6, 1]) YPO(7, 1])
      "s_xor_b32 %[sy], %[sy], " XSTR(YSLOT_REAL) "\n\ts_branch 30b\n\t"
      WP(200, 201f) "35:\n\t" MCA(100) BUD("36f") "s_branch 32f\n\t"
      "36:\n\ts_add_u32 %[tq], %[tq], " XSTR(GRID) "+" XSTR(XT) "\n\ts_xor_b32 %[sa], %[sa], %[flp]\n\t" WP(201, 202f)
      SPIN(tq, "37", "38", "39") "buffer_load_dword %[sv], %[sm], %[D], %[sa] offen sc1\n\t" WP(202, 203f) "s_waitcnt vmcnt(0)\n\t"
      "v_cmp_eq_u32 vcc, -1, %[sv]\n\ts_cbranch_vccnz 35b\n"
      "32:\n\t" TIMES "s_branch 33b\n" WP(203, 204f)
      "59:\n\t" WP(204, 205f) "s_waitcnt lgkmcnt(0)\n\ts_lshr_b32 %[tt], %[lev], 8\n\ts_sub_u32 %[k], vcc_lo, %[tt]\n\ts_sub_u32 %[k], %[k], " XSTR(RTOL) "\n\t"
      "34:\n\ts_add_u32 %[tq], %[tq], " XSTR(GRID) "\n\ts_add_u32 %[t0], %[t0], " XSTR(XT) "\n\ts_sub_u32 %[r], %[tq], %[k]\n\ts_cmp_lt_i32 %[r], 0\n\ts_cbranch_scc1 34b\n\t"
      WP(205, 206f) TIMES "s_branch 75b\n\t" WP(206, 207f)
      "58:\n\ts_cmp_lg_u32 %[s], 0\n\ts_cbranch_scc1 102f\n\ts_mov_b32 %[su], 0\n\ts_branch 29b\n102:\n\ts_memrealtime vcc\n\ts_xor_b32 %[sq], %[sq], %[flp]\n\t"
      WP(207, 208f) DZ6(0) WP(208, 209f) DZ6(1) WP(209, 210f) DZ6(2) WP(210, 211f) DZ6(3) WP(211, 212f) DZ6(4) WP(212, 213f)
      ".rept 4\n\tv_mov_b64 v[120+2*\\+:121+2*\\+], 0\n\t.endr\n\ts_branch 59b\n\t" WP(213, 214f)
      "50:\n\t" FML MCA(1) BUD("58b") "v_min_u32 v61, -2, v61\n\ts_branch 51b\n\t" WP(214, 215f)
      "99:\n\ts_memrealtime vcc\n\ts_xor_b32 %[sq], %[sq], %[flp]\n\ts_branch 59b\n\t"
      "61:\n\t" FME MCA(1) BUD("99b") "s_branch 62b\n\t" WP(215, 299b)
      "60:\n\ts_waitcnt vmcnt(0)"
      : [tq] "+&s"(tq), [lev] "+&s"(lev), [bud] "+&s"(bud), [s] "+&s"(s), [su] "+&s"(su), [sy] "+&s"(sy), [sp] "+&s"(sp), [tt] "=&s"(tt), [sa] "=&s"(sa),
        [sn] "=&s"(sn), [sb] "=&s"(sb), [sc] "=&s"(sc), [tn] "=&s"(tn), [ln] "=&s"(ln), [ue] "=&s"(ue), [t0] "=&s"(t0), [sx] "=&s"(sx), [sq] "=&s"(sq), [cw] "=&v"(cw), [sv] "=&v"(sv), [ua] "+&v"(ua), [ub] "=&v"(ub), [uq] "=&v"(uq), [ufl] "+&v"(ufl), [ox] "+&v"(ox), [r] "=&s"(r), [k] "=&s"(k), [nr] "+&s"(nr), [cv] "+&v"(cv),
        [ev0] "+&v"(ev0), [ev1] "+&v"(ev1), [ev2] "+&v"(ev2), [ev3] "+&v"(ev3) MCYO
      : [D] "s"(D), [u0] "v"(uo[0]), [u1] "v"(uo[1]), [u2] "v"(uo[2]), [u3] "v"(uo[3]), [u4] "v"(uo[4]), [u5] "v"(uo[5]), [u6] "v"(uo[6]), [u7] "v"(uo[7]), [e0] "v"(eo[0]), [e1] "v"(eo[1]), [e2] "v"(eo[2]),
        [e3] "v"(eo[3]), [y0] "v"(y0), [y1] "v"(y0 + 4096), [pm] "v"(pm), [al] "v"(al), [mo] "v"(mo), [mc] "v"(mc), [an] "v"(an), [wp] "v"(wp),
        [ie] "v"(ie), [ip] "v"(ip), [ia] "v"(ia), [sel] "v"(sel), [one] "v"(one), [sm] "v"(sm), [uf] "v"(uf), [es] "v"(lane < 32 ? (unsigned)nearoff(NEAR_E + lane * 512 + (lane & 1) * (64 - 512)) + fo : OOB), [rdy] "v"(lane == 0 ? (unsigned)(FL_MRDY + m * 4) : OOB), [own] "s"(own), [flp] "s"(CPY2 + CPY1),
        [up] "v"(OFF_ZF + (unsigned)(m * 40960 + (lane & 31) * 1024 + 1020))
      : "memory", "vcc", "scc", "s68", "s69", "s70", "s71", "s72", "s73", "s74", "s75", "s76", "s77", "s78", "s79", "s80", "s81", "s84", "s85", "s86", "s87", "s88", "s89", "s90", "s91",
        "v59", VR8("v", 6), VR8("v", 7), VR8("v", 8), VR8("v", 9), VR8("v", 10), VR8("v", 11), VR8("v", 12), VR8("v", 13), VR8("v", 14), VR8("v", 15),
        VR8("v", 16), VR8("v", 17), VR8("v", 18), VR8("v", 19), VR8("v", 20), VR8("v", 21), VR8("v", 22), VR8("v", 23), VR8("v", 24),
        "v250", "v251", "v252", "v253", "v254", "v255",
        "a0", "a1", "a2", "a3", "a4", "a5", "a6", "a7", "a8", "a9", VR8("a", 1), VR8("a", 2), VR8("a", 3), VR8("a", 4), VR8("a", 5), VR8("a", 6),
        VR8("a", 7), VR8("a", 8), VR8("a", 9), VR8("a", 10), VR8("a", 11), VR8("a", 12), VR8("a", 13), VR8("a", 14), VR8("a", 15),
        VR8("a", 16), VR8("a", 17), VR8("a", 18), VR8("a", 19), VR8("a", 20), VR8("a", 21), VR8("a", 22), VR8("a", 23), VR8("a", 24),
        "a250", "a251", "a252", "a253", "a254", "a255");
  RT(7);
}

struct XL { rsrc_t X; i32x4 D; unsigned vo; int wr, rd; };
struct XP { lds_char* p[2]; };
__device__ __forceinline__ void x_init(XL& xl, const u16* x, int wave, int lane) {
  xl.X = __builtin_amdgcn_make_buffer_rsrc((void*)x, 0, S_LEN * DM * 2, 0x00020000);
  xl.D = (i32x4){(int)(unsigned)(u64)x, (int)(unsigned)((u64)x >> 32), S_LEN * DM * 2, 0x00020000};
  xl.vo = (unsigned)((wave * 8 + (lane >> 3)) * 1152 + ((lane ^ (lane >> 3)) & 7) * 16);
  xl.wr = wave * 9216 + lane * 16;
  xl.rd = ((lane >> 3) & 1) * 9216 + (lane & 7) * 128 + ((lane >> 4) ^ (lane & 7)) * 16;
}
__device__ __forceinline__ XP x_at(lds_char* buf, const XL& xl) {
  XP r = {{buf + xl.rd, buf + (xl.rd ^ 64)}};
  pin_v(r.p[0]); pin_v(r.p[1]);
  return r;
}
__device__ __forceinline__ void x_m0(unsigned b, int k) { asm volatile("s_add_u32 m0, %0, %1" :: "s"(b), "n"(k * 896) : "scc"); }
__device__ __forceinline__ void x_dma(const XL& xl, unsigned so, int k) {
  asm volatile("buffer_load_dwordx4 %0, %1, %2 offen offset:%3 lds" :: "v"(xl.vo), "s"(xl.D), "s"(so), "n"(k * 128) : "memory");
}
__device__ __forceinline__ void x_dma0(const XL& xl, unsigned so, int k) {
  asm volatile("s_nop 0\n\tbuffer_load_dwordx4 %0, %1, %2 offen offset:%3 " XNT " lds" :: "v"(xl.vo), "s"(xl.D), "s"(so), "n"(k * 128) : "memory");
}
__device__ __forceinline__ i32x4 wdesc(const u16* p) { return (i32x4){(int)(unsigned)(u64)p, (int)(unsigned)((u64)p >> 32), DM * HD * 2, 0x00020000}; }
__device__ __forceinline__ void w_dma(i32x4 D, unsigned vo, unsigned so, unsigned b, int o) {
  asm volatile("s_add_u32 m0, %3, %4\n\ts_nop 0\n\tbuffer_load_dwordx4 %0, %1, %2 offen nt lds" :: "v"(vo), "s"(D), "s"(so), "s"(b), "n"(o) : "memory", "scc");
}
#define VB_OFF 73728
#define VX_OFF (VB_OFF + 73728)
constexpr int vbuf(int j) { return j % 5 == 3 ? VX_OFF : VB_OFF + ((j + 1) % 5) * 18432; }
__device__ __forceinline__ void x_move(lds_char*& p, unsigned d) { asm volatile("v_add_u32 %0, %1, %0" : "+v"(p) : "s"(d)); }
__device__ __forceinline__ u32x4 x_ld1(const XL& xl, int s, int k) { return __builtin_amdgcn_raw_buffer_load_b128(xl.X, xl.vo + k * 128, (unsigned)s * 36864u, 0); }
__device__ __forceinline__ void x_gload(u32x4 (&xr)[9], const XL& xl, int s) {
#pragma unroll
  for (int k = 0; k < 9; ++k) xr[k] = x_ld1(xl, s, k);
}
__device__ __forceinline__ void x_swrite(const u32x4 (&xr)[9], lds_char* buf, const XL& xl) {
#pragma unroll
  for (int k = 0; k < 9; ++k) *(lds_u32x4*)(buf + xl.wr + k * 1024) = xr[k];
}
#define XF(buf, tt, ks) (*(lds_u32x4*)((buf).p[(ks) & 1] + (tt) * 18432 + ((ks) >> 1) * 1024))
__device__ __forceinline__ void proj4(lds_char* cbuf, lds_char* nbuf, u32x4 (&xr)[9], const XL& xl, int snext, int wave, int lane,
                                      const u32x4 (&w0)[18], const u32x4 (&w1)[18], const u32x4 (&w2)[18], const u32x4 (&w3)[18],
                                      f32x4 (&acc)[4][2]) {
  u32x4 bb[3][2];
  const XP buf = x_at(cbuf, xl);
  nbuf += xl.wr;
#pragma unroll
  for (int ks = 0; ks < 2; ++ks) { bb[ks][0] = XF(buf, 0, ks); bb[ks][1] = XF(buf, 1, ks); }
#pragma unroll
  for (int t = 0; t < 4; ++t) { acc[t][0] = (f32x4){0.f, 0.f, 0.f, 0.f}; acc[t][1] = acc[t][0]; }
#pragma unroll
  for (int ks = 0; ks < 18; ++ks) {
    const u32x4 b0 = bb[ks % 3][0], b1 = bb[ks % 3][1];
    SB();
    acc[0][0] = mfma(w0[ks], b0, acc[0][0]); SB();
    if (ks + 2 < 18) bb[(ks + 2) % 3][0] = XF(buf, 0, ks + 2);
    SB();
    acc[0][1] = mfma(w0[ks], b1, acc[0][1]); SB();
    if (ks + 2 < 18) bb[(ks + 2) % 3][1] = XF(buf, 1, ks + 2);
    SB();
    acc[1][0] = mfma(w1[ks], b0, acc[1][0]); SB();
    if ((ks & 1) == 0) *(lds_u32x4*)(nbuf + (ks >> 1) * 1024) = xr[ks >> 1];
    SB();
    acc[1][1] = mfma(w1[ks], b1, acc[1][1]);
    acc[2][0] = mfma(w2[ks], b0, acc[2][0]); SB();
    if ((ks & 1) == 0) xr[ks >> 1] = x_ld1(xl, snext, ks >> 1);
    SB();
    acc[2][1] = mfma(w2[ks], b1, acc[2][1]);
    acc[3][0] = mfma(w3[ks], b0, acc[3][0]); acc[3][1] = mfma(w3[ks], b1, acc[3][1]);
    SB();
  }
}
#define PFL 13
#define STG0 6
#define STGD 0
#define NOST 0
#define NOFL 0
#define FG 7
#define UFL 5
__device__ __forceinline__ int clampseg(int s) { return s < NSEG ? s : NSEG - 1; }

__device__ __forceinline__ void role_prj(const Args& a, rsrc_t R, const Go& go, lds_char* lds, const u16* W0, const u16* W1, unsigned o0, unsigned o1, unsigned flag,
                                         int tid, int wave, int lane) {
  const int l15 = lane & 15, g = lane >> 4;
  u32x4 w[2][2][18];
  u32x4 xr[9], xq[9];
  XL xl; x_init(xl, a.x, wave, lane);
  x_gload(xq, xl, 0);
  x_gload(xr, xl, 1);
  const unsigned fo = wave * 1024 + lane * 16;
  const unsigned pf = lane == 0 ? flag + wave * 4 : OOB;
  f32x4 acc[2][2][2];
  {
    u32x4 sg[PDEP][8];
    unsigned g1;
    const rsrc_t B0 = __builtin_amdgcn_make_buffer_rsrc((void*)W0, 0, DM * HD * 2, 0x00020000), B1 = __builtin_amdgcn_make_buffer_rsrc((void*)W1, 0, DM * HD * 2, 0x00020000);
    const unsigned wof = (unsigned)((tid >> 4) * (HD * 2) + (tid & 15) * 16);
    auto gl = [&](int i, int k) {
      if (i < 10 && (i % 5 < 4 || k < 4)) { sg[i % PDEP][k] = __builtin_amdgcn_raw_buffer_load_b128(i < 5 ? B0 : B1, wof, (NOCW & 4) ? 0u : (unsigned)(128 * (i % 5) + 16 * k) * (HD * 2), (NTM & 4) ? 2 : 0); SB(); }
    };
#pragma unroll
    for (int k = 0; k < 8 * PDEP; ++k) gl(k >> 3, k & 7);
    if (wave == 0) WALK(100); else if (wave == 1) WALK(101); else if (wave == 2) WALK(102); else WALK(103);
    RT(2);
    PADS(100);
    lds_char *x0p = lds + X0_OFF, *x1p = lds + X1_OFF;
    pin_v(x0p); pin_v(x1p);
    x_swrite(xq, x0p, xl);
    PAD();
    x_swrite(xr, x1p, xl);
    PAD();
    const XP x0r = x_at(x0p, xl), x1r = x_at(x1p, xl);
    lds_char *wp = lds + (tid >> 4) * 288 + (tid & 15) * 16, *rp = lds + (g * 8 + (l15 >> 2)) * 288 + wave * 64 + (l15 & 3) * 8;
    pin_v(wp); pin_v(rp);
    auto put = [&](int i, int k) {
      if (i < 10 && (i % 5 < 4 || k < 4)) { *(lds_u32x4*)(wp + (i & 1) * STG_BYTES + k * (16 * 288)) = sg[i % PDEP][k]; SB(); }
    };
#pragma unroll
    for (int k = 0; k < 8; ++k) { put(0, k); gl(PDEP, k); }
    auto chunk = [&](auto IC) {
      constexpr int i = decltype(IC)::value, m = i / 5, c = i % 5, nk = c < 4 ? 4 : 2;
      if (c == 0) {
#pragma unroll
        for (int k = 0; k < 8; ++k) { acc[k >> 2][(k >> 1) & 1][k & 1] = (f32x4){0.f, 0.f, 0.f, 0.f}; pin_v(acc[k >> 2][(k >> 1) & 1][k & 1]); }
      }
      if (i == 4) { PADE(); PADS(101); } else if (i == 8) { PADE(); PADS(102); } else PAD();
      BAR();
      if (i == 0) RT(3);
      if (i == 2) RT(4);
      if (i == 4) RT(5);
      if (i == 6) RT(6);
      if (i == 7) RT(8);
      if (i == 8) RT(9);
      u32x4 xb[2][4];
      u32x2 wh[2][4];
      auto rd = [&](int kk, int j) {
        if (kk >= nk) return;
        if (j & 1) xb[kk & 1][j >> 1] = XF((j & 4) ? x1r : x0r, (j >> 1) & 1, 4 * c + kk);
        else wh[kk & 1][j >> 1] = trd(rp + (i & 1) * STG_BYTES + (kk * 32 + (j & 2) * 2) * 288 + (j >> 2) * 32);
        SB();
      };
#pragma unroll
      for (int j = 0; j < 8; ++j) rd(0, j);
#pragma unroll
      for (int k = 0; k < 8 * nk; ++k) {
        const int kk = k >> 3;
        f32x4& d = acc[(k >> 2) & 1][k & 1][(k >> 1) & 1];
        u32x4& f = w[m][k & 1][4 * c + kk];
        if ((k & 7) < 2) {
          f = (u32x4){wh[kk & 1][(k & 1) * 2][0], wh[kk & 1][(k & 1) * 2][1], wh[kk & 1][(k & 1) * 2 + 1][0], wh[kk & 1][(k & 1) * 2 + 1][1]};
          if (c < 4) pin_a(f); else pin_v(f);
        }
        if (c < 4) mfA(d, f, xb[kk & 1][(k >> 1) & 3]); else mfa(d, f, xb[kk & 1][(k >> 1) & 3]);
        SB();
        rd(kk + 1, k & 7);
        if (k % nk == 0) put(i + 1, k / nk);
        if (k % nk == 1) gl(i + 1 + PDEP, k / nk);
        if (k % 12 == 11 || k == 8 * nk - 1) PAD();
      }
      if (i == 2) g1 = ld32(R, go.vo, 0);
      if (i == 5) x_gload(xq, xl, 2);
      if (i == 6) x_gload(xr, xl, 3);
      if (i == KFI) {
        SB();
        asm volatile("s_waitcnt vmcnt(%0)" :: "n"(KFI == 5 ? 13 : 22) : "memory");
        if (__all(g1 != go.v0)) go_ack(R, go, lane); else go_wait(R, go, lane);
        st32(0u, R, pf, 0);
      }
      SB();
      if (c == 4) {
        asm volatile("s_nop 7"); SB();
#pragma unroll
        for (int k = 0; k < 4; ++k) {
          PAD();
          const f32x4 (&e)[2][2] = acc[k >> 1];
          const int tt = k & 1;
          st128((u32x4){pk2(e[0][tt][0], e[0][tt][1]), pk2(e[0][tt][2], e[0][tt][3]), pk2(e[1][tt][0], e[1][tt][1]), pk2(e[1][tt][2], e[1][tt][3])}, R,
                (m ? o1 : o0) + fo + tt * 4096, (unsigned)(k >> 1) * 262144u);
        }
        SB();
      }
    };
#define CH(n) chunk(std::integral_constant<int, n>())
    CH(0); CH(1); CH(2); CH(3); CH(4); CH(5); CH(6); CH(7); CH(8); CH(9);
#undef CH
    BAR();
  }
  RT(12);
  PAD();
  x_swrite(xq, lds, xl);
  x_swrite(xr, lds + STG_BYTES, xl);
  PAD();
  u32x4 bb[3][2];
  XP rd0 = x_at(lds, xl), rd1 = x_at(lds + STG_BYTES, xl);
  unsigned b0 = 0, b1 = STG_BYTES, b2 = X1_OFF;
  const unsigned xw = (unsigned)(uintptr_t)lds + __builtin_amdgcn_readfirstlane(wave) * 9216;
  BAR();
#pragma unroll
  for (int ks = 0; ks < 2; ++ks) { bb[ks][0] = XF(rd0, 0, ks); bb[ks][1] = XF(rd0, 1, ks); }
  unsigned oa = o0 + fo, ob = o1 + fo, tc0 = 0, tr0 = 0;
  pin_v(oa); pin_v(ob);
  auto half = [&](auto HC, int s) {
    constexpr int H = decltype(HC)::value & 1, P = decltype(HC)::value >> 1;
    XP& buf = P ? rd1 : rd0;
    XP& nrd = H ? (P ? rd0 : rd1) : buf;
    const unsigned oo = H ? oa : ob;
    unsigned so, sx, xm, xd;
    f32x4 (&c)[2][2] = acc[H];
    f32x4 (&e)[2][2] = acc[H ^ 1];
    u32x4 o;
#define PM(t, tt) do { if (ks == 0) c[t][tt] = mfA0(w[H][t][0], bb[0][tt]); else if (ks < 16) mfA(c[t][tt], w[H][t][ks], bb[ks % 3][tt]); \
                       else mfa(c[t][tt], w[H][t][ks], bb[ks % 3][tt]); SB(); } while (0)
#define PR(tt) do { bb[(ks + 2) % 3][tt] = ks < 16 ? XF(buf, tt, ks + 2) : XF(nrd, tt, ks - 16); SB(); } while (0)
#pragma unroll
    for (int ks = 0; ks < 18; ++ks) {
      const int xi = H * 5 + ((ks - 7) >> 1);
      const bool xs = ks >= 7 && ks < 17 && xi < 9 && (H == 0 || ks < 15);
      if (H == 1 && ks == 16) { __builtin_amdgcn_s_waitcnt(0xC27F); __builtin_amdgcn_s_barrier(); SB(); }
      else { __builtin_amdgcn_s_waitcnt(0xC27F); SB(); }
      PM(0, 0); PR(0);
      if (ks == 0) {
        so = (unsigned)(s + H - 1) * 262144u;
        sx = (unsigned)clampseg(s + 2) * 36864u;
        asm volatile("" : "+s"(so), "+s"(sx)); SB();
      }
      if (ks == 1) {
        xm = xw + b2; xd = b2 - b0;
        if (H) { const unsigned t = b0; b0 = b1; b1 = b2; b2 = t; }
        asm volatile("" : "+s"(xm), "+s"(xd), "+s"(b0), "+s"(b1), "+s"(b2)); SB();
      }
      PM(0, 1); PR(1);
      PM(1, 0);
      if (ks == 1 || ks == 2) { o[(ks - 1) * 2] = pkv(e[ks - 1][0][0], e[ks - 1][0][1]); SB(); }
      if (ks == 4 || ks == 5) { o[(ks - 4) * 2] = pkv(e[ks - 4][1][0], e[ks - 4][1][1]); SB(); }
      if (ks == 3 && NOST < 2) { st128(o, R, oo, so); SB(); }
      if (ks == 6 && NOST < 2) { st128(o, R, oo + 4096, so); SB(); }
      if (xs && (ks & 1)) { x_m0(xm, xi); SB(); }
      if (xs && !(ks & 1)) { x_dma(xl, sx, xi); SB(); }
      if (H && ks >= 16) { x_move(buf.p[ks & 1], xd); SB(); }
      PM(1, 1);
      if (ks % 3 == 0) { if (H == 1 && P == 0 && ks == 0) { PADE(); PADS(103); } else PAD(); SB(); }
      if (ks == 1 || ks == 2) { o[(ks - 1) * 2 + 1] = pkv(e[ks - 1][0][2], e[ks - 1][0][3]); SB(); }
      if (ks == 4 || ks == 5) { o[(ks - 4) * 2 + 1] = pkv(e[ks - 4][1][2], e[ks - 4][1][3]); SB(); }
      if (ks == PFL) {
        asm volatile("s_waitcnt vmcnt(%0)" :: "n"((H ? 8 : 7) + (PFL - 6) / 2) : "memory");
        st32((unsigned)(H || P || s > 2 ? 2 * s + H - 2 : 0), R, pf, 0); SB();
      }
      if (ks == 17 && H == 1 && UNLIKELY(s < KEF)) { RT(10); VMW(5); st32((unsigned)(2 * s), R, pf, 0); RT(11); SB(); }
    }
#undef PM
#undef PR
  };
#pragma nounroll
  for (int s = 2; s < NSEG; ++s) {
    half(std::integral_constant<int, 0>(), s);
    half(std::integral_constant<int, 1>(), s);
    STEPRT(0, 0);
    ++s;
    half(std::integral_constant<int, 2>(), s);
    half(std::integral_constant<int, 3>(), s);
    STEPRT(0, 0);
  }
  PADE();
  mf_wait2(acc[1][0][0], acc[1][0][1]); mf_wait2(acc[1][1][0], acc[1][1][1]);
#pragma unroll
  for (int tt = 0; tt < 2; ++tt) {
    const u32x4 o = {pk2(acc[1][0][tt][0], acc[1][0][tt][1]), pk2(acc[1][0][tt][2], acc[1][0][tt][3]), pk2(acc[1][1][tt][0], acc[1][1][tt][1]), pk2(acc[1][1][tt][2], acc[1][1][tt][3])};
    st128(o, R, o1 + fo + tt * 4096, (unsigned)((NSEG - 1) & RMASK) * 262144u);
  }
  VMW(0);
  st32((unsigned)(2 * NSEG - 1), R, pf, 0);
  RT(7);
}

__device__ __forceinline__ void role_px(const Args& a, rsrc_t R, const Go& go, int pi, int wave, int lane) {
  const int l15 = lane & 15, g = lane >> 4;
  const int h = pi * 2 + (wave >> 1), par = wave & 1;
  const int wz = h >> 3, tq = (h >> 1) & 3, hp = h & 1;
  const int gy = ((tq & 1) << 1) | (tq >> 1);
  const unsigned kmo = OFF_KMF + (unsigned)h * 4096u + lane * 16;
  const unsigned pmo = OFF_PXF + (unsigned)((32 + wz * 2 + hp) * 1024 + (gy * 16 + l15) * 16 + g * 4), pxo = OFF_PXF + (unsigned)(h * 1024 + lane * 16);
  const unsigned ppf = (lane == 0) ? (unsigned)(FL_PXPROG + h * 128 + par * 4) : OOB;
  const unsigned qo = OFF_QF + (unsigned)h * 8192u + lane * 16, ko = OFF_KF + (unsigned)h * 8192u + lane * 16;
  const unsigned kmf = lane == 4 ? FL_KMPROG + h * 128 : (lane < 4 ? FL_PROG + h * 128 + lane * 4 : OOB);
  int fm = -1;
  RT(1);
  u32x4 qf[2][4], kf[2][4];
  auto qk = [&](int s) {
#pragma unroll
    for (int k = 0; k < 8; ++k) { kf[k >> 2][k & 3] = ld128(R, ko + k * 1024, (unsigned)s * 262144u); qf[k >> 2][k & 3] = ld128(R, qo + k * 1024, (unsigned)s * 262144u); }
  };
  if (!go_wait(R, go, lane) || !wait_flag_c<16>(R, lane == 4 ? kmf : OOB, lane == 4 ? 0 : -1, fm)) return;
  qk(par);
  for (int s = par; s < NSEG - 1; s += 2) {
    u32x4 km[4], qc[2][4];
    f32x4 T[2][2];
    float mx[2], sx[2];
#pragma unroll
    for (int qt = 0; qt < 2; ++qt) {
      T[qt][0] = T[qt][1] = (f32x4){0.f, 0.f, 0.f, 0.f};
#pragma unroll
      for (int ks = 0; ks < 4; ++ks) {
        T[qt][0] = mfma(kf[0][ks], qf[qt][ks], T[qt][0]);
        if (qt) T[1][1] = mfma(kf[1][ks], qf[1][ks], T[1][1]);
        qc[qt][ks] = qf[qt][ks];
      }
      px_mask(T[qt][0], T[qt][1], qt, lane);
      px_seg(T[qt][0], T[qt][1], mx[qt], sx[qt]);
      pin_v(T[qt][0]); pin_v(T[qt][1]); pin_v(mx[qt]); pin_v(sx[qt]);
    }
    VMW(0);
    st32((unsigned)(s - 2), R, ppf, 0);
    STEPRT(2, 0);
    const int need = lane < 4 ? min(2 * s + 5, 2 * NSEG - 1) : (lane == 4 ? s : -1);
    for (unsigned spins = 0; !__all(fm >= need); ++spins) {
      if (spins > SPINCAP) return;
      if (spins) __builtin_amdgcn_s_sleep(PXS);
      CBAR();
      fm = max(fm, (int)ld32(R, fm < need && (lane == 4 || __builtin_amdgcn_readlane(fm, 4) >= s) ? kmf : OOB, 0));
    }
    CBAR();
#pragma unroll
    for (int ks = 0; ks < 4; ++ks) km[ks] = ld128(R, kmo + ks * 1024, (unsigned)s * 131072u);
    SB();
    qk(clampseg(s + 2));
    CBAR();
#pragma unroll
    for (int qt = 0; qt < 2; ++qt) {
      f32x4 G = {0.f, 0.f, 0.f, 0.f};
#pragma unroll
      for (int ks = 0; ks < 4; ++ks) G = mfma(km[ks], qc[qt][ks], G);
      u32x4 px; unsigned pw;
      px_fin(G, T[qt][0], T[qt][1], mx[qt], sx[qt], px, pw, lane);
      st128(px, R, pxo, (unsigned)s * 81920u + qt * 40960);
      st32(pw, R, pmo, (unsigned)s * 81920u + qt * 40960);
    }
    CBAR();
    fm = (int)ld32(R, kmf, 0);
    CBAR();
  }
  VMW(0);
  st32((unsigned)(NSEG - 2 - par), R, ppf, 0);
  RT(7);
}

__device__ __forceinline__ void role_vuw(const Args& a, rsrc_t R, const Go& go, lds_char* lds, int h, int tid, int wave, int lane) {
  const int l15 = lane & 15, g = lane >> 4;
  u32x4 wv[2][18], wo[9][4];
  XL xl; x_init(xl, a.x, wave, lane);
  unsigned zo = OFF_ZF + (unsigned)(wave * 9 * 40960 + lane * 16) + h * 1024;
  const unsigned vpf = (lane == 0) ? (unsigned)(FL_UPROG + h * 128 + wave * 4) : OOB;
  const unsigned lb = (unsigned)(uintptr_t)lds, xw = lb + wave * 9216;
  lds_char *vr = lds + VX_OFF + lane * 16, *vw = vr + wave * 1024;
  f32x4 cc[2][2], av[2][2][2];
  u32x4 u1[9];
  TKD;
  {
    Go g0 = go;
    const i32x4 DV = wdesc(a.Wv + h * 128), DO = wdesc(a.Wo + (size_t)h * 128 * DM), D = mkdesc(a.wb);
    const unsigned wa = (unsigned)((16 * wave + g) * (HD * 2) + (l15 ^ (2 * g)) * 16), wb = wa ^ 128, ww = lb + wave * 4096, w1 = lb + wave * 1024;
    auto stage = [&](int j) {
      x_m0(xw, j); x_dma0(xl, 0, j);
      x_m0(xw + 36864, j); x_dma0(xl, 36864, j);
#pragma unroll
      for (int k = 0; k < 4; ++k) w_dma(DV, k < 2 ? wa : wb, (64 * j + 4 * k) * (HD * 2), ww, vbuf(j) + k * 1024);
      SB();
    };
#pragma unroll
    for (int j = 0; j < 3; ++j) stage(j);
    TK(0);
    unsigned oo[9], g1, g2;
#pragma unroll
    for (int i = 0; i < 9; ++i) { const int u = (4 * i + wave) * 64 + lane, r = u / 72; oo[i] = r * 1152 + ((u % 72) ^ (r & 6)) * 16; pin_v(oo[i]); }
    auto o_dma = [&](int k, int i0, int i1) {
#pragma unroll
      for (int i = i0; i < i1; ++i) w_dma(DO, oo[i], k * 36864, w1, ((k + 2) & 3) * 36864 + i * 4096);
      SB();
    };
    const XP x0r = x_at(lds, xl), x1r = x_at(lds + 36864, xl);
    lds_char* rp[2][2];
#pragma unroll
    for (int k = 0; k < 4; ++k) {
      rp[k >> 1][k & 1] = lds + ((k >> 1) ? VX_OFF : VB_OFF) + (g * 8 + (l15 >> 2)) * 256 + ((4 * wave + 2 * (k & 1)) ^ (2 * (l15 >> 2) + 8 * (g & 1))) * 16 + (l15 & 3) * 8;
      pin_v(rp[k >> 1][k & 1]);
    }
    asm volatile("s_load_dword %0, %1, %2\n\ts_waitcnt lgkmcnt(0)" : "=s"(g0.v0) : "s"(a.wb), "s"(__builtin_amdgcn_readfirstlane(go.vo)) : "memory");
    const unsigned pf = lane < 9 ? (unsigned)(FL_MPOI + (wave * 9 + lane) * 4) : OOB;
    TK(1);
    stage(3); stage(4);
    TK(2);
    auto vst = [&](auto JC) {
      constexpr int j = decltype(JC)::value, n = j == 0 ? 24 : (j < 4 ? 18 : (j < 6 ? 19 : (j < 7 ? 17 : 15))), hi = vbuf(j) == VX_OFF, bo = hi ? 0 : vbuf(j) - VB_OFF;
      asm volatile("s_waitcnt vmcnt(%0)" :: "n"(n) : "memory");
      __builtin_amdgcn_s_barrier(); CBAR(); SB();
      if (!(j & 1)) TK(3 + j / 2);
      u32x4 xb[2][4];
      u32x2 wh[2][4];
#pragma unroll
      for (int k = 0; k < 16; ++k) {
        const int kk = k >> 3, q = (k >> 1) & 3;
        if (k & 1) xb[kk][q] = XF((q & 2) ? x1r : x0r, q & 1, 2 * j + kk);
        else wh[kk][q] = trd(rp[hi][q >> 1] + bo + (kk * 32 + (q & 1) * 4) * 256);
        SB();
      }
      if (j == 3) { asm volatile("buffer_load_dword %0, %1, %2, 0 offen sc1" : "=v"(g1) : "v"(go.vo), "s"(D) : "memory"); SB(); }
      if (j == 7) {
        asm volatile("" : "+v"(g1));
        if (__all(g1 != g0.v0)) go_ack(R, g0, lane); else go_wait(R, g0, lane);
        asm volatile("buffer_load_dword %0, %1, %2, 0 offen sc1" : "=v"(g2) : "v"(pf), "s"(D) : "memory"); SB();
      }
      if (j == 0) {} else if (j < 5) stage(j + 4); else o_dma((j - 5) >> 1, (j & 1) ? 0 : 4, (j & 1) ? 4 : 9);
#pragma unroll
      for (int k = 0; k < 16; ++k) {
        const int kk = k >> 3;
        u32x4& f = wv[k & 1][2 * j + kk];
        if ((k & 7) < 2) { f = (u32x4){wh[kk][(k & 1) * 2][0], wh[kk][(k & 1) * 2][1], wh[kk][(k & 1) * 2 + 1][0], wh[kk][(k & 1) * 2 + 1][1]}; pin_a(f); }
        f32x4& d = av[(k >> 2) & 1][k & 1][(k >> 1) & 1];
        if (j == 0 && k < 8) d = mfA0(f, xb[kk][(k >> 1) & 3]); else mfA(d, f, xb[kk][(k >> 1) & 3]);
        SB();
      }
    };
#define CH(n) vst(std::integral_constant<int, n>())
    CH(0); CH(1); CH(2); CH(3); CH(4); CH(5); CH(6); CH(7); CH(8);
#undef CH
    BAR();
    TK(8);
    o_dma(2, 0, 9); o_dma(3, 0, 9);
    TK(9);
#pragma unroll
    for (int k = 0; k < 4; ++k) {
      const f32x4 (&e)[2][2] = av[k >> 1];
      const int tt = k & 1;
      *(lds_u32x4*)(vw + k * 4096) = (u32x4){pkv(e[0][tt][0], e[0][tt][1]), pkv(e[0][tt][2], e[0][tt][3]), pkv(e[1][tt][0], e[1][tt][1]), pkv(e[1][tt][2], e[1][tt][3])};
    }
    SB();
    lds_char* op[4];
#pragma unroll
    for (int k = 0; k < 4; ++k) {
      const int T = 9 * wave + k;
      op[k] = lds + VB_OFF + (4 * g + (l15 >> 2)) * 1152 + (T ^ ((2 * g + (l15 >> 3)) & 3)) * 32 + (l15 & 3) * 8;
      pin_v(op[k]);
    }
    f32x4 ua[18][2];
    auto ust = [&](auto KC) {
      constexpr int k = decltype(KC)::value;
      asm volatile("s_waitcnt vmcnt(%0) lgkmcnt(0)" :: "n"(k == 0 ? 29 : (k < 3 ? 18 : 9)) : "memory");
      __builtin_amdgcn_s_barrier(); CBAR(); SB();
      TK(10 + k);
      if (k == 2) {
#pragma unroll
        for (int i = 0; i < 4; ++i) x_move(op[i], -VB_OFF);
      }
      u32x4 f[4];
      u32x2 wh[9][2];
#pragma unroll
      for (int i = 0; i < 2; ++i) { f[i] = *(lds_u32x4*)(vr + i * 4096 + k * 1024); SB(); }
#pragma unroll
      for (int i = 0; i < 18; ++i) { wh[i >> 1][i & 1] = trd(op[(i >> 1) & 3] + (k & 1) * 36864 + (i & 1) * 18432 + (i >> 3) * 128); SB(); }
#pragma unroll
      for (int i = 2; i < 4; ++i) { f[i] = *(lds_u32x4*)(vr + i * 4096 + k * 1024); SB(); }
      if (k == 1) {
#pragma unroll
        for (int i = 0; i < 9; ++i) { x_m0(xw + VB_OFF, i); x_dma0(xl, 2 * 36864, i); }
        SB();
      }
      if (k == 2) {
        asm volatile("" : "+v"(g2));
        int c = (int)g2;
        wait_flag_c<4>(R, pf, 0, c); SB();
      }
#pragma unroll
      for (int t = 0; t < 19; ++t) {
        const int ct = t % 9;
        if (t < 9) { wo[ct][k] = (u32x4){wh[ct][0][0], wh[ct][0][1], wh[ct][1][0], wh[ct][1][1]}; if (ct < 7) pin_a(wo[ct][k]); else pin_v(wo[ct][k]); }
#pragma unroll
        for (int tt = 0; tt < 2; ++tt) {
          if (t == 18) break;
          f32x4& d = ua[t][tt];
          const u32x4& v = f[(t / 9) * 2 + tt];
          if (ct < 7) { if (k == 0) d = mfB0(v, wo[ct][k]); else mfB(d, v, wo[ct][k]); }
          else { if (k == 0) d = mf0(v, wo[ct][k]); else mfa(d, v, wo[ct][k]); }
          SB();
        }
        if (k == 3 && t > 0) {
          f32x4 (&e)[2] = ua[t - 1];
          if (t == 18) mf_wait2(e[0], e[1]);
          const u32x4 uo = {pkv(e[0][0], e[0][1]), pkv(e[0][2], e[0][3]), pkv(e[1][0], e[1][1]), pkv(e[1][2], e[1][3])};
          if (t < 10) st128(uo, R, zo, (t - 1) * 40960); else { u1[t < 10 ? 0 : t - 10] = uo; pin_v(u1[t < 10 ? 0 : t - 10]); }
          SB();
        }
      }
    };
    ust(std::integral_constant<int, 0>()); ust(std::integral_constant<int, 1>()); ust(std::integral_constant<int, 2>()); ust(std::integral_constant<int, 3>());
    asm volatile("" :: "v"(go.v0));
#pragma unroll
    for (int i = 0; i < 9; ++i) { x_m0(xw + VB_OFF + 36864, i); x_dma0(xl, 3 * 36864, i); }
    SB();
  }
  TK(14);
  XP rd0 = x_at(lds + VB_OFF, xl), rd1 = x_at(lds + VB_OFF + 36864, xl);
  unsigned b0 = VB_OFF, b1 = VB_OFF + 36864, b2 = 0;
  pin_v(vr); pin_v(vw); pin_v(zo);
  u32x4 bb[3][2], vf[2][2][4];
#pragma unroll
  for (int k = 0; k < 4; ++k) cc[k >> 1][k & 1] = (f32x4){0.f, 0.f, 0.f, 0.f};
#pragma unroll
  for (int k = 0; k < 16; ++k) asm volatile("" : "=v"(vf[k >> 3][(k >> 2) & 1][k & 3]));
  VMW(18);
  BAR();
#pragma unroll
  for (int ks = 0; ks < 2; ++ks) { bb[ks][0] = XF(rd0, 0, ks); bb[ks][1] = XF(rd0, 1, ks); }
  auto iter = [&](auto PC, int s) {
    constexpr int P = decltype(PC)::value & 1, W = 0, NOV = 0, NOU = decltype(PC)::value >> 1;
    XP& buf = P ? rd1 : rd0;
    XP& nrd = P ? rd0 : rd1;
    f32x4 (&c)[2][2] = av[P];
    f32x4 (&e)[2][2] = av[P ^ 1];
    u32x4 o, q;
    unsigned s1, s2, sx, xm, xd, sf;
#define VM(ks, t, tt) do { if (NOV) break; if ((ks) == 0) c[t][tt] = mfA0(wv[t][ks], bb[(ks) % 3][tt]); else mfA(c[t][tt], wv[t][ks], bb[(ks) % 3][tt]); SB(); } while (0)
#define UM(k, tt) do { if (NOU) break; f32x4& d = cc[(gq + P) & 1][tt]; const u32x4& f = vf[gq < 2 ? P : P ^ 1][tt][k];                                      \
                       if (ct < 7) { if ((k) == 0) d = mfB0(f, wo[ct][k]); else mfB(d, f, wo[ct][k]); }                                       \
                       else { if ((k) == 0) d = mf0(f, wo[ct][k]); else mfa(d, f, wo[ct][k]); } SB(); } while (0)
#define BRD(ks, tt) do { if (NOV) break; bb[(ks) % 3][tt] = (ks) < 18 ? XF(buf, tt, (ks) % 18) : XF(nrd, tt, (ks) % 18); SB(); } while (0)
#define CV(j) do { if (NOU) break; o[j] = pkv(cc[(gq + P + 1) & 1][(j) >> 1][((j) & 1) * 2], cc[(gq + P + 1) & 1][(j) >> 1][((j) & 1) * 2 + 1]); SB(); } while (0)
#define CE(j, tt) do { if (gq == 0 && !NOU) { q[j] = pkv(e[(j) >> 1][tt][((j) & 1) * 2], e[(j) >> 1][tt][((j) & 1) * 2 + 1]); SB(); } } while (0)
#define VR(j) do { if (gq == 1 && !NOU) { vf[P ^ 1][(j) & 1][(j) >> 1] = *(lds_u32x4*)(vr + P * 8192 + ((j) & 1) * 4096 + ((j) >> 1) * 1024); SB(); } } while (0)
#define LW(n) do { __builtin_amdgcn_s_waitcnt(0xC07F | ((n) << 8)); SB(); } while (0)
#pragma unroll
    for (int gq = NOV ? 2 : 0; gq < 9; ++gq) {
      const int ct = (gq + 7) % 9, k0 = 2 * gq, k1 = 2 * gq + 1, pt = (gq + 6) % 9;
      if (NOU || gq == 0 || gq > 2) LW(2); else if (gq == 1) LW(6); else LW(8);
      if (gq == 8 && !NOV) { __builtin_amdgcn_s_barrier(); SB(); }
      VM(k0, 0, 0); BRD(k0 + 2, 0);
      if (gq == (NOV ? 2 : 0)) {
        s1 = (unsigned)(s - 1) < NSEG ? (unsigned)(s - 1) * ZS : OOB; s2 = s >= 4 ? (unsigned)(s - 2) * ZS : OOB; sx = (unsigned)clampseg(s + 2) * 36864u;
        s1 = __builtin_amdgcn_readfirstlane(s1); s2 = __builtin_amdgcn_readfirstlane(s2); sx = __builtin_amdgcn_readfirstlane(sx);
        asm volatile("" : "+s"(s1), "+s"(s2), "+s"(sx)); SB();
      }
      UM(0, 0); CV(0);
      unsigned su = (gq < 3 ? s2 : s1) + pt * 40960;
      asm volatile("" : "+s"(su)); SB();
      VM(k0, 0, 1); BRD(k0 + 2, 1);
      if (!NOV && gq == 0) { sf = FG >= 7 ? (unsigned)(s - 2) : (s == 2 ? ~1u : (unsigned)(s - 3)); pin_v(sf); SB(); }
      if (!NOV && gq == 2) {
        xm = xw + b2; xd = b2 - b0;
        const unsigned t = b0; b0 = b1; b1 = b2; b2 = t;
        asm volatile("" : "+s"(xm), "+s"(xd), "+s"(b0), "+s"(b1), "+s"(b2)); SB();
      }
      UM(0, 1); CV(1);
      if (gq == 1 && !NOU) { LW(2); __builtin_amdgcn_s_barrier(); SB(); }
      VM(k0, 1, 0); VR(0); CV(2);
      UM(1, 0); VR(1); CV(3);
#define ST(n) do { if (!NOU && !NOST && STG0 + W * STGD == n) { st128(o, R, zo, su); SB(); } } while (0)
#define VW2(j, tt) do { if (gq == 0 && !NOU) { *(lds_u32x2*)(vw + P * 8192 + (tt) * 4096 + (j) * 4) = (u32x2){q[j], q[(j) + 1]}; SB(); } } while (0)
      ST(5);
      if (NOU && gq < 5) { st128(u1[gq < 5 ? 2 * gq : 0], R, zo, ZS + 2 * gq * 40960); SB(); }
      VM(k0, 1, 1); ST(6);
      UM(1, 1); CE(0, 0); CE(1, 0); VR(2); ST(7);
      if (gq == 1 && !NOU) LW(5); else LW(2);
      VM(k1, 0, 0); BRD(k1 + 2, 0); CE(2, 0); ST(8);
      UM(2, 0); VW2(0, 0); VR(3); ST(9);
      if (!NOV && gq >= 2 && gq < 7) { x_m0(xm, gq - 2); SB(); }
      if (!NOV && gq == 8) { x_move(buf.p[0], xd); x_move(buf.p[1], xd); SB(); }
      VM(k1, 0, 1); BRD(k1 + 2, 1); CE(3, 0); ST(10);
      UM(2, 1); VW2(2, 0); VR(4); ST(11);
      if (!NOV && gq >= 2 && gq < 7) { x_dma(xl, sx, gq - 2); SB(); }
      VM(k1, 1, 0); VR(5); ST(12);
      CE(0, 1); CE(1, 1);
      if (!NOV && gq >= 3 && gq < 7) { x_m0(xm, gq + 2); SB(); }
      UM(3, 0); VW2(0, 1); VR(6); ST(13);
      if (!NOV && gq == 0) { PADE(); if (P) PADS(113); else PADS(112); } else PAD();
      SB();
      VM(k1, 1, 1); CE(2, 1); CE(3, 1); VR(7); ST(14);
      if (!NOV && gq >= 3 && gq < 7) { x_dma(xl, sx, gq + 2); SB(); }
      UM(3, 1); VW2(2, 1); ST(15);
      if (NOU && gq < 4) { st128(u1[gq < 4 ? 2 * gq + 1 : 0], R, zo, ZS + (2 * gq + 1) * 40960); SB(); }
#undef ST
#undef VW2
      if (NOU && gq == 7) { VMW(18); SB(); }
      if (!NOU && P && gq == EG1 && UNLIKELY(s == 3)) { asm volatile("s_waitcnt vmcnt(%0)" :: "n"(EG1 + 7) : "memory"); st32(sf, R, vpf, 0); SB(); }
      if (!NOU && gq == FG && !NOFL) {
        asm volatile("s_waitcnt vmcnt(%0)" :: "n"(FG >= 7 ? FG + 7 : (FG < 2 ? FG + 16 : 20)) : "memory");
        st32(sf, R, vpf, 0); SB();
      }
      if (!NOU && gq == 7 && FG != 7) { VMW(20); SB(); }
      if (!NOU && gq == EG && UNLIKELY((unsigned)(s - 4) < UEF || s >= NSEG)) { asm volatile("s_waitcnt vmcnt(%0)" :: "n"(3 * EG - 5) : "memory"); st32(sf, R, vpf, 0); SB(); }
    }
#undef VM
#undef UM
#undef BRD
#undef CV
#undef CE
#undef VR
#undef LW
  };
  iter(std::integral_constant<int, 2>(), 2);
  RT(8);
  TKS;
#pragma nounroll
  for (int s = 3;; s += 2) {
    iter(std::integral_constant<int, 1>(), s);
    if (s == NSEG + 1) break;
    iter(std::integral_constant<int, 0>(), s + 1);
  }
  PADE();
  VMW(0);
  RT(7);
}

#define RING_OFF (2 * STG_BYTES)
#define RING_STR 456
#define OST_OFF (RING_OFF + 64 * RING_STR)
__device__ __forceinline__ float dot2(unsigned a, unsigned b, float c) {
  return __builtin_amdgcn_fdot2_f32_bf16(__builtin_bit_cast(bf16x2, a), __builtin_bit_cast(bf16x2, b), c, false);
}
__device__ __forceinline__ float swp1(float x) { return __uint_as_float(__builtin_amdgcn_update_dpp(0u, __float_as_uint(x), 0xB1, 0xf, 0xf, false)); }
__device__ __forceinline__ void role_aw(const Args& a, rsrc_t R, const Go& go, lds_char* lds, int ai, int tid, int wave, int lane) {
  const int l15 = lane & 15, g = lane >> 4;
  const int ga = ai & 7, rr = ai >> 3;
  u32x4 w[4][18];
  {
    u32x4 sg[3][7];
    unsigned gv[9], wo[7], lo[7];
    const rsrc_t W = __builtin_amdgcn_make_buffer_rsrc((void*)a.wqkv, 0, DM * 1728 * 2, 0x00020000);
    const rsrc_t G = __builtin_amdgcn_make_buffer_rsrc((void*)a.gw, 0, DM * 2, 0x00020000);
#pragma unroll
    for (int k = 0; k < 7; ++k) {
      const int u = tid + 256 * k, row = u / 27, cu = u % 27;
      wo[k] = row < 64 ? (unsigned)(row * 3456 + (cu / 9) * 1152 + ga * 144 + (cu % 9) * 16) : OOB;
      lo[k] = row < 64 ? (unsigned)(row * 544 + cu * 16) : (unsigned)((tid & 63) * 544 + 448);
    }
    auto gl = [&](u32x4 (&st)[7], int c) {
#pragma unroll
      for (int k = 0; k < 7; ++k) st[k] = __builtin_amdgcn_raw_buffer_load_b128(W, wo[k], (unsigned)c * (64u * 3456u), (NTM & 16) ? 2 : 0);
    };
#pragma unroll
    for (int c = 0; c < 9; ++c) gv[c] = __builtin_amdgcn_raw_buffer_load_b16(G, (unsigned)(tid & 63) * 2, (unsigned)c * 128u, 0);
    gl(sg[0], 0); if (ASLP > 0) __builtin_amdgcn_s_sleep(ASLP);
    gl(sg[1], 1); if (ASLP > 0) __builtin_amdgcn_s_sleep(ASLP);
    gl(sg[2], 2);
#pragma unroll
    for (int c = 0; c < 9; ++c) {
      lds_char* buf = lds + (c & 1) * STG_BYTES;
#pragma unroll
      for (int k = 0; k < 7; ++k) *(lds_u32x4*)(buf + lo[k]) = sg[c % 3][k];
      *(lds_u32x4*)(buf + (tid & 63) * 544 + 432) = (u32x4){gv[c], 0u, 0u, 0u};
      if (ASLP > 0) __builtin_amdgcn_s_sleep(ASLP);
      if (c + 3 < 9) gl(sg[c % 3], c + 3);
      BAR();
#pragma unroll
      for (int kk = 0; kk < 2; ++kk)
#pragma unroll
        for (int t = 0; t < 4; ++t) {
          lds_char* p = buf + (kk * 32 + g * 8 + (l15 >> 2)) * 544 + (wave * 64 + t * 16) * 2 + (l15 & 3) * 8;
          u32x2 v0 = trd(p), v1 = trd(p + 4 * 544);
          w[t][2 * c + kk] = (u32x4){v0[0], v0[1], v1[0], v1[1]};
        }
      SB();
#pragma unroll
      for (int k = 0; k < 8; ++k) { if (c < 7) pin_a(w[k & 3][2 * c + (k >> 2)]); else pin_v(w[k & 3][2 * c + (k >> 2)]); }
    }
    BAR();
  }
  RT(1);
  go_wait(R, go, lane);
  auto sgm = [&](int it) { const int j = it / 5; return min(16 * j + 4 * rr + it - 5 * j - 1, NSEG - 1); };
  const int i0 = rr > 0 ? 0 : 1;
  u32x4 xr[9];
  XL xl; x_init(xl, a.x, wave, lane);
  x_gload(xr, xl, sgm(i0));
  x_swrite(xr, lds + i0 * STG_BYTES, xl);
  x_gload(xr, xl, sgm(i0 + 1));
  lds_char* ring = lds + RING_OFF;
  lds_char* ost = lds + OST_OFF;
  const int t = tid >> 3, hh = (tid >> 1) & 3, half = tid & 1;
  const float sc = 0.23570226039551584f * 1.4426950408889634f;
  auto done = [&](int s) { __builtin_amdgcn_raw_ptr_buffer_atomic_add_i32(1, R, tid == 0 && s >= 0 ? (unsigned)(FL_APROG + s * 128) : OOB, 0, SC1); };
  const unsigned gto = (ga == 0 && wave == 3 && g == 2) ? OFF_GATE + l15 * 4 : OOB;
  int sp = -1;
  for (int it = i0; it < 20; ++it) {
    const int s = sgm(it);
    BAR();
    {
      f32x4 acc[4][2];
      proj4(lds + (it & 1) * STG_BYTES, lds + ((it + 1) & 1) * STG_BYTES, xr, xl, sgm(it + 2), wave, lane, w[0], w[1], w[2], w[3], acc);
      VMW(9);
#pragma unroll
      for (int tl = 0; tl < 4; ++tl)
#pragma unroll
        for (int tt = 0; tt < 2; ++tt) {
          const int col = wave * 64 + tl * 16 + g * 4;
          u32x2 o = {pk2(acc[tl][tt][0], acc[tl][tt][1]), pk2(acc[tl][tt][2], acc[tl][tt][3])};
          if (col < 216) *(__attribute__((address_space(3))) u32x2*)(ring + (((s & 1) * 32 + tt * 16 + l15) * RING_STR) + col * 2) = o;
        }
#pragma unroll
      for (int tt = 0; tt < 2; ++tt) {
        float z = acc[1][tt][0];
        float gv = __builtin_amdgcn_rcpf(1.f + __builtin_amdgcn_exp2f(-1.4426950408889634f * z));
        st32(__float_as_uint(gv), R, gto + tt * 64, (unsigned)s * 128u);
      }
    }
    BAR();
    done(sp);
    sp = it % 5 != 0 ? s : -1;
    { const int s = it; STEPRT(2, 0); }
    if (it % 5 != 0) {
      const int row = s * 32 + t;
      unsigned q[9];
      lds_char* qp = ring + (row & 63) * RING_STR + hh * 36;
#pragma unroll
      for (int j = 0; j < 9; ++j) q[j] = *(lds_u32*)(qp + j * 4);
      float lg[4];
      lds_char* vp[4];
#pragma unroll
      for (int wi = 0; wi < 4; ++wi) {
        const int r2 = row - 2 * (half * 4 + wi);
        const bool ok = r2 >= 0;
        lds_char* kp = ring + ((ok ? r2 : row) & 63) * RING_STR + hh * 36 + 144;
        vp[wi] = kp + 144;
        float d = 0.f;
#pragma unroll
        for (int j = 0; j < 9; ++j) d = dot2(q[j], *(lds_u32*)(kp + j * 4), d);
        lg[wi] = ok ? d : -__builtin_inff();
      }
      float m = fmaxf(fmaxf(lg[0], lg[1]), fmaxf(lg[2], lg[3]));
      m = fmaxf(m, swp1(m));
      const float ms = -m * sc;
      float sum = 0.f;
#pragma unroll
      for (int wi = 0; wi < 4; ++wi) { lg[wi] = __builtin_amdgcn_exp2f(fmaf(lg[wi], sc, ms)); sum += lg[wi]; }
      sum += swp1(sum);
      const float inv = __builtin_amdgcn_rcpf(sum);
      const unsigned p01 = pk2(lg[0] * inv, lg[1] * inv), p23 = pk2(lg[2] * inv, lg[3] * inv);
#pragma unroll
      for (int j = 0; j < 9; ++j) {
        const unsigned v0 = *(lds_u32*)(vp[0] + j * 4), v1 = *(lds_u32*)(vp[1] + j * 4), v2 = *(lds_u32*)(vp[2] + j * 4), v3 = *(lds_u32*)(vp[3] + j * 4);
        float lo = dot2(__builtin_amdgcn_perm(v1, v0, 0x05040100u), p01, 0.f);
        lo = dot2(__builtin_amdgcn_perm(v3, v2, 0x05040100u), p23, lo);
        float hi = dot2(__builtin_amdgcn_perm(v1, v0, 0x07060302u), p01, 0.f);
        hi = dot2(__builtin_amdgcn_perm(v3, v2, 0x07060302u), p23, hi);
        lo += swp1(lo); hi += swp1(hi);
        if ((j & 1) == half) *(lds_u32*)(ost + t * 144 + hh * 36 + j * 4) = pk2(lo, hi);
      }
      BAR();
#pragma unroll
      for (int k = 0; k < 2; ++k) {
        const int u = tid + 256 * k;
        const bool ok = u < 288;
        const int ur = u & 31, uc = ok ? u >> 5 : 0, cc = ga * 9 + uc;
        u32x4 o = *(lds_u32x4*)(ost + ur * 144 + uc * 16);
        st128(o, R, ok ? OFF_OA + (unsigned)(((ur >> 4) * 18 + (cc >> 2)) * 1024 + ((cc & 3) * 16 + (ur & 15)) * 16) : OOB, (unsigned)s * 36864u);
      }
      CBAR();
    }
  }
  VMW(0);
  BAR();
  done(sp);
  RT(7);
}

__device__ __forceinline__ void role_out(const Args& a, rsrc_t R, const Go& go, lds_char* lds, int o, int tid, int wave, int lane) {
  const int l15 = lane & 15, g = lane >> 4;
  const int cb = o % 9, oj = o / 9, sw = cb == 8 ? OSW : 0, grp = oj < 4 ? oj : (oj == 4 ? sw + 4 : NSEG - 16 + oj);
  auto nxt = [&](int s) { return s < sw ? s + 4 : (s < NSEG - 16 ? s + 5 : s + 8); };
  u32x4 wf[4][5];
  {
    u32x4 st[18];
#pragma unroll
    for (int k = 0; k < 18; ++k) { int u = tid + 256 * k; st[k] = *(const u32x4*)(a.wo + (size_t)(u >> 3) * DM + cb * 64 + (u & 7) * 8); }
#pragma unroll
    for (int k = 0; k < 18; ++k) { int u = tid + 256 * k; *(lds_u32x4*)(lds + (u >> 3) * 144 + (u & 7) * 16) = st[k]; }
    BAR();
#pragma unroll
    for (int i = 0; i < 5; ++i)
#pragma unroll
      for (int ct = 0; ct < 4; ++ct) {
        const int ks = wave + 4 * i;
        lds_char* p = lds + ((ks < 18 ? ks : 17) * 32 + g * 8 + (l15 >> 2)) * 144 + ct * 32 + (l15 & 3) * 8;
        u32x2 v0 = trd(p), v1 = trd(p + 4 * 144);
        wf[ct][i] = (ks < 18) ? (u32x4){v0[0], v0[1], v1[0], v1[1]} : (u32x4){0u, 0u, 0u, 0u};
        pin_a(wf[ct][i]);
      }
    BAR();
  }
  const unsigned zb = OFF_ZF + (unsigned)(4 * cb) * 40960u + lane * 16;
  const unsigned flk = lane == 32 ? FL_KMPROG + ((o * 4 + wave) & 31) * 128 : OOB;
  const bool isu = (lane & 24) == 8 && lane < 48;
  auto faddr = [&](int s) -> unsigned {
    return lane < 8 ? (lane == 0 ? FL_APROG + s * 128 : OOB)
                    : (isu ? FL_UPROG + (wave * 8 + (lane & 7)) * 128 + ((cb * 64 + (lane < 16 ? 0 : 63)) / 144) * 4
                           : (lane < 32 ? (s == NSEG - 1 ? FL_PXLAST + (wave * 16 + lane - 16) * 128 : FL_PXPROG + (wave * 8 + (lane & 7)) * 128 + (s & 1) * 4)
                                        : (s + 3 < NSEG ? flk : FL_YPROG + (wave * 8 + (lane & 7)) * 16 + ((cb * 64 + (lane < 48 ? 0 : 63)) / 144) * 4 + (lane < 56 ? 0 : OOB))));
  };
  const int t = tid >> 3, n8 = tid & 7;
  const rsrc_t O = __builtin_amdgcn_make_buffer_rsrc(a.out, 0, S_LEN * DM * 2, 0x00020000);
  const i32x4 D = mkdesc(a.wb);
  int fv = -1;
  RT(1);
  const unsigned ofo = OFF_OA + (unsigned)(wave * 1024 + lane * 16), ofl = wave < 2 ? ofo : OOB, gto = OFF_GATE + l15 * 4;
  const unsigned pfo = OFF_PXF + (unsigned)(wave * 8192 + lane * 16), pfy = OFF_PXF + (unsigned)((24 + wave * 2) * 1024 + lane * 16);
  u32x4 zu[4][8];
  int fu = -1;
  const unsigned fua = isu ? faddr(0) : OOB;
  const unsigned zw = zb + wave * 8192;
#pragma unroll
  for (int k = 0; k < 32; ++k) asm volatile("" : "=a"(zu[k >> 3][k & 7]));
  auto zu4 = [&](unsigned vo, int s, int hh) {
#pragma unroll
    for (int ct = 0; ct < 4; ++ct)
      asm volatile("buffer_load_dwordx4 %0, %1, %2, %3 offen offset:%4 sc1" : "+a"(zu[ct][hh])
                   : "v"(vo), "s"(D), "s"((unsigned)(s & RMASK) * ZS + ct * 40960 + (hh >> 2) * 4096), "n"((hh & 3) * 1024) : "memory");
  };
  auto zuld = [&](int s) -> bool {
    unsigned spins = 0;
    while (ballot(isu && fu < s) != 0) {
      if (++spins > SPINCAP) return false;
      if (spins > 1) __builtin_amdgcn_s_sleep(16);
      if (s == NSEG - 1 && spins > 1) __builtin_amdgcn_s_sleep(100);
      CBAR();
      fu = (int)ld32(R, fua, 0);
    }
#pragma unroll
    for (int hh = 0; hh < 8; ++hh) zu4(zw, s, hh);
    SB();
    return true;
  };
  if ((o < 44 && o != 40 && !go_wait(R, go, lane)) || !zuld(grp)) return;
  unsigned acc_t[8] = {0, 0, 0, 0, 0, 0, 0, 0}, t0 = 0, t1 = 0, t2 = 0, t3 = 0, t4 = 0, t5 = 0, t6 = 0;
  for (int s = grp; s < NSEG; s = nxt(s)) {
    TS(t0);
    const unsigned fa = faddr(s);
    const int need = isu || fa >= OOB ? -1 : (lane == 0 ? 7 : ((lane >= 32 && s + 3 < NSEG) ? s + 2 : s));
    const bool far = lane >= 8 && lane < 48 && (isu || (lane < 32 && s < NSEG - 1));
    unsigned spins = 0;
    while ((ballot(fv < need) & 0xFFull) != 0) {
      if (++spins > SPINCAP) return;
      if (spins > 1) __builtin_amdgcn_s_sleep(32);
      CBAR();
      const int nv = (int)lda(R, fv < need && lane < 8 ? fa : OOB);
      fv = lane < 8 ? max(fv, nv) : fv;
    }
    TS(t1);
#define OLD(d, vo, so, o) asm volatile("buffer_load_dwordx4 %0, %1, %2, %3 offen offset:%4 sc1" : "=v"(d) : "v"(vo), "s"(D), "s"(so), "n"(o) : "memory")
    u32x4 zy[4][2], pf[2][10], of[2][5];
    float gt[2];
#pragma unroll
    for (int qt = 0; qt < 2; ++qt) {
#pragma unroll
      for (int i = 0; i < 5; ++i) OLD(of[qt][i], i < 4 ? ofo : ofl, (unsigned)s * 36864u + qt * 18432 + i * 4096, 0);
      asm volatile("buffer_load_dword %0, %1, %2, %3 offen offset:%4 sc1" : "=v"(gt[qt]) : "v"(gto), "s"(D), "s"((unsigned)s * 128u), "n"(qt * 64) : "memory");
    }
    SB();
#define FWAIT(m) do { spins = 0;                                                                                          \
      while ((ballot(fv < need) & (m)) != 0) {                                                                            \
        if (++spins > SPINCAP) return;                                                                                    \
        if (spins > 1) { if (s < OFAST) __builtin_amdgcn_s_sleep(OSLP); else __builtin_amdgcn_s_sleep(OSL2); }            \
        CBAR();                                                                                                           \
        const int n0 = (int)lda(R, fv < need && !far ? fa : OOB), n1 = (int)ld32(R, fv < need && far ? fa : OOB, 0), nv = far ? n1 : n0;                                                             \
        fv = max(fv, nv);                                                                                                 \
      } } while (0)
#define ZYLD(zv) do { _Pragma("unroll") for (int hp = 0; hp < 2; ++hp) _Pragma("unroll") for (int ct = 0; ct < 4; ++ct)                        \
                        asm volatile("buffer_load_dwordx4 %0, %1, %2, %3 offen offset:%4 sc1" : "+v"(zy[ct][hp])                                   \
                                     : "v"(zv), "s"(D), "s"((unsigned)(s & RMASK) * ZS + ct * 40960 + 32768 + wave * 2048), "n"(hp * 1024) : "memory"); } while (0)
#define MFS(hh) do { _Pragma("unroll") for (int qt = 0; qt < 2; ++qt) _Pragma("unroll") for (int ct = 0; ct < 4; ++ct) {                           \
                        ab[ct][qt] = mfma(hh < 8 ? zu[ct][hh & 7] : zy[ct][hh & 1], pf[qt][hh], hh ? ab[ct][qt] : (f32x4){0.f, 0.f, 0.f, 0.f}); SB(); } } while (0)
    TS(t2);
#pragma unroll
    for (int k = 0; k < 8; ++k) asm volatile("" : "=v"(zy[k >> 1][k & 1]));
    bool yr = false;
#define ZYW() asm volatile("s_waitcnt vmcnt(0)" : "+v"(zy[0][0]), "+v"(zy[0][1]), "+v"(zy[1][0]), "+v"(zy[1][1]), "+v"(zy[2][0]), "+v"(zy[2][1]), "+v"(zy[3][0]), "+v"(zy[3][1]) :: "memory")
#define ZYTRY(w) do { if (!yr && (ballot(fv < need) >> 32) == 0) { ZYLD(zb); if (w) ZYW(); yr = true; } } while (0)
    spins = 0;
#pragma nounroll
    while ((unsigned)ballot(fv < need) != 0) {
      if (++spins > SPINCAP) return;
      if (spins > 1) { if (s < OFAST) __builtin_amdgcn_s_sleep(OSLP); else __builtin_amdgcn_s_sleep(OSL2); }
      CBAR();
      const int nv = (int)ld32(R, fv < need ? fa : OOB, 0);
      CBAR();
      ZYTRY(1);
      fv = max(fv, nv);
    }
    TS(t3);
#pragma unroll
    for (int hh = 0; hh < 10; ++hh)
#pragma unroll
      for (int qt = 0; qt < 2; ++qt) OLD(pf[qt][hh], hh < 8 ? pfo : pfy, (unsigned)(s & RMASK) * 81920u + qt * 40960 + (hh >> 2) * 4096, (hh & 3) * 1024);
    ZYTRY(0);
    const int sn = nxt(s) < NSEG ? nxt(s) : s;
    int n1;
    asm volatile("buffer_load_dword %0, %1, %2, 0 offen sc1" : "=v"(n1) : "v"(faddr(sn)), "s"(D) : "memory");
    const bool uok = sn != s && ballot(isu && fu < sn) == 0;
    unsigned zn = uok ? zw : OOB;
    pin_v(zn);
    asm volatile("s_waitcnt vmcnt(21)" : "+v"(of[0][0]), "+v"(of[0][1]), "+v"(of[0][2]), "+v"(of[0][3]), "+v"(of[0][4]), "+v"(of[1][0]), "+v"(of[1][1]),
                 "+v"(of[1][2]), "+v"(of[1][3]), "+v"(of[1][4]), "+v"(gt[0]), "+v"(gt[1]) :: "memory");
    SB();
    f32x4 aa[4][2];
#pragma unroll
    for (int ct = 0; ct < 4; ++ct)
#pragma unroll
      for (int qt = 0; qt < 2; ++qt) {
        f32x4 acc = {0.f, 0.f, 0.f, 0.f};
#pragma unroll
        for (int i = 0; i < 5; ++i) acc = mfma(wf[ct][i], of[qt][i], acc);
        aa[ct][qt] = acc * gt[qt];
        pin_v(aa[ct][qt]);
      }
    SB();
    f32x4 ab[4][2];
#pragma unroll
    for (int hh = 0; hh < 8; ++hh) {
      asm volatile("s_waitcnt vmcnt(%2)" : "+v"(pf[0][hh]), "+v"(pf[1][hh]) : "n"(19 + 2 * hh) : "memory");
      SB();
      MFS(hh);
      zu4(zn, sn, hh);
      SB();
    }
    TS(t4);
    asm volatile("s_waitcnt vmcnt(32)" : "+v"(n1), "+v"(pf[0][8]), "+v"(pf[1][8]), "+v"(pf[0][9]), "+v"(pf[1][9]) :: "memory");
    fu = isu ? n1 : fu;
    if (sn != s && !uok && !zuld(sn)) return;
    if (!yr) { FWAIT(~0ull); ZYLD(zb); ZYW(); }
#pragma unroll
    for (int k = 0; k < 8; ++k) pin_v(zy[k >> 1][k & 1]);
    fv = n1;
    SB();
    MFS(8); MFS(9);
    TS(t5);
#undef OLD
#undef ZYTRY
#undef ZYW
#undef FWAIT
#undef ZYLD
#undef MFS
#pragma unroll
    for (int qt = 0; qt < 2; ++qt)
#pragma unroll
      for (int ct = 0; ct < 4; ++ct)
        *(lds_f32x4*)(lds + wave * 8704 + (qt * 16 + l15) * 272 + (ct * 16 + g * 4) * 4) = aa[ct][qt] + ab[ct][qt] * (1.f - gt[qt]);
    BAR();
    f32x4 b0 = *(lds_f32x4*)(lds + t * 272 + n8 * 32), b1 = *(lds_f32x4*)(lds + t * 272 + n8 * 32 + 16);
#pragma unroll
    for (int w = 1; w < 4; ++w) { b0 += *(lds_f32x4*)(lds + w * 8704 + t * 272 + n8 * 32); b1 += *(lds_f32x4*)(lds + w * 8704 + t * 272 + n8 * 32 + 16); }
    u32x4 ov = {pk2(b0[0], b0[1]), pk2(b0[2], b0[3]), pk2(b1[0], b1[1]), pk2(b1[2], b1[3])};
    __builtin_amdgcn_raw_buffer_store_b128(ov, O, (unsigned)((s * 32 + t) * DM + cb * 64 + n8 * 8) * 2, 0, OAUX);
    BAR();
    if (s < 4) RT(2);
    TS(t6); ACC(0, 4 * (t1 - t0)); ACC(1, 4 * (t2 - t1)); ACC(2, 4 * (t3 - t2)); ACC(3, 4 * (t4 - t3)); ACC(4, 4 * (t5 - t4)); ACC(5, 4 * (t6 - t5));
    STEPRT(0, 0);
  }
  for (int k = 0; k < 8; ++k) SV(16 + k, acc_t[k]);
  RT(7);
}

#define LDS_BYTES 163840
__device__ __forceinline__ void code_touch(u64 pc, unsigned len, unsigned off, unsigned ld) {
  const i32x4 P = {(int)(unsigned)pc, (int)(unsigned)(pc >> 32), (int)len, 0x00020000};
  asm volatile("s_mov_b32 m0, %2\n\ts_nop 0\n\tbuffer_load_dword %0, %1, 0 offen lds" :: "v"(off), "s"(P), "s"(ld) : "memory");
}
__global__ void __launch_bounds__(256, 1) mega(Args a) {
  __shared__ __attribute__((aligned(16))) char smem[LDS_BYTES];
  lds_char* lds = (lds_char*)smem;
  const int b = blockIdx.x, tid = threadIdx.x, lane = tid & 63, wave = __builtin_amdgcn_readfirstlane(tid >> 6);
  const int x = b & 7, sl = b >> 3;
  const rsrc_t R = __builtin_amdgcn_make_buffer_rsrc(a.wb, 0, (int)OFF_END, 0x00020000);
  const Go go = go_look(R, b + wave * 4);
  if (b == 244) go_lead(R, tid, wave, lane);
  u64 pc; unsigned len;
  const unsigned tst = rt_now();
  {
    asm volatile("s_getpc_b64 %0\n.Lcode_here:\n\ts_mov_b32 %1, zz_code_end-.Lcode_here" : "=s"(pc), "=s"(len));
    if (wave == CPF) code_touch(pc, len, (unsigned)(sl * 8192 + lane * 128), (unsigned)(uintptr_t)lds + (x >= 4 && sl >= 8 && sl < 16 ? VB_OFF + 16384 : 153600));
  }
  RT(0);
  if (wave == 3 && (sl == 24 || sl == 29)) {
    unsigned d[4];
#pragma unroll
    for (int k = 0; k < 3; ++k) d[k] = ld32(R, (unsigned)(k * 64 + lane) << 20, 0);
    d[3] = *(const unsigned*)(a.out + min(lane, 2) * (1 << 19));
    asm volatile("" :: "v"(d[0]), "v"(d[1]), "v"(d[2]), "v"(d[3]));
  }
  if (x < 4 ? sl >= 25 : sl >= 16) {
    const unsigned t0 = rt_now();
    const int nw = x < 4 ? 28 : 56, id = __builtin_amdgcn_readfirstlane((sl - (x < 4 ? 25 : 16)) * 4 + wave);
    const int fa = x < 4 ? HF1 : HF3, fb = x < 4 ? HF0 : HF2, na = 6 * (18 - fa);
    unsigned d = 0;
    for (int e = id < nw ? id : 999; e < na + 3 * (18 - fb); e += nw) {
      const int j = e % 3, q = e / 3, blk = e < na ? fa + (q >> 1) : q - 2 * (18 - fa) + fb;
      const u16* W = e < na && (q & 1) ? a.Wk : ((e < na) == (x < 4) ? a.Wq : a.Wv);
      const int c = (int)((u64)W >> 8), hh = j < 2 ? (x & 3) * 8 + ((1 - c) & 3) + 4 * j : (31 - c) & 31;
      if ((hh >> 3) != (x & 3) || !HLP) continue;
      const u16* p = W + (size_t)(blk * 32 + (lane >> 1)) * HD + hh * 128 + (lane & 1) * 64;
      asm volatile("global_load_dword %0, %1, off nt" : "+v"(d) : "v"(p) : "memory");
    }
    for (int k = 0; k < GSL; ++k) __builtin_amdgcn_s_sleep(16);
    for (int e = id < nw ? id + 16 * (x < 4 ? G0 : G1) : 999; e < 576; e += nw) {
      const int t = e >> 4, k = e & 7, rb = e & 8, late = t >= 18, blk = x < 4 && !rb ? t >> 1 : t - 18 * late;
      const u16* W = rb ? a.Wv : ((x < 4 ? t & 1 : !late) ? a.Wk : a.Wq);
      const u16* p = rb && late == (x >= 4) ? a.Wo + (size_t)((x & 3) * 8 + k) * (128 * DM) + (blk * 64 + lane) * 64
                                            : W + (size_t)(blk * 32 + k * 4 + (lane >> 4)) * HD + (x & 3) * 1024 + (lane & 15) * 64;
      if (GM >> (rb * 2 / 8 + (x >> 2)) & 1) asm volatile("global_load_dword %0, %1, off nt" : "+v"(d) : "v"(p) : "memory");
    }
    asm volatile("s_waitcnt vmcnt(0)" : "+v"(d) :: "memory");
    if (x >= 4 && sl >= 20 && sl < 30) {
#pragma unroll
      for (int m = 0; m < 2; ++m) {
        const bool xm = (m == 0) == (XPF < APF);
        if (!(xm ? XPF : APF)) continue;
        for (int k = 0; k < 64 && rt_now() - t0 < (xm ? XPF : APF); ++k) __builtin_amdgcn_s_sleep(8);
        for (int e = (id - 16) * 4 + (x & 3); e < (xm ? 288 - XP0 : 243); e += 160)
          asm volatile("global_load_dword %0, %1, off" : "+v"(d) : "v"((xm ? a.x + XP0 * 4096 : a.wqkv) + e * 4096 + lane * 64) : "memory");
      }
      asm volatile("s_waitcnt vmcnt(0)" : "+v"(d) :: "memory");
      RT(13);
    }
    if (sl >= 20) for (int k = 0; k < 64 && rt_now() - t0 < (x < 4 || sl == 31 ? AWT : XPT); ++k) __builtin_amdgcn_s_sleep(8);
    if (AL2 && x < 4) {
      for (int e = id; e < 243; e += 28) asm volatile("global_load_dword %0, %1, off" : "+v"(d) : "v"(a.wqkv + (e * 64 + lane) * 64) : "memory");
      asm volatile("s_waitcnt vmcnt(0)" : "+v"(d) :: "memory");
    }
  }
  if (x < 4 && wave == 1 && sl >= 16 && sl < 24) calibrate(R, go, x + (sl & 4), sl & 3, lane);
  if (CT0 && sl >= 16 && (x < 4 ? wave == 1 && sl < 24 : sl < 18)) {
    for (int k = 0; k < 200 && rt_now() - tst < (x < 4 ? CT0 : CT1); ++k) __builtin_amdgcn_s_sleep(8);
#pragma unroll
    for (int k = 0; k < 4; ++k) code_touch(pc, len, (unsigned)((k * 8 + (x < 4 ? sl - 16 : (sl - 16) * 4 + wave)) * 8192 + lane * 128), (unsigned)(uintptr_t)lds + 153600);
    VMW(0);
  }
  if (x < 4) {
    if (sl < 16) for (int k = 0; k < QDL; ++k) __builtin_amdgcn_s_sleep(32);
    if (sl < 8) role_qkc(a, R, go, lds, x * 8 + sl, tid, wave, lane);
    else if (sl < 16) role_vcu(a, R, go, lds, x * 8 + sl - 8, tid, wave, lane);
    else if (sl < 25) role_mix(a, R, go, x * 9 + sl - 16, wave, lane);
    else role_aw(a, R, go, lds, x * 8 + sl - 25, tid, wave, lane);
  } else {
    const int y = x - 4;
    if (sl < 8) {
      const int h = y * 8 + ((sl + HSH) & 7);
      role_prj(a, R, go, lds, a.Wk + h * 128, a.Wq + h * 128, OFF_KF + h * 8192u, OFF_QF + h * 8192u, FL_PROG + h * 128, tid, wave, lane);
    } else if (sl < 16) role_vuw(a, R, go, lds, y * 8 + sl - 8, tid, wave, lane);
    else if (sl < 20) { role_px(a, R, go, y * 4 + sl - 16, wave, lane); __builtin_amdgcn_s_waitcnt(0x0F70); }
    else {
      if (sl == 31) { role_aw(a, R, go, lds, y * 8 + 7, tid, wave, lane); __builtin_amdgcn_s_waitcnt(0x0F70); }
      if (sl == 29 && !XPF) {
        const rsrc_t X = __builtin_amdgcn_make_buffer_rsrc((void*)a.x, 0, S_LEN * DM * 2, 0x00020000);
#pragma unroll 1
        for (int k = 0; k < 3; ++k) {
          unsigned t[12];
#pragma unroll
          for (int j = 0; j < 12; ++j) t[j] = __builtin_amdgcn_raw_buffer_load_b32(X, (unsigned)(((k * 12 + j) * 16 + y * 4 + wave) * 64 + lane) * 64, 0, 0);
#pragma unroll
          for (int j = 0; j < 12; ++j) asm volatile("" :: "v"(t[j]));
        }
      }
    }
    const int o = sl < 8 ? 45 + y * 8 + sl : (sl < 20 ? 99 : (sl < 29 ? y * 9 + sl - 20 : (sl < 31 ? 36 + (sl - 29) * 4 + y : 44 + y * 99)));
    if (o < 72) { BAR(); role_out(a, R, go, lds, o, tid, wave, lane); }
  }
}

extern "C" __device__ __attribute__((noinline, used, visibility("hidden"))) void zz_code_end() {}
extern "C" void kernel_launch(void* const* d_in, const int* in_sizes, int n_in, void* d_out, int out_size, void* d_ws,
                              size_t ws_size, hipStream_t stream) {
  char* wb = (char*)(((uintptr_t)d_ws + 0x1FFFFF) & ~(uintptr_t)0x1FFFFF);
  Args a;
  a.x = (const u16*)d_in[0]; a.mem0 = (const u16*)d_in[4]; a.Wq = (const u16*)d_in[5]; a.Wk = (const u16*)d_in[6]; a.Wv = (const u16*)d_in[7];
  a.Wo = (const u16*)d_in[8]; a.wqkv = (const u16*)d_in[1]; a.wo = (const u16*)d_in[2]; a.gw = (const u16*)d_in[10]; a.out = (u16*)d_out; a.wb = wb;
  mega<<<256, 256, 0, stream>>>(a);
}
```

```cpp
#define S1 26
#define S2 40
#define T1D 125
#define DE0 96
#define DELTA0 164
#define DEA 82
#define KA_ 64
#define DE 80
#define K2 64
#define DEC 78
#define KC_ 62
#define G1 10
#define GSL 3
#define CT0 1100
#define CT1 950
#define EG 5
#define APF 600
#define AWT 900
#define AL2 1
#define XPF 800
#define MSL 2
#define XNT ""
#define UEF 1
#define OSW 28
#define HLP 1
#define HF0 9
#define HF1 4
#define HF2 0
#define HF3 4
#define XPT 760
#define HSH 0
#define NOCW 0
#define EFIRST
#define CSLP 1
#define RTOL 2
#define QDL 0
#define LTOL 16
#define GRID 48
#define XT 6
#define NOAT
#define AWD 8
#define XPD 8
#define NTM 15
#define FIXL
#define NSC 8
#define WRM 3
#define VED 0
#define NOYC 0
#define G0 36
#define GM 15
#define KFI 6
#define EG1 0
#define XP0 18
#define KEF 3
#include <hip/hip_runtime.h>
#include <stdint.h>
#include <type_traits>

typedef unsigned short u16;
typedef __attribute__((ext_vector_type(2))) __bf16 bf16x2;
typedef __attribute__((ext_vector_type(4))) __bf16 bf16x4;
typedef __attribute__((ext_vector_type(8))) __bf16 bf16x8;
typedef __attribute__((ext_vector_type(16))) __bf16 bf16x16;
typedef __attribute__((ext_vector_type(2))) float f32x2;
typedef __attribute__((ext_vector_type(4))) float f32x4;
typedef __attribute__((ext_vector_type(2))) unsigned u32x2;
typedef __attribute__((ext_vector_type(4))) unsigned u32x4;
typedef __attribute__((ext_vector_type(8))) unsigned u32x8;
typedef __attribute__((ext_vector_type(4))) short s16x4;
typedef __attribute__((address_space(3))) s16x4 lds_s16x4;
typedef __attribute__((address_space(3))) u32x4 lds_u32x4;
typedef __attribute__((address_space(3))) f32x4 lds_f32x4;
typedef __attribute__((address_space(3))) unsigned lds_u32;
typedef __attribute__((address_space(3))) u32x2 lds_u32x2;
typedef __attribute__((address_space(3))) char lds_char;

#define S_LEN 2048
#define DM 576
#define NSEG 64
#define HD 4096

#define SLOT_REAL  131072
#define NEAR_PM    (8192 + 1024)
#define NEAR_AL    (8192 + 5120)
#define NEAR_E     16384
#define OFF_FLAGS  0x100000
#define FL_FLIP    (OFF_FLAGS + 22016)
#define FL_WP      (OFF_FLAGS + 16384)
#define FL_AN      (OFF_FLAGS + 18432)
#define FL_MRDY    (OFF_FLAGS + 21504)
#define FL_MPOI    (OFF_FLAGS + 21760)
#define FL_PROG    (OFF_FLAGS + 4096)
#define FL_UPROG   (OFF_FLAGS + 0)
#define FL_KMPROG  (OFF_FLAGS + 49152)
#define FL_YPROG   (OFF_FLAGS + 20480)
#define FL_PXPROG  (OFF_FLAGS + 24576)
#define FL_APROG   (OFF_FLAGS + 40960)
#define FL_PXLAST  (OFF_FLAGS + 32768)
#define FL_ACK     (OFF_FLAGS + 30720)
#define FLAG_BYTES 0x10000
#define FL_GO      (OFF_FLAGS + FLAG_BYTES)
#define FXI 2
#define PXS 6
#define FX(a, cls, k) ((unsigned)(a) ^ (((unsigned)(cls) ^ (FXI >> (k))) & 1u) * 8192u)
#define NACK       945
#define REAL_CAL   0x140000
#define OFF_YR     0x200000
#define YSLOT_REAL 0x100000
#define CPY1       16384
#define CPY2       0x80000
#define OFF_KF   (4u << 20)
#define OFF_QF   (OFF_KF + (16u << 20))
#define OFF_KMF  (OFF_QF + (16u << 20))
#define OFF_PXF  (OFF_KMF + (8u << 20))
#define OFF_ZF   (OFF_PXF + (6u << 20))
#define ZS       (36u * 40960u)
#define OFF_OA   (OFF_ZF + (91u << 20))
#define OFF_GATE (OFF_OA + (3u << 20))
#define OFF_END  (OFF_GATE + (1u << 20))

#define RMASK 63
#define SPINCAP (1u << 19)
#define SC1 16

#define BAR() do { asm volatile("s_waitcnt lgkmcnt(0)" ::: "memory"); __builtin_amdgcn_s_barrier(); asm volatile("" ::: "memory"); } while (0)
#define VMW(n) asm volatile("s_waitcnt vmcnt(" #n ")" ::: "memory")
#define CBAR() asm volatile("" ::: "memory")
#define PADA
#define PADN "s_nop 0\n\t"
#define PADM
#define PADZ
#define PAD()
#define PADS(id)
#define PADE()
#define WALK(id)

#define TKD
#define TK(i)
#define TKS
#define NOSTEP 0
#define OFF_STAMP (200u << 20)
#define TS(v)
#define RT(i)
#define SV(i, v)
#define ACC(k, d)
#define STEPRT(j, f)
#define EVASM
#define EV(v)
#define EVK(v)
#define EVPUT(j, v)
#define EVPUTI(j, v)
#define EVDECL
#define EVDUMP()
#define CKDECL
#define CKS()
#define CKDUMP()
typedef unsigned long long u64;
struct Args {
  const u16* x; const u16* mem0; const u16* Wq; const u16* Wk; const u16* Wv; const u16* Wo;
  const u16* wqkv; const u16* wo; const u16* gw; u16* out; char* wb;
};

__device__ __forceinline__ float bf2f(u16 v) { return __uint_as_float(((unsigned)v) << 16); }
__device__ __forceinline__ unsigned pk2(float a, float b) {
  f32x2 v = {a, b};
  bf16x2 r = __builtin_convertvector(v, bf16x2);
  return __builtin_bit_cast(unsigned, r);
}
__device__ __forceinline__ int nearoff(int o) { return ((o >> 13) << 15) + (o & 8191); }
__device__ __forceinline__ unsigned own_cpy(int cls, int x) { return ((cls >> (2 * x)) & 2) ? CPY1 : 0u; }
__device__ __forceinline__ unsigned ywin(int cls, int m) {
  const int x = m / 9, row = (x >> 1) * 9 + m - 9 * x;
  const int se = (cls >> (4 * (x >> 1) + 1)) & 1, so = (cls >> (4 * (x >> 1) + 3)) & 1, sd = se == so ? x & 1 : ((x & 1) ? so : se);
  return (unsigned)(OFF_YR + row * 32768 + ((cls >> 8) & 1) * 8192 + ((sd ^ (cls >> 1) ^ (cls >> 9) ^ (row >> 4)) & 1) * 16384);
}
__device__ __forceinline__ u32x2 trd(lds_char* p) {
  s16x4 a = __builtin_amdgcn_ds_read_tr16_b64_v4i16((lds_s16x4*)p);
  return __builtin_bit_cast(u32x2, a);
}
__device__ __forceinline__ f32x4 mfma(u32x4 a, u32x4 b, f32x4 c) {
  return __builtin_amdgcn_mfma_f32_16x16x32_bf16(__builtin_bit_cast(bf16x8, a), __builtin_bit_cast(bf16x8, b), c, 0, 0, 0);
}
__device__ __forceinline__ f32x4 mfv0(u32x4 a, u32x4 b) {
  f32x4 d; asm volatile("s_nop 1\n\tv_mfma_f32_16x16x32_bf16 %0, %1, %2, 0" : "=&v"(d) : "v"(a), "v"(b)); return d;
}
__device__ __forceinline__ f32x4 mfvc(u32x4 a, u32x4 b, f32x4 c) {
  f32x4 d; asm volatile("s_nop 1\n\tv_mfma_f32_16x16x32_bf16 %0, %1, %2, %3" : "=&v"(d) : "v"(a), "v"(b), "v"(c)); return d;
}
__device__ __forceinline__ void mfva(f32x4& d, u32x4 a, u32x4 b) {
  asm volatile("s_nop 1\n\tv_mfma_f32_16x16x32_bf16 %0, %1, %2, %0" : "+v"(d) : "v"(a), "v"(b));
}
__device__ __forceinline__ void mf_wait(f32x4& d) { asm volatile("s_nop 7" : "+v"(d)); }
__device__ __forceinline__ void mf_wait2(f32x4& d, f32x4& e) { asm volatile("s_nop 7" : "+v"(d), "+v"(e)); }
__device__ __forceinline__ f32x4 smf(u32x4 a, u32x8 b, f32x4 c, int idx) {
  return __builtin_amdgcn_smfmac_f32_16x16x64_bf16(__builtin_bit_cast(bf16x8, a), __builtin_bit_cast(bf16x16, b), c, idx, 0, 0);
}
__device__ __forceinline__ float xmax(float x) {
  auto s = __builtin_amdgcn_permlane16_swap(__float_as_uint(x), __float_as_uint(x), false, false);
  x = fmaxf(__uint_as_float(s[0]), __uint_as_float(s[1]));
  auto t = __builtin_amdgcn_permlane32_swap(__float_as_uint(x), __float_as_uint(x), false, false);
  return fmaxf(__uint_as_float(t[0]), __uint_as_float(t[1]));
}
__device__ __forceinline__ float xsum(float x) {
  auto s = __builtin_amdgcn_permlane16_swap(__float_as_uint(x), __float_as_uint(x), false, false);
  x = __uint_as_float(s[0]) + __uint_as_float(s[1]);
  auto t = __builtin_amdgcn_permlane32_swap(__float_as_uint(x), __float_as_uint(x), false, false);
  return __uint_as_float(t[0]) + __uint_as_float(t[1]);
}
__device__ __forceinline__ f32x4 halfadd(f32x4 a, f32x4 b) {
  f32x4 o;
#pragma unroll
  for (int r = 0; r < 4; ++r) {
    auto s = __builtin_amdgcn_permlane32_swap(__float_as_uint(a[r]), __float_as_uint(b[r]), false, false);
    o[r] = __uint_as_float(s[0]) + __uint_as_float(s[1]);
  }
  return o;
}
template <class T> __device__ __forceinline__ void pin_a(T& v) { asm volatile("" : "+a"(v)); }
typedef __amdgpu_buffer_rsrc_t rsrc_t;
__device__ __forceinline__ u32x4 ld128(rsrc_t R, unsigned vo, unsigned so) { return __builtin_amdgcn_raw_buffer_load_b128(R, vo, so, SC1); }
__device__ __forceinline__ unsigned ld32(rsrc_t R, unsigned vo, unsigned so) { return __builtin_amdgcn_raw_buffer_load_b32(R, vo, so, SC1); }
__device__ __forceinline__ void st128(u32x4 v, rsrc_t R, unsigned vo, unsigned so) { __builtin_amdgcn_raw_buffer_store_b128(v, R, vo, so, SC1); }
__device__ __forceinline__ void st32(unsigned v, rsrc_t R, unsigned vo, unsigned so) { __builtin_amdgcn_raw_buffer_store_b32(v, R, vo, so, SC1); }
#define OOB 0x7ff00000
__device__ __forceinline__ unsigned lda(rsrc_t R, unsigned vo) { return ld32(R, vo, 0); }

struct Go { unsigned vo, v0; };
__device__ __forceinline__ Go go_look(rsrc_t R, int b) {
  Go g; g.vo = FL_GO + (b & 15) * 128; g.v0 = ld32(R, g.vo, 0); return g;
}
__device__ __forceinline__ void go_ack(rsrc_t R, const Go& g, int lane) {
  __builtin_amdgcn_raw_ptr_buffer_atomic_add_i32(1, R, lane == 0 ? g.vo - FL_GO + FL_ACK : OOB, 0, SC1);
}
__device__ __forceinline__ bool go_wait(rsrc_t R, const Go& g, int lane) {
  for (unsigned spins = 0; spins < SPINCAP; ++spins) {
    CBAR();
    if (__all(ld32(R, g.vo, 0) != g.v0)) {
      go_ack(R, g, lane);
      return true;
    }
    __builtin_amdgcn_s_sleep(6);
  }
  return false;
}
__device__ __forceinline__ void go_lead(rsrc_t R, int tid, int wave, int lane) {
#pragma unroll
  for (int k = 0; k < FLAG_BYTES / 4096; ++k) st128((u32x4){~0u, ~0u, ~0u, ~0u}, R, OFF_FLAGS + k * 4096 + tid * 16, 0);
  VMW(0);
  BAR();
  if (wave != 0) return;
  for (unsigned k = 0; k < SPINCAP; ++k) {
    st32(k, R, lane < 16 ? FL_GO + lane * 128 : OOB, 0);
    int n = lane < 16 ? (int)ld32(R, FL_ACK + lane * 128, 0) + 1 : 0;
#pragma unroll
    for (int d = 1; d < 16; d <<= 1) n += __shfl_xor(n, d);
    if (__builtin_amdgcn_readfirstlane(n) >= NACK) break;
    __builtin_amdgcn_s_sleep(4);
  }
}
__device__ __forceinline__ bool wait_flag(rsrc_t R, unsigned vo, int need) {
  for (unsigned spins = 0; spins < SPINCAP; ++spins) {
    CBAR();
    int v = (int)ld32(R, vo, 0);
    if (__all(v >= need)) return true;
    __builtin_amdgcn_s_sleep(8);
  }
  return false;
}

template <int SLP, int AT = 0>
__device__ __forceinline__ bool wait_flag_c(rsrc_t R, unsigned vo, int need, int& cache) {
  if (__all(cache >= need)) return true;
  for (unsigned spins = 0; spins < SPINCAP; ++spins) {
    CBAR();
    cache = (int)(AT ? lda(R, vo) : ld32(R, vo, 0));
    if (__all(cache >= need)) return true;
    __builtin_amdgcn_s_sleep(SLP);
  }
  return false;
}
__device__ __forceinline__ unsigned umax3(unsigned a, unsigned b, unsigned c) { return max(max(a, b), c); }

template <int NTB> __device__ __forceinline__ u32x4 gld(const u16* p) {
  if (NTB) return __builtin_nontemporal_load((const u32x4*)p);
  return *(const u32x4*)p;
}
#define XSTR2(x) #x
#define XSTR(x) XSTR2(x)
#define LSLP 3
#define ESLP 0
#define MSLP 0
#define TSLP 2
#define XSLP 2
#define KSLP 10
#define OSLP 24
#define OSL2 4
#define UE 1
#define YL
#define CPF 3
#define PARK 58
#define OFAST 60
#define CPS 8
#define CSL 2
#define TSL 6
#define UGO 5
#define UPO 1
#define CALN 4
#define QDEP 4
#define PDEP 3
#define WDEP 3
#define VSLP 2
#define QLN 24
#define VLN 8
#define PLN 32
#define LMASK(n) ((n) >= 64 ? ~0ull : ((1ull << ((n) & 63)) - 1))
#define UP3 0
#define ORDT 0x73652410u
#define USLP 8
#define UT 0
#define UTD 1
#define EMF 0
#define EMV 0
#define MWPOS 1
#define DSLP "s_sleep 1\n\t"
typedef __attribute__((ext_vector_type(4))) int i32x4;
__device__ __forceinline__ u64 ballot(bool c) { return __builtin_amdgcn_ballot_w64(c); }
#define UNLIKELY(c) __builtin_expect(!!(c), 0)
#define DELTA 164
#define D1 98
#define DVQ 4
#define LEV0 1
#define LEV3 1
#define LEVMAX 7
#define CL0 8
#define HEAT 3
#define COOL0 4
#define PSLP 8
#define VK0 64
#define AWE 0
#define ASLP 0
#define LEVF 99
#define RX 0
#define VSL 6
#define D0 14
#define WLAST 41
#define UDL 8
#define MGAP 0
#define SMIN 8
#define K1 4
#define PAD0 4
#define PADD 4
#define PADMAX 16
#define OAUX 0
__device__ __forceinline__ unsigned rt_now() {
  u64 t; asm volatile("s_memrealtime %0\n\ts_waitcnt lgkmcnt(0)" : "=s"(t) :: "memory"); return (unsigned)t;
}
#define S0 3
#define PACE0 ((DE0 << 8) | DELTA0)
#define PACEA ((DEA << 8) | (DEA + KA_))
#define PACEB ((DE << 8) | (DE + K2))
#define PACEC ((DEC << 8) | (DEC + KC_))
__device__ __forceinline__ void next_time(unsigned& t, int s) {
  t += (s < S0 ? PACE0 : (s < S1 ? PACEA : (s < S2 ? PACEB : PACEC))) & 255u;
  t = __builtin_amdgcn_readfirstlane(t);
  asm volatile("" : "+s"(t));
}
#define SNOP 1
#define SPIN(T, a, b, c) a ":\n\ts_memrealtime vcc\n\ts_waitcnt lgkmcnt(0)\n\ts_sub_u32 %[r], %[" #T "], vcc_lo\n\ts_sub_u32 %[k], %[r], 4\n\t"   \
               "s_cmp_lt_u32 %[k], 2000\n\ts_cbranch_scc1 " a "b\n\t"                                                                       \
               "s_sub_u32 %[k], %[r], 1\n\ts_cmp_lt_u32 %[k], 3\n\ts_cbranch_scc0 " c "f\n\t"                                               \
               b ":\n\ts_nop " XSTR(SNOP) "\n\ts_sub_u32 %[k], %[k], 1\n\ts_cbranch_scc0 " b "b\n\t" c ":\n\t"
__device__ __forceinline__ int spin_until(unsigned T) {
  int r, k;
  asm volatile(SPIN(T, "1", "2", "3") : [r] "=&s"(r), [k] "=&s"(k) : [T] "s"(T) : "memory", "vcc", "scc");
  return r;
}
#define KA "v_mov_b32 v240, v240\n\ts_nop 2\n\t"
#define KAN KA KA
#define EVA(n)
#define EVC(k)
#define BUD(l) "s_sub_u32 %[bud], %[bud], 1\n\ts_cbranch_scc0 " l "\n\ts_mov_b32 %[bud], 0\n\t"
__device__ __forceinline__ i32x4 mkdesc(char* wb) {
  const u64 p = (u64)wb;
  return (i32x4){(int)(unsigned)p, (int)(unsigned)(p >> 32), (int)OFF_END, 0x00020000};
}
template <class T> __device__ __forceinline__ void pin_v(T& v) { asm volatile("" : "+v"(v)); }
#define SB() __builtin_amdgcn_sched_barrier(0)
__device__ __forceinline__ unsigned pkv(float a, float b) {
  unsigned d; asm volatile("v_cvt_pk_bf16_f32 %0, %1, %2" : "=v"(d) : "v"(a), "v"(b)); return d;
}
__device__ __forceinline__ f32x4 mfB0(u32x4 a, u32x4 b) {
  f32x4 d; asm volatile("v_mfma_f32_16x16x32_bf16 %0, %1, %2, 0" : "=&v"(d) : "v"(a), "a"(b)); return d;
}
__device__ __forceinline__ void mfB(f32x4& d, u32x4 a, u32x4 b) {
  asm volatile("v_mfma_f32_16x16x32_bf16 %0, %1, %2, %0" : "+v"(d) : "v"(a), "a"(b));
}

template <int NTB>
__device__ __forceinline__ void qk_gload(u32x4 (&st)[8], const Args& a, int h, int c, int tid) {
#pragma unroll
  for (int k = 0; k < 8; ++k) {
    int u = tid + 256 * k; int row = u >> 5, cu = u & 31;
    const u16* W = (cu < 16) ? a.Wq : a.Wk;
    st[k] = gld<NTB>(W + (((size_t)(64 * c + row) * HD + h * 128 + (cu & 15) * 8) & ((NOCW & 1) ? 2047 : ~0ull)));
  }
}
__device__ __forceinline__ void qk_swrite(const u32x4 (&st)[8], lds_char* buf, int tid) {
#pragma unroll
  for (int k = 0; k < 8; ++k) { int u = tid + 256 * k; int row = u >> 5, cu = u & 31; *(lds_u32x4*)(buf + row * 544 + cu * 16) = st[k]; }
}
#define STG_BYTES 38912
#define X0_OFF (2 * STG_BYTES)
#define X1_OFF (X0_OFF + 37888)

__device__ __forceinline__ int cls_look(rsrc_t R, int lane) { return (int)ld32(R, FL_FLIP + (lane & 31) * 4, 0); }
__device__ __forceinline__ int rowsum(int v) {
  v += __builtin_amdgcn_update_dpp(v, v, 0x128, 0xf, 0xf, false);
  v += __builtin_amdgcn_update_dpp(v, v, 0x124, 0xf, 0xf, false);
  v += __builtin_amdgcn_update_dpp(v, v, 0x122, 0xf, 0xf, false);
  return v + __builtin_amdgcn_update_dpp(v, v, 0x121, 0xf, 0xf, false);
}
__device__ __forceinline__ int get_cls(rsrc_t R, int lane, int v = -1) {
  for (unsigned spins = 0; spins < SPINCAP; ++spins) {
    if (__all(v >= 0)) {
      const int n = rowsum((lane & 1) ? -v : v) > 0;
      const int d = rowsum((lane & 1) != n ? 0 : (((lane >> 1) ^ (lane >> 2)) & 1 ? -v : v)) > 0;
      const int c = n | (d << 1);
      return (__builtin_amdgcn_readlane(c, 0) * 0x55 ^ 0x88) | (__builtin_amdgcn_readlane(c, 16) << 8);
    }
    if (spins) __builtin_amdgcn_s_sleep(CPS);
    CBAR();
    v = cls_look(R, lane);
  }
  return -1;
}
__device__ __forceinline__ void calibrate(rsrc_t R, const Go& go, int r, int k, int lane) {
  unsigned best = 0xFFFFFFu;
  for (int it = 0; it < CALN; ++it) {
    unsigned long long t0, t1;
    asm volatile("s_waitcnt vmcnt(0)\n\ts_memtime %0\n\ts_waitcnt lgkmcnt(0)" : "=s"(t0) :: "memory");
    st32((unsigned)it, R, (lane == 0) ? (unsigned)((r >> 2) * OFF_YR + REAL_CAL + (r & 3) * 256 + k * 8192 + (it & 1) * 4096) : OOB, 0);
    asm volatile("s_waitcnt vmcnt(0)\n\ts_memtime %0\n\ts_waitcnt lgkmcnt(0)" : "=s"(t1) :: "memory");
    best = min(best, (unsigned)(t1 - t0));
  }
  if (go_wait(R, go, lane)) st32(best, R, lane == 0 ? (unsigned)(FL_FLIP + (r * 4 + k) * 4) : OOB, 0);
}
__device__ __forceinline__ unsigned get_t1(rsrc_t R, unsigned vo, unsigned& bud) {
  unsigned v = 0;
#pragma nounroll
  for (; bud != 0; --bud) {
    CBAR();
    v = __builtin_amdgcn_readfirstlane(ld32(R, vo, 0));
    if (v != ~0u) break;
    __builtin_amdgcn_s_sleep(2);
  }
  return v;
}

#define F8_OFF 18432
#define QSTG 34816
#define ISOT()
#define ISOQ(n)
__device__ __forceinline__ float vmax(float a, float b) { float d; asm("v_max_f32 %0, %1, %2" : "=v"(d) : "v"(a), "v"(b)); return d; }
__device__ __forceinline__ float vmax3(float a, float b, float c) { float d; asm("v_max3_f32 %0, %1, %2, %3" : "=v"(d) : "v"(a), "v"(b), "v"(c)); return d; }
__device__ __forceinline__ float max32(float x) {
  auto t = __builtin_amdgcn_permlane32_swap(__float_as_uint(x), __float_as_uint(x), false, false);
  return vmax(__uint_as_float(t[0]), __uint_as_float(t[1]));
}
__device__ __forceinline__ float max16(float x) {
  auto t = __builtin_amdgcn_permlane16_swap(__float_as_uint(x), __float_as_uint(x), false, false);
  return vmax(__uint_as_float(t[0]), __uint_as_float(t[1]));
}
__device__ __forceinline__ float sum32(float x) {
  auto t = __builtin_amdgcn_permlane32_swap(__float_as_uint(x), __float_as_uint(x), false, false);
  return __uint_as_float(t[0]) + __uint_as_float(t[1]);
}
template <int r> __device__ __forceinline__ void swp32(f32x4& a, f32x4& b) {
  auto t = __builtin_amdgcn_permlane32_swap(__float_as_uint(a[r]), __float_as_uint(b[r]), false, false);
  a[r] = __uint_as_float(t[0]); b[r] = __uint_as_float(t[1]);
}
__device__ __forceinline__ float vadd(float a, float b) { float d; asm("v_add_f32 %0, %1, %2" : "=v"(d) : "v"(a), "v"(b)); return d; }
__device__ __forceinline__ float max8(float x) {
  float d; asm("s_nop 1\n\tv_max_f32_dpp %0, %1, %1 row_ror:8 row_mask:0xf bank_mask:0xf" : "=&v"(d) : "v"(x)); return d;
}
__device__ __forceinline__ float sum8(float x) {
  float d; asm("s_nop 1\n\tv_add_f32_dpp %0, %1, %1 row_ror:8 row_mask:0xf bank_mask:0xf" : "=&v"(d) : "v"(x)); return d;
}
__device__ __forceinline__ float sum16(float x) {
  auto t = __builtin_amdgcn_permlane16_swap(__float_as_uint(x), __float_as_uint(x), false, false);
  return __uint_as_float(t[0]) + __uint_as_float(t[1]);
}
__device__ __forceinline__ void st64(u32x2 v, rsrc_t R, unsigned vo, unsigned so) { __builtin_amdgcn_raw_buffer_store_b64(v, R, vo, so, SC1); }
template <int N>
__device__ __forceinline__ void mem_issue2(i32x4 D, unsigned m0, unsigned m1, unsigned m2, unsigned so, unsigned l0, unsigned l1, unsigned l2, u64& ew) {
  asm volatile("s_mov_b32 m0, %[l0]\n\ts_barrier\n\t" EVASM
               "buffer_load_dwordx4 %[m0], %[D], %[so] offen sc1 lds\n\t"
               "s_mov_b32 m0, %[l1]\n\ts_nop 0\n\t"
               "buffer_load_dwordx4 %[m1], %[D], %[so] offen sc1 lds\n\t"
               ".if %[n] == 3\n\ts_mov_b32 m0, %[l2]\n\ts_nop 0\n\t"
               "buffer_load_dwordx4 %[m2], %[D], %[so] offen sc1 lds\n\t.endif"
               : [ew] "=&s"(ew)
               : [m0] "v"(m0), [m1] "v"(m1), [m2] "v"(m2), [D] "s"(D), [so] "s"(so), [l0] "s"(l0), [l1] "s"(l1), [l2] "s"(l2), [n] "n"(N)
               : "memory");
}
#define TRYB ".if %[id] >= 0\n\ts_cbranch_vccnz .Ltry%[id]\n.Lend%[id]:\n\t.endif\n\t"
#define TRYC "vcc", "scc", "v236", "v237", "v238", "v239", "v240"
template <int ID>
__device__ __forceinline__ void try_top(unsigned& nt, unsigned& ms, unsigned& tm, unsigned& rcnt, unsigned ia) {
  asm volatile(".Ltry%[id]:\n\ts_waitcnt lgkmcnt(0)\n\ts_cmp_ge_i32 %[nt], " XSTR(NSC) "\n\ts_cbranch_scc1 1f\n"
               "2:\n\ts_nop 4\n\ts_xor_b32 %[ms], %[ms], %[fl]\n\ts_add_u32 %[tm], %[tm], " XSTR(GRID) "+" XSTR(XT) "\n\ts_add_u32 %[nt], %[nt], 1\n\t.subsection 1\n"
               "1:\n\tv_mov_b32 v240, 0\n\t"
               ".irp o, 0, 1024, 2048, 3072, 4096, 5120, 6144, 7168, " XSTR(F8_OFF) "\n\tds_read_b128 v[236:239], %[ia] offset:\\o\n\ts_waitcnt lgkmcnt(0)\n\t"
               "v_max3_u32 v240, v240, v236, v237\n\tv_max3_u32 v240, v240, v238, v239\n\t.endr\n\t"
               "v_cmp_eq_u32 vcc, -1, v240\n\ts_cbranch_vccz .Lend%[id]\n\ts_add_u32 %[rc], %[rc], 1\n\ts_cmp_lt_u32 %[rc], %[cap]\n\t"
               "s_cbranch_scc1 2b\n\ts_branch .Lend%[id]\n\t.subsection 0"
               : [nt] "+s"(nt), [ms] "+s"(ms), [tm] "+s"(tm), [rc] "+s"(rcnt)
               : [fl] "n"(CPY2 + CPY1), [id] "n"(ID), [ia] "v"(ia), [cap] "n"(SPINCAP / 2) : "memory", TRYC);
}
__device__ __forceinline__ void try_end(unsigned ia, unsigned nt, unsigned ms, unsigned tm, unsigned rcnt,
                                        unsigned m0, unsigned m1, unsigned m2, unsigned l0, unsigned l1, unsigned l2, i32x4 D) {
  asm volatile("" :: "s"(rcnt), "s"(nt), "s"(ms), "s"(tm), "v"(ia), "v"(m0), "v"(m1), "v"(m2), "s"(l0), "s"(l1), "s"(l2), "s"(D) : "memory");
}
__device__ __forceinline__ int spin_late(unsigned& t, unsigned d) {
  int r, k; unsigned T;
  asm volatile("s_add_u32 %[T], %[t], %[d]\n\t" SPIN(T, "1", "2", "3") : [r] "=&s"(r), [k] "=&s"(k), [T] "=&s"(T), [t] "+s"(t) : [d] "n"(d) : "memory", "vcc", "scc");
  return r;
}
__device__ __forceinline__ void move_on(unsigned& t, int r) {
  asm volatile("1:\n\ts_cmp_lt_i32 %1, -" XSTR(LTOL) "\n\ts_cbranch_scc0 2f\n\ts_add_u32 %0, %0, " XSTR(GRID) "+" XSTR(XT) "\n\ts_add_u32 %1, %1, " XSTR(GRID) "+" XSTR(XT) "\n\ts_branch 1b\n2:"
               : "+s"(t), "+s"(r) :: "scc");
}
__device__ __forceinline__ void gate_wait(rsrc_t R, unsigned vo, int need, int& cache, unsigned& bud) {
#pragma nounroll
  for (; bud != 0; --bud) {
    if (__all(cache >= need)) break;
    CBAR();
    cache = (int)ld32(R, vo, 0);
    __builtin_amdgcn_s_sleep(4);
  }
}
__device__ __forceinline__ unsigned um3v(unsigned a, unsigned b, unsigned c) {
  unsigned d; asm volatile("v_max3_u32 %0, %1, %2, %3" : "=v"(d) : "v"(a), "v"(b), "v"(c)); return d;
}
template <int OFF>
__device__ __forceinline__ void k_put(f32x4 k, unsigned ad) {
  unsigned t0, t1;
  asm volatile("v_cvt_pk_bf16_f32 %0, %2, %3\n\tv_cvt_pk_bf16_f32 %1, %4, %5\n\tds_write2_b32 %6, %0, %1 offset0:%7 offset1:%8"
               : "=&v"(t0), "=&v"(t1) : "v"(k[0]), "v"(k[1]), "v"(k[2]), "v"(k[3]), "v"(ad), "n"(OFF), "n"(OFF + 2) : "memory");
}
template <int OFF>
__device__ __forceinline__ void q_put(f32x4 k, unsigned ad) {
  unsigned t0, t1;
  asm volatile("v_cvt_pk_bf16_f32 %0, %2, %3\n\tv_cvt_pk_bf16_f32 %1, %4, %5\n\tds_write2_b32 %6, %0, %1 offset0:%7 offset1:%8"
               : "=&v"(t0), "=&v"(t1) : "v"(k[0]), "v"(k[1]), "v"(k[2]), "v"(k[3]), "v"(ad), "n"(OFF), "n"(OFF + 1) : "memory");
}
__device__ __forceinline__ f32x4 mf0(u32x4 a, u32x4 b) {
  f32x4 d; asm volatile("v_mfma_f32_16x16x32_bf16 %0, %1, %2, 0" : "=&v"(d) : "v"(a), "v"(b)); return d;
}
__device__ __forceinline__ void mfa(f32x4& d, u32x4 a, u32x4 b) {
  asm volatile("v_mfma_f32_16x16x32_bf16 %0, %1, %2, %0" : "+v"(d) : "v"(a), "v"(b));
}
__device__ __forceinline__ f32x4 mfA0(u32x4 a, u32x4 b) {
  f32x4 d; asm volatile("v_mfma_f32_16x16x32_bf16 %0, %1, %2, 0" : "=&v"(d) : "a"(a), "v"(b)); return d;
}
__device__ __forceinline__ void mfA(f32x4& d, u32x4 a, u32x4 b) {
  asm volatile("v_mfma_f32_16x16x32_bf16 %0, %1, %2, %0" : "+v"(d) : "a"(a), "v"(b));
}
#define DPPLO " row_ror:8 row_mask:0xf bank_mask:0x3\n\t"
#define DPPHI " row_ror:8 row_mask:0xf bank_mask:0xc\n\t"
#define DPPAL " row_ror:8 row_mask:0xf bank_mask:0xf\n\t"
#define SCK "0x3e0293ee"
#define SCN "0xbe0293ee"
__device__ __forceinline__ void px_mask(f32x4& T0, f32x4& T1, int qt, int lane) {
  const float ninf = -__builtin_inff();
#pragma unroll
  for (int r = 0; r < 4; ++r) {
    const bool diag = (4 * (lane >> 4) + r) <= (lane & 15);
    T0[r] = (qt == 1 || diag) ? T0[r] : ninf;
    T1[r] = (qt == 1 && diag) ? T1[r] : ninf;
  }
}
__device__ __forceinline__ void px_seg(f32x4& T0, f32x4& T1, float& mx, float& sx) {
  const float sc = 0.08838834764831845f * 1.4426950408889634f;
  mx = xmax(fmaxf(fmaxf(fmaxf(T0[0], T0[1]), fmaxf(T0[2], T0[3])), fmaxf(fmaxf(T1[0], T1[1]), fmaxf(T1[2], T1[3]))));
  const float ms = -mx * sc;
  sx = 0.f;
#pragma unroll
  for (int r = 0; r < 4; ++r) {
    T0[r] = __builtin_amdgcn_exp2f(fmaf(T0[r], sc, ms)); T1[r] = __builtin_amdgcn_exp2f(fmaf(T1[r], sc, ms));
    sx += T0[r] + T1[r];
  }
}
__device__ __forceinline__ void px_fin(f32x4 G, f32x4 T0, f32x4 T1, float mx, float sx, u32x4& px, unsigned& pw, int lane) {
  const float sc = 0.08838834764831845f * 1.4426950408889634f;
  float g0 = (lane & 16) ? G[2] : G[0], g1 = (lane & 16) ? G[3] : G[1];
  const float ms = -xmax(fmaxf(fmaxf(g0, g1), mx)) * sc;
  g0 = __builtin_amdgcn_exp2f(fmaf(g0, sc, ms)); g1 = __builtin_amdgcn_exp2f(fmaf(g1, sc, ms));
  const float f = __builtin_amdgcn_exp2f(fmaf(mx, sc, ms));
  asm volatile("" : "+v"(g0), "+v"(g1)); PAD();
  const float inv = __builtin_amdgcn_rcpf(xsum(fmaf(f, sx, g0 + g1))), fi = f * inv;
  px = (u32x4){pk2(T0[0] * fi, T0[1] * fi), pk2(T0[2] * fi, T0[3] * fi), pk2(T1[0] * fi, T1[1] * fi), pk2(T1[2] * fi, T1[3] * fi)};
  pw = pk2(g0 * inv, g1 * inv);
}
#define WPOS 4
#define QPOS 1
#define LPOS 1
#define QIM_OFF 8192
#define KIM_OFF 10240
#define FLG_OFF 17408
#define F8W 3
#define KX_OFF 36864
typedef __attribute__((ext_vector_type(16))) float f32x16;
typedef __attribute__((ext_vector_type(16))) unsigned u32x16;
__device__ __forceinline__ void ds_flag(unsigned ad, unsigned v) { asm volatile("ds_write_b32 %0, %1" :: "v"(ad), "v"(v) : "memory"); }
#define SMX(d, j, w) "v_smfmac_f32_16x16x64_bf16 v[" d "], %[a" #j "], %[" #w "], %[ix]\n\t"
#define CVX(d, x, y) "v_cvt_pk_bf16_f32 v" #d ", v" #x ", v" #y "\n\t"
#define PKA(d, b) "v_pk_add_f32 v[" #d ":" #d "+1], v[" #b ":" #b "+1], v[" #b "+2:" #b "+3]\n\t"
#define IMW(a, o) "ds_write_b32 " a ", v232 offset:" o "\n\t"
template <int T, int AG>
__device__ __forceinline__ void smo(f32x4& c, u32x4 a, u32x8 b, int ix) {
  if (T == 0 && AG) asm volatile(SMX("140:143", 0, w) : "+{v[140:143]}"(c) : [a0] "v"(a), [w] "a"(b), [ix] "v"(ix));
  if (T == 0 && !AG) asm volatile(SMX("140:143", 0, w) : "+{v[140:143]}"(c) : [a0] "v"(a), [w] "v"(b), [ix] "v"(ix));
  if (T == 1 && AG) asm volatile(SMX("144:147", 0, w) : "+{v[144:147]}"(c) : [a0] "v"(a), [w] "a"(b), [ix] "v"(ix));
  if (T == 1 && !AG) asm volatile(SMX("144:147", 0, w) : "+{v[144:147]}"(c) : [a0] "v"(a), [w] "v"(b), [ix] "v"(ix));
}
__device__ __forceinline__ void v_zero(f32x4& v0, f32x4& v1) {
  asm volatile("v_mov_b64 v[140:141], 0\n\tv_mov_b64 v[142:143], 0\n\tv_mov_b64 v[144:145], 0\n\tv_mov_b64 v[146:147], 0\n\ts_nop 1"
               : "={v[140:143]}"(v0), "={v[144:147]}"(v1));
}
__device__ __forceinline__ void k_zero(f32x4& k0, f32x4& k1, f32x4& k2) {
  asm volatile(".rept 6\n\tv_mov_b64 v[148+2*\\+:149+2*\\+], 0\n\t.endr" : "={v[148:151]}"(k0), "={v[152:155]}"(k1), "={v[156:159]}"(k2));
}
#define TZERO ".rept 6\n\tv_mov_b64 v[200+2*\\+:201+2*\\+], 0\n\t.endr\n\t"
__device__ __forceinline__ void t_zero(f32x4& k0, f32x16& T) {
  asm volatile("v_mov_b64 v[148:149], 0\n\tv_mov_b64 v[150:151], 0\n\t" TZERO : "={v[148:151]}"(k0), "={v[200:215]}"(T));
}
#define AD0(b) "v_add_f32 v230, v[" #b "], v[" #b "+2]\n\t"
#define AD1(b) "v_add_f32 v231, v[" #b "+1], v[" #b "+3]\n\t"
#define CVP CVX(232, 230, 231)
#define Q1(j) SMX("144:147", j, w##j)
template <int ID>
__device__ __forceinline__ void q_mid(const u32x4 (&af)[9], const u32x8 (&w)[9], int ix, unsigned qw, f32x4 q0, f32x4& q1) {
  asm volatile(Q1(3) Q1(4) AD0(140) Q1(5) AD1(140) "v_cmp_u_f32 vcc, v230, v231\n\t" Q1(6) CVP Q1(7) TRYB IMW("%[qw]", "0") Q1(8)
               : "+{v[144:147]}"(q1)
               : "{v[140:143]}"(q0), [a3] "v"(af[3]), [a4] "v"(af[4]), [a5] "v"(af[5]), [a6] "v"(af[6]), [a7] "v"(af[7]), [a8] "v"(af[8]),
                 [w3] "a"(w[3]), [w4] "a"(w[4]), [w5] "a"(w[5]), [w6] "a"(w[6]), [w7] "a"(w[7]), [w8] "v"(w[8]), [ix] "v"(ix), [qw] "v"(qw), [id] "n"(ID)
               : "memory", "v230", "v231", "v232", TRYC);
}
__device__ __forceinline__ void k_last(unsigned kw, f32x4 q1, unsigned fw, unsigned fv) {
  asm volatile("s_nop 7\n\t" PKA(230, 144) CVP IMW("%[kw]", "1024") "ds_write_b32 %[fw], %[fv]"
               :: "{v[144:147]}"(q1), [kw] "v"(kw), [fw] "v"(fw), [fv] "v"(fv) : "memory", "v230", "v231", "v232");
}
#define K0(j) SMX("148:151", j, u##j)
#define K1(j) SMX("152:155", j, u##j)
#define AF9 [a0] "v"(af[0]), [a1] "v"(af[1]), [a2] "v"(af[2]), [a3] "v"(af[3]), [a4] "v"(af[4]), [a5] "v"(af[5]), [a6] "v"(af[6]), [a7] "v"(af[7]), [a8] "v"(af[8])
#define WK9 [u0] "a"(wk[0]), [u1] "a"(wk[1]), [u2] "a"(wk[2]), [u3] "a"(wk[3]), [u4] "a"(wk[4]), [u5] "a"(wk[5]), [u6] "a"(wk[6]), [u7] "a"(wk[7]), [u8] "v"(wk[8])
__device__ __forceinline__ void k_one(const u32x4 (&af)[9], const u32x8 (&wk)[9], const u32x8 (&wp)[3], int ix, unsigned qw, f32x4 q1, f32x4& k0, f32x4& k2, float& t) {
  asm volatile(K0(8) K0(0) AD0(144) AD1(144) CVP K0(4) IMW("%[qw]", "1024") K0(1) K0(2) K0(3) K0(5) PADA K0(6) K0(7)
               SMX("156:159", 0, p0) SMX("156:159", 4, p1) AD0(148)
               : "+{v[148:151]}"(k0), "+{v[156:159]}"(k2), "=&{v230}"(t)
               : "{v[144:147]}"(q1), AF9, WK9, [p0] "v"(wp[0]), [p1] "v"(wp[1]), [ix] "v"(ix), [qw] "v"(qw) : "memory", "v231", "v232");
}
#define KB
template <int PO, int JC>
__device__ __forceinline__ void k_end(const u32x4 (&af)[9], const u32x8 (&wk)[9], u32x8 wp, int ix, f32x4 k0, f32x4 k1, f32x4 k2, float t, unsigned kw, unsigned fw, unsigned fv) {
  asm volatile(".if %[jc] == 3\n\t" SMX("156:159", 3, p2) ".endif\n\t.if %[jc] == 6\n\t" SMX("156:159", 6, p2) ".endif\n\t.if %[jc] == 8\n\t" SMX("156:159", 8, p2) ".endif\n\t"
               AD1(148) K1(8) CVP K1(0) IMW("%[kw]", "0") K1(4) AD0(156) K1(1) AD1(156) K1(2) CVP K1(3) IMW("%[kw]", "%[po]") K1(5) PADA K1(6) K1(7)
               "s_nop 7\n\t" PKA(230, 152) CVP IMW("%[kw]", "1024") KB "ds_write_b32 %[fw], %[fv]\n\t"
               :: "{v[148:151]}"(k0), "{v[152:155]}"(k1), "{v[156:159]}"(k2), "{v230}"(t), AF9, WK9, [p2] "v"(wp), [ix] "v"(ix), [kw] "v"(kw), [fw] "v"(fw),
                  [fv] "v"(fv), [po] "n"(PO), [jc] "n"(JC)
               : "memory", "v231", "v232");
}
__device__ __forceinline__ unsigned add3bf(unsigned a, unsigned b, unsigned c) {
  return pk2(__uint_as_float(a << 16) + __uint_as_float(b << 16) + __uint_as_float(c << 16),
             __uint_as_float(a & 0xffff0000u) + __uint_as_float(b & 0xffff0000u) + __uint_as_float(c & 0xffff0000u));
}
#define IS(n)
#define cKW "v112"
#define cKA "v113"
#define cQ0 "v114"
#define cQ1 "v115"
#define cQ3 "v116"
#define cFA "v117"
#define cEO "v118"
#define cLO "v119"
#define cC0 "v120"
#define cC1 "v121"
#define cC2 "v122"
#define cFW "v123"
#define cQW "v124"
#define cKX "v125"
#define TRD(r, a, o) "ds_read_b64_tr_b16 v[" #r ":" #r "+1], " a " offset:" #o "\n\t"
#define LGK(n) "s_waitcnt lgkmcnt(" #n ")\n\t"
#define SXM(t, c) "v_smfmac_f32_16x16x64_bf16 v[200+4*" #t ":203+4*" #t "], v[160+4*" #c ":163+4*" #c "], v[64+16*" #t "+8*" #c ":71+16*" #t "+8*" #c "], %[ix]\n\t"
#define SMM(c) "v_smfmac_f32_16x16x64_bf16 v[208:211], v[160+4*" #c ":163+4*" #c "], v[176+8*" #c ":183+8*" #c "], %[ix]\n\t"
#define PQ ".if pq\n97:\n\ts_cbranch_execz 97f\n\t.endif\n\t"
#define PQ0 ".set pq, 0\n\t"
#define PQA ".set pq, %[wid] >= 0\n\t.if pq\n.Lwa%[wid]:\n\t.endif\n\t" PQ
#define PQB ".if pq\n97:\n\ts_cbranch_execz .Lwb%[wid]\n\t.endif\n\t" PQ0
template <int A> __device__ __forceinline__ void pq_walk() {
  asm volatile(".if %1\n\ts_mov_b64 exec, 0\n\ts_branch .Lwa%0\n\t.endif\n.Lwb%0:\n\ts_mov_b64 exec, -1" :: "n"(A), "n"(WRM >> 1 & 1) : "memory");
}
#define QRE TRD(160, cQ0, 0) TRD(162, cQ1, 0)
#define QRL TRD(164, cQ0, 1024) TRD(166, cQ1, 1024)
#define CN(k)
#define QE 0
#define QL 1
#define KL -1
#define QP(n) ".if " XSTR(QE) " == " #n "\n\t" QRE ".endif\n\t"
#define QQ(n) ".if " XSTR(QL) " == " #n "\n\t" QRL ".endif\n\t"
template <int PH>
__device__ __forceinline__ void kx_get(u32x16 cst, u32x16& x0, u32x16& x1) {
#define KXR(k) "ds_read_b128 v[64+4*" #k ":67+4*" #k "], " cKX " offset:%[o]+1024*" #k "\n\tds_read_b128 v[80+4*" #k ":83+4*" #k "], " cKX " offset:%[o]+1024*" #k "+16\n\t"
  asm volatile(KXR(0) KXR(1) KXR(2) KXR(3)
               : "={v[64:79]}"(x0), "={v[80:95]}"(x1) : "{v[112:127]}"(cst), [o] "n"(KX_OFF + PH * 8192) : "memory");
}
#define NK 2
#define KIF(c, t) ".if " XSTR(NK) c "\n\t" t ".endif\n\t"
__device__ __forceinline__ void s_first(const u32x4 (&af)[9], const u32x8 (&wk)[9], int ix, u32x16 cst, u32x16 x0, u32x16 x1, f32x4 q1, f32x4& k0, f32x16& T, u32x8& Q) {
  asm volatile(K0(8) QP(0) K0(0) AD0(144) AD1(144) CVP K0(4) IMW(cQW, "1024") K0(1) QP(1) QQ(0) K0(2) QQ(1) K0(3) QQ(2) KIF(" == 0", K0(5))
               LGK(2) SXM(0, 0) SXM(1, 0) LGK(0) SXM(0, 1) SXM(1, 1)
               : "+{v[148:151]}"(k0), "+{v[200:215]}"(T), "=&{v[160:167]}"(Q)
               : "{v[144:147]}"(q1), [a0] "v"(af[0]), [a1] "v"(af[1]), [a2] "v"(af[2]), [a3] "v"(af[3]), [a4] "v"(af[4]), [a5] "v"(af[5]), [a8] "v"(af[8]),
                 [u0] "a"(wk[0]), [u1] "a"(wk[1]), [u2] "a"(wk[2]), [u3] "a"(wk[3]), [u4] "a"(wk[4]), [u5] "a"(wk[5]), [u8] "v"(wk[8]),
                 [ix] "v"(ix), "{v[112:127]}"(cst), "{v[64:79]}"(x0), "{v[80:95]}"(x1)
               : "memory", "v230", "v231", "v232");
}
#define DPPF " row_mask:0xf bank_mask:0xf\n\t"
#define DX1 " quad_perm:[1,0,3,2]" DPPF
#define DX2 " quad_perm:[2,3,0,1]" DPPF
#define DX4 " row_half_mirror" DPPF
template <int WID>
__device__ __forceinline__ void el_phase(i32x4 D, const u32x4 (&af)[9], const u32x8 (&wk)[9], int ix, u32x16 cst, u32x16 x0, u32x16 x1, u32x8& Q, f32x4& k0, f32x16& T,
                                         unsigned& bud, u64& e1, u64& e4, unsigned& nr, u32x8 sc, unsigned fv) {
#define EST(s) "buffer_store_dwordx2 v[222:223], " cEO ", %[D], " s " offen sc1\n\t"
#define LST(s) "buffer_store_dword v227, " cLO ", %[D], " s " offen sc1\n\t"
#define EPART(KA, KB_)                                                                                                             \
               IS(4) KA "v_add_f32 v212, v200, v202\n\t" KB_ "v_add_f32 v213, v201, v203\n\tv_add_f32 v214, v204, v206\n\tv_add_f32 v215, v205, v207\n\t" \
               "v_max_f32 v216, v212, v214\n\tv_max_f32 v217, v213, v215\n\tv_mov_b32 v219, v212\n\t"                              \
               "v_max_f32_dpp v218, v216, v216" DPPLO "v_max_f32_dpp v218, v217, v217" DPPHI PQ                                    \
               "v_mov_b32 v220, v214\n\ts_nop 0\n\t"                                                                             \
               "v_max_f32_dpp v218, v218, v218" DX1 "v_mov_b32_dpp v212, v213" DPPHI "v_mov_b32_dpp v214, v215" DPPHI              \
               "v_max_f32_dpp v218, v218, v218" DX2 "v_mov_b32_dpp v213, v219" DPPLO "v_mov_b32_dpp v215, v220" DPPLO              \
               PQ "v_max_f32_dpp v218, v218, v218" DX4 IS(5)                                                                       \
               "v_mul_f32 v216, " SCN ", v218\n\t"                                                                                 \
               "v_pk_fma_f32 v[212:213], v[212:213], s[44:45], v[216:217] op_sel_hi:[1,0,0]\n\t"                                   \
               "v_pk_fma_f32 v[214:215], v[214:215], s[44:45], v[216:217] op_sel_hi:[1,0,0]\n\t"                                   \
               "v_exp_f32 v212, v212\n\tv_exp_f32 v214, v214\n\tv_exp_f32 v213, v213\n\tv_exp_f32 v215, v215\n\t"                  \
               PQ CVX(222, 212, 214) CVX(223, 213, 215)
#define MPART(KA, KB_, KBW)                                                                                                        \
               KA EST("s40") TRD(168, cQ3, 0) TRD(192, cKA, 2048) EST("s41") EVA(4) IS(6) PQ TRD(194, cKA, 2304) TRD(176, cKA, 0)   \
               KB_ TRD(178, cKA, 256) TRD(180, cKA, 512) TRD(182, cKA, 768)                                                        \
               "v_add_f32 v212, v212, v214\n\tv_add_f32 v213, v213, v215\n\tv_mov_b32 v225, " cC2 "\n\tv_add_f32 v212, v212, v213\n\t"  \
               PQ LGK(4) PKA(230, 148) CVP "v_smfmac_f32_16x16x32_bf16 v[208:211], v[168:169], v[192:195], %[ix]\n\t"                 \
               IMW(cKW, "1024") TRD(190, cKA, 1792) KBW TRD(184, cKA, 1024) PQ TRD(186, cKA, 1280) TRD(188, cKA, 1536) LGK(5) SMM(0)  \
               EST("s42") "ds_write_b32 " cFW ", %[fv]\n\t" LGK(1) SMM(1) IS(7)
#define GADD   "v_add_f32_dpp v220, v208, v210 quad_perm:[0,1,2,3] row_mask:0xf bank_mask:0x3\n\t"                                 \
               "v_add_f32_dpp v220, v209, v211 quad_perm:[0,1,2,3] row_mask:0xf bank_mask:0xc\n\t"
#define LEND   "v_add_f32_dpp v217, v217, v217" DX4 "v_rcp_f32 v218, v217\n\t" PADN                                                \
               "v_pk_mul_f32 v[224:225], v[224:225], v[218:219] op_sel_hi:[1,0]\n\t"                                               \
               CVX(227, 224, 225) IS(8) LST("s40") LST("s41") EVA(1) IS(9) LST("s42") EST("s43") LST("s43")
#define KBF
#define EVO
  asm volatile(PQ0 EPART(K0(6) KIF(" == 2", "s_nop 1\n\t"), KIF(" < 2", K0(7)))
               MPART(KIF(" > 0", K0(5)), KIF(" == 2", K0(7)), KBF)
               "s_nop 1\n\tv_add_f32_dpp v212, v212, v212" DX1 "s_nop 4\n\t" GADD
               "v_fmamk_f32 v221, v220, " SCK ", v216\n14:\n\tv_exp_f32 v220, v221\n\tv_cmp_nge_f32 vcc, s45, v221\n\tv_add_f32 v217, v220, v212\n\t"
               "v_add_f32_dpp v217, v220, v217" DX1 "v_fma_f32 v224, v220, " cC0 ", " cC1 "\n\tv_fmac_f32_dpp v225, v220, " cC0 DX1
               "v_add_f32_dpp v217, v217, v217" DX2 "s_cmp_eq_u64 vcc, 0\n\ts_cselect_b64 exec, -1, 0\n\t" LEND "s_cbranch_scc0 19f\n"
               "13:\n\t.subsection 1\n19:\n\ts_mov_b64 exec, -1\n\t" PQA CN(10000)
               "20:\n\tds_read_b32 v224, " cFA "\n\t" LGK(0) "v_cmp_gt_i32 vcc, %[fv], v224\n\ts_cbranch_vccz 21f\n\t" BUD("20b")
               "21:\n\t" QRE PQ QRL PQ TZERO PQ LGK(0) SXM(0, 0) SXM(1, 0) SXM(0, 1) SXM(1, 1) PQ
               EPART("s_nop 7\n\t", "") MPART("", "", "") PQ
               "v_mov_b32 v213, v212\n\tv_add_f32_dpp v212, v212, v212" DX1 "s_nop 5\n\t" GADD
               "v_fmamk_f32 v221, v220, " SCK ", v216\n\tv_cmp_nge_f32 vcc, s45, v221\n\ts_cbranch_vccz 14b\n\t" PQB
               "v_max_f32 v216, v220, v218\n\tv_mul_f32 v221, " SCK ", v218\n\ts_nop 0\n\t"
               "v_max_f32_dpp v216, v216, v216" DX1 "v_mul_f32 v220, " SCK ", v220\n\ts_nop 0\n\t"
               "v_max_f32_dpp v216, v216, v216" DX2 "s_nop 1\n\t"
               "v_max_f32_dpp v216, v216, v216" DX4
               "v_pk_fma_f32 v[220:221], v[216:217], s[44:45], v[220:221] op_sel_hi:[0,0,1] neg_lo:[1,0,0] neg_hi:[1,0,0]\n\t"
               "v_exp_f32 v221, v221\n\tv_exp_f32 v220, v220\n\t"
               "v_mul_f32 v224, v221, " cC1 "\n\tv_fma_f32 v217, v221, v213, v220\n\t"
               "v_mul_f32 v225, v221, " cC2 "\n\tv_fmac_f32 v224, v220, " cC0 "\n\t"
               "v_add_f32_dpp v217, v217, v217" DX1 "v_fmac_f32_dpp v225, v220, " cC0 DX1 "s_nop 0\n\t"
               "v_add_f32_dpp v217, v217, v217" DX2 "s_nop 1\n\t" LEND "s_branch 13b\n\t.subsection 0"
               : [bud] "+s"(bud), "+{v[160:167]}"(Q), "+{v[148:151]}"(k0), "+{v[200:215]}"(T) EVO
               : [a5] "v"(af[5]), [a6] "v"(af[6]), [a7] "v"(af[7]), [u5] "a"(wk[5]), [u6] "a"(wk[6]), [u7] "a"(wk[7]), [ix] "v"(ix),
                 "{v[112:127]}"(cst), "{v[64:79]}"(x0), "{v[80:95]}"(x1), [D] "s"(D), "{s[40:47]}"(sc), [fv] "v"(fv), [wid] "n"(WID)
               : "memory", "vcc", "scc", "v168", "v169", "v176", "v177", "v178", "v179", "v180", "v181", "v182", "v183", "v184",
                 "v185", "v186", "v187", "v188", "v189", "v190", "v191", "v192", "v193", "v194", "v195",
                 "v216", "v217", "v218", "v219", "v220", "v221", "v222", "v223", "v224", "v225", "v227", "v230", "v231", "v232");
}
#define YMAX 0x42700000u
#define TWO 2
#define ARD(j, p) do { af[j] = *(lds_u32x4*)(p); SB(); } while (0)
#define SECOND(n, NXV) do { if (TWO == n) { asm volatile("s_waitcnt vmcnt(%0)" :: "n"(NXV) : "memory"); __builtin_amdgcn_s_barrier(); CBAR(); SB(); \
                         ARD(4, ard0 + 4096); ARD(5, ard1 + 4096); } if (TWO + 1 == n) { ARD(6, ard2 + 4096); ARD(7, ard3 + 4096); }               \
                         if (TWO + 2 == n) ARD(8, lds + F8_OFF + ar); } while (0)
#define HEAD(M, NXV, H7) do {                                                                                                                     \
    ARD(0, ard0); __builtin_amdgcn_s_barrier(); CBAR(); SB(); ARD(1, ard1); ARD(2, ard2); ARD(3, ard3);                                       \
    if (!TWO) { ARD(4, ard0 + 4096); ARD(5, ard1 + 4096); }                                                                                   \
    M(0, 0); SECOND(1, NXV); M(1, 0); SECOND(2, NXV); M(0, 1); SECOND(3, NXV); M(1, 1); SECOND(4, NXV); M(0, 2); SECOND(5, NXV);              \
    if (!(H7)) M(1, 2);                                                                                                                       \
    SECOND(6, NXV); M(0, 3); M(0, 4); M(0, 5); M(0, 6); M(0, 7); M(0, 8); } while (0)
__device__ __forceinline__ void role_qkc(const Args& a, rsrc_t R, const Go& go, lds_char* lds, int h, int tid, int hw, int lane) {
  const int wave = 3 - hw;
  const int l15 = lane & 15, g = lane >> 4;
  u32x8 wq[2][9], wk[2][9], wp[3];
  int cls = -1, kv0 = -1;
  unsigned g1;
  u32x4 af0[3];
  const int wz = h >> 3, tq = (h >> 1) & 3, hp = h & 1, qi = 2 * g + (l15 >> 3), kq = l15 & 7;
  unsigned pxo = (unsigned)nearoff(NEAR_E + (h >> 1) * 1024 + ((kq >> 1) * 16 + 8 * (qi >> 2) + 4 * hp + (qi & 3)) * 16 + (kq & 1) * 8);
  unsigned plo = !(kq & 1) ? (unsigned)nearoff(NEAR_PM + wz * 1024 + ((2 * hp + (tq >> 1)) * 16 + 4 * (qi >> 1) + (kq & 2) + (qi & 1)) * 16 + (tq & 1) * 8 + (kq >> 2) * 4)
                           : (kq == 1 ? (unsigned)nearoff(NEAR_AL + wz * 256 + ((tq >> 1) * 8 + qi) * 16 + (2 * hp + (tq & 1)) * 4) : OOB);
  {
    u32x4 sg[QDEP][8];
#pragma unroll
    for (int c = 0; c < QDEP; ++c) qk_gload<NTM & 1>(sg[c], a, h, c, tid);
#define WK3(b) do { WALK(b + 2); WALK(b + 1); WALK(b); } while (0)
    if (wave == 0) WK3(200); else if (wave == 1) WK3(208); else if (wave == 2) WK3(216); else WK3(224);
    if (wave == 0) pq_walk<24>(); else if (wave == 1) pq_walk<25>();
    const int q = l15 >> 2;
    const int lrow = 16 * (g >> 1) + 4 * (g & 1) + 8 * (q >> 1) + (q & 1);
    auto wrd = [&](lds_char* p) {
      u32x2 v0 = trd(p), v1 = trd(p + 2 * 544), v2 = trd(p + 32 * 544), v3 = trd(p + 34 * 544);
      return (u32x8){v0[0], v0[1], v1[0], v1[1], v2[0], v2[1], v3[0], v3[1]};
    };
#pragma unroll
    for (int ph = 0; ph < 3; ++ph) {
      if (ph == 2) {
        cls = get_cls(R, lane, cls);
        pxo += (unsigned)(cls & 1) * 8192u; plo += (unsigned)(cls & 1) * 8192u;
        CBAR();
        kv0 = (int)ld32(R, FX(FL_PROG, cls, 5) + h * 128 + (lane & 3) * 4, 0);
#pragma unroll
        for (int f = 0; f < 3; ++f) {
          const int u = (f == 2 ? 8 : f * 4 + wave) * 64 + lane;
          af0[f] = *(const u32x4*)(a.mem0 + (u & 7) * DM + (u >> 3) * 8);
        }
        CBAR();
      }
#pragma unroll
      for (int i = 0; i < 4; ++i) {
        const int c = ph * 4 + i;
        if (c < 9) {
          qk_swrite(sg[c % QDEP], lds + i * QSTG, tid);
          if (c + QDEP < 9) qk_gload<NTM & 1>(sg[c % QDEP], a, h, c + QDEP, tid);
          CBAR();
        }
      }
      RT(8 + ph);
      if (ph == 0) { g1 = ld32(R, go.vo, 0); CBAR(); }
      if (ph == 1) {
        if (__all(g1 != go.v0)) go_ack(R, go, lane); else go_wait(R, go, lane);
        cls = cls_look(R, lane);
        CBAR();
      }
      if (ph == 2) {
        if (wave < 3) {
#pragma unroll
          for (int k = 0; k < 4; ++k) {
            const unsigned so = wave * SLOT_REAL + (k & 1) * CPY1 + (k >> 1) * CPY2;
            st64((u32x2){~0u, ~0u}, R, pxo, so); st32(~0u, R, plo, so);
          }
        }
        CBAR();
        RT(11);
      }
      BAR();
#pragma unroll
      for (int i = 0; i < 4; ++i) {
        const int j = ph * 4 + i;
        if (j < 9) {
          lds_char* buf = lds + (ph < 2 ? ((i + wave) & 3) : 0) * QSTG + lrow * 544 + (l15 & 3) * 8;
#pragma unroll
          for (int t = 0; t < 4; ++t) {
            u32x8& w = t < 2 ? wq[t & 1][j] : wk[t & 1][j];
            w = wrd(buf + t * 128 + wave * 32);
          }
          if (j == 0 || j == 4) wp[j >> 2] = wrd(buf + 256 + 96);
        }
      }
      SB();
#pragma unroll
      for (int k = 0; k < 16; ++k) {
        const int j = ph * 4 + (k >> 2);
        if (j < 9) { u32x8& w = (k & 2) ? wk[k & 1][j] : wq[k & 1][j]; if (j < 8) pin_a(w); else pin_v(w); }
      }
      if (ph < 2) pin_v(wp[ph]);
      if (ph == 0 || wave == ph) wp[2] = wrd(lds + (ph < 2 ? 3 : 0) * QSTG + lrow * 544 + (l15 & 3) * 8 + 256 + 96);
      pin_v(wp[2]);
      BAR();
    }
  }
  RT(1);
  if (cls < 0) return;
  RT(2);
  auto run = [&](auto ROLEC) {
  constexpr int ROLE = decltype(ROLEC)::value;
  lds_char* img = lds;
  const unsigned fo = (unsigned)(cls & 1) * 8192u;
  const int r0 = ROLE * 1024 + lane * 16, r1 = r0 + 4096, r2 = 8192 + lane * 16;
  const unsigned m0 = nearoff(r0) + fo, m1 = nearoff(r1) + fo, m2 = nearoff(r2) + fo;
  const unsigned lb = (unsigned)(uintptr_t)lds, lw0 = lb + ROLE * 1024;
  const i32x4 D = mkdesc(a.wb);
  const int ar = g * 256 + ((l15 >> 1) & 1) * 128 + (2 * (l15 >> 2) + (l15 & 1)) * 16;
  lds_char* ard0 = img + ROLE * 1024 + ar;
  lds_char* ard1 = img + ((ROLE + 1) & 3) * 1024 + ar;
  lds_char* ard2 = img + ((ROLE + 2) & 3) * 1024 + ar;
  lds_char* ard3 = img + ((ROLE + 3) & 3) * 1024 + ar;
  const int gy = ((tq & 1) << 1) | (tq >> 1);
  const unsigned pcl = lane < 32 ? (unsigned)nearoff(NEAR_E + (h >> 1) * 1024 + ((lane >> 2) * 8 + 4 * hp + (lane & 3)) * 16) + fo
                     : (lane < 40 ? (unsigned)nearoff(NEAR_PM + wz * 1024 + ((2 * hp + (tq >> 1)) * 16 + (tq & 1) * 8 + (lane & 7)) * 16) + fo
                                  : (lane < 42 ? (unsigned)nearoff(NEAR_AL + wz * 256 + ((tq >> 1) * 8 + (2 * hp + (tq & 1)) * 2 + (lane & 1)) * 16) + fo : OOB));
  const unsigned kfo = OFF_KF + (unsigned)h * 8192u + lane * 16;
  constexpr int QT = ROLE >> 1;
  const unsigned kmo = OFF_KMF + (unsigned)h * 4096u + lane * 16;
  const unsigned pxs = OFF_PXF + (unsigned)((QT * 40 + h) * 1024 + lane * 16);
  const unsigned pws = OFF_PXF + (unsigned)((QT * 40 + 32 + wz * 2 + hp) * 1024 + (gy * 16 + l15) * 16 + g * 4);
  const unsigned kpf = FX(FL_PROG, cls, 5) + h * 128 + (lane & 3) * 4;
  const unsigned kmf = (lane == 0) ? FX(FL_KMPROG, cls, 3) + h * 128 : OOB;
  const unsigned wpo = lane == 0 ? FX(FL_WP, cls, 0) + h * 32 : OOB;
  const int idx = (lane & 2) ? 0xEEEE : 0x4444;
  const int jr = l15 >> 2;
  const unsigned qw = lb + QIM_OFF + (ROLE * 8 + ((2 * jr + (l15 & 1)) ^ ((ROLE & 1) << 1))) * 32 + g * 8 + (l15 & 2) * 2;
  const unsigned kw = lb + KIM_OFF + ROLE * 256 + l15 * 16 + g * 4;
  lds_char* krd = lds + KIM_OFF + (4 * g + jr) * 16 + (l15 & 2) * 4;
  unsigned fw = lb + FLG_OFF + ROLE * 4;
  pin_v(fw);
  lds_char* frd = lds + FLG_OFF + (lane & 3) * 4;
  const unsigned qa = lb + QIM_OFF + (16 * (g >> 1) + 4 * (g & 1) + (jr & 1) + 10 * (jr >> 1)) * 32 + (l15 & 3) * 8;
  u32x16 cst = {kw, lb + KIM_OFF + (4 * g + jr) * 16 + (l15 & 1) * 8, qa, qa ^ 64, lb + QIM_OFF + ((24 + 2 * g + (jr & 1)) ^ 2) * 32 + (l15 & 3) * 8,
                lb + FLG_OFF + min(lane & 3, 2) * 4, pxo, plo, (l15 & 1) ? 0u : 0x3f800000u, l15 == 1 ? 0x3f800000u : 0u, l15 == 9 ? 0x3f800000u : 0u, fw, qw,
                lb + (l15 >> 3) * 4096 + (g * 16 + 4 * (l15 & 3) + 2 * (jr & 1)) * 16, 0u, 0u};
  const unsigned own = own_cpy(cls, h >> 3);
  unsigned ima = lb + lane * 16;
  pin_v(ima);
  ds_flag(fw, 0u);
  ds_flag(fw + 16, 0u);
  auto poison = [&]() {
#pragma unroll
    for (int f = 0; f < 5; ++f) *(lds_u32x4*)(lds + QIM_OFF + f * 1024 + lane * 16) = (u32x4){~0u, ~0u, ~0u, ~0u};
    asm volatile("s_waitcnt lgkmcnt(0)" ::: "memory");
  };
  if (ROLE == 0) poison();

  int fl = kv0;
  auto k_dma = [&](int sg, int half) {
#pragma unroll
    for (int f = 0; f < 4; ++f)
      asm volatile("s_mov_b32 m0, %2\n\ts_nop 0\n\tbuffer_load_dwordx4 %0, %1, %3 offen offset:%4 sc1 lds"
                   :: "v"(kfo), "s"(D), "s"(lb + KX_OFF + (sg & 1) * 8192 + half * 4096), "s"((unsigned)(sg & RMASK) * 262144u + half * 4096), "n"(f * 1024) : "memory");
  };
  u32x4 qq[4], F[4];
#pragma unroll
  for (int f = 0; f < 4; ++f) { qq[f] = af0[0]; F[f] = af0[0]; }
  f32x4 T0 = {0.f, 0.f, 0.f, 0.f}, T1 = T0;
  float tmx = 0.f, tsx = 0.f;
  auto q_ld = [&](u32x4 (&qx)[4], int sg) {
#pragma unroll
    for (int f = 0; f < 4; ++f)
      asm volatile("buffer_load_dwordx4 %0, %1, %2, %3 offen offset:%4 sc1"
                   : "+v"(qx[f]) : "v"(kfo), "s"(D), "s"((unsigned)sg * 262144u + (OFF_QF - OFF_KF) + QT * 4096), "n"(f * 1024) : "memory");
  };
  auto t_part = [&](u32x4 (&qx)[4], int par, auto NC) {
    asm volatile("s_waitcnt vmcnt(%4)" : "+v"(qx[0]), "+v"(qx[1]), "+v"(qx[2]), "+v"(qx[3]) : "n"(decltype(NC)::value) : "memory");
    T0 = (f32x4){0.f, 0.f, 0.f, 0.f}; T1 = T0;
#pragma unroll
    for (int ks = 0; ks < 4; ++ks) {
      T0 = mfma(*(lds_u32x4*)(lds + KX_OFF + par * 8192 + ks * 1024 + lane * 16), qx[ks], T0);
      if (QT) T1 = mfma(*(lds_u32x4*)(lds + KX_OFF + par * 8192 + 4096 + ks * 1024 + lane * 16), qx[ks], T1);
    }
    pin_v(T0); pin_v(T1); PAD();
    px_mask(T0, T1, QT, lane);
    pin_v(T0); pin_v(T1); PAD();
    px_seg(T0, T1, tmx, tsx);
    pin_v(T0); pin_v(T1); pin_v(tmx); pin_v(tsx); PAD();
  };
  if (ROLE == 3) {
    const bool ok = wait_flag_c<8>(R, kpf, 0, fl);
    k_dma(0, 0); k_dma(0, 1);
    if (!ok) return;
  }
  VMW(0);

  unsigned iso_acc = 0, bud = SPINCAP, tm = 0, rcnt = 0;
  unsigned cv = 0;
  const unsigned cwo = FX(FL_AN, cls, 0) + (h & 15) * 128;
  EVDECL; CKDECL;
  auto k_get = [&](u32x4 (&F)[4], int s, bool parts) {
#pragma nounroll
    for (; bud != 0; --bud) {
      CBAR();
      if (__all((int)*(lds_u32*)frd > s)) break;
      __builtin_amdgcn_s_sleep(1);
    }
    CBAR();
#pragma unroll
    for (int f = 0; f < 4; ++f) { u32x2 lo = trd(krd + f * 512), hi = trd(krd + f * 512 + 256); F[f] = (u32x4){lo[0], lo[1], hi[0], hi[1]}; }
    if (!parts) return;
    const u32x2 p0 = trd(krd + 2048), p1 = trd(krd + 2304);
    F[1][2] = add3bf(F[1][2], p0[0], p1[0]); F[1][3] = add3bf(F[1][3], p0[1], p1[1]);
  };
  auto step = [&](auto PHC, int s) {
    constexpr int PH = decltype(PHC)::value & 1, FIRST = (decltype(PHC)::value >> 1) & 1, LAST = decltype(PHC)::value >> 2;
    constexpr int JC = ROLE == 0 ? 3 : (ROLE == 1 ? 6 : 8);
    u64 e0 = 0, e1 = 0, e3 = 0, e4 = 0; unsigned nr0 = 0;
    u32x4 af[9];
    f32x4 q0, q1, k0, k1, k2;
    PADS(200 + ROLE * 8 + decltype(PHC)::value);
    unsigned f3 = s + 1;
    pin_v(f3);
    constexpr int RD = ROLE == 3 && !LAST;
    f32x16 T;
    u32x16 x0, x1;
    if (RD) t_zero(k0, T); else if (!LAST) k_zero(k0, k1, k2);
    const u32x8 (&wf)[2][9] = LAST ? wk : wq;
#define MQ(t, j) do { smo<t, (j) < 8>(q##t, af[j], wf[t][j], idx); SB(); } while (0)
    lds_char* f8 = lds + F8_OFF + lane * 16;
    bud = __builtin_amdgcn_readfirstlane(bud);
    asm volatile("" : "+s"(bud));
    unsigned nt = 0; int rq = 0;
    tm = __builtin_amdgcn_readfirstlane(tm); rcnt = __builtin_amdgcn_readfirstlane(rcnt);
    asm volatile("" : "+s"(tm), "+s"(rcnt));
    if (RD) asm volatile("" : "+{v[112:127]}"(cst));
    unsigned p0 = (s & 7) * SLOT_REAL, p1 = p0 + CPY1, p2 = p0 ^ CPY2, p3 = p2 + CPY1;
    unsigned ms = (p2 + own) ^ ((s & 4) ? 0 : CPY1);
    u32x8 sc = {p0, p1, p2, p3, 0x3e0293eeu, YMAX, 0u, 0u};
    if (RD) asm volatile("" : "+{s[40:47]}"(sc));
    constexpr int NX = ROLE == 3 || LAST ? 0 : (ROLE == 0 ? 8 : 4);
    constexpr int ID = FIRST ? -1 : ROLE * 8 + decltype(PHC)::value;
    auto head = [&]() {
    v_zero(q0, q1);
    if (!FIRST) {
      int r = 0;
      if (ROLE == 1) r = spin_late(tm, 0);
      rq = r;
      mem_issue2<ROLE == F8W ? 3 : 2>(D, m0, m1, m2, ms, lw0, lw0 + 4096, lb + F8_OFF, e3);
      if (ROLE == 1) { move_on(tm, r); SB(); }
      PAD();
      if (RD) { kx_get<PH>(cst, x0, x1); asm volatile("s_waitcnt lgkmcnt(0)" ::: "memory"); }
      if (ROLE == 3) asm volatile("s_waitcnt vmcnt(%0)" :: "n"(TWO ? 1 + (F8W == 3) : 0) : "memory");
      else if (nt != 0) VMW(0);
      else {
      __builtin_amdgcn_s_sleep(XSLP);
      if (!LAST && (ROLE == 1 || ROLE == 2)) k_dma(s + 1, ROLE - 1);
      if (ROLE == 0 && !LAST) {
#pragma unroll
        for (int ks = 0; ks < 4; ++ks) st128(F[ks], R, kmo + ks * 1024, (unsigned)(s - 1) * 131072u);
      }
      if (ROLE == 0 && !LAST) {
        st128((u32x4){~0u, ~0u, ~0u, ~0u}, R, pcl, (unsigned)((s + 2) & 3) * SLOT_REAL);
        st128((u32x4){~0u, ~0u, ~0u, ~0u}, R, pcl, (unsigned)((s + 2) & 3) * SLOT_REAL + CPY1);
        st128((u32x4){~0u, ~0u, ~0u, ~0u}, R, pcl, (unsigned)((s + 2) & 3) * SLOT_REAL + CPY2);
        st128((u32x4){~0u, ~0u, ~0u, ~0u}, R, pcl, (unsigned)((s + 2) & 3) * SLOT_REAL + CPY2 + CPY1);
      }
      PAD();
      CBAR();
      asm volatile("s_waitcnt vmcnt(%0)" :: "n"(NX + (TWO ? 1 + (ROLE == F8W) : 0)) : "memory");
      }
    } else {
      *(lds_u32x4*)(img + r0) = af0[0]; *(lds_u32x4*)(img + r1) = af0[1]; *(lds_u32x4*)f8 = af0[2];
      asm volatile("s_waitcnt lgkmcnt(0)" ::: "memory");
    }
    SB();
    HEAD(MQ, NX, 0);
    if (s == 0) RT(3);
    if (s == 1) RT(4);
    if (s == 8) RT(5);
    if (s == 60) RT(6);
    ISOQ(1);
    q_mid<ID>(af, wf[1], idx, LAST ? kw : qw, q0, q1); SB();
    };
    if (!FIRST) {
      tm -= GRID + XT; nt = ~0u;
      try_top<ID>(nt, ms, tm, rcnt, ima); SB();
      head();
      try_end(ima, nt, ms, tm, rcnt, m0, m1, m2, lw0, lw0 + 4096, lb + F8_OFF, D); SB();
    } else
    {
      if (FIRST && RD) { kx_get<PH>(cst, x0, x1); asm volatile("s_waitcnt lgkmcnt(0)" ::: "memory"); }
      head();
    }
    ISOQ(2);
    EV(e0);
    if (LAST) k_last(kw, q1, fw, f3);
    if (ROLE < 3) {
      if (!LAST) {
        float t;
        k_one(af, wk[0], wp, idx, qw, q1, k0, k2, t); SB();
        ISOQ(4);
        if (ROLE == 1 && FIRST) { st32(f3, R, wpo, 0); SB(); }
        k_end<ROLE == 2 ? 256 : 2048, JC>(af, wk[1], wp[2], idx, k0, k1, k2, t, kw, fw, f3); SB();
      }
      ISOQ(6);
      PAD();
      if (ROLE == 1 && !LAST) {
        nr0 = nt != 0;
        if (!FIRST) next_time(tm, s);
      }
      if (ROLE == 0 && !LAST) {
        __builtin_amdgcn_s_sleep(TSLP);
        k_get(F, s, true);
        ds_flag(lb + FLG_OFF + 20, f3);
        pin_v(F[0]); pin_v(F[1]); pin_v(F[2]); pin_v(F[3]);
        VMW(0);
        st32((unsigned)(s - 1), R, kmf, 0);
        if (!PH && !FIRST && UNLIKELY(s == NSEG - 2)) {
#pragma unroll
          for (int ks = 0; ks < 4; ++ks) st128(F[ks], R, kmo + ks * 1024, (unsigned)s * 131072u);
          VMW(0);
          st32((unsigned)s, R, kmf, 0);
        }
      }
      if (ROLE == 2 && !LAST) {
        if (FIRST) { k_dma(1, 0); k_dma(1, 1); }
        gate_wait(R, kpf, min(2 * s + 4 + (s >= NSEG - 4), 2 * NSEG - 1), fl, bud);
        fl = (int)ld32(R, kpf, 0);
        VMW(0);
        pin_v(fl);
      }
      if ((ROLE == 0 || ROLE == 2) && !PH && !FIRST && UNLIKELY(s == NSEG - 2)) {
        k_dma(s + 1, 0);
        q_ld(qq, s + 1);
        t_part(qq, 1, std::integral_constant<int, 0>());
      }
      if ((ROLE == 0 || ROLE == 2) && LAST) {
        f32x4 G = {0.f, 0.f, 0.f, 0.f};
        u32x4 px; unsigned pw;
        k_get(F, s, false);
#pragma unroll
        for (int ks = 0; ks < 4; ++ks) G = mfma(F[ks], qq[ks], G);
        px_fin(G, T0, T1, tmx, tsx, px, pw, lane);
        st128(px, R, pxs, (unsigned)s * 81920u); st32(pw, R, pws, (unsigned)s * 81920u);
        VMW(0);
        st32((unsigned)s, R, lane == 0 ? FX(FL_PXLAST, cls, 1) + h * 256 + QT * 128 : OOB, 0);
      }
      CBAR();
      if (ROLE == 0) CKS();
    } else if (!LAST) {
      u32x8 Q;
      s_first(af, wk[1], idx, cst, x0, x1, q1, k0, T, Q); SB();
      el_phase<ID>(D, af, wk[1], idx, cst, x0, x1, Q, k0, T, bud, e1, e4, nr0, sc, f3);
      ds_flag(fw + 4, f3);
      EVPUT(2, e1); EVPUT(4, e4);
    }
    EVPUT(0, e0);
    EVPUTI(1, nr0); if (!FIRST) EVPUT(3, e3);
    if (ROLE == 1 && !LAST) {
#pragma nounroll
      for (; bud != 0; --bud) {
        CBAR();
        if (__all((int)*(lds_u32*)(lds + FLG_OFF + 16 + (lane & 1) * 4) > s)) break;
        __builtin_amdgcn_s_sleep(1);
      }
      poison();
      VMW(0);
      if (FIRST) { tm = get_t1(R, cwo, bud); asm volatile("" : "+s"(tm)); }
    }
    if (PH && !LAST && UNLIKELY(s == WLAST)) WALK(200 + ROLE * 8 + 5);
    PADE();
#undef MQ
  };
  step(std::integral_constant<int, 2>(), 0);
#pragma nounroll
  for (int s = 1; s < NSEG - 1; s += 2) {
    step(std::integral_constant<int, 1>(), s);
    step(std::integral_constant<int, 0>(), s + 1);
  }
  step(std::integral_constant<int, 5>(), NSEG - 1);
  EVDUMP();
  if (ROLE == 0) CKDUMP();
  };
  if (hw == 0) run(std::integral_constant<int, 3>());
  else if (hw == 1) run(std::integral_constant<int, 2>());
  else if (hw == 2) run(std::integral_constant<int, 1>());
  else run(std::integral_constant<int, 0>());
  RT(7);
  SV(15, cls);
}

#define VIM_OFF 8192
#define NOP(n) do { asm volatile("s_nop " #n); SB(); } while (0)
#define cVW "v112"
#define cRA "v113"
#define cRB "v114"
#define cHA "v115"
#define cHB "v116"
#define cVF "v117"
#define cVR "v118"
#define cIY "v119"
#define YP(p, c) "v_smfmac_f32_16x16x64_bf16 v[148+4*" #p ":151+4*" #p "], v[168+4*" #c "+4*(" #c ">>1):171+4*" #c "+4*(" #c ">>1)], %[w" #p #c "], " cIY "\n\t"
#define Y8(k) "v_smfmac_f32_16x16x64_bf16 v[164:167], v[176+12*" #k ":179+12*" #k "], %[w8" #k "], " cIY "\n\t"
#define HA(d, b) "v_add_f32 v" #d ", v[" b "], v[" b "+2]\n\t"
#define HB(d, b) "v_add_f32 v" #d ", v[" b "+1], v[" b "+3]\n\t"
#define YC(p, e) "v_cvt_pk_bf16_f32 v[192+2*" #p "+" #e "], v[148+4*" #p "+2*" #e "], v[149+4*" #p "+2*" #e "]\n\t"
#define YS(p) ".if %[st]\n\tbuffer_store_dwordx2 v[192+2*" #p ":193+2*" #p "], v[120+" #p "], %[D], %[ys] offen sc1\n\t.endif\n\t"
#define S16(a, b) "v_permlane16_swap_b32 v" #a ", v" #b "\n\t"
#define Z2(r) "v_mov_b64 v[" #r ":" #r "+1], 0\n\tv_mov_b64 v[" #r "+2:" #r "+3], 0\n\t"
#define YZERO Z2(148) Z2(152) Z2(156) Z2(160) Z2(164)
#define VR0 TRD(168, cRA, 0) TRD(170, cRB, 0)
#define VR1 TRD(172, cRA, 512) TRD(174, cRB, 512) TRD(176, cHA, 0) TRD(178, cHB, 0)
__device__ __forceinline__ void y_zero(f32x16& ya, f32x4& yc) {
  asm volatile(YZERO : "={v[148:163]}"(ya), "={v[164:167]}"(yc));
}
#define HV 0
#define VO 1
#define VR 1
#define YEO
#define YEN
template <int ID>
__device__ __forceinline__ void v_end(const u32x4 (&af)[9], const u32x8 (&w)[9], int ix, u32x16 cst, f32x4 v0, f32x4& v1, u32x16& A,
                                      const u32x8 (&wo)[4][4], f32x16& ya, u64& e1) {
#define VH0 HA(201, "140") HB(202, "140") CVX(203, 201, 202)
#define VH1 HA(201, "144") HB(202, "144") CVX(203, 201, 202)
#define VW0 "ds_write_b32 " cVW ", v203\n\t"
#define VW1 "ds_write_b32 " cVW ", v203 offset:1024\n\t"
#define VCM "v_cmp_u_f32 vcc, v201, v202\n\t"
#define TRYL TRYB
  asm volatile(
               Q1(3) Q1(4) VH0 Q1(5) VW0 Q1(6) "s_barrier\n\t" VR0 Q1(7) TRD(172, cRA, 512) TRD(174, cRB, 512) Q1(8) TRD(176, cHA, 0) TRD(178, cHB, 0) VCM
               LGK(4) YP(0, 0) IS(16) VH1 YP(1, 0) VW1 TRYL
               : "+{v[144:147]}"(v1), "=&{v[168:183]}"(A), "+{v[148:163]}"(ya) YEO
               : "{v[140:143]}"(v0), [a2] "v"(af[2]), [a3] "v"(af[3]), [a4] "v"(af[4]), [a5] "v"(af[5]), [a6] "v"(af[6]), [a7] "v"(af[7]), [a8] "v"(af[8]),
                 [w2] "a"(w[2]), [w3] "a"(w[3]), [w4] "a"(w[4]), [w5] "a"(w[5]), [w6] "a"(w[6]), [w7] "a"(w[7]), [w8] "v"(w[8]), [ix] "v"(ix), "{v[112:127]}"(cst), [id] "n"(ID),
                 [w00] "a"(wo[0][0]), [w10] "a"(wo[1][0])
               : "memory", "v201", "v202", "v203", TRYC);
}
template <int ST, int WID>
__device__ __forceinline__ void y_all(const u32x8 (&wo)[4][4], const u32x8 (&w8)[2], u32x16 cst, f32x16 ya, f32x4 yc, u32x16 A, i32x4 D,
                                      unsigned ys, unsigned fv, u32x8& o, unsigned& o8, u64& e1, unsigned& nr) {
#define RH1 TRD(188, cHA, 1024) TRD(190, cHB, 1024)
#define RQ2 TRD(180, cRA, 1024) TRD(182, cRB, 1024)
#define RQ3 TRD(184, cRA, 1536) TRD(186, cRB, 1536)
#define VFL "ds_write_b32 " cVF ", %[fv]\n\t"
#define YK0 YP(2, 0) YP(3, 0) RH1 LGK(3) YP(0, 1) RQ2 Y8(0) RQ3 YP(1, 1) VFL IS(17)
#define YK1    LGK(5) Y8(1) IS(18) YP(2, 1) YP(3, 1) S16(164, 166) LGK(3) YP(0, 2) PQ S16(165, 167) LGK(1) YP(0, 3) HA(201, "164") YP(1, 2) HB(202, "164") CVX(200, 201, 202) PQ \
               YP(1, 3) ".if %[st]\n\tbuffer_store_dword v200, v124, %[D], %[ys] offen sc1\n\t.endif\n\t" YC(0, 0) YP(2, 2) YC(0, 1) YP(2, 3) PQ YS(0) YC(1, 0)\
               YP(3, 2) YC(1, 1) YP(3, 3) IS(19) YS(1) PQ YC(2, 0) YC(2, 1) YS(2) "s_nop 5-2*%[st]\n\t" YC(3, 0) YC(3, 1) YS(3) IS(20) EVA(1)
  asm volatile(PQ0 YK0 YK1
               "v_cmp_u_f32 vcc, v148, v150\n\tv_cmp_u_f32 s[46:47], v201, v202\n\ts_or_b64 vcc, vcc, s[46:47]\n\ts_cbranch_scc1 19f\n"
               "13:\n\t.subsection 1\n"
               PQA "19:\n\tds_read_b32 v204, " cVR "\n\t" LGK(0) "v_cmp_gt_i32 vcc, %[fv], v204\n\ts_cbranch_vccnz 19b\n\t"
               CN(100) VR0 PQ VR1 RH1 PQ RQ2 RQ3 Z2(148) PQ Z2(152) Z2(156) Z2(160) PQ Z2(164) LGK(0) YP(0, 0) YP(1, 0) YP(2, 0) YP(3, 0) PQ YP(0, 1) Y8(0) YP(1, 1) PQ YK1
               "s_branch 13b\n\t" PQB ".subsection 0"
               : "=&{v[192:199]}"(o), "=&{v200}"(o8) YEO YEN
               : [w00] "a"(wo[0][0]), [w01] "a"(wo[0][1]), [w02] "a"(wo[0][2]), [w03] "a"(wo[0][3]), [w10] "a"(wo[1][0]), [w11] "a"(wo[1][1]),
                 [w12] "a"(wo[1][2]), [w13] "a"(wo[1][3]), [w20] "a"(wo[2][0]), [w21] "a"(wo[2][1]), [w22] "a"(wo[2][2]), [w23] "a"(wo[2][3]),
                 [w30] "a"(wo[3][0]), [w31] "a"(wo[3][1]), [w32] "a"(wo[3][2]), [w33] "a"(wo[3][3]), [w80] "v"(w8[0]), [w81] "v"(w8[1]),
                 "{v[112:127]}"(cst), "{v[148:163]}"(ya), "{v[164:167]}"(yc), "{v[168:183]}"(A),
                 [D] "s"(D), [ys] "s"(ys), [fv] "v"(fv), [st] "n"(ST), [wid] "n"(WID)
               : "memory", "vcc", "scc", "s46", "s47", "v184", "v185", "v186", "v187", "v188", "v189", "v190", "v191", "v201", "v202", "v204");
}
__device__ __forceinline__ void role_vcu(const Args& a, rsrc_t R, const Go& go, lds_char* lds, int h, int tid, int wave, int lane) {
  const int l15 = lane & 15, g = lane >> 4;
  u32x8 wv[2][9], wo[4][4], w8[2];
  int cv0 = -1;
  unsigned g1;
  u32x4 af0[3];
  {
    u32x4 st[36], so[36];
#pragma unroll
    for (int k = 0; k < 36; ++k) so[k] = gld<NTM & 2>(a.Wo + (((size_t)h * 128 * DM + (tid + 256 * k) * 8) & ((NOCW & 2) ? 2047 : ~0ull)));
    if (wave == 0) WK3(232); else if (wave == 1) WK3(240); else if (wave == 2) WK3(248); else WK3(256);
    if (wave == 0) { pq_walk<32>(); pq_walk<33>(); } else if (wave == 1) { pq_walk<40>(); pq_walk<41>(); }
    else if (wave == 2) { pq_walk<48>(); pq_walk<49>(); } else { pq_walk<56>(); pq_walk<57>(); }
    const int q = l15 >> 2;
    const int lrow = 16 * (g >> 1) + 4 * (g & 1) + 8 * (q >> 1) + (q & 1), lrow2 = 8 * (g >> 1) + 4 * (g & 1) + (q & 1), up2 = (g & 1) ? 12 : 20;
    auto wrd = [&](lds_char* p, int rs, int up) {
      u32x2 v0 = trd(p), v1 = trd(p + 2 * rs), v2 = trd(p + up * rs), v3 = trd(p + (up + 2) * rs);
      return (u32x8){v0[0], v0[1], v1[0], v1[1], v2[0], v2[1], v3[0], v3[1]};
    };
#pragma unroll
    for (int ph = 0; ph < 4; ++ph) {
      if (ph < 2) {
#pragma unroll
        for (int k = 0; k < 18; ++k) { const int u = tid + 256 * k; *(lds_u32x4*)(lds + (u / 72) * 1184 + (u % 72) * 16) = so[ph * 18 + k]; }
#pragma unroll
        for (int k = ph * 18; k < ph * 18 + 18; ++k) { const int u = tid + 256 * k; st[k] = gld<NTM & 2>(a.Wv + (((size_t)(u >> 4) * HD + h * 128 + (u & 15) * 8) & ((NOCW & 2) ? 2047 : ~0ull))); }
      } else {
#pragma unroll
        for (int k = (ph - 2) * 16; k < (ph == 2 ? 16 : 36); ++k) { const int u = tid + 256 * k; *(lds_u32x4*)(lds + ((u >> 4) - (ph - 2) * 256) * 288 + (u & 15) * 16) = st[k]; }
      }
      if (ph == 0) { CBAR(); g1 = ld32(R, go.vo, 0); CBAR(); }
      if (ph == 2) {
        if (__all(g1 != go.v0)) go_ack(R, go, lane); else go_wait(R, go, lane);
        cv0 = cls_look(R, lane);
#pragma unroll
        for (int f = 0; f < 3; ++f) {
          const int u = (f == 2 ? 8 : f * 4 + wave) * 64 + lane;
          af0[f] = *(const u32x4*)(a.mem0 + (u & 7) * DM + (u >> 3) * 8);
        }
        CBAR();
      }
      BAR();
      if (ph < 2) {
#pragma unroll
        for (int k = 0; k < 8; ++k) wo[k >> 1][2 * ph + (k & 1)] = wrd(lds + (lrow2 + 32 * (k & 1)) * 1184 + (wave * 9 + (k & 6) + (q >> 1)) * 32 + (l15 & 3) * 8, 1184, up2);
        w8[ph] = wrd(lds + (lrow2 + 32 * (q >> 1)) * 1184 + (wave * 9 + 8) * 32 + (l15 & 3) * 8, 1184, up2);
        SB();
#pragma unroll
        for (int k = 0; k < 8; ++k) pin_a(wo[k >> 1][2 * ph + (k & 1)]);
        pin_v(w8[ph]);
      } else {
#pragma unroll
        for (int k = 0; k < (ph == 2 ? 8 : 10); ++k) {
          const int i = k >> 1, t = k & 1;
          wv[t][(ph - 2) * 4 + i] = wrd(lds + ((i < 4 ? (i + wave) & 3 : 4) * 64 + lrow) * 288 + (64 * t + 16 * wave) * 2 + (l15 & 3) * 8, 288, 32);
        }
        SB();
#pragma unroll
        for (int k = 0; k < (ph == 2 ? 8 : 10); ++k) { if (k < 8) pin_a(wv[k & 1][(ph - 2) * 4 + (k >> 1)]); else pin_v(wv[k & 1][8]); }
      }
      BAR();
    }
  }
  RT(1);
  const int cls = get_cls(R, lane, cv0);
  if (cls < 0) return;
  RT(2);
  auto run = [&](auto ROLEC) {
  constexpr int ROLE = decltype(ROLEC)::value;
  lds_char* img = lds;
  const unsigned fo = (unsigned)(cls & 1) * 8192u;
  const int r0 = ROLE * 1024 + lane * 16, r1 = r0 + 4096, r2 = 8192 + lane * 16;
  const unsigned m0 = nearoff(r0) + fo, m1 = nearoff(r1) + fo, m2 = nearoff(r2) + fo;
  const unsigned lb = (unsigned)(uintptr_t)lds, lw0 = lb + ROLE * 1024;
  const i32x4 D = mkdesc(a.wb);
  const int ar = g * 256 + ((l15 >> 1) & 1) * 128 + (2 * (l15 >> 2) + (l15 & 1)) * 16;
  lds_char* ard0 = img + ROLE * 1024 + ar;
  lds_char* ard1 = img + ((ROLE + 1) & 3) * 1024 + ar;
  lds_char* ard2 = img + ((ROLE + 2) & 3) * 1024 + ar;
  lds_char* ard3 = img + ((ROLE + 3) & 3) * 1024 + ar;
  const int wz = h >> 3, tq = (h >> 1) & 3, hp = h & 1;
  const int gh = ((tq & 1) << 1) | (tq >> 1);
  unsigned yr[5], yz[5];
#pragma unroll
  for (int q = 0; q < 5; ++q) {
    const int m = ROLE * 9 + q * 2 + (q < 4 ? g & 1 : 0), o = l15 * 16 + (q < 4 ? (g >> 1) * 8 : g * 4);
    yr[q] = ywin(cls, m) + (unsigned)((wz * 2 + hp) * 1024 + tq * 256 + o);
    yz[q] = NOYC ? OOB : OFF_ZF + (unsigned)(m * 40960 + (32 + wz * 2 + hp) * 1024 + gh * 256 + o);
  }
  lds_char* vw = lds + VIM_OFF + (ROLE * 16 + l15) * 16 + g * 4;
  const unsigned ra = lb + VIM_OFF + (8 * g + 4 * (g >> 1) + (l15 >> 2)) * 16 + (l15 & 2) * 4, rh = ra + (l15 & 1) * 512;
  u32x16 cst = {(unsigned)(uintptr_t)vw, ra, ra ^ 64, rh, rh ^ 64, lb + FLG_OFF + ROLE * 4, lb + FLG_OFF + (lane & 3) * 4, (lane & 4) ? 0xEEEEu : 0x4444u,
                yr[0], yr[1], yr[2], yr[3], yr[4], 0u, 0u, 0u};
  ds_flag(lb + FLG_OFF + ROLE * 4, 0u);
  const unsigned own = own_cpy(cls, h >> 3);
  unsigned ima = lb + lane * 16;
  pin_v(ima);
  const int idx = (lane & 2) ? 0xEEEE : 0x4444;
  unsigned tm = 0, rcnt = 0, cv = 0, iso_acc = 0, o8 = 0, bud = SPINCAP;
  u32x8 o = {0u, 0u, 0u, 0u, 0u, 0u, 0u, 0u};
  auto y_copy = [&](int s) {
#pragma unroll
    for (int q = 0; q < 4; ++q) st64((u32x2){o[2 * q], o[2 * q + 1]}, R, yz[q], (unsigned)s * ZS);
    st32(o8, R, yz[4], (unsigned)s * ZS);
  };
  auto poison = [&]() { asm volatile("v_mov_b32 v203, -1\n\tds_write2st64_b32 " cVW ", v203, v203 offset1:4" :: "{v[112:127]}"(cst) : "memory", "v203"); };
  const unsigned cwo = FX(FL_AN, cls, 0) + ((h + 8) & 15) * 128;
  EVDECL;
  auto step = [&](auto PHC, int s) {
    constexpr int PH = decltype(PHC)::value & 1, FIRST = (decltype(PHC)::value >> 1) & 1, LAST = decltype(PHC)::value >> 2;
    u64 e0 = 0, e1 = 0, e3 = 0, e4 = 0, e5 = 0, e6 = 0;
    u32x4 af[9];
    f32x4 v0, v1, yc;
    f32x16 ya;
    u32x16 A;
    unsigned nsl = 0;
    PADS(232 + ROLE * 8 + decltype(PHC)::value);
    unsigned f3 = s + 1;
    pin_v(f3);
    asm volatile("" : "+{v[112:127]}"(cst));
    PAD();
    unsigned nt = 0;
    tm = __builtin_amdgcn_readfirstlane(tm); rcnt = __builtin_amdgcn_readfirstlane(rcnt);
    asm volatile("" : "+s"(tm), "+s"(rcnt));
#define MV(t, j) do { smo<t, (j) < 8>(v##t, af[j], wv[t][j], idx); SB(); } while (0)
    unsigned ms = ((((s & 7) * SLOT_REAL) ^ CPY2) + own) ^ ((s & 4) ? 0 : CPY1);
    constexpr int ID = FIRST ? -1 : 32 + ROLE * 8 + decltype(PHC)::value;
    auto head = [&]() {
      y_zero(ya, yc);
      v_zero(v0, v1);
      if (!FIRST) {
        int r = 0;
        if (ROLE == 1) r = spin_late(tm, DVQ);
        mem_issue2<ROLE == F8W ? 3 : 2>(D, m0, m1, m2, ms, lw0, lw0 + 4096, lb + F8_OFF, e3);
        if (ROLE == 1) { move_on(tm, r); SB(); }
        poison();
        PAD();
        if (LAST || nt != 0) VMW(0);
        else {
          __builtin_amdgcn_s_sleep(XSLP);
          y_copy(s - 1);
          CBAR();
          asm volatile("s_waitcnt vmcnt(%0)" :: "n"(5 + (TWO ? 1 + (ROLE == F8W) : 0)) : "memory");
        }
      } else {
        *(lds_u32x4*)(img + r0) = af0[0]; *(lds_u32x4*)(img + r1) = af0[1]; *(lds_u32x4*)(lds + F8_OFF + lane * 16) = af0[2];
        poison();
        asm volatile("s_waitcnt lgkmcnt(0)" ::: "memory");
      }
      SB();
      EV(e6);
      HEAD(MV, 5, HV);
      ISOQ(11);
      v_end<ID>(af, wv[1], idx, cst, v0, v1, A, wo, ya, e0); SB();
    };
    if (!FIRST) {
      tm -= GRID + XT; nt = ~0u;
      try_top<ID>(nt, ms, tm, rcnt, ima); SB();
      head();
      try_end(ima, nt, ms, tm, rcnt, m0, m1, m2, lw0, lw0 + 4096, lb + F8_OFF, D); SB();
    } else
    head();
    if (LAST) { st32((unsigned)(s - 1), R, lane == 0 ? FX(FL_YPROG, cls, 2) + h * 16 + ROLE * 4 : OOB, 0); SB(); }
    EV(e4); ISOQ(13);
    y_all<!LAST, LAST ? -1 : ID>(wo, w8, cst, ya, yc, A, D, (unsigned)(s & 1) * YSLOT_REAL, f3, o, o8, e5, nsl); SB();
    ISOQ(15);
    PAD();
#undef MV
    if (FIRST) {
      VMW(0); BAR();
      st32(0u, R, tid == 0 ? FX(FL_WP, cls, 0) + (32 + h) * 32 : OOB, 0);
    }
    if (LAST || (!PH && !FIRST && UNLIKELY(s == NSEG - 2))) y_copy(s);
    CBAR();
    if (ROLE == 1 && !LAST) {
      if (FIRST) { tm = get_t1(R, cwo, bud); asm volatile("" : "+s"(tm)); }
      else
      next_time(tm, s);
    }
    if (PH && !LAST && UNLIKELY(s == WLAST)) WALK(232 + ROLE * 8 + 5);
    PADE();
    EVPUT(0, e0); EVPUT(6, e6); EVPUT(4, e4);
    EVPUTI(1, nt); EVPUT(2, e1); if (!FIRST) EVPUT(3, e3);
  };
  step(std::integral_constant<int, 2>(), 0);
#pragma nounroll
  for (int s = 1; s < NSEG - 1; s += 2) {
    step(std::integral_constant<int, 1>(), s);
    step(std::integral_constant<int, 0>(), s + 1);
  }
  step(std::integral_constant<int, 5>(), NSEG - 1);
  VMW(0);
  st32((unsigned)(NSEG - 1), R, lane == 0 ? FX(FL_YPROG, cls, 2) + h * 16 + ROLE * 4 : OOB, 0);
  EVDUMP();
  };
  if (wave == 0) run(std::integral_constant<int, 0>());
  else if (wave == 1) run(std::integral_constant<int, 1>());
  else if (wave == 2) run(std::integral_constant<int, 2>());
  else run(std::integral_constant<int, 3>());
  RT(7);
}

#define NG 4
#define UL(k, o) "buffer_load_dwordx4 v[128+4*" #k ":128+4*" #k "+3], %[u" #o ", %[D], %[su] offen offset:(" #k "&3)*1024 sc1\n\t"
#define UL4(c, k0, k1, k2, k3, o) ".if " #k0 c "4*" XSTR(NG) "\n\t" UL(k0, o) UL(k1, o) UL(k2, o) UL(k3, o) ".endif\n\t"
#define ULD(c) UL4(c, 0, 1, 2, 3, 0]) UL4(c, 4, 5, 6, 7, 1]) UL4(c, 8, 9, 10, 11, 2]) UL4(c, 12, 13, 14, 15, 3])   \
               UL4(c, 16, 17, 18, 19, 4]) UL4(c, 20, 21, 22, 23, 5]) UL4(c, 24, 25, 26, 27, 6]) UL4(c, 28, 29, 30, 31, 7])
#define UG(G, k0, k1, k2, k3, o) "s_bfe_u32 %[k], %[r], 0x40000+4*" #G "\n\ts_cmp_lg_u32 %[k], 15\n\ts_cbranch_scc1 23f\n\t" \
                                 UL(k0, o) UL(k1, o) UL(k2, o) UL(k3, o) "s_or_b32 %[sa], %[sa], 15<<(4*" #G ")\n23:\n\t"
#define USL 2
#define ULD0 ULD(" < ")
#define ULD1 ULD(" >= ")
#define TB 12
#define UM(T, lo, hi, n) ".if " #T " >= " lo " && " #T " < " hi "\n\t.if !(" #T " & 1)\n\ts_waitcnt vmcnt(" n "+28-2*" #T ")\n\t.endif\n\t"   \
                         "v_swap_b32 v[129+8*" #T "], v[132+8*" #T "]\n\tv_swap_b32 v[131+8*" #T "], v[134+8*" #T "]\n\t.endif\n\t"
#define UMS(...) UM(0, __VA_ARGS__) UM(1, __VA_ARGS__) UM(2, __VA_ARGS__) UM(3, __VA_ARGS__) UM(4, __VA_ARGS__) UM(5, __VA_ARGS__) UM(6, __VA_ARGS__) UM(7, __VA_ARGS__) \
                 UM(8, __VA_ARGS__) UM(9, __VA_ARGS__) UM(10, __VA_ARGS__) UM(11, __VA_ARGS__) UM(12, __VA_ARGS__) UM(13, __VA_ARGS__) UM(14, __VA_ARGS__) UM(15, __VA_ARGS__)
#define TIMES "s_and_b32 %[tt], %[lev], 255\n\ts_add_u32 %[tn], %[tq], %[tt]\n\ts_add_u32 %[tn], %[tn], %[t0]\n\t" \
              "s_lshr_b32 %[tt], %[lev], 8\n\ts_add_u32 %[tt], %[tt], %[tq]\n\t"
#define DZERO ".rept 32\n\tv_mov_b64 v[64+2*\\+:65+2*\\+], 0\n\t.endr\n\tv_mov_b64 v[60:61], 0\n\tv_mov_b64 v[62:63], 0\n\t"
#define WP(a, b) #a ":\n\ts_cbranch_execz " #b "\n\t"
#define DZ6(k) ".rept 6\n\tv_mov_b64 v[60+12*" #k "+2*\\+:61+12*" #k "+2*\\+], 0\n\t.endr\n\t"
#define YLD(k, o) "buffer_load_dwordx4 a[64+4*" #k ":64+4*" #k "+3], %[y" #o ", %[D], %[sy] offen offset:(" #k "&3)*1024 sc1\n\t"
#define YPO(k, o) "buffer_store_dwordx4 %[one], %[y" #o ", %[D], %[sy] offen offset:(" #k "&3)*1024 sc1\n\t"
#define ELD(T, o) "buffer_load_dwordx4 a[4*" #T ":4*" #T "+3], %[e" #o ", %[D], %[sq] offen offset:(" #T "&3)*1024 sc1\n\t"
#define PLD(G) "buffer_load_dwordx4 a[96+4*" #G ":96+4*" #G "+3], %[pm], %[D], %[sq] offen offset:" #G "*1024 sc1\n\t"
#define ALD(G) "buffer_load_dwordx4 a[112+4*" #G ":112+4*" #G "+3], %[al], %[D], %[sq] offen offset:" #G "*256 sc1\n\t"
#define VW(X, n) "s_waitcnt vmcnt(1+" n ")\n\t"
#define EM(T) "v_smfmac_f32_16x16x64_bf16 v[64+4*" T ":64+4*" T "+3], a[4*" T ":4*" T "+3], v[128+8*" T ":128+8*" T "+7], %[ie]\n\t"
#define WC0(T) "v_cvt_pk_bf16_f32 v[64+2*" #T "], v[64+4*" #T "], v[64+4*" #T "+1]\n\t"
#define WC1(T) "v_cvt_pk_bf16_f32 v[65+2*" #T "], v[64+4*" #T "+2], v[64+4*" #T "+3]\n\t"
#define WCV(T) WC0(T) WC1(T)
#define EM2_0(T, C, o) VW("0", "30-" #T) EM(#T) WC0(C) EM("(" #T "+1)") WC1(C)

#define PMM(X, G) VW(X, "7-" #G) "v_smfmac_f32_16x16x64_bf16 v[60:63], a[96+4*" #G ":96+4*" #G "+3], a[64+8*" #G ":64+8*" #G "+7], %[ip]\n\t"
#define AMM(X, G) VW(X, "3-" #G) "v_smfmac_f32_16x16x64_bf16 v[60:63], a[112+4*" #G ":112+4*" #G "+3], v[64+8*" #G ":64+8*" #G "+7], %[ia]\n\t"
#define TRY(X, EM2, a, b, c, d)                                                                                                       \
      SPIN(tt, a, b, c) MEV(1) MCY(0)                                                                                                \
      "buffer_load_dword v59, %[es], %[D], %[sq] offen sc1\n\t" ELD(0, 0]) ELD(1, 0]) ELD(2, 0]) ELD(3, 0]) ELD(4, 1]) ELD(5, 1]) ELD(6, 1]) ELD(7, 1])                                        \
      ELD(8, 2]) ELD(9, 2]) ELD(10, 2]) ELD(11, 2]) ELD(12, 3]) ELD(13, 3]) ELD(14, 3]) ELD(15, 3])                                  \
      YLD(0, 0]) YLD(1, 0]) YLD(2, 0]) YLD(3, 0]) YLD(4, 1]) YLD(5, 1]) YLD(6, 1]) YLD(7, 1])                                        \
      PLD(0) PLD(1) PLD(2) PLD(3) ALD(0) ALD(1) ALD(2) ALD(3) MCY(1) "buffer_load_dword %[ub], %[uq], %[D], 0 offen sc1\n\ts_cmp_lg_u32 %[sx], 0\n\ts_cbranch_scc1 77f\n\t" UMS(XSTR(TB), "16", "43") "s_mov_b32 %[sx], 1\n77:\n\t"                                                                 \
      VW("0", "32") "v_cmp_eq_u32 vcc, -1, v59\n\t" VW("0", "30") "s_cbranch_vccnz 61f\n62:\n\t" EM("0") MCY(2) EM("1")                                                                                           \
      EM2(2, 0, 0]) EM2(4, 1, 0]) EM2(6, 2, 0]) EM2(8, 3, 0]) EM2(10, 4, 1]) EM2(12, 5, 1]) EM2(14, 6, 1]) MCY(3)                           \
      "s_cmp_lt_i32 %[r], -" XSTR(LTOL) "\n\ts_cbranch_scc0 " d "f\n\t"                              \
      "63:\n\ts_add_u32 %[tq], %[tq], " XSTR(GRID) "\n\ts_add_u32 %[t0], %[t0], " XSTR(XT) "\n\ts_add_u32 %[tn], %[tn], " XSTR(GRID) "+" XSTR(XT) "\n\ts_add_u32 %[r], %[r], " XSTR(GRID) "\n\t"   \
      "s_cmp_lt_i32 %[r], -" XSTR(LTOL) "\n\ts_cbranch_scc1 63b\n\t" MCA(1000)                                                        \
      d ":\n\t" WCV(7) WCV(8) WCV(9) WCV(10) WCV(11) WCV(12) WCV(13) WCV(14) WCV(15)                                                 \
      PMM(X, 0) MCY(4) PMM(X, 1) PMM(X, 2) PMM(X, 3) AMM(X, 0) AMM(X, 1) AMM(X, 2) AMM(X, 3) MCY(5)                                  \
      PADN "s_nop 6\n\t"                                                                                                             \
      "v_pk_add_f32 v[60:61], v[60:61], v[62:63]\n\t"                                                                                \
      "v_cvt_pk_bf16_f32 v62, v60, v61\n\tv_cmp_u_f32 vcc, v60, v61\n\ts_nop 0\n\t"                                                  \
      "v_mov_b32_dpp v63, v62 quad_perm:[1,0,3,2] row_mask:0xf bank_mask:0xf\n\t"                                                    \
      "v_perm_b32 v61, v63, v62, %[sel]\n\ts_cbranch_vccnz 50f\n\t"
#define MEV(n)
#define MEVPUT
#define MCY(n)
#define MCYPUT
#define MCYO
#define MCN
#define MCA(k)
#define FME
#define FML
#define FMZ
#define VR8(p, b) p #b "0", p #b "1", p #b "2", p #b "3", p #b "4", p #b "5", p #b "6", p #b "7", p #b "8", p #b "9"
__device__ __forceinline__ void role_mix(const Args& a, rsrc_t R, const Go& go, int m, int wave, int lane) {
  if (wave >= 2) {
    unsigned t[4];
#pragma unroll
    for (int k = 0; k < 4; ++k) {
      const int u = ((m * 2 + wave - 2) * 4 + k) * 64 + lane;
      t[k] = ld32(R, u < 8192 ? (unsigned)u * 128u : (u < 12800 ? OFF_YR + (unsigned)(u - 8192) * 128u : (u < 17408 ? OFF_YR + YSLOT_REAL + (unsigned)(u - 12800) * 128u : OOB)), 0);
    }
    asm volatile("" :: "v"(t[0]), "v"(t[1]), "v"(t[2]), "v"(t[3]));
  }
  if (wave != 0 && (wave != 2 || m != 0)) return;
  const int l15 = lane & 15, g = lane >> 4;
  if (wave == 0) {
#pragma unroll
    for (int k = 0; k < 32; ++k) st128((u32x4){~0u, ~0u, ~0u, ~0u}, R, OFF_ZF + (unsigned)(m * 40960 + lane * 16), k * 1024);
  }
  if (!go_wait(R, go, lane)) return;
  VMW(0);
  st32(0u, R, lane == 0 && wave == 0 ? (unsigned)(FL_MPOI + m * 4) : OOB, 0);
  RT(3);
  if (wave == 2) {
    for (int k = 0; k < CSL; ++k) __builtin_amdgcn_s_sleep(32);
    const int cls = get_cls(R, lane);
    for (int k = CSL; k < TSL; ++k) __builtin_amdgcn_s_sleep(32);
    for (unsigned spins = 0; spins < SPINCAP; ++spins) {
      CBAR();
      if (__all((int)(ld32(R, FX(FL_MRDY, cls, 0) + min(lane, 35) * 4, 0) | ld32(R, FX(FL_WP, cls, 0) + lane * 32, 0)) >= 0)) break;
      __builtin_amdgcn_s_sleep(1);
    }
    st32((rt_now() + T1D) & ~1u, R, lane < 16 ? FX(FL_AN, cls, 0) + lane * 128 : OOB, 0);
    return;
  }
  for (int k = 0; k < CSL; ++k) __builtin_amdgcn_s_sleep(32);
  const int cls = get_cls(R, lane);
  if (cls < 0) return;
  RT(4);
  const unsigned fo = (unsigned)(cls & 1) * 8192u, y0 = ywin(cls, m) + lane * 16;
  const u32x4 one = {~0u, ~0u, ~0u, ~0u};
  const unsigned mc = (unsigned)nearoff(m * 256 + l15 * 16) + fo + (g & 1) * CPY1 + (g >> 1) * CPY2;
#pragma unroll
  for (int k = 0; k < 8; ++k) st128(one, R, y0 + k * 1024, YSLOT_REAL);
  st128(one, R, mc, SLOT_REAL);
  RT(1);
  unsigned uo[8], eo[4];
#pragma unroll
  for (int k = 0; k < 8; ++k) uo[k] = OFF_ZF + (unsigned)(m * 40960 + k * 4096 + lane * 16);
#pragma unroll
  for (int k = 0; k < 4; ++k) {
    eo[k] = (unsigned)nearoff(NEAR_E + k * 4096 + lane * 16) + fo;
  }
  const unsigned pm = (unsigned)nearoff(NEAR_PM + lane * 16) + fo;
  const int qi = 2 * (l15 >> 2) + (l15 & 1);
  const unsigned al = !(l15 & 2) && (g & 1) == (qi >> 2) ? (unsigned)nearoff(NEAR_AL + ((g >> 1) * 8 + qi) * 16) + fo : OOB;
  const unsigned mo = (unsigned)nearoff((2 * m + (l15 >> 3)) * 128 + (2 * g + (l15 & 1)) * 16 + ((l15 & 7) >> 1) * 4) + fo;
  const unsigned sm = lane < 36 ? (unsigned)nearoff(lane * 256) + fo : OOB, own = own_cpy(cls, m / 9);
  const unsigned an = FX(FL_AN, cls, 0) + (m & 15) * 128, wp = FX(FL_WP, cls, 0) + lane * 32;
  const unsigned ie = (lane & 4) ? 0xEEEE : 0x4444, ip = (lane & 2) ? 0xEEEE : 0x4444, ia = ie;
  const unsigned sel = (lane & 1) ? 0x03020706u : 0x05040100u;
  const i32x4 D = mkdesc(a.wb);
  const unsigned uf = lane < 32 ? FX(FL_UPROG, cls, 4) + lane * 128 + (m / 9) * 4 : OOB;
  for (int k = 0; k < MSL; ++k) __builtin_amdgcn_s_sleep(16);
  RT(2);
  unsigned tq = 0, lev = PACE0, bud = SPINCAP, s = 0, su = 0, sy = 0, sp = 0, tt, sa, sb, sc, sn, tn, ln, ue, t0, sx, sq, cw, sv, ua = ~0u, ub, uq, nr = 0, cv = 0;
  int ufl = lane < 32 ? -1 : 0x7fffffff;
  unsigned ox = lane < 32 ? OOB : an;
  unsigned ev0 = 0, ev1 = 0, ev2 = 0, ev3 = 0, ec[7] = {0, 0, 0, 0, 0, 0, 0};
  int r, k;
  asm volatile(
      "s_mov_b64 exec, 0\n\ts_branch 200f\n299:\n\ts_mov_b64 exec, -1\n\t"
      "s_mov_b32 %[sa], 0\n\ts_mov_b32 %[sb], " XSTR(D0) "\n"
      "27:\n\tbuffer_load_dword v59, %[up], %[D], 0 offen sc1\n\tbuffer_load_dword %[cw], %[wp], %[D], 0 offen sc1\n\t"
      "s_mov_b32 %[tq], %[sb]\n\ts_waitcnt vmcnt(0)\n\tv_cmp_ne_u32 vcc, -1, v59\n\ts_andn2_b32 %[r], vcc_lo, %[sa]\n\ts_cbranch_scc0 26f\n\t"
      UG(0, 0, 1, 2, 3, 0]) UG(1, 4, 5, 6, 7, 1]) UG(2, 8, 9, 10, 11, 2]) UG(3, 12, 13, 14, 15, 3])
      UG(4, 16, 17, 18, 19, 4]) UG(5, 20, 21, 22, 23, 5]) UG(6, 24, 25, 26, 27, 6]) UG(7, 28, 29, 30, 31, 7])
      "s_cmp_lg_u32 %[sa], -1\n\ts_cbranch_scc1 26f\n\tv_mov_b32 v59, 0\n\tbuffer_store_dword v59, %[rdy], %[D], 0 offen sc1\n"
      "26:\n\tv_cmp_gt_i32 vcc, 0, %[cw]\n\ts_cbranch_vccnz 25f\n\ts_mov_b32 %[sb], 0\n\ts_cmp_lg_u32 %[sa], -1\n\ts_cbranch_scc0 24f\n"
      "25:\n\ts_sleep " XSTR(USL) "\n\t" BUD("27b")
      "24:\n\ts_memrealtime vcc\n\ts_waitcnt lgkmcnt(0)\n\ts_add_u32 %[tq], %[tq], vcc_lo\n\ts_sub_u32 %[tq], %[tq], " XSTR(DE0) "\n\ts_branch 28f\n"
      "29:\n\t" ULD0 ULD1 "28:\n\ts_mov_b32 %[t0], 0\n\ts_mov_b32 %[sb], 0\n\ts_mov_b32 %[ue], 2*" XSTR(SLOT_REAL) "\n\ts_waitcnt vmcnt(0)\n\t"
      "30:\n\t" FMZ DZERO
      "s_add_u32 %[su], %[s], 1\n\ts_and_b32 %[su], %[su], " XSTR(RMASK) "\n\ts_mul_i32 %[su], %[su], 36*40960\n\t"
      "s_add_u32 %[sn], %[sp], " XSTR(SLOT_REAL) "\n\ts_and_b32 %[sn], %[sn], 7*" XSTR(SLOT_REAL) "\n\t"
      "s_lshr_b32 %[sq], %[sp], 5\n\ts_and_b32 %[sq], %[sq], " XSTR(CPY1) "\n\ts_xor_b32 %[sq], %[sq], %[own]\n\ts_add_u32 %[sq], %[sq], %[sp]\n\t"
      "s_movk_i32 %[ln], " XSTR(PACEC) "\n\ts_cmp_lt_u32 %[s], " XSTR(S2) "-1\n\ts_cmovk_i32 %[ln], " XSTR(PACEB) "\n\t"
      "s_cmp_lt_u32 %[s], " XSTR(S1) "-1\n\ts_cmovk_i32 %[ln], " XSTR(PACEA) "\n\ts_cmp_lt_u32 %[s], " XSTR(S0) "-1\n\ts_cmovk_i32 %[ln], " XSTR(PACE0) "\n\t"
      "s_add_u32 %[sc], %[sn], " XSTR(CPY1) "\n\ts_xor_b32 %[sb], %[sn], " XSTR(CPY2) "\n\ts_mov_b32 %[sx], 0\n\t" TIMES
      UMS("0", "2*" XSTR(NG), "8") "s_cmp_lg_u32 %[s], 0\n\t"
      "buffer_store_dwordx4 %[one], %[mc], %[D], %[ue] offen sc1\n\tv_max_i32 %[ufl], %[ufl], %[ua]\n\ts_cbranch_scc0 33f\n\t"
      "v_cmp_eq_u32 vcc, -1, %[sv]\n\ts_cbranch_vccnz 35f\n"
      "33:\n\tv_cmp_ge_i32 vcc, %[s], %[ufl]\n\ts_nop 0\n\tv_cndmask_b32 %[uq], %[ox], %[uf], vcc\n\t"
      UMS("2*" XSTR(NG), XSTR(TB), "9") MEV(3)
      "75:\n\t" TRY("0", EM2_0, "40", "41", "42", "43")
      "51:\n\tbuffer_store_dword v61, %[mo], %[D], %[sn] offen sc1\n\tbuffer_store_dword v61, %[mo], %[D], %[sc] offen sc1\n\ts_memrealtime vcc\n\t" MEV(2) MCY(6)
      "buffer_store_dword v61, %[mo], %[D], %[sb] offen sc1\n\ts_add_u32 %[sb], %[sb], " XSTR(CPY1) "\n\tbuffer_store_dword v61, %[mo], %[D], %[sb] offen sc1\n\t"
      "s_waitcnt vmcnt(4) lgkmcnt(0)\n\ts_add_u32 %[k], vcc_lo, " XSTR(MGAP) "\n"
      "97:\n\ts_sub_u32 %[r], %[tn], %[k]\n\ts_cmp_lt_i32 %[r], 0\n\ts_cbranch_scc0 98f\n\ts_add_u32 %[tn], %[tn], " XSTR(GRID) "+" XSTR(XT) "\n\ts_branch 97b\n"
      "98:\n\t" MEVPUT MCYPUT MCN
      "s_add_u32 %[s], %[s], 1\n\ts_cmp_ge_u32 %[s], 63\n\ts_cbranch_scc1 60f\n\t"
      "v_max_i32 %[ufl], %[ufl], %[ub]\n\tv_cmp_gt_i32 vcc, %[s], %[ufl]\n\ts_mov_b32 %[sx], 1\n\ts_cbranch_vccnz 100f\n\t"
      "s_mov_b32 %[sx], 0\n\t" ULD0 "100:\n\t"
      "s_cmp_lg_u32 %[s], 1\n\ts_cbranch_scc1 67f\n\t"
      "v_readlane_b32 %[tn], %[ub], 32\n\tv_mov_b32 %[ox], " XSTR(OOB) "\n\ts_cmp_lg_u32 %[tn], -1\n\ts_cbranch_scc1 67f\n"
      "66:\n\tbuffer_load_dword %[cw], %[an], %[D], 0 offen sc1\n\ts_waitcnt vmcnt(0)\n\tv_readfirstlane_b32 %[tn], %[cw]\n\t"
      "s_cmp_lg_u32 %[tn], -1\n\ts_cbranch_scc1 67f\n\t" BUD("66b")
      "67:\n\t"
      "s_xor_b32 %[ue], %[sn], 2*" XSTR(SLOT_REAL) "\n\ts_and_b32 %[ue], %[ue], 3*" XSTR(SLOT_REAL) "\n\ts_mov_b32 %[sp], %[sn]\n\t"
      "s_mov_b32 %[tq], %[tn]\n\ts_mov_b32 %[t0], 0\n\ts_mov_b32 %[lev], %[ln]\n\t"
      "v_cmp_ge_i32 vcc, %[s], %[ufl]\n\tv_mov_b32 %[ua], " XSTR(OOB) "\n\tv_cndmask_b32 %[ua], %[ua], %[uf], vcc\n\t"
      "s_lshr_b32 %[sa], %[sn], 5\n\ts_and_b32 %[sa], %[sa], " XSTR(CPY1) "\n\ts_xor_b32 %[sa], %[sa], %[own]\n\ts_add_u32 %[sa], %[sa], %[sn]\n\t"
      SPIN(tq, "52", "53", "54") "buffer_load_dword %[sv], %[sm], %[D], %[sa] offen sc1\n\t"
      "buffer_load_dword %[ua], %[ua], %[D], 0 offen sc1\n\t"
      "s_cmp_eq_u32 %[sx], 0\n\ts_cbranch_scc1 101f\n\t"
      "55:\n\ts_waitcnt vmcnt(0)\n\tv_max_i32 %[ufl], %[ufl], %[ua]\n\tv_cmp_gt_i32 vcc, %[s], %[ufl]\n\ts_cbranch_vccz 57f\n\t" MCA(100000)
      "buffer_load_dword %[ua], %[uf], %[D], 0 offen sc1\n\t" BUD("55b")
      "57:\n\t" ULD0 "101:\n\t" ULD1 YPO(0, 0]) YPO(1, 0]) YPO(2, 0]) YPO(3, 0]) YPO(4, 1]) YPO(5, 1]) YPO(6, 1]) YPO(7, 1])
      "s_xor_b32 %[sy], %[sy], " XSTR(YSLOT_REAL) "\n\ts_branch 30b\n\t"
      WP(200, 201f) "35:\n\t" MCA(100) BUD("36f") "s_branch 32f\n\t"
      "36:\n\ts_add_u32 %[tq], %[tq], " XSTR(GRID) "+" XSTR(XT) "\n\ts_xor_b32 %[sa], %[sa], %[flp]\n\t" WP(201, 202f)
      SPIN(tq, "37", "38", "39") "buffer_load_dword %[sv], %[sm], %[D], %[sa] offen sc1\n\t" WP(202, 203f) "s_waitcnt vmcnt(0)\n\t"
      "v_cmp_eq_u32 vcc, -1, %[sv]\n\ts_cbranch_vccnz 35b\n"
      "32:\n\t" TIMES "s_branch 33b\n" WP(203, 204f)
      "59:\n\t" WP(204, 205f) "s_waitcnt lgkmcnt(0)\n\ts_lshr_b32 %[tt], %[lev], 8\n\ts_sub_u32 %[k], vcc_lo, %[tt]\n\ts_sub_u32 %[k], %[k], " XSTR(RTOL) "\n\t"
      "34:\n\ts_add_u32 %[tq], %[tq], " XSTR(GRID) "\n\ts_add_u32 %[t0], %[t0], " XSTR(XT) "\n\ts_sub_u32 %[r], %[tq], %[k]\n\ts_cmp_lt_i32 %[r], 0\n\ts_cbranch_scc1 34b\n\t"
      WP(205, 206f) TIMES "s_branch 75b\n\t" WP(206, 207f)
      "58:\n\ts_cmp_lg_u32 %[s], 0\n\ts_cbranch_scc1 102f\n\ts_mov_b32 %[su], 0\n\ts_branch 29b\n102:\n\ts_memrealtime vcc\n\ts_xor_b32 %[sq], %[sq], %[flp]\n\t"
      WP(207, 208f) DZ6(0) WP(208, 209f) DZ6(1) WP(209, 210f) DZ6(2) WP(210, 211f) DZ6(3) WP(211, 212f) DZ6(4) WP(212, 213f)
      ".rept 4\n\tv_mov_b64 v[120+2*\\+:121+2*\\+], 0\n\t.endr\n\ts_branch 59b\n\t" WP(213, 214f)
      "50:\n\t" FML MCA(1) BUD("58b") "v_min_u32 v61, -2, v61\n\ts_branch 51b\n\t" WP(214, 215f)
      "99:\n\ts_memrealtime vcc\n\ts_xor_b32 %[sq], %[sq], %[flp]\n\ts_branch 59b\n\t"
      "61:\n\t" FME MCA(1) BUD("99b") "s_branch 62b\n\t" WP(215, 299b)
      "60:\n\ts_waitcnt vmcnt(0)"
      : [tq] "+&s"(tq), [lev] "+&s"(lev), [bud] "+&s"(bud), [s] "+&s"(s), [su] "+&s"(su), [sy] "+&s"(sy), [sp] "+&s"(sp), [tt] "=&s"(tt), [sa] "=&s"(sa),
        [sn] "=&s"(sn), [sb] "=&s"(sb), [sc] "=&s"(sc), [tn] "=&s"(tn), [ln] "=&s"(ln), [ue] "=&s"(ue), [t0] "=&s"(t0), [sx] "=&s"(sx), [sq] "=&s"(sq), [cw] "=&v"(cw), [sv] "=&v"(sv), [ua] "+&v"(ua), [ub] "=&v"(ub), [uq] "=&v"(uq), [ufl] "+&v"(ufl), [ox] "+&v"(ox), [r] "=&s"(r), [k] "=&s"(k), [nr] "+&s"(nr), [cv] "+&v"(cv),
        [ev0] "+&v"(ev0), [ev1] "+&v"(ev1), [ev2] "+&v"(ev2), [ev3] "+&v"(ev3) MCYO
      : [D] "s"(D), [u0] "v"(uo[0]), [u1] "v"(uo[1]), [u2] "v"(uo[2]), [u3] "v"(uo[3]), [u4] "v"(uo[4]), [u5] "v"(uo[5]), [u6] "v"(uo[6]), [u7] "v"(uo[7]), [e0] "v"(eo[0]), [e1] "v"(eo[1]), [e2] "v"(eo[2]),
        [e3] "v"(eo[3]), [y0] "v"(y0), [y1] "v"(y0 + 4096), [pm] "v"(pm), [al] "v"(al), [mo] "v"(mo), [mc] "v"(mc), [an] "v"(an), [wp] "v"(wp),
        [ie] "v"(ie), [ip] "v"(ip), [ia] "v"(ia), [sel] "v"(sel), [one] "v"(one), [sm] "v"(sm), [uf] "v"(uf), [es] "v"(lane < 32 ? (unsigned)nearoff(NEAR_E + lane * 512 + (lane & 1) * (64 - 512)) + fo : OOB), [rdy] "v"(lane == 0 ? FX(FL_MRDY, cls, 0) + m * 4 : OOB), [own] "s"(own), [flp] "s"(CPY2 + CPY1),
        [up] "v"(OFF_ZF + (unsigned)(m * 40960 + (lane & 31) * 1024 + 1020))
      : "memory", "vcc", "scc", "s68", "s69", "s70", "s71", "s72", "s73", "s74", "s75", "s76", "s77", "s78", "s79", "s80", "s81", "s84", "s85", "s86", "s87", "s88", "s89", "s90", "s91",
        "v59", VR8("v", 6), VR8("v", 7), VR8("v", 8), VR8("v", 9), VR8("v", 10), VR8("v", 11), VR8("v", 12), VR8("v", 13), VR8("v", 14), VR8("v", 15),
        VR8("v", 16), VR8("v", 17), VR8("v", 18), VR8("v", 19), VR8("v", 20), VR8("v", 21), VR8("v", 22), VR8("v", 23), VR8("v", 24),
        "v250", "v251", "v252", "v253", "v254", "v255",
        "a0", "a1", "a2", "a3", "a4", "a5", "a6", "a7", "a8", "a9", VR8("a", 1), VR8("a", 2), VR8("a", 3), VR8("a", 4), VR8("a", 5), VR8("a", 6),
        VR8("a", 7), VR8("a", 8), VR8("a", 9), VR8("a", 10), VR8("a", 11), VR8("a", 12), VR8("a", 13), VR8("a", 14), VR8("a", 15),
        VR8("a", 16), VR8("a", 17), VR8("a", 18), VR8("a", 19), VR8("a", 20), VR8("a", 21), VR8("a", 22), VR8("a", 23), VR8("a", 24),
        "a250", "a251", "a252", "a253", "a254", "a255");
  RT(7);
}

struct XL { rsrc_t X; i32x4 D; unsigned vo; int wr, rd; };
struct XP { lds_char* p[2]; };
__device__ __forceinline__ void x_init(XL& xl, const u16* x, int wave, int lane) {
  xl.X = __builtin_amdgcn_make_buffer_rsrc((void*)x, 0, S_LEN * DM * 2, 0x00020000);
  xl.D = (i32x4){(int)(unsigned)(u64)x, (int)(unsigned)((u64)x >> 32), S_LEN * DM * 2, 0x00020000};
  xl.vo = (unsigned)((wave * 8 + (lane >> 3)) * 1152 + ((lane ^ (lane >> 3)) & 7) * 16);
  xl.wr = wave * 9216 + lane * 16;
  xl.rd = ((lane >> 3) & 1) * 9216 + (lane & 7) * 128 + ((lane >> 4) ^ (lane & 7)) * 16;
}
__device__ __forceinline__ XP x_at(lds_char* buf, const XL& xl) {
  XP r = {{buf + xl.rd, buf + (xl.rd ^ 64)}};
  pin_v(r.p[0]); pin_v(r.p[1]);
  return r;
}
__device__ __forceinline__ void x_m0(unsigned b, int k) { asm volatile("s_add_u32 m0, %0, %1" :: "s"(b), "n"(k * 896) : "scc"); }
__device__ __forceinline__ void x_dma(const XL& xl, unsigned so, int k) {
  asm volatile("buffer_load_dwordx4 %0, %1, %2 offen offset:%3 lds" :: "v"(xl.vo), "s"(xl.D), "s"(so), "n"(k * 128) : "memory");
}
__device__ __forceinline__ void x_dma0(const XL& xl, unsigned so, int k) {
  asm volatile("s_nop 0\n\tbuffer_load_dwordx4 %0, %1, %2 offen offset:%3 " XNT " lds" :: "v"(xl.vo), "s"(xl.D), "s"(so), "n"(k * 128) : "memory");
}
__device__ __forceinline__ i32x4 wdesc(const u16* p) { return (i32x4){(int)(unsigned)(u64)p, (int)(unsigned)((u64)p >> 32), DM * HD * 2, 0x00020000}; }
__device__ __forceinline__ void w_dma(i32x4 D, unsigned vo, unsigned so, unsigned b, int o) {
  asm volatile("s_add_u32 m0, %3, %4\n\ts_nop 0\n\tbuffer_load_dwordx4 %0, %1, %2 offen nt lds" :: "v"(vo), "s"(D), "s"(so), "s"(b), "n"(o) : "memory", "scc");
}
#define VB_OFF 73728
#define VX_OFF (VB_OFF + 73728)
constexpr int vbuf(int j) { return j % 5 == 3 ? VX_OFF : VB_OFF + ((j + 1) % 5) * 18432; }
__device__ __forceinline__ void x_move(lds_char*& p, unsigned d) { asm volatile("v_add_u32 %0, %1, %0" : "+v"(p) : "s"(d)); }
__device__ __forceinline__ u32x4 x_ld1(const XL& xl, int s, int k) { return __builtin_amdgcn_raw_buffer_load_b128(xl.X, xl.vo + k * 128, (unsigned)s * 36864u, 0); }
__device__ __forceinline__ void x_gload(u32x4 (&xr)[9], const XL& xl, int s) {
#pragma unroll
  for (int k = 0; k < 9; ++k) xr[k] = x_ld1(xl, s, k);
}
__device__ __forceinline__ void x_swrite(const u32x4 (&xr)[9], lds_char* buf, const XL& xl) {
#pragma unroll
  for (int k = 0; k < 9; ++k) *(lds_u32x4*)(buf + xl.wr + k * 1024) = xr[k];
}
#define XF(buf, tt, ks) (*(lds_u32x4*)((buf).p[(ks) & 1] + (tt) * 18432 + ((ks) >> 1) * 1024))
__device__ __forceinline__ void proj4(lds_char* cbuf, lds_char* nbuf, u32x4 (&xr)[9], const XL& xl, int snext, int wave, int lane,
                                      const u32x4 (&w0)[18], const u32x4 (&w1)[18], const u32x4 (&w2)[18], const u32x4 (&w3)[18],
                                      f32x4 (&acc)[4][2]) {
  u32x4 bb[3][2];
  const XP buf = x_at(cbuf, xl);
  nbuf += xl.wr;
#pragma unroll
  for (int ks = 0; ks < 2; ++ks) { bb[ks][0] = XF(buf, 0, ks); bb[ks][1] = XF(buf, 1, ks); }
#pragma unroll
  for (int t = 0; t < 4; ++t) { acc[t][0] = (f32x4){0.f, 0.f, 0.f, 0.f}; acc[t][1] = acc[t][0]; }
#pragma unroll
  for (int ks = 0; ks < 18; ++ks) {
    const u32x4 b0 = bb[ks % 3][0], b1 = bb[ks % 3][1];
    SB();
    acc[0][0] = mfma(w0[ks], b0, acc[0][0]); SB();
    if (ks + 2 < 18) bb[(ks + 2) % 3][0] = XF(buf, 0, ks + 2);
    SB();
    acc[0][1] = mfma(w0[ks], b1, acc[0][1]); SB();
    if (ks + 2 < 18) bb[(ks + 2) % 3][1] = XF(buf, 1, ks + 2);
    SB();
    acc[1][0] = mfma(w1[ks], b0, acc[1][0]); SB();
    if ((ks & 1) == 0) *(lds_u32x4*)(nbuf + (ks >> 1) * 1024) = xr[ks >> 1];
    SB();
    acc[1][1] = mfma(w1[ks], b1, acc[1][1]);
    acc[2][0] = mfma(w2[ks], b0, acc[2][0]); SB();
    if ((ks & 1) == 0) xr[ks >> 1] = x_ld1(xl, snext, ks >> 1);
    SB();
    acc[2][1] = mfma(w2[ks], b1, acc[2][1]);
    acc[3][0] = mfma(w3[ks], b0, acc[3][0]); acc[3][1] = mfma(w3[ks], b1, acc[3][1]);
    SB();
  }
}
#define PFL 13
#define STG0 6
#define STGD 0
#define NOST 0
#define NOFL 0
#define FG 7
#define UFL 5
__device__ __forceinline__ int clampseg(int s) { return s < NSEG ? s : NSEG - 1; }

__device__ __forceinline__ void role_prj(const Args& a, rsrc_t R, const Go& go, lds_char* lds, const u16* W0, const u16* W1, unsigned o0, unsigned o1, unsigned flag,
                                         int tid, int wave, int lane) {
  const int l15 = lane & 15, g = lane >> 4;
  u32x4 w[2][2][18];
  u32x4 xr[9], xq[9];
  XL xl; x_init(xl, a.x, wave, lane);
  x_gload(xq, xl, 0);
  x_gload(xr, xl, 1);
  const unsigned fo = wave * 1024 + lane * 16;
  const unsigned pf = lane < 2 ? flag + lane * 8192 + wave * 4 : OOB;
  f32x4 acc[2][2][2];
  {
    u32x4 sg[PDEP][8];
    unsigned g1;
    const rsrc_t B0 = __builtin_amdgcn_make_buffer_rsrc((void*)W0, 0, DM * HD * 2, 0x00020000), B1 = __builtin_amdgcn_make_buffer_rsrc((void*)W1, 0, DM * HD * 2, 0x00020000);
    const unsigned wof = (unsigned)((tid >> 4) * (HD * 2) + (tid & 15) * 16);
    auto gl = [&](int i, int k) {
      if (i < 10 && (i % 5 < 4 || k < 4)) { sg[i % PDEP][k] = __builtin_amdgcn_raw_buffer_load_b128(i < 5 ? B0 : B1, wof, (NOCW & 4) ? 0u : (unsigned)(128 * (i % 5) + 16 * k) * (HD * 2), (NTM & 4) ? 2 : 0); SB(); }
    };
#pragma unroll
    for (int k = 0; k < 8 * PDEP; ++k) gl(k >> 3, k & 7);
    if (wave == 0) WALK(100); else if (wave == 1) WALK(101); else if (wave == 2) WALK(102); else WALK(103);
    RT(2);
    PADS(100);
    lds_char *x0p = lds + X0_OFF, *x1p = lds + X1_OFF;
    pin_v(x0p); pin_v(x1p);
    x_swrite(xq, x0p, xl);
    PAD();
    x_swrite(xr, x1p, xl);
    PAD();
    const XP x0r = x_at(x0p, xl), x1r = x_at(x1p, xl);
    lds_char *wp = lds + (tid >> 4) * 288 + (tid & 15) * 16, *rp = lds + (g * 8 + (l15 >> 2)) * 288 + wave * 64 + (l15 & 3) * 8;
    pin_v(wp); pin_v(rp);
    auto put = [&](int i, int k) {
      if (i < 10 && (i % 5 < 4 || k < 4)) { *(lds_u32x4*)(wp + (i & 1) * STG_BYTES + k * (16 * 288)) = sg[i % PDEP][k]; SB(); }
    };
#pragma unroll
    for (int k = 0; k < 8; ++k) { put(0, k); gl(PDEP, k); }
    auto chunk = [&](auto IC) {
      constexpr int i = decltype(IC)::value, m = i / 5, c = i % 5, nk = c < 4 ? 4 : 2;
      if (c == 0) {
#pragma unroll
        for (int k = 0; k < 8; ++k) { acc[k >> 2][(k >> 1) & 1][k & 1] = (f32x4){0.f, 0.f, 0.f, 0.f}; pin_v(acc[k >> 2][(k >> 1) & 1][k & 1]); }
      }
      if (i == 4) { PADE(); PADS(101); } else if (i == 8) { PADE(); PADS(102); } else PAD();
      BAR();
      if (i == 0) RT(3);
      if (i == 2) RT(4);
      if (i == 4) RT(5);
      if (i == 6) RT(6);
      if (i == 7) RT(8);
      if (i == 8) RT(9);
      u32x4 xb[2][4];
      u32x2 wh[2][4];
      auto rd = [&](int kk, int j) {
        if (kk >= nk) return;
        if (j & 1) xb[kk & 1][j >> 1] = XF((j & 4) ? x1r : x0r, (j >> 1) & 1, 4 * c + kk);
        else wh[kk & 1][j >> 1] = trd(rp + (i & 1) * STG_BYTES + (kk * 32 + (j & 2) * 2) * 288 + (j >> 2) * 32);
        SB();
      };
#pragma unroll
      for (int j = 0; j < 8; ++j) rd(0, j);
#pragma unroll
      for (int k = 0; k < 8 * nk; ++k) {
        const int kk = k >> 3;
        f32x4& d = acc[(k >> 2) & 1][k & 1][(k >> 1) & 1];
        u32x4& f = w[m][k & 1][4 * c + kk];
        if ((k & 7) < 2) {
          f = (u32x4){wh[kk & 1][(k & 1) * 2][0], wh[kk & 1][(k & 1) * 2][1], wh[kk & 1][(k & 1) * 2 + 1][0], wh[kk & 1][(k & 1) * 2 + 1][1]};
          if (c < 4) pin_a(f); else pin_v(f);
        }
        if (c < 4) mfA(d, f, xb[kk & 1][(k >> 1) & 3]); else mfa(d, f, xb[kk & 1][(k >> 1) & 3]);
        SB();
        rd(kk + 1, k & 7);
        if (k % nk == 0) put(i + 1, k / nk);
        if (k % nk == 1) gl(i + 1 + PDEP, k / nk);
        if (k % 12 == 11 || k == 8 * nk - 1) PAD();
      }
      if (i == 2) g1 = ld32(R, go.vo, 0);
      if (i == 5) x_gload(xq, xl, 2);
      if (i == 6) x_gload(xr, xl, 3);
      if (i == KFI) {
        SB();
        asm volatile("s_waitcnt vmcnt(%0)" :: "n"(KFI == 5 ? 13 : 22) : "memory");
        if (__all(g1 != go.v0)) go_ack(R, go, lane); else go_wait(R, go, lane);
        st32(0u, R, pf, 0);
      }
      SB();
      if (c == 4) {
        asm volatile("s_nop 7"); SB();
#pragma unroll
        for (int k = 0; k < 4; ++k) {
          PAD();
          const f32x4 (&e)[2][2] = acc[k >> 1];
          const int tt = k & 1;
          st128((u32x4){pk2(e[0][tt][0], e[0][tt][1]), pk2(e[0][tt][2], e[0][tt][3]), pk2(e[1][tt][0], e[1][tt][1]), pk2(e[1][tt][2], e[1][tt][3])}, R,
                (m ? o1 : o0) + fo + tt * 4096, (unsigned)(k >> 1) * 262144u);
        }
        SB();
      }
    };
#define CH(n) chunk(std::integral_constant<int, n>())
    CH(0); CH(1); CH(2); CH(3); CH(4); CH(5); CH(6); CH(7); CH(8); CH(9);
#undef CH
    BAR();
  }
  RT(12);
  PAD();
  x_swrite(xq, lds, xl);
  x_swrite(xr, lds + STG_BYTES, xl);
  PAD();
  u32x4 bb[3][2];
  XP rd0 = x_at(lds, xl), rd1 = x_at(lds + STG_BYTES, xl);
  unsigned b0 = 0, b1 = STG_BYTES, b2 = X1_OFF;
  const unsigned xw = (unsigned)(uintptr_t)lds + __builtin_amdgcn_readfirstlane(wave) * 9216;
  BAR();
#pragma unroll
  for (int ks = 0; ks < 2; ++ks) { bb[ks][0] = XF(rd0, 0, ks); bb[ks][1] = XF(rd0, 1, ks); }
  unsigned oa = o0 + fo, ob = o1 + fo, tc0 = 0, tr0 = 0;
  pin_v(oa); pin_v(ob);
  auto half = [&](auto HC, int s) {
    constexpr int H = decltype(HC)::value & 1, P = decltype(HC)::value >> 1;
    XP& buf = P ? rd1 : rd0;
    XP& nrd = H ? (P ? rd0 : rd1) : buf;
    const unsigned oo = H ? oa : ob;
    unsigned so, sx, xm, xd;
    f32x4 (&c)[2][2] = acc[H];
    f32x4 (&e)[2][2] = acc[H ^ 1];
    u32x4 o;
#define PM(t, tt) do { if (ks == 0) c[t][tt] = mfA0(w[H][t][0], bb[0][tt]); else if (ks < 16) mfA(c[t][tt], w[H][t][ks], bb[ks % 3][tt]); \
                       else mfa(c[t][tt], w[H][t][ks], bb[ks % 3][tt]); SB(); } while (0)
#define PR(tt) do { bb[(ks + 2) % 3][tt] = ks < 16 ? XF(buf, tt, ks + 2) : XF(nrd, tt, ks - 16); SB(); } while (0)
#pragma unroll
    for (int ks = 0; ks < 18; ++ks) {
      const int xi = H * 5 + ((ks - 7) >> 1);
      const bool xs = ks >= 7 && ks < 17 && xi < 9 && (H == 0 || ks < 15);
      if (H == 1 && ks == 16) { __builtin_amdgcn_s_waitcnt(0xC27F); __builtin_amdgcn_s_barrier(); SB(); }
      else { __builtin_amdgcn_s_waitcnt(0xC27F); SB(); }
      PM(0, 0); PR(0);
      if (ks == 0) {
        so = (unsigned)(s + H - 1) * 262144u;
        sx = (unsigned)clampseg(s + 2) * 36864u;
        asm volatile("" : "+s"(so), "+s"(sx)); SB();
      }
      if (ks == 1) {
        xm = xw + b2; xd = b2 - b0;
        if (H) { const unsigned t = b0; b0 = b1; b1 = b2; b2 = t; }
        asm volatile("" : "+s"(xm), "+s"(xd), "+s"(b0), "+s"(b1), "+s"(b2)); SB();
      }
      PM(0, 1); PR(1);
      PM(1, 0);
      if (ks == 1 || ks == 2) { o[(ks - 1) * 2] = pkv(e[ks - 1][0][0], e[ks - 1][0][1]); SB(); }
      if (ks == 4 || ks == 5) { o[(ks - 4) * 2] = pkv(e[ks - 4][1][0], e[ks - 4][1][1]); SB(); }
      if (ks == 3 && NOST < 2) { st128(o, R, oo, so); SB(); }
      if (ks == 6 && NOST < 2) { st128(o, R, oo + 4096, so); SB(); }
      if (xs && (ks & 1)) { x_m0(xm, xi); SB(); }
      if (xs && !(ks & 1)) { x_dma(xl, sx, xi); SB(); }
      if (H && ks >= 16) { x_move(buf.p[ks & 1], xd); SB(); }
      PM(1, 1);
      if (ks % 3 == 0) { if (H == 1 && P == 0 && ks == 0) { PADE(); PADS(103); } else PAD(); SB(); }
      if (ks == 1 || ks == 2) { o[(ks - 1) * 2 + 1] = pkv(e[ks - 1][0][2], e[ks - 1][0][3]); SB(); }
      if (ks == 4 || ks == 5) { o[(ks - 4) * 2 + 1] = pkv(e[ks - 4][1][2], e[ks - 4][1][3]); SB(); }
      if (ks == PFL) {
        asm volatile("s_waitcnt vmcnt(%0)" :: "n"((H ? 8 : 7) + (PFL - 6) / 2) : "memory");
        st32((unsigned)(H || P || s > 2 ? 2 * s + H - 2 : 0), R, pf, 0); SB();
      }
      if (ks == 17 && H == 1 && UNLIKELY(s < KEF)) { RT(10); VMW(5); st32((unsigned)(2 * s), R, pf, 0); RT(11); SB(); }
    }
#undef PM
#undef PR
  };
#pragma nounroll
  for (int s = 2; s < NSEG; ++s) {
    half(std::integral_constant<int, 0>(), s);
    half(std::integral_constant<int, 1>(), s);
    STEPRT(0, 0);
    ++s;
    half(std::integral_constant<int, 2>(), s);
    half(std::integral_constant<int, 3>(), s);
    STEPRT(0, 0);
  }
  PADE();
  mf_wait2(acc[1][0][0], acc[1][0][1]); mf_wait2(acc[1][1][0], acc[1][1][1]);
#pragma unroll
  for (int tt = 0; tt < 2; ++tt) {
    const u32x4 o = {pk2(acc[1][0][tt][0], acc[1][0][tt][1]), pk2(acc[1][0][tt][2], acc[1][0][tt][3]), pk2(acc[1][1][tt][0], acc[1][1][tt][1]), pk2(acc[1][1][tt][2], acc[1][1][tt][3])};
    st128(o, R, o1 + fo + tt * 4096, (unsigned)((NSEG - 1) & RMASK) * 262144u);
  }
  VMW(0);
  st32((unsigned)(2 * NSEG - 1), R, pf, 0);
  RT(7);
}

__device__ __forceinline__ void role_px(const Args& a, rsrc_t R, const Go& go, int pi, int wave, int lane) {
  const int l15 = lane & 15, g = lane >> 4;
  const int h = pi * 2 + (wave >> 1), par = wave & 1;
  const int wz = h >> 3, tq = (h >> 1) & 3, hp = h & 1;
  const int gy = ((tq & 1) << 1) | (tq >> 1);
  const unsigned kmo = OFF_KMF + (unsigned)h * 4096u + lane * 16;
  const unsigned pmo = OFF_PXF + (unsigned)((32 + wz * 2 + hp) * 1024 + (gy * 16 + l15) * 16 + g * 4), pxo = OFF_PXF + (unsigned)(h * 1024 + lane * 16);
  const unsigned qo = OFF_QF + (unsigned)h * 8192u + lane * 16, ko = OFF_KF + (unsigned)h * 8192u + lane * 16;
  int fm = -1;
  RT(1);
  u32x4 qf[2][4], kf[2][4];
  auto qk = [&](int s) {
#pragma unroll
    for (int k = 0; k < 8; ++k) { kf[k >> 2][k & 3] = ld128(R, ko + k * 1024, (unsigned)s * 262144u); qf[k >> 2][k & 3] = ld128(R, qo + k * 1024, (unsigned)s * 262144u); }
  };
  if (!go_wait(R, go, lane)) return;
  for (int k = 0; k < PXS; ++k) __builtin_amdgcn_s_sleep(32);
  const int cls = get_cls(R, lane);
  const unsigned ppf = (lane == 0) ? FX(FL_PXPROG, cls, 0) + h * 128 + par * 4 : OOB;
  const unsigned kmf = lane == 4 ? FX(FL_KMPROG, cls, 3) + h * 128 : (lane < 4 ? FX(FL_PROG + 8192, cls, 6) + h * 128 + lane * 4 : OOB);
  if (!wait_flag_c<16>(R, lane == 4 ? kmf : OOB, lane == 4 ? 0 : -1, fm)) return;
  qk(par);
  for (int s = par; s < NSEG - 1; s += 2) {
    u32x4 km[4], qc[2][4];
    f32x4 T[2][2];
    float mx[2], sx[2];
#pragma unroll
    for (int qt = 0; qt < 2; ++qt) {
      T[qt][0] = T[qt][1] = (f32x4){0.f, 0.f, 0.f, 0.f};
#pragma unroll
      for (int ks = 0; ks < 4; ++ks) {
        T[qt][0] = mfma(kf[0][ks], qf[qt][ks], T[qt][0]);
        if (qt) T[1][1] = mfma(kf[1][ks], qf[1][ks], T[1][1]);
        qc[qt][ks] = qf[qt][ks];
      }
      px_mask(T[qt][0], T[qt][1], qt, lane);
      px_seg(T[qt][0], T[qt][1], mx[qt], sx[qt]);
      pin_v(T[qt][0]); pin_v(T[qt][1]); pin_v(mx[qt]); pin_v(sx[qt]);
    }
    VMW(0);
    st32((unsigned)(s - 2), R, ppf, 0);
    STEPRT(2, 0);
    const int need = lane < 4 ? min(2 * s + 5, 2 * NSEG - 1) : (lane == 4 ? s : -1);
    for (unsigned spins = 0; !__all(fm >= need); ++spins) {
      if (spins > SPINCAP) return;
      if (spins) __builtin_amdgcn_s_sleep(PXS);
      CBAR();
      fm = max(fm, (int)ld32(R, fm < need && (lane == 4 || __builtin_amdgcn_readlane(fm, 4) >= s) ? kmf : OOB, 0));
    }
    CBAR();
#pragma unroll
    for (int ks = 0; ks < 4; ++ks) km[ks] = ld128(R, kmo + ks * 1024, (unsigned)s * 131072u);
    SB();
    qk(clampseg(s + 2));
    CBAR();
#pragma unroll
    for (int qt = 0; qt < 2; ++qt) {
      f32x4 G = {0.f, 0.f, 0.f, 0.f};
#pragma unroll
      for (int ks = 0; ks < 4; ++ks) G = mfma(km[ks], qc[qt][ks], G);
      u32x4 px; unsigned pw;
      px_fin(G, T[qt][0], T[qt][1], mx[qt], sx[qt], px, pw, lane);
      st128(px, R, pxo, (unsigned)s * 81920u + qt * 40960);
      st32(pw, R, pmo, (unsigned)s * 81920u + qt * 40960);
    }
    CBAR();
    fm = (int)ld32(R, kmf, 0);
    CBAR();
  }
  VMW(0);
  st32((unsigned)(NSEG - 2 - par), R, ppf, 0);
  RT(7);
}

__device__ __forceinline__ void role_vuw(const Args& a, rsrc_t R, const Go& go, lds_char* lds, int h, int tid, int wave, int lane) {
  const int l15 = lane & 15, g = lane >> 4;
  u32x4 wv[2][18], wo[9][4];
  XL xl; x_init(xl, a.x, wave, lane);
  unsigned zo = OFF_ZF + (unsigned)(wave * 9 * 40960 + lane * 16) + h * 1024;
  const unsigned vpf = lane < 2 ? (unsigned)(FL_UPROG + lane * 8192 + h * 128 + wave * 4) : OOB;
  const unsigned lb = (unsigned)(uintptr_t)lds, xw = lb + wave * 9216;
  lds_char *vr = lds + VX_OFF + lane * 16, *vw = vr + wave * 1024;
  f32x4 cc[2][2], av[2][2][2];
  u32x4 u1[9];
  TKD;
  {
    Go g0 = go;
    const i32x4 DV = wdesc(a.Wv + h * 128), DO = wdesc(a.Wo + (size_t)h * 128 * DM), D = mkdesc(a.wb);
    const unsigned wa = (unsigned)((16 * wave + g) * (HD * 2) + (l15 ^ (2 * g)) * 16), wb = wa ^ 128, ww = lb + wave * 4096, w1 = lb + wave * 1024;
    auto stage = [&](int j) {
      x_m0(xw, j); x_dma0(xl, 0, j);
      x_m0(xw + 36864, j); x_dma0(xl, 36864, j);
#pragma unroll
      for (int k = 0; k < 4; ++k) w_dma(DV, k < 2 ? wa : wb, (64 * j + 4 * k) * (HD * 2), ww, vbuf(j) + k * 1024);
      SB();
    };
#pragma unroll
    for (int j = 0; j < 3; ++j) stage(j);
    TK(0);
    unsigned oo[9], g1, g2;
#pragma unroll
    for (int i = 0; i < 9; ++i) { const int u = (4 * i + wave) * 64 + lane, r = u / 72; oo[i] = r * 1152 + ((u % 72) ^ (r & 6)) * 16; pin_v(oo[i]); }
    auto o_dma = [&](int k, int i0, int i1) {
#pragma unroll
      for (int i = i0; i < i1; ++i) w_dma(DO, oo[i], k * 36864, w1, ((k + 2) & 3) * 36864 + i * 4096);
      SB();
    };
    const XP x0r = x_at(lds, xl), x1r = x_at(lds + 36864, xl);
    lds_char* rp[2][2];
#pragma unroll
    for (int k = 0; k < 4; ++k) {
      rp[k >> 1][k & 1] = lds + ((k >> 1) ? VX_OFF : VB_OFF) + (g * 8 + (l15 >> 2)) * 256 + ((4 * wave + 2 * (k & 1)) ^ (2 * (l15 >> 2) + 8 * (g & 1))) * 16 + (l15 & 3) * 8;
      pin_v(rp[k >> 1][k & 1]);
    }
    asm volatile("s_load_dword %0, %1, %2\n\ts_waitcnt lgkmcnt(0)" : "=s"(g0.v0) : "s"(a.wb), "s"(__builtin_amdgcn_readfirstlane(go.vo)) : "memory");
    const unsigned pf = lane < 9 ? (unsigned)(FL_MPOI + (wave * 9 + lane) * 4) : OOB;
    TK(1);
    stage(3); stage(4);
    TK(2);
    auto vst = [&](auto JC) {
      constexpr int j = decltype(JC)::value, n = j == 0 ? 24 : (j < 4 ? 18 : (j < 6 ? 19 : (j < 7 ? 17 : 15))), hi = vbuf(j) == VX_OFF, bo = hi ? 0 : vbuf(j) - VB_OFF;
      asm volatile("s_waitcnt vmcnt(%0)" :: "n"(n) : "memory");
      __builtin_amdgcn_s_barrier(); CBAR(); SB();
      if (!(j & 1)) TK(3 + j / 2);
      u32x4 xb[2][4];
      u32x2 wh[2][4];
#pragma unroll
      for (int k = 0; k < 16; ++k) {
        const int kk = k >> 3, q = (k >> 1) & 3;
        if (k & 1) xb[kk][q] = XF((q & 2) ? x1r : x0r, q & 1, 2 * j + kk);
        else wh[kk][q] = trd(rp[hi][q >> 1] + bo + (kk * 32 + (q & 1) * 4) * 256);
        SB();
      }
      if (j == 3) { asm volatile("buffer_load_dword %0, %1, %2, 0 offen sc1" : "=v"(g1) : "v"(go.vo), "s"(D) : "memory"); SB(); }
      if (j == 7) {
        asm volatile("" : "+v"(g1));
        if (__all(g1 != g0.v0)) go_ack(R, g0, lane); else go_wait(R, g0, lane);
        asm volatile("buffer_load_dword %0, %1, %2, 0 offen sc1" : "=v"(g2) : "v"(pf), "s"(D) : "memory"); SB();
      }
      if (j == 0) {} else if (j < 5) stage(j + 4); else o_dma((j - 5) >> 1, (j & 1) ? 0 : 4, (j & 1) ? 4 : 9);
#pragma unroll
      for (int k = 0; k < 16; ++k) {
        const int kk = k >> 3;
        u32x4& f = wv[k & 1][2 * j + kk];
        if ((k & 7) < 2) { f = (u32x4){wh[kk][(k & 1) * 2][0], wh[kk][(k & 1) * 2][1], wh[kk][(k & 1) * 2 + 1][0], wh[kk][(k & 1) * 2 + 1][1]}; pin_a(f); }
        f32x4& d = av[(k >> 2) & 1][k & 1][(k >> 1) & 1];
        if (j == 0 && k < 8) d = mfA0(f, xb[kk][(k >> 1) & 3]); else mfA(d, f, xb[kk][(k >> 1) & 3]);
        SB();
      }
    };
#define CH(n) vst(std::integral_constant<int, n>())
    CH(0); CH(1); CH(2); CH(3); CH(4); CH(5); CH(6); CH(7); CH(8);
#undef CH
    BAR();
    TK(8);
    o_dma(2, 0, 9); o_dma(3, 0, 9);
    TK(9);
#pragma unroll
    for (int k = 0; k < 4; ++k) {
      const f32x4 (&e)[2][2] = av[k >> 1];
      const int tt = k & 1;
      *(lds_u32x4*)(vw + k * 4096) = (u32x4){pkv(e[0][tt][0], e[0][tt][1]), pkv(e[0][tt][2], e[0][tt][3]), pkv(e[1][tt][0], e[1][tt][1]), pkv(e[1][tt][2], e[1][tt][3])};
    }
    SB();
    lds_char* op[4];
#pragma unroll
    for (int k = 0; k < 4; ++k) {
      const int T = 9 * wave + k;
      op[k] = lds + VB_OFF + (4 * g + (l15 >> 2)) * 1152 + (T ^ ((2 * g + (l15 >> 3)) & 3)) * 32 + (l15 & 3) * 8;
      pin_v(op[k]);
    }
    f32x4 ua[18][2];
    auto ust = [&](auto KC) {
      constexpr int k = decltype(KC)::value;
      asm volatile("s_waitcnt vmcnt(%0) lgkmcnt(0)" :: "n"(k == 0 ? 29 : (k < 3 ? 18 : 9)) : "memory");
      __builtin_amdgcn_s_barrier(); CBAR(); SB();
      TK(10 + k);
      if (k == 2) {
#pragma unroll
        for (int i = 0; i < 4; ++i) x_move(op[i], -VB_OFF);
      }
      u32x4 f[4];
      u32x2 wh[9][2];
#pragma unroll
      for (int i = 0; i < 2; ++i) { f[i] = *(lds_u32x4*)(vr + i * 4096 + k * 1024); SB(); }
#pragma unroll
      for (int i = 0; i < 18; ++i) { wh[i >> 1][i & 1] = trd(op[(i >> 1) & 3] + (k & 1) * 36864 + (i & 1) * 18432 + (i >> 3) * 128); SB(); }
#pragma unroll
      for (int i = 2; i < 4; ++i) { f[i] = *(lds_u32x4*)(vr + i * 4096 + k * 1024); SB(); }
      if (k == 1) {
#pragma unroll
        for (int i = 0; i < 9; ++i) { x_m0(xw + VB_OFF, i); x_dma0(xl, 2 * 36864, i); }
        SB();
      }
      if (k == 2) {
        asm volatile("" : "+v"(g2));
        int c = (int)g2;
        wait_flag_c<4>(R, pf, 0, c); SB();
      }
#pragma unroll
      for (int t = 0; t < 19; ++t) {
        const int ct = t % 9;
        if (t < 9) { wo[ct][k] = (u32x4){wh[ct][0][0], wh[ct][0][1], wh[ct][1][0], wh[ct][1][1]}; if (ct < 7) pin_a(wo[ct][k]); else pin_v(wo[ct][k]); }
#pragma unroll
        for (int tt = 0; tt < 2; ++tt) {
          if (t == 18) break;
          f32x4& d = ua[t][tt];
          const u32x4& v = f[(t / 9) * 2 + tt];
          if (ct < 7) { if (k == 0) d = mfB0(v, wo[ct][k]); else mfB(d, v, wo[ct][k]); }
          else { if (k == 0) d = mf0(v, wo[ct][k]); else mfa(d, v, wo[ct][k]); }
          SB();
        }
        if (k == 3 && t > 0) {
          f32x4 (&e)[2] = ua[t - 1];
          if (t == 18) mf_wait2(e[0], e[1]);
          const u32x4 uo = {pkv(e[0][0], e[0][1]), pkv(e[0][2], e[0][3]), pkv(e[1][0], e[1][1]), pkv(e[1][2], e[1][3])};
          if (t < 10) st128(uo, R, zo, (t - 1) * 40960); else { u1[t < 10 ? 0 : t - 10] = uo; pin_v(u1[t < 10 ? 0 : t - 10]); }
          SB();
        }
      }
    };
    ust(std::integral_constant<int, 0>()); ust(std::integral_constant<int, 1>()); ust(std::integral_constant<int, 2>()); ust(std::integral_constant<int, 3>());
    asm volatile("" :: "v"(go.v0));
#pragma unroll
    for (int i = 0; i < 9; ++i) { x_m0(xw + VB_OFF + 36864, i); x_dma0(xl, 3 * 36864, i); }
    SB();
  }
  TK(14);
  XP rd0 = x_at(lds + VB_OFF, xl), rd1 = x_at(lds + VB_OFF + 36864, xl);
  unsigned b0 = VB_OFF, b1 = VB_OFF + 36864, b2 = 0;
  pin_v(vr); pin_v(vw); pin_v(zo);
  u32x4 bb[3][2], vf[2][2][4];
#pragma unroll
  for (int k = 0; k < 4; ++k) cc[k >> 1][k & 1] = (f32x4){0.f, 0.f, 0.f, 0.f};
#pragma unroll
  for (int k = 0; k < 16; ++k) asm volatile("" : "=v"(vf[k >> 3][(k >> 2) & 1][k & 3]));
  VMW(18);
  BAR();
#pragma unroll
  for (int ks = 0; ks < 2; ++ks) { bb[ks][0] = XF(rd0, 0, ks); bb[ks][1] = XF(rd0, 1, ks); }
  auto iter = [&](auto PC, int s) {
    constexpr int P = decltype(PC)::value & 1, W = 0, NOV = 0, NOU = decltype(PC)::value >> 1;
    XP& buf = P ? rd1 : rd0;
    XP& nrd = P ? rd0 : rd1;
    f32x4 (&c)[2][2] = av[P];
    f32x4 (&e)[2][2] = av[P ^ 1];
    u32x4 o, q;
    unsigned s1, s2, sx, xm, xd, sf;
#define VM(ks, t, tt) do { if (NOV) break; if ((ks) == 0) c[t][tt] = mfA0(wv[t][ks], bb[(ks) % 3][tt]); else mfA(c[t][tt], wv[t][ks], bb[(ks) % 3][tt]); SB(); } while (0)
#define UM(k, tt) do { if (NOU) break; f32x4& d = cc[(gq + P) & 1][tt]; const u32x4& f = vf[gq < 2 ? P : P ^ 1][tt][k];                                      \
                       if (ct < 7) { if ((k) == 0) d = mfB0(f, wo[ct][k]); else mfB(d, f, wo[ct][k]); }                                       \
                       else { if ((k) == 0) d = mf0(f, wo[ct][k]); else mfa(d, f, wo[ct][k]); } SB(); } while (0)
#define BRD(ks, tt) do { if (NOV) break; bb[(ks) % 3][tt] = (ks) < 18 ? XF(buf, tt, (ks) % 18) : XF(nrd, tt, (ks) % 18); SB(); } while (0)
#define CV(j) do { if (NOU) break; o[j] = pkv(cc[(gq + P + 1) & 1][(j) >> 1][((j) & 1) * 2], cc[(gq + P + 1) & 1][(j) >> 1][((j) & 1) * 2 + 1]); SB(); } while (0)
#define CE(j, tt) do { if (gq == 0 && !NOU) { q[j] = pkv(e[(j) >> 1][tt][((j) & 1) * 2], e[(j) >> 1][tt][((j) & 1) * 2 + 1]); SB(); } } while (0)
#define VR(j) do { if (gq == 1 && !NOU) { vf[P ^ 1][(j) & 1][(j) >> 1] = *(lds_u32x4*)(vr + P * 8192 + ((j) & 1) * 4096 + ((j) >> 1) * 1024); SB(); } } while (0)
#define LW(n) do { __builtin_amdgcn_s_waitcnt(0xC07F | ((n) << 8)); SB(); } while (0)
#pragma unroll
    for (int gq = NOV ? 2 : 0; gq < 9; ++gq) {
      const int ct = (gq + 7) % 9, k0 = 2 * gq, k1 = 2 * gq + 1, pt = (gq + 6) % 9;
      if (NOU || gq == 0 || gq > 2) LW(2); else if (gq == 1) LW(6); else LW(8);
      if (gq == 8 && !NOV) { __builtin_amdgcn_s_barrier(); SB(); }
      VM(k0, 0, 0); BRD(k0 + 2, 0);
      if (gq == (NOV ? 2 : 0)) {
        s1 = (unsigned)(s - 1) < NSEG ? (unsigned)(s - 1) * ZS : OOB; s2 = s >= 4 ? (unsigned)(s - 2) * ZS : OOB; sx = (unsigned)clampseg(s + 2) * 36864u;
        s1 = __builtin_amdgcn_readfirstlane(s1); s2 = __builtin_amdgcn_readfirstlane(s2); sx = __builtin_amdgcn_readfirstlane(sx);
        asm volatile("" : "+s"(s1), "+s"(s2), "+s"(sx)); SB();
      }
      UM(0, 0); CV(0);
      unsigned su = (gq < 3 ? s2 : s1) + pt * 40960;
      asm volatile("" : "+s"(su)); SB();
      VM(k0, 0, 1); BRD(k0 + 2, 1);
      if (!NOV && gq == 0) { sf = FG >= 7 ? (unsigned)(s - 2) : (s == 2 ? ~1u : (unsigned)(s - 3)); pin_v(sf); SB(); }
      if (!NOV && gq == 2) {
        xm = xw + b2; xd = b2 - b0;
        const unsigned t = b0; b0 = b1; b1 = b2; b2 = t;
        asm volatile("" : "+s"(xm), "+s"(xd), "+s"(b0), "+s"(b1), "+s"(b2)); SB();
      }
      UM(0, 1); CV(1);
      if (gq == 1 && !NOU) { LW(2); __builtin_amdgcn_s_barrier(); SB(); }
      VM(k0, 1, 0); VR(0); CV(2);
      UM(1, 0); VR(1); CV(3);
#define ST(n) do { if (!NOU && !NOST && STG0 + W * STGD == n) { st128(o, R, zo, su); SB(); } } while (0)
#define VW2(j, tt) do { if (gq == 0 && !NOU) { *(lds_u32x2*)(vw + P * 8192 + (tt) * 4096 + (j) * 4) = (u32x2){q[j], q[(j) + 1]}; SB(); } } while (0)
      ST(5);
      if (NOU && gq < 5) { st128(u1[gq < 5 ? 2 * gq : 0], R, zo, ZS + 2 * gq * 40960); SB(); }
      VM(k0, 1, 1); ST(6);
      UM(1, 1); CE(0, 0); CE(1, 0); VR(2); ST(7);
      if (gq == 1 && !NOU) LW(5); else LW(2);
      VM(k1, 0, 0); BRD(k1 + 2, 0); CE(2, 0); ST(8);
      UM(2, 0); VW2(0, 0); VR(3); ST(9);
      if (!NOV && gq >= 2 && gq < 7) { x_m0(xm, gq - 2); SB(); }
      if (!NOV && gq == 8) { x_move(buf.p[0], xd); x_move(buf.p[1], xd); SB(); }
      VM(k1, 0, 1); BRD(k1 + 2, 1); CE(3, 0); ST(10);
      UM(2, 1); VW2(2, 0); VR(4); ST(11);
      if (!NOV && gq >= 2 && gq < 7) { x_dma(xl, sx, gq - 2); SB(); }
      VM(k1, 1, 0); VR(5); ST(12);
      CE(0, 1); CE(1, 1);
      if (!NOV && gq >= 3 && gq < 7) { x_m0(xm, gq + 2); SB(); }
      UM(3, 0); VW2(0, 1); VR(6); ST(13);
      if (!NOV && gq == 0) { PADE(); if (P) PADS(113); else PADS(112); } else PAD();
      SB();
      VM(k1, 1, 1); CE(2, 1); CE(3, 1); VR(7); ST(14);
      if (!NOV && gq >= 3 && gq < 7) { x_dma(xl, sx, gq + 2); SB(); }
      UM(3, 1); VW2(2, 1); ST(15);
      if (NOU && gq < 4) { st128(u1[gq < 4 ? 2 * gq + 1 : 0], R, zo, ZS + (2 * gq + 1) * 40960); SB(); }
#undef ST
#undef VW2
      if (NOU && gq == 7) { VMW(18); SB(); }
      if (!NOU && P && gq == EG1 && UNLIKELY(s == 3)) { asm volatile("s_waitcnt vmcnt(%0)" :: "n"(EG1 + 7) : "memory"); st32(sf, R, vpf, 0); SB(); }
      if (!NOU && gq == FG && !NOFL) {
        asm volatile("s_waitcnt vmcnt(%0)" :: "n"(FG >= 7 ? FG + 7 : (FG < 2 ? FG + 16 : 20)) : "memory");
        st32(sf, R, vpf, 0); SB();
      }
      if (!NOU && gq == 7 && FG != 7) { VMW(20); SB(); }
      if (!NOU && gq == EG && UNLIKELY((unsigned)(s - 4) < UEF || s >= NSEG)) { asm volatile("s_waitcnt vmcnt(%0)" :: "n"(3 * EG - 5) : "memory"); st32(sf, R, vpf, 0); SB(); }
    }
#undef VM
#undef UM
#undef BRD
#undef CV
#undef CE
#undef VR
#undef LW
  };
  iter(std::integral_constant<int, 2>(), 2);
  RT(8);
  TKS;
#pragma nounroll
  for (int s = 3;; s += 2) {
    iter(std::integral_constant<int, 1>(), s);
    if (s == NSEG + 1) break;
    iter(std::integral_constant<int, 0>(), s + 1);
  }
  PADE();
  VMW(0);
  RT(7);
}

#define RING_OFF (2 * STG_BYTES)
#define RING_STR 456
#define OST_OFF (RING_OFF + 64 * RING_STR)
__device__ __forceinline__ float dot2(unsigned a, unsigned b, float c) {
  return __builtin_amdgcn_fdot2_f32_bf16(__builtin_bit_cast(bf16x2, a), __builtin_bit_cast(bf16x2, b), c, false);
}
__device__ __forceinline__ float swp1(float x) { return __uint_as_float(__builtin_amdgcn_update_dpp(0u, __float_as_uint(x), 0xB1, 0xf, 0xf, false)); }
__device__ __forceinline__ void role_aw(const Args& a, rsrc_t R, const Go& go, lds_char* lds, int ai, int tid, int wave, int lane) {
  const int l15 = lane & 15, g = lane >> 4;
  const int ga = ai & 7, rr = ai >> 3;
  go_wait(R, go, lane);
  const int cls = get_cls(R, lane);
  u32x4 w[4][18];
  {
    u32x4 sg[3][7];
    unsigned gv[9], wo[7], lo[7];
    const rsrc_t W = __builtin_amdgcn_make_buffer_rsrc((void*)a.wqkv, 0, DM * 1728 * 2, 0x00020000);
    const rsrc_t G = __builtin_amdgcn_make_buffer_rsrc((void*)a.gw, 0, DM * 2, 0x00020000);
#pragma unroll
    for (int k = 0; k < 7; ++k) {
      const int u = tid + 256 * k, row = u / 27, cu = u % 27;
      wo[k] = row < 64 ? (unsigned)(row * 3456 + (cu / 9) * 1152 + ga * 144 + (cu % 9) * 16) : OOB;
      lo[k] = row < 64 ? (unsigned)(row * 544 + cu * 16) : (unsigned)((tid & 63) * 544 + 448);
    }
    auto gl = [&](u32x4 (&st)[7], int c) {
#pragma unroll
      for (int k = 0; k < 7; ++k) st[k] = __builtin_amdgcn_raw_buffer_load_b128(W, wo[k], (unsigned)c * (64u * 3456u), (NTM & 16) ? 2 : 0);
    };
#pragma unroll
    for (int c = 0; c < 9; ++c) gv[c] = __builtin_amdgcn_raw_buffer_load_b16(G, (unsigned)(tid & 63) * 2, (unsigned)c * 128u, 0);
    gl(sg[0], 0); if (ASLP > 0) __builtin_amdgcn_s_sleep(ASLP);
    gl(sg[1], 1); if (ASLP > 0) __builtin_amdgcn_s_sleep(ASLP);
    gl(sg[2], 2);
#pragma unroll
    for (int c = 0; c < 9; ++c) {
      lds_char* buf = lds + (c & 1) * STG_BYTES;
#pragma unroll
      for (int k = 0; k < 7; ++k) *(lds_u32x4*)(buf + lo[k]) = sg[c % 3][k];
      *(lds_u32x4*)(buf + (tid & 63) * 544 + 432) = (u32x4){gv[c], 0u, 0u, 0u};
      if (ASLP > 0) __builtin_amdgcn_s_sleep(ASLP);
      if (c + 3 < 9) gl(sg[c % 3], c + 3);
      BAR();
#pragma unroll
      for (int kk = 0; kk < 2; ++kk)
#pragma unroll
        for (int t = 0; t < 4; ++t) {
          lds_char* p = buf + (kk * 32 + g * 8 + (l15 >> 2)) * 544 + (wave * 64 + t * 16) * 2 + (l15 & 3) * 8;
          u32x2 v0 = trd(p), v1 = trd(p + 4 * 544);
          w[t][2 * c + kk] = (u32x4){v0[0], v0[1], v1[0], v1[1]};
        }
      SB();
#pragma unroll
      for (int k = 0; k < 8; ++k) { if (c < 7) pin_a(w[k & 3][2 * c + (k >> 2)]); else pin_v(w[k & 3][2 * c + (k >> 2)]); }
    }
    BAR();
  }
  RT(1);
  auto sgm = [&](int it) { const int j = it / 5; return min(16 * j + 4 * rr + it - 5 * j - 1, NSEG - 1); };
  const int i0 = rr > 0 ? 0 : 1;
  u32x4 xr[9];
  XL xl; x_init(xl, a.x, wave, lane);
  x_gload(xr, xl, sgm(i0));
  x_swrite(xr, lds + i0 * STG_BYTES, xl);
  x_gload(xr, xl, sgm(i0 + 1));
  lds_char* ring = lds + RING_OFF;
  lds_char* ost = lds + OST_OFF;
  const int t = tid >> 3, hh = (tid >> 1) & 3, half = tid & 1;
  const float sc = 0.23570226039551584f * 1.4426950408889634f;
  auto done = [&](int s) { __builtin_amdgcn_raw_ptr_buffer_atomic_add_i32(1, R, tid == 0 && s >= 0 ? FX(FL_APROG, cls, 1) + s * 128 : OOB, 0, SC1); };
  const unsigned gto = (ga == 0 && wave == 3 && g == 2) ? OFF_GATE + l15 * 4 : OOB;
  int sp = -1;
  for (int it = i0; it < 20; ++it) {
    const int s = sgm(it);
    BAR();
    {
      f32x4 acc[4][2];
      proj4(lds + (it & 1) * STG_BYTES, lds + ((it + 1) & 1) * STG_BYTES, xr, xl, sgm(it + 2), wave, lane, w[0], w[1], w[2], w[3], acc);
      VMW(9);
#pragma unroll
      for (int tl = 0; tl < 4; ++tl)
#pragma unroll
        for (int tt = 0; tt < 2; ++tt) {
          const int col = wave * 64 + tl * 16 + g * 4;
          u32x2 o = {pk2(acc[tl][tt][0], acc[tl][tt][1]), pk2(acc[tl][tt][2], acc[tl][tt][3])};
          if (col < 216) *(__attribute__((address_space(3))) u32x2*)(ring + (((s & 1) * 32 + tt * 16 + l15) * RING_STR) + col * 2) = o;
        }
#pragma unroll
      for (int tt = 0; tt < 2; ++tt) {
        float z = acc[1][tt][0];
        float gv = __builtin_amdgcn_rcpf(1.f + __builtin_amdgcn_exp2f(-1.4426950408889634f * z));
        st32(__float_as_uint(gv), R, gto + tt * 64, (unsigned)s * 128u);
      }
    }
    BAR();
    done(sp);
    sp = it % 5 != 0 ? s : -1;
    { const int s = it; STEPRT(2, 0); }
    if (it % 5 != 0) {
      const int row = s * 32 + t;
      unsigned q[9];
      lds_char* qp = ring + (row & 63) * RING_STR + hh * 36;
#pragma unroll
      for (int j = 0; j < 9; ++j) q[j] = *(lds_u32*)(qp + j * 4);
      float lg[4];
      lds_char* vp[4];
#pragma unroll
      for (int wi = 0; wi < 4; ++wi) {
        const int r2 = row - 2 * (half * 4 + wi);
        const bool ok = r2 >= 0;
        lds_char* kp = ring + ((ok ? r2 : row) & 63) * RING_STR + hh * 36 + 144;
        vp[wi] = kp + 144;
        float d = 0.f;
#pragma unroll
        for (int j = 0; j < 9; ++j) d = dot2(q[j], *(lds_u32*)(kp + j * 4), d);
        lg[wi] = ok ? d : -__builtin_inff();
      }
      float m = fmaxf(fmaxf(lg[0], lg[1]), fmaxf(lg[2], lg[3]));
      m = fmaxf(m, swp1(m));
      const float ms = -m * sc;
      float sum = 0.f;
#pragma unroll
      for (int wi = 0; wi < 4; ++wi) { lg[wi] = __builtin_amdgcn_exp2f(fmaf(lg[wi], sc, ms)); sum += lg[wi]; }
      sum += swp1(sum);
      const float inv = __builtin_amdgcn_rcpf(sum);
      const unsigned p01 = pk2(lg[0] * inv, lg[1] * inv), p23 = pk2(lg[2] * inv, lg[3] * inv);
#pragma unroll
      for (int j = 0; j < 9; ++j) {
        const unsigned v0 = *(lds_u32*)(vp[0] + j * 4), v1 = *(lds_u32*)(vp[1] + j * 4), v2 = *(lds_u32*)(vp[2] + j * 4), v3 = *(lds_u32*)(vp[3] + j * 4);
        float lo = dot2(__builtin_amdgcn_perm(v1, v0, 0x05040100u), p01, 0.f);
        lo = dot2(__builtin_amdgcn_perm(v3, v2, 0x05040100u), p23, lo);
        float hi = dot2(__builtin_amdgcn_perm(v1, v0, 0x07060302u), p01, 0.f);
        hi = dot2(__builtin_amdgcn_perm(v3, v2, 0x07060302u), p23, hi);
        lo += swp1(lo); hi += swp1(hi);
        if ((j & 1) == half) *(lds_u32*)(ost + t * 144 + hh * 36 + j * 4) = pk2(lo, hi);
      }
      BAR();
#pragma unroll
      for (int k = 0; k < 2; ++k) {
        const int u = tid + 256 * k;
        const bool ok = u < 288;
        const int ur = u & 31, uc = ok ? u >> 5 : 0, cc = ga * 9 + uc;
        u32x4 o = *(lds_u32x4*)(ost + ur * 144 + uc * 16);
        st128(o, R, ok ? OFF_OA + (unsigned)(((ur >> 4) * 18 + (cc >> 2)) * 1024 + ((cc & 3) * 16 + (ur & 15)) * 16) : OOB, (unsigned)s * 36864u);
      }
      CBAR();
    }
  }
  VMW(0);
  BAR();
  done(sp);
  RT(7);
}

__device__ __forceinline__ void role_out(const Args& a, rsrc_t R, const Go& go, lds_char* lds, int o, int tid, int wave, int lane) {
  const int l15 = lane & 15, g = lane >> 4;
  const int cb = o % 9, oj = o / 9, sw = cb == 8 ? OSW : 0, grp = oj < 4 ? oj : (oj == 4 ? sw + 4 : NSEG - 16 + oj);
  auto nxt = [&](int s) { return s < sw ? s + 4 : (s < NSEG - 16 ? s + 5 : s + 8); };
  u32x4 wf[4][5];
  {
    u32x4 st[18];
#pragma unroll
    for (int k = 0; k < 18; ++k) { int u = tid + 256 * k; st[k] = *(const u32x4*)(a.wo + (size_t)(u >> 3) * DM + cb * 64 + (u & 7) * 8); }
#pragma unroll
    for (int k = 0; k < 18; ++k) { int u = tid + 256 * k; *(lds_u32x4*)(lds + (u >> 3) * 144 + (u & 7) * 16) = st[k]; }
    BAR();
#pragma unroll
    for (int i = 0; i < 5; ++i)
#pragma unroll
      for (int ct = 0; ct < 4; ++ct) {
        const int ks = wave + 4 * i;
        lds_char* p = lds + ((ks < 18 ? ks : 17) * 32 + g * 8 + (l15 >> 2)) * 144 + ct * 32 + (l15 & 3) * 8;
        u32x2 v0 = trd(p), v1 = trd(p + 4 * 144);
        wf[ct][i] = (ks < 18) ? (u32x4){v0[0], v0[1], v1[0], v1[1]} : (u32x4){0u, 0u, 0u, 0u};
        pin_a(wf[ct][i]);
      }
    BAR();
  }
  const unsigned zb = OFF_ZF + (unsigned)(4 * cb) * 40960u + lane * 16;
  if (o < 44 && o != 40 && !go_wait(R, go, lane)) return;
  const int cls = get_cls(R, lane);
  const unsigned flk = lane == 32 ? FX(FL_KMPROG, cls, 3) + ((o * 4 + wave) & 31) * 128 : OOB;
  const bool isu = (lane & 24) == 8 && lane < 48;
  auto faddr = [&](int s) -> unsigned {
    return lane < 8 ? (lane == 0 ? FX(FL_APROG, cls, 1) + s * 128 : OOB)
                    : (isu ? FX(FL_UPROG + 8192, cls, 6) + (wave * 8 + (lane & 7)) * 128 + ((cb * 64 + (lane < 16 ? 0 : 63)) / 144) * 4
                           : (lane < 32 ? (s == NSEG - 1 ? FX(FL_PXLAST, cls, 1) + (wave * 16 + lane - 16) * 128 : FX(FL_PXPROG, cls, 0) + (wave * 8 + (lane & 7)) * 128 + (s & 1) * 4)
                                        : (s + 3 < NSEG ? flk : FX(FL_YPROG, cls, 2) + (wave * 8 + (lane & 7)) * 16 + ((cb * 64 + (lane < 48 ? 0 : 63)) / 144) * 4 + (lane < 56 ? 0 : OOB))));
  };
  const int t = tid >> 3, n8 = tid & 7;
  const rsrc_t O = __builtin_amdgcn_make_buffer_rsrc(a.out, 0, S_LEN * DM * 2, 0x00020000);
  const i32x4 D = mkdesc(a.wb);
  int fv = -1;
  RT(1);
  const unsigned ofo = OFF_OA + (unsigned)(wave * 1024 + lane * 16), ofl = wave < 2 ? ofo : OOB, gto = OFF_GATE + l15 * 4;
  const unsigned pfo = OFF_PXF + (unsigned)(wave * 8192 + lane * 16), pfy = OFF_PXF + (unsigned)((24 + wave * 2) * 1024 + lane * 16);
  u32x4 zu[4][8];
  int fu = -1;
  const unsigned fua = isu ? faddr(0) : OOB;
  const unsigned zw = zb + wave * 8192;
#pragma unroll
  for (int k = 0; k < 32; ++k) asm volatile("" : "=a"(zu[k >> 3][k & 7]));
  auto zu4 = [&](unsigned vo, int s, int hh) {
#pragma unroll
    for (int ct = 0; ct < 4; ++ct)
      asm volatile("buffer_load_dwordx4 %0, %1, %2, %3 offen offset:%4 sc1" : "+a"(zu[ct][hh])
                   : "v"(vo), "s"(D), "s"((unsigned)(s & RMASK) * ZS + ct * 40960 + (hh >> 2) * 4096), "n"((hh & 3) * 1024) : "memory");
  };
  auto zuld = [&](int s) -> bool {
    unsigned spins = 0;
    while (ballot(isu && fu < s) != 0) {
      if (++spins > SPINCAP) return false;
      if (spins > 1) __builtin_amdgcn_s_sleep(16);
      if (s == NSEG - 1 && spins > 1) __builtin_amdgcn_s_sleep(100);
      CBAR();
      fu = (int)ld32(R, fua, 0);
    }
#pragma unroll
    for (int hh = 0; hh < 8; ++hh) zu4(zw, s, hh);
    SB();
    return true;
  };
  if (!zuld(grp)) return;
  unsigned acc_t[8] = {0, 0, 0, 0, 0, 0, 0, 0}, t0 = 0, t1 = 0, t2 = 0, t3 = 0, t4 = 0, t5 = 0, t6 = 0;
  for (int s = grp; s < NSEG; s = nxt(s)) {
    TS(t0);
    const unsigned fa = faddr(s);
    const int need = isu || fa >= OOB ? -1 : (lane == 0 ? 7 : ((lane >= 32 && s + 3 < NSEG) ? s + 2 : s));
    const bool far = lane >= 8 && lane < 48 && (isu || (lane < 32 && s < NSEG - 1));
    unsigned spins = 0;
    while ((ballot(fv < need) & 0xFFull) != 0) {
      if (++spins > SPINCAP) return;
      if (spins > 1) __builtin_amdgcn_s_sleep(32);
      CBAR();
      const int nv = (int)lda(R, fv < need && lane < 8 ? fa : OOB);
      fv = lane < 8 ? max(fv, nv) : fv;
    }
    TS(t1);
#define OLD(d, vo, so, o) asm volatile("buffer_load_dwordx4 %0, %1, %2, %3 offen offset:%4 sc1" : "=v"(d) : "v"(vo), "s"(D), "s"(so), "n"(o) : "memory")
    u32x4 zy[4][2], pf[2][10], of[2][5];
    float gt[2];
#pragma unroll
    for (int qt = 0; qt < 2; ++qt) {
#pragma unroll
      for (int i = 0; i < 5; ++i) OLD(of[qt][i], i < 4 ? ofo : ofl, (unsigned)s * 36864u + qt * 18432 + i * 4096, 0);
      asm volatile("buffer_load_dword %0, %1, %2, %3 offen offset:%4 sc1" : "=v"(gt[qt]) : "v"(gto), "s"(D), "s"((unsigned)s * 128u), "n"(qt * 64) : "memory");
    }
    SB();
#define FWAIT(m) do { spins = 0;                                                                                          \
      while ((ballot(fv < need) & (m)) != 0) {                                                                            \
        if (++spins > SPINCAP) return;                                                                                    \
        if (spins > 1) { if (s < OFAST) __builtin_amdgcn_s_sleep(OSLP); else __builtin_amdgcn_s_sleep(OSL2); }            \
        CBAR();                                                                                                           \
        const int n0 = (int)lda(R, fv < need && !far ? fa : OOB), n1 = (int)ld32(R, fv < need && far ? fa : OOB, 0), nv = far ? n1 : n0;                                                             \
        fv = max(fv, nv);                                                                                                 \
      } } while (0)
#define ZYLD(zv) do { _Pragma("unroll") for (int hp = 0; hp < 2; ++hp) _Pragma("unroll") for (int ct = 0; ct < 4; ++ct)                        \
                        asm volatile("buffer_load_dwordx4 %0, %1, %2, %3 offen offset:%4 sc1" : "+v"(zy[ct][hp])                                   \
                                     : "v"(zv), "s"(D), "s"((unsigned)(s & RMASK) * ZS + ct * 40960 + 32768 + wave * 2048), "n"(hp * 1024) : "memory"); } while (0)
#define MFS(hh) do { _Pragma("unroll") for (int qt = 0; qt < 2; ++qt) _Pragma("unroll") for (int ct = 0; ct < 4; ++ct) {                           \
                        ab[ct][qt] = mfma(hh < 8 ? zu[ct][hh & 7] : zy[ct][hh & 1], pf[qt][hh], hh ? ab[ct][qt] : (f32x4){0.f, 0.f, 0.f, 0.f}); SB(); } } while (0)
    TS(t2);
#pragma unroll
    for (int k = 0; k < 8; ++k) asm volatile("" : "=v"(zy[k >> 1][k & 1]));
    bool yr = false;
#define ZYW() asm volatile("s_waitcnt vmcnt(0)" : "+v"(zy[0][0]), "+v"(zy[0][1]), "+v"(zy[1][0]), "+v"(zy[1][1]), "+v"(zy[2][0]), "+v"(zy[2][1]), "+v"(zy[3][0]), "+v"(zy[3][1]) :: "memory")
#define ZYTRY(w) do { if (!yr && (ballot(fv < need) >> 32) == 0) { ZYLD(zb); if (w) ZYW(); yr = true; } } while (0)
    spins = 0;
#pragma nounroll
    while ((unsigned)ballot(fv < need) != 0) {
      if (++spins > SPINCAP) return;
      if (spins > 1) { if (s < OFAST) __builtin_amdgcn_s_sleep(OSLP); else __builtin_amdgcn_s_sleep(OSL2); }
      CBAR();
      const int nv = (int)ld32(R, fv < need ? fa : OOB, 0);
      CBAR();
      ZYTRY(1);
      fv = max(fv, nv);
    }
    TS(t3);
#pragma unroll
    for (int hh = 0; hh < 10; ++hh)
#pragma unroll
      for (int qt = 0; qt < 2; ++qt) OLD(pf[qt][hh], hh < 8 ? pfo : pfy, (unsigned)(s & RMASK) * 81920u + qt * 40960 + (hh >> 2) * 4096, (hh & 3) * 1024);
    ZYTRY(0);
    const int sn = nxt(s) < NSEG ? nxt(s) : s;
    int n1;
    asm volatile("buffer_load_dword %0, %1, %2, 0 offen sc1" : "=v"(n1) : "v"(faddr(sn)), "s"(D) : "memory");
    const bool uok = sn != s && ballot(isu && fu < sn) == 0;
    unsigned zn = uok ? zw : OOB;
    pin_v(zn);
    asm volatile("s_waitcnt vmcnt(21)" : "+v"(of[0][0]), "+v"(of[0][1]), "+v"(of[0][2]), "+v"(of[0][3]), "+v"(of[0][4]), "+v"(of[1][0]), "+v"(of[1][1]),
                 "+v"(of[1][2]), "+v"(of[1][3]), "+v"(of[1][4]), "+v"(gt[0]), "+v"(gt[1]) :: "memory");
    SB();
    f32x4 aa[4][2];
#pragma unroll
    for (int ct = 0; ct < 4; ++ct)
#pragma unroll
      for (int qt = 0; qt < 2; ++qt) {
        f32x4 acc = {0.f, 0.f, 0.f, 0.f};
#pragma unroll
        for (int i = 0; i < 5; ++i) acc = mfma(wf[ct][i], of[qt][i], acc);
        aa[ct][qt] = acc * gt[qt];
        pin_v(aa[ct][qt]);
      }
    SB();
    f32x4 ab[4][2];
#pragma unroll
    for (int hh = 0; hh < 8; ++hh) {
      asm volatile("s_waitcnt vmcnt(%2)" : "+v"(pf[0][hh]), "+v"(pf[1][hh]) : "n"(19 + 2 * hh) : "memory");
      SB();
      MFS(hh);
      zu4(zn, sn, hh);
      SB();
    }
    TS(t4);
    asm volatile("s_waitcnt vmcnt(32)" : "+v"(n1), "+v"(pf[0][8]), "+v"(pf[1][8]), "+v"(pf[0][9]), "+v"(pf[1][9]) :: "memory");
    fu = isu ? n1 : fu;
    if (sn != s && !uok && !zuld(sn)) return;
    if (!yr) { FWAIT(~0ull); ZYLD(zb); ZYW(); }
#pragma unroll
    for (int k = 0; k < 8; ++k) pin_v(zy[k >> 1][k & 1]);
    fv = n1;
    SB();
    MFS(8); MFS(9);
    TS(t5);
#undef OLD
#undef ZYTRY
#undef ZYW
#undef FWAIT
#undef ZYLD
#undef MFS
#pragma unroll
    for (int qt = 0; qt < 2; ++qt)
#pragma unroll
      for (int ct = 0; ct < 4; ++ct)
        *(lds_f32x4*)(lds + wave * 8704 + (qt * 16 + l15) * 272 + (ct * 16 + g * 4) * 4) = aa[ct][qt] + ab[ct][qt] * (1.f - gt[qt]);
    BAR();
    f32x4 b0 = *(lds_f32x4*)(lds + t * 272 + n8 * 32), b1 = *(lds_f32x4*)(lds + t * 272 + n8 * 32 + 16);
#pragma unroll
    for (int w = 1; w < 4; ++w) { b0 += *(lds_f32x4*)(lds + w * 8704 + t * 272 + n8 * 32); b1 += *(lds_f32x4*)(lds + w * 8704 + t * 272 + n8 * 32 + 16); }
    u32x4 ov = {pk2(b0[0], b0[1]), pk2(b0[2], b0[3]), pk2(b1[0], b1[1]), pk2(b1[2], b1[3])};
    __builtin_amdgcn_raw_buffer_store_b128(ov, O, (unsigned)((s * 32 + t) * DM + cb * 64 + n8 * 8) * 2, 0, OAUX);
    BAR();
    if (s < 4) RT(2);
    TS(t6); ACC(0, 4 * (t1 - t0)); ACC(1, 4 * (t2 - t1)); ACC(2, 4 * (t3 - t2)); ACC(3, 4 * (t4 - t3)); ACC(4, 4 * (t5 - t4)); ACC(5, 4 * (t6 - t5));
    STEPRT(0, 0);
  }
  for (int k = 0; k < 8; ++k) SV(16 + k, acc_t[k]);
  RT(7);
}

#define LDS_BYTES 163840
__device__ __forceinline__ void code_touch(u64 pc, unsigned len, unsigned off, unsigned ld) {
  const i32x4 P = {(int)(unsigned)pc, (int)(unsigned)(pc >> 32), (int)len, 0x00020000};
  asm volatile("s_mov_b32 m0, %2\n\ts_nop 0\n\tbuffer_load_dword %0, %1, 0 offen lds" :: "v"(off), "s"(P), "s"(ld) : "memory");
}
__global__ void __launch_bounds__(256, 1) mega(Args a) {
  __shared__ __attribute__((aligned(16))) char smem[LDS_BYTES];
  lds_char* lds = (lds_char*)smem;
  const int b = blockIdx.x, tid = threadIdx.x, lane = tid & 63, wave = __builtin_amdgcn_readfirstlane(tid >> 6);
  const int x = b & 7, sl = b >> 3;
  const rsrc_t R = __builtin_amdgcn_make_buffer_rsrc(a.wb, 0, (int)OFF_END, 0x00020000);
  const Go go = go_look(R, b + wave * 4);
  if (b == 244) go_lead(R, tid, wave, lane);
  u64 pc; unsigned len;
  const unsigned tst = rt_now();
  {
    asm volatile("s_getpc_b64 %0\n.Lcode_here:\n\ts_mov_b32 %1, zz_code_end-.Lcode_here" : "=s"(pc), "=s"(len));
    if (wave == CPF) code_touch(pc, len, (unsigned)(sl * 8192 + lane * 128), (unsigned)(uintptr_t)lds + (x >= 4 && sl >= 8 && sl < 16 ? VB_OFF + 16384 : 153600));
  }
  RT(0);
  if (wave == 3 && (sl == 24 || sl == 29)) {
    unsigned d[4];
#pragma unroll
    for (int k = 0; k < 3; ++k) d[k] = ld32(R, (unsigned)(k * 64 + lane) << 20, 0);
    d[3] = *(const unsigned*)(a.out + min(lane, 2) * (1 << 19));
    asm volatile("" :: "v"(d[0]), "v"(d[1]), "v"(d[2]), "v"(d[3]));
  }
  if (x < 4 ? sl >= 25 : sl >= 16) {
    const unsigned t0 = rt_now();
    const int nw = x < 4 ? 28 : 56, id = __builtin_amdgcn_readfirstlane((sl - (x < 4 ? 25 : 16)) * 4 + wave);
    const int fa = x < 4 ? HF1 : HF3, fb = x < 4 ? HF0 : HF2, na = 6 * (18 - fa);
    unsigned d = 0;
    for (int e = id < nw ? id : 999; e < na + 3 * (18 - fb); e += nw) {
      const int j = e % 3, q = e / 3, blk = e < na ? fa + (q >> 1) : q - 2 * (18 - fa) + fb;
      const u16* W = e < na && (q & 1) ? a.Wk : ((e < na) == (x < 4) ? a.Wq : a.Wv);
      const int c = (int)((u64)W >> 8), hh = j < 2 ? (x & 3) * 8 + ((1 - c) & 3) + 4 * j : (31 - c) & 31;
      if ((hh >> 3) != (x & 3) || !HLP) continue;
      const u16* p = W + (size_t)(blk * 32 + (lane >> 1)) * HD + hh * 128 + (lane & 1) * 64;
      asm volatile("global_load_dword %0, %1, off nt" : "+v"(d) : "v"(p) : "memory");
    }
    for (int k = 0; k < GSL; ++k) __builtin_amdgcn_s_sleep(16);
    for (int e = id < nw ? id + 16 * (x < 4 ? G0 : G1) : 999; e < 576; e += nw) {
      const int t = e >> 4, k = e & 7, rb = e & 8, late = t >= 18, blk = x < 4 && !rb ? t >> 1 : t - 18 * late;
      const u16* W = rb ? a.Wv : ((x < 4 ? t & 1 : !late) ? a.Wk : a.Wq);
      const u16* p = rb && late == (x >= 4) ? a.Wo + (size_t)((x & 3) * 8 + k) * (128 * DM) + (blk * 64 + lane) * 64
                                            : W + (size_t)(blk * 32 + k * 4 + (lane >> 4)) * HD + (x & 3) * 1024 + (lane & 15) * 64;
      if (GM >> (rb * 2 / 8 + (x >> 2)) & 1) asm volatile("global_load_dword %0, %1, off nt" : "+v"(d) : "v"(p) : "memory");
    }
    asm volatile("s_waitcnt vmcnt(0)" : "+v"(d) :: "memory");
    if (x >= 4 && sl >= 20 && sl < 30) {
#pragma unroll
      for (int m = 0; m < 2; ++m) {
        const bool xm = (m == 0) == (XPF < APF);
        if (!(xm ? XPF : APF)) continue;
        for (int k = 0; k < 64 && rt_now() - t0 < (xm ? XPF : APF); ++k) __builtin_amdgcn_s_sleep(8);
        for (int e = (id - 16) * 4 + (x & 3); e < (xm ? 288 - XP0 : 243); e += 160)
          asm volatile("global_load_dword %0, %1, off" : "+v"(d) : "v"((xm ? a.x + XP0 * 4096 : a.wqkv) + e * 4096 + lane * 64) : "memory");
      }
      asm volatile("s_waitcnt vmcnt(0)" : "+v"(d) :: "memory");
      RT(13);
    }
    if (sl >= 20) for (int k = 0; k < 64 && rt_now() - t0 < (x < 4 || sl == 31 ? AWT : XPT); ++k) __builtin_amdgcn_s_sleep(8);
    if (AL2 && x < 4) {
      for (int e = id; e < 243; e += 28) asm volatile("global_load_dword %0, %1, off" : "+v"(d) : "v"(a.wqkv + (e * 64 + lane) * 64) : "memory");
      asm volatile("s_waitcnt vmcnt(0)" : "+v"(d) :: "memory");
    }
  }
  if (x < 4 && wave == 1 && sl >= 16 && sl < 24) calibrate(R, go, x + (sl & 4), sl & 3, lane);
  if (CT0 && sl >= 16 && (x < 4 ? wave == 1 && sl < 24 : sl < 18)) {
    for (int k = 0; k < 200 && rt_now() - tst < (x < 4 ? CT0 : CT1); ++k) __builtin_amdgcn_s_sleep(8);
#pragma unroll
    for (int k = 0; k < 4; ++k) code_touch(pc, len, (unsigned)((k * 8 + (x < 4 ? sl - 16 : (sl - 16) * 4 + wave)) * 8192 + lane * 128), (unsigned)(uintptr_t)lds + 153600);
    VMW(0);
  }
  if (x < 4) {
    if (sl < 16) for (int k = 0; k < QDL; ++k) __builtin_amdgcn_s_sleep(32);
    if (sl < 8) role_qkc(a, R, go, lds, x * 8 + sl, tid, wave, lane);
    else if (sl < 16) role_vcu(a, R, go, lds, x * 8 + sl - 8, tid, wave, lane);
    else if (sl < 25) role_mix(a, R, go, x * 9 + sl - 16, wave, lane);
    else role_aw(a, R, go, lds, x * 8 + sl - 25, tid, wave, lane);
  } else {
    const int y = x - 4;
    if (sl < 8) {
      const int h = y * 8 + ((sl + HSH) & 7);
      role_prj(a, R, go, lds, a.Wk + h * 128, a.Wq + h * 128, OFF_KF + h * 8192u, OFF_QF + h * 8192u, FL_PROG + h * 128, tid, wave, lane);
    } else if (sl < 16) role_vuw(a, R, go, lds, y * 8 + sl - 8, tid, wave, lane);
    else if (sl < 20) { role_px(a, R, go, y * 4 + sl - 16, wave, lane); __builtin_amdgcn_s_waitcnt(0x0F70); }
    else {
      if (sl == 31) { role_aw(a, R, go, lds, y * 8 + 7, tid, wave, lane); __builtin_amdgcn_s_waitcnt(0x0F70); }
      if (sl == 29 && !XPF) {
        const rsrc_t X = __builtin_amdgcn_make_buffer_rsrc((void*)a.x, 0, S_LEN * DM * 2, 0x00020000);
#pragma unroll 1
        for (int k = 0; k < 3; ++k) {
          unsigned t[12];
#pragma unroll
          for (int j = 0; j < 12; ++j) t[j] = __builtin_amdgcn_raw_buffer_load_b32(X, (unsigned)(((k * 12 + j) * 16 + y * 4 + wave) * 64 + lane) * 64, 0, 0);
#pragma unroll
          for (int j = 0; j < 12; ++j) asm volatile("" :: "v"(t[j]));
        }
      }
    }
    const int o = sl < 8 ? 45 + y * 8 + sl : (sl < 20 ? 99 : (sl < 29 ? y * 9 + sl - 20 : (sl < 31 ? 36 + (sl - 29) * 4 + y : 44 + y * 99)));
    if (o < 72) { BAR(); role_out(a, R, go, lds, o, tid, wave, lane); }
  }
}

extern "C" __device__ __attribute__((noinline, used, visibility("hidden"))) void zz_code_end() {}
extern "C" void kernel_launch(void* const* d_in, const int* in_sizes, int n_in, void* d_out, int out_size, void* d_ws,
                              size_t ws_size, hipStream_t stream) {
  char* wb = (char*)(((uintptr_t)d_ws + 0x1FFFFF) & ~(uintptr_t)0x1FFFFF);
  Args a;
  a.x = (const u16*)d_in[0]; a.mem0 = (const u16*)d_in[4]; a.Wq = (const u16*)d_in[5]; a.Wk = (const u16*)d_in[6]; a.Wv = (const u16*)d_in[7];
  a.Wo = (const u16*)d_in[8]; a.wqkv = (const u16*)d_in[1]; a.wo = (const u16*)d_in[2]; a.gw = (const u16*)d_in[10]; a.out = (u16*)d_out; a.wb = wb;
  mega<<<256, 256, 0, stream>>>(a);
}
```

```cpp
#define T1D 140
#define DE0 100
#define DELTA0 168
#define DE 78
#define DEC 76
#define AWT 1000
#define G1 10
#define GSL 3
#define CT0 1100
#define CT1 950
#define EG 5
#define APF 600
#define AL2 1
#define XPF 800
#define FXI 6
#define S1 26
#define S2 40
#define DEA 82
#define KA_ 64
#define K2 64
#define KC_ 62
#define MSL 2
#define XNT ""
#define UEF 1
#define OSW 28
#define HLP 1
#define HF0 9
#define HF1 4
#define HF2 0
#define HF3 4
#define XPT 760
#define HSH 0
#define NOCW 0
#define EFIRST
#define CSLP 1
#define RTOL 2
#define QDL 0
#define LTOL 16
#define GRID 48
#define XT 6
#define NOAT
#define AWD 8
#define XPD 8
#define NTM 15
#define FIXL
#define NSC 8
#define WRM 3
#define VED 0
#define NOYC 0
#define G0 36
#define GM 15
#define KFI 6
#define EG1 0
#define XP0 18
#define KEF 3
#include <hip/hip_runtime.h>
#include <stdint.h>
#include <type_traits>

typedef unsigned short u16;
typedef __attribute__((ext_vector_type(2))) __bf16 bf16x2;
typedef __attribute__((ext_vector_type(4))) __bf16 bf16x4;
typedef __attribute__((ext_vector_type(8))) __bf16 bf16x8;
typedef __attribute__((ext_vector_type(16))) __bf16 bf16x16;
typedef __attribute__((ext_vector_type(2))) float f32x2;
typedef __attribute__((ext_vector_type(4))) float f32x4;
typedef __attribute__((ext_vector_type(2))) unsigned u32x2;
typedef __attribute__((ext_vector_type(4))) unsigned u32x4;
typedef __attribute__((ext_vector_type(8))) unsigned u32x8;
typedef __attribute__((ext_vector_type(4))) short s16x4;
typedef __attribute__((address_space(3))) s16x4 lds_s16x4;
typedef __attribute__((address_space(3))) u32x4 lds_u32x4;
typedef __attribute__((address_space(3))) f32x4 lds_f32x4;
typedef __attribute__((address_space(3))) unsigned lds_u32;
typedef __attribute__((address_space(3))) u32x2 lds_u32x2;
typedef __attribute__((address_space(3))) char lds_char;

#define S_LEN 2048
#define DM 576
#define NSEG 64
#define HD 4096

#define SLOT_REAL  131072
#define NEAR_PM    (8192 + 1024)
#define NEAR_AL    (8192 + 5120)
#define NEAR_E     16384
#define OFF_FLAGS  0x100000
#define FL_FLIP    (OFF_FLAGS + 22016)
#define FL_WP      (OFF_FLAGS + 16384)
#define FL_AN      (OFF_FLAGS + 18432)
#define FL_MRDY    (OFF_FLAGS + 21504)
#define FL_MPOI    (OFF_FLAGS + 21760)
#define FL_PROG    (OFF_FLAGS + 53248)
#define FL_UPROG   (OFF_FLAGS + 0)
#define UH 128
#define UW 4
#define FL_KMPROG  (OFF_FLAGS + 49152)
#define FL_YPROG   (OFF_FLAGS + 20480)
#define FL_PXPROG  (OFF_FLAGS + 24576)
#define FL_APROG   (OFF_FLAGS + 40960)
#define FL_PXLAST  (OFF_FLAGS + 32768)
#define FL_ACK     (OFF_FLAGS + 30720)
#define FLAG_BYTES 0x10000
#define FL_GO      (OFF_FLAGS + FLAG_BYTES)
#define PXW 6
#define FX(a, cls, k) ((unsigned)(a) ^ (((unsigned)(cls) ^ (FXI >> (k))) & 1u) * 8192u)
#define NACK       945
#define REAL_CAL   0x140000
#define OFF_YR     0x200000
#define YSLOT_REAL 0x100000
#define CPY1       16384
#define CPY2       0x80000
#define OFF_KF   (4u << 20)
#define OFF_QF   (OFF_KF + (16u << 20))
#define OFF_KMF  (OFF_QF + (16u << 20))
#define OFF_PXF  (OFF_KMF + (8u << 20))
#define OFF_ZF   (OFF_PXF + (6u << 20))
#define ZS       (36u * 40960u)
#define OFF_OA   (OFF_ZF + (91u << 20))
#define OFF_GATE (OFF_OA + (3u << 20))
#define OFF_END  (OFF_GATE + (1u << 20))

#define RMASK 63
#define SPINCAP (1u << 19)
#define SC1 16

#define BAR() do { asm volatile("s_waitcnt lgkmcnt(0)" ::: "memory"); __builtin_amdgcn_s_barrier(); asm volatile("" ::: "memory"); } while (0)
#define VMW(n) asm volatile("s_waitcnt vmcnt(" #n ")" ::: "memory")
#define CBAR() asm volatile("" ::: "memory")
#define PADA
#define PADN "s_nop 0\n\t"
#define PADM
#define PADZ
#define PAD()
#define PADS(id)
#define PADE()
#define WALK(id)

#define TKD
#define TK(i)
#define TKS
#define NOSTEP 0
#define OFF_STAMP (200u << 20)
#define TS(v)
#define RT(i)
#define SV(i, v)
#define ACC(k, d)
#define STEPRT(j, f)
#define EVASM
#define EV(v)
#define EVK(v)
#define EVPUT(j, v)
#define EVPUTI(j, v)
#define EVDECL
#define EVDUMP()
#define CKDECL
#define CKS()
#define CKDUMP()
typedef unsigned long long u64;
struct Args {
  const u16* x; const u16* mem0; const u16* Wq; const u16* Wk; const u16* Wv; const u16* Wo;
  const u16* wqkv; const u16* wo; const u16* gw; u16* out; char* wb;
};

__device__ __forceinline__ float bf2f(u16 v) { return __uint_as_float(((unsigned)v) << 16); }
__device__ __forceinline__ unsigned pk2(float a, float b) {
  f32x2 v = {a, b};
  bf16x2 r = __builtin_convertvector(v, bf16x2);
  return __builtin_bit_cast(unsigned, r);
}
__device__ __forceinline__ int nearoff(int o) { return ((o >> 13) << 15) + (o & 8191); }
__device__ __forceinline__ unsigned own_cpy(int cls, int x) { return ((cls >> (2 * x)) & 2) ? CPY1 : 0u; }
__device__ __forceinline__ unsigned ywin(int cls, int m) {
  const int x = m / 9, row = (x >> 1) * 9 + m - 9 * x;
  const int se = (cls >> (4 * (x >> 1) + 1)) & 1, so = (cls >> (4 * (x >> 1) + 3)) & 1, sd = se == so ? x & 1 : ((x & 1) ? so : se);
  return (unsigned)(OFF_YR + row * 32768 + ((cls >> 8) & 1) * 8192 + ((sd ^ (cls >> 1) ^ (cls >> 9) ^ (row >> 4)) & 1) * 16384);
}
__device__ __forceinline__ u32x2 trd(lds_char* p) {
  s16x4 a = __builtin_amdgcn_ds_read_tr16_b64_v4i16((lds_s16x4*)p);
  return __builtin_bit_cast(u32x2, a);
}
__device__ __forceinline__ f32x4 mfma(u32x4 a, u32x4 b, f32x4 c) {
  return __builtin_amdgcn_mfma_f32_16x16x32_bf16(__builtin_bit_cast(bf16x8, a), __builtin_bit_cast(bf16x8, b), c, 0, 0, 0);
}
__device__ __forceinline__ f32x4 mfv0(u32x4 a, u32x4 b) {
  f32x4 d; asm volatile("s_nop 1\n\tv_mfma_f32_16x16x32_bf16 %0, %1, %2, 0" : "=&v"(d) : "v"(a), "v"(b)); return d;
}
__device__ __forceinline__ f32x4 mfvc(u32x4 a, u32x4 b, f32x4 c) {
  f32x4 d; asm volatile("s_nop 1\n\tv_mfma_f32_16x16x32_bf16 %0, %1, %2, %3" : "=&v"(d) : "v"(a), "v"(b), "v"(c)); return d;
}
__device__ __forceinline__ void mfva(f32x4& d, u32x4 a, u32x4 b) {
  asm volatile("s_nop 1\n\tv_mfma_f32_16x16x32_bf16 %0, %1, %2, %0" : "+v"(d) : "v"(a), "v"(b));
}
__device__ __forceinline__ void mf_wait(f32x4& d) { asm volatile("s_nop 7" : "+v"(d)); }
__device__ __forceinline__ void mf_wait2(f32x4& d, f32x4& e) { asm volatile("s_nop 7" : "+v"(d), "+v"(e)); }
__device__ __forceinline__ f32x4 smf(u32x4 a, u32x8 b, f32x4 c, int idx) {
  return __builtin_amdgcn_smfmac_f32_16x16x64_bf16(__builtin_bit_cast(bf16x8, a), __builtin_bit_cast(bf16x16, b), c, idx, 0, 0);
}
__device__ __forceinline__ float xmax(float x) {
  auto s = __builtin_amdgcn_permlane16_swap(__float_as_uint(x), __float_as_uint(x), false, false);
  x = fmaxf(__uint_as_float(s[0]), __uint_as_float(s[1]));
  auto t = __builtin_amdgcn_permlane32_swap(__float_as_uint(x), __float_as_uint(x), false, false);
  return fmaxf(__uint_as_float(t[0]), __uint_as_float(t[1]));
}
__device__ __forceinline__ float xsum(float x) {
  auto s = __builtin_amdgcn_permlane16_swap(__float_as_uint(x), __float_as_uint(x), false, false);
  x = __uint_as_float(s[0]) + __uint_as_float(s[1]);
  auto t = __builtin_amdgcn_permlane32_swap(__float_as_uint(x), __float_as_uint(x), false, false);
  return __uint_as_float(t[0]) + __uint_as_float(t[1]);
}
__device__ __forceinline__ f32x4 halfadd(f32x4 a, f32x4 b) {
  f32x4 o;
#pragma unroll
  for (int r = 0; r < 4; ++r) {
    auto s = __builtin_amdgcn_permlane32_swap(__float_as_uint(a[r]), __float_as_uint(b[r]), false, false);
    o[r] = __uint_as_float(s[0]) + __uint_as_float(s[1]);
  }
  return o;
}
template <class T> __device__ __forceinline__ void pin_a(T& v) { asm volatile("" : "+a"(v)); }
typedef __amdgpu_buffer_rsrc_t rsrc_t;
__device__ __forceinline__ u32x4 ld128(rsrc_t R, unsigned vo, unsigned so) { return __builtin_amdgcn_raw_buffer_load_b128(R, vo, so, SC1); }
__device__ __forceinline__ unsigned ld32(rsrc_t R, unsigned vo, unsigned so) { return __builtin_amdgcn_raw_buffer_load_b32(R, vo, so, SC1); }
__device__ __forceinline__ void st128(u32x4 v, rsrc_t R, unsigned vo, unsigned so) { __builtin_amdgcn_raw_buffer_store_b128(v, R, vo, so, SC1); }
__device__ __forceinline__ void st32(unsigned v, rsrc_t R, unsigned vo, unsigned so) { __builtin_amdgcn_raw_buffer_store_b32(v, R, vo, so, SC1); }
#define OOB 0x7ff00000
__device__ __forceinline__ unsigned lda(rsrc_t R, unsigned vo) { return ld32(R, vo, 0); }

struct Go { unsigned vo, v0; };
__device__ __forceinline__ Go go_look(rsrc_t R, int b) {
  Go g; g.vo = FL_GO + (b & 15) * 128; g.v0 = ld32(R, g.vo, 0); return g;
}
__device__ __forceinline__ void go_ack(rsrc_t R, const Go& g, int lane) {
  __builtin_amdgcn_raw_ptr_buffer_atomic_add_i32(1, R, lane == 0 ? g.vo - FL_GO + FL_ACK : OOB, 0, SC1);
}
__device__ __forceinline__ bool go_wait(rsrc_t R, const Go& g, int lane) {
  for (unsigned spins = 0; spins < SPINCAP; ++spins) {
    CBAR();
    if (__all(ld32(R, g.vo, 0) != g.v0)) {
      go_ack(R, g, lane);
      return true;
    }
    __builtin_amdgcn_s_sleep(6);
  }
  return false;
}
__device__ __forceinline__ void go_lead(rsrc_t R, int tid, int wave, int lane) {
#pragma unroll
  for (int k = 0; k < FLAG_BYTES / 4096; ++k) st128((u32x4){~0u, ~0u, ~0u, ~0u}, R, OFF_FLAGS + k * 4096 + tid * 16, 0);
  VMW(0);
  BAR();
  if (wave != 0) return;
  for (unsigned k = 0; k < SPINCAP; ++k) {
    st32(k, R, lane < 16 ? FL_GO + lane * 128 : OOB, 0);
    int n = lane < 16 ? (int)ld32(R, FL_ACK + lane * 128, 0) + 1 : 0;
#pragma unroll
    for (int d = 1; d < 16; d <<= 1) n += __shfl_xor(n, d);
    if (__builtin_amdgcn_readfirstlane(n) >= NACK) break;
    __builtin_amdgcn_s_sleep(4);
  }
}
__device__ __forceinline__ bool wait_flag(rsrc_t R, unsigned vo, int need) {
  for (unsigned spins = 0; spins < SPINCAP; ++spins) {
    CBAR();
    int v = (int)ld32(R, vo, 0);
    if (__all(v >= need)) return true;
    __builtin_amdgcn_s_sleep(8);
  }
  return false;
}

template <int SLP, int AT = 0>
__device__ __forceinline__ bool wait_flag_c(rsrc_t R, unsigned vo, int need, int& cache) {
  if (__all(cache >= need)) return true;
  for (unsigned spins = 0; spins < SPINCAP; ++spins) {
    CBAR();
    cache = (int)(AT ? lda(R, vo) : ld32(R, vo, 0));
    if (__all(cache >= need)) return true;
    __builtin_amdgcn_s_sleep(SLP);
  }
  return false;
}
__device__ __forceinline__ unsigned umax3(unsigned a, unsigned b, unsigned c) { return max(max(a, b), c); }

template <int NTB> __device__ __forceinline__ u32x4 gld(const u16* p) {
  if (NTB) return __builtin_nontemporal_load((const u32x4*)p);
  return *(const u32x4*)p;
}
#define XSTR2(x) #x
#define XSTR(x) XSTR2(x)
#define LSLP 3
#define ESLP 0
#define MSLP 0
#define PXS 6
#define TSLP 2
#define XSLP 2
#define KSLP 10
#define OSLP 24
#define OSL2 4
#define UE 1
#define YL
#define CPF 3
#define PARK 58
#define OFAST 60
#define CPS 8
#define CSL 2
#define TSL 6
#define UGO 5
#define UPO 1
#define CALN 4
#define QDEP 4
#define PDEP 3
#define WDEP 3
#define VSLP 2
#define QLN 24
#define VLN 8
#define PLN 32
#define LMASK(n) ((n) >= 64 ? ~0ull : ((1ull << ((n) & 63)) - 1))
#define UP3 0
#define ORDT 0x73652410u
#define USLP 8
#define UT 0
#define UTD 1
#define EMF 0
#define EMV 0
#define MWPOS 1
#define DSLP "s_sleep 1\n\t"
typedef __attribute__((ext_vector_type(4))) int i32x4;
__device__ __forceinline__ u64 ballot(bool c) { return __builtin_amdgcn_ballot_w64(c); }
#define UNLIKELY(c) __builtin_expect(!!(c), 0)
#define DELTA 164
#define D1 98
#define DVQ 4
#define LEV0 1
#define LEV3 1
#define LEVMAX 7
#define CL0 8
#define HEAT 3
#define COOL0 4
#define PSLP 8
#define VK0 64
#define AWE 0
#define ASLP 0
#define LEVF 99
#define RX 0
#define VSL 6
#define D0 14
#define WLAST 41
#define UDL 8
#define MGAP 0
#define SMIN 8
#define K1 4
#define PAD0 4
#define PADD 4
#define PADMAX 16
#define OAUX 0
__device__ __forceinline__ unsigned rt_now() {
  u64 t; asm volatile("s_memrealtime %0\n\ts_waitcnt lgkmcnt(0)" : "=s"(t) :: "memory"); return (unsigned)t;
}
#define S0 3
#define PACE0 ((DE0 << 8) | DELTA0)
#define PACEA ((DEA << 8) | (DEA + KA_))
#define PACEB ((DE << 8) | (DE + K2))
#define PACEC ((DEC << 8) | (DEC + KC_))
__device__ __forceinline__ void next_time(unsigned& t, int s) {
  t += (s < S0 ? PACE0 : (s < S1 ? PACEA : (s < S2 ? PACEB : PACEC))) & 255u;
  t = __builtin_amdgcn_readfirstlane(t);
  asm volatile("" : "+s"(t));
}
#define SNOP 1
#define SPIN(T, a, b, c) a ":\n\ts_memrealtime vcc\n\ts_waitcnt lgkmcnt(0)\n\ts_sub_u32 %[r], %[" #T "], vcc_lo\n\ts_sub_u32 %[k], %[r], 4\n\t"   \
               "s_cmp_lt_u32 %[k], 2000\n\ts_cbranch_scc1 " a "b\n\t"                                                                       \
               "s_sub_u32 %[k], %[r], 1\n\ts_cmp_lt_u32 %[k], 3\n\ts_cbranch_scc0 " c "f\n\t"                                               \
               b ":\n\ts_nop " XSTR(SNOP) "\n\ts_sub_u32 %[k], %[k], 1\n\ts_cbranch_scc0 " b "b\n\t" c ":\n\t"
__device__ __forceinline__ int spin_until(unsigned T) {
  int r, k;
  asm volatile(SPIN(T, "1", "2", "3") : [r] "=&s"(r), [k] "=&s"(k) : [T] "s"(T) : "memory", "vcc", "scc");
  return r;
}
#define KA "v_mov_b32 v240, v240\n\ts_nop 2\n\t"
#define KAN KA KA
#define EVA(n)
#define EVC(k)
#define BUD(l) "s_sub_u32 %[bud], %[bud], 1\n\ts_cbranch_scc0 " l "\n\ts_mov_b32 %[bud], 0\n\t"
__device__ __forceinline__ i32x4 mkdesc(char* wb) {
  const u64 p = (u64)wb;
  return (i32x4){(int)(unsigned)p, (int)(unsigned)(p >> 32), (int)OFF_END, 0x00020000};
}
template <class T> __device__ __forceinline__ void pin_v(T& v) { asm volatile("" : "+v"(v)); }
#define SB() __builtin_amdgcn_sched_barrier(0)
__device__ __forceinline__ unsigned pkv(float a, float b) {
  unsigned d; asm volatile("v_cvt_pk_bf16_f32 %0, %1, %2" : "=v"(d) : "v"(a), "v"(b)); return d;
}
__device__ __forceinline__ f32x4 mfB0(u32x4 a, u32x4 b) {
  f32x4 d; asm volatile("v_mfma_f32_16x16x32_bf16 %0, %1, %2, 0" : "=&v"(d) : "v"(a), "a"(b)); return d;
}
__device__ __forceinline__ void mfB(f32x4& d, u32x4 a, u32x4 b) {
  asm volatile("v_mfma_f32_16x16x32_bf16 %0, %1, %2, %0" : "+v"(d) : "v"(a), "a"(b));
}

template <int NTB>
__device__ __forceinline__ void qk_gload(u32x4 (&st)[8], const Args& a, int h, int c, int tid) {
#pragma unroll
  for (int k = 0; k < 8; ++k) {
    int u = tid + 256 * k; int row = u >> 5, cu = u & 31;
    const u16* W = (cu < 16) ? a.Wq : a.Wk;
    st[k] = gld<NTB>(W + (((size_t)(64 * c + row) * HD + h * 128 + (cu & 15) * 8) & ((NOCW & 1) ? 2047 : ~0ull)));
  }
}
__device__ __forceinline__ void qk_swrite(const u32x4 (&st)[8], lds_char* buf, int tid) {
#pragma unroll
  for (int k = 0; k < 8; ++k) { int u = tid + 256 * k; int row = u >> 5, cu = u & 31; *(lds_u32x4*)(buf + row * 544 + cu * 16) = st[k]; }
}
#define STG_BYTES 38912
#define X0_OFF (2 * STG_BYTES)
#define X1_OFF (X0_OFF + 37888)

__device__ __forceinline__ int cls_look(rsrc_t R, int lane) { return (int)ld32(R, FL_FLIP + (lane & 31) * 4, 0); }
__device__ __forceinline__ int rowsum(int v) {
  v += __builtin_amdgcn_update_dpp(v, v, 0x128, 0xf, 0xf, false);
  v += __builtin_amdgcn_update_dpp(v, v, 0x124, 0xf, 0xf, false);
  v += __builtin_amdgcn_update_dpp(v, v, 0x122, 0xf, 0xf, false);
  return v + __builtin_amdgcn_update_dpp(v, v, 0x121, 0xf, 0xf, false);
}
__device__ __forceinline__ int get_cls(rsrc_t R, int lane, int v = -1) {
  for (unsigned spins = 0; spins < SPINCAP; ++spins) {
    if (__all(v >= 0)) {
      const int n = rowsum((lane & 1) ? -v : v) > 0;
      const int d = rowsum((lane & 1) != n ? 0 : (((lane >> 1) ^ (lane >> 2)) & 1 ? -v : v)) > 0;
      const int c = n | (d << 1);
      return (__builtin_amdgcn_readlane(c, 0) * 0x55 ^ 0x88) | (__builtin_amdgcn_readlane(c, 16) << 8);
    }
    if (spins) __builtin_amdgcn_s_sleep(CPS);
    CBAR();
    v = cls_look(R, lane);
  }
  return -1;
}
__device__ __forceinline__ void calibrate(rsrc_t R, const Go& go, int r, int k, int lane) {
  unsigned best = 0xFFFFFFu;
  for (int it = 0; it < CALN; ++it) {
    unsigned long long t0, t1;
    asm volatile("s_waitcnt vmcnt(0)\n\ts_memtime %0\n\ts_waitcnt lgkmcnt(0)" : "=s"(t0) :: "memory");
    st32((unsigned)it, R, (lane == 0) ? (unsigned)((r >> 2) * OFF_YR + REAL_CAL + (r & 3) * 256 + k * 8192 + (it & 1) * 4096) : OOB, 0);
    asm volatile("s_waitcnt vmcnt(0)\n\ts_memtime %0\n\ts_waitcnt lgkmcnt(0)" : "=s"(t1) :: "memory");
    best = min(best, (unsigned)(t1 - t0));
  }
  if (go_wait(R, go, lane)) st32(best, R, lane == 0 ? (unsigned)(FL_FLIP + (r * 4 + k) * 4) : OOB, 0);
}
__device__ __forceinline__ unsigned get_t1(rsrc_t R, unsigned vo, unsigned& bud) {
  unsigned v = 0;
#pragma nounroll
  for (; bud != 0; --bud) {
    CBAR();
    v = __builtin_amdgcn_readfirstlane(ld32(R, vo, 0));
    if (v != ~0u) break;
    __builtin_amdgcn_s_sleep(2);
  }
  return v;
}

#define F8_OFF 18432
#define QSTG 34816
#define ISOT()
#define ISOQ(n)
__device__ __forceinline__ float vmax(float a, float b) { float d; asm("v_max_f32 %0, %1, %2" : "=v"(d) : "v"(a), "v"(b)); return d; }
__device__ __forceinline__ float vmax3(float a, float b, float c) { float d; asm("v_max3_f32 %0, %1, %2, %3" : "=v"(d) : "v"(a), "v"(b), "v"(c)); return d; }
__device__ __forceinline__ float max32(float x) {
  auto t = __builtin_amdgcn_permlane32_swap(__float_as_uint(x), __float_as_uint(x), false, false);
  return vmax(__uint_as_float(t[0]), __uint_as_float(t[1]));
}
__device__ __forceinline__ float max16(float x) {
  auto t = __builtin_amdgcn_permlane16_swap(__float_as_uint(x), __float_as_uint(x), false, false);
  return vmax(__uint_as_float(t[0]), __uint_as_float(t[1]));
}
__device__ __forceinline__ float sum32(float x) {
  auto t = __builtin_amdgcn_permlane32_swap(__float_as_uint(x), __float_as_uint(x), false, false);
  return __uint_as_float(t[0]) + __uint_as_float(t[1]);
}
template <int r> __device__ __forceinline__ void swp32(f32x4& a, f32x4& b) {
  auto t = __builtin_amdgcn_permlane32_swap(__float_as_uint(a[r]), __float_as_uint(b[r]), false, false);
  a[r] = __uint_as_float(t[0]); b[r] = __uint_as_float(t[1]);
}
__device__ __forceinline__ float vadd(float a, float b) { float d; asm("v_add_f32 %0, %1, %2" : "=v"(d) : "v"(a), "v"(b)); return d; }
__device__ __forceinline__ float max8(float x) {
  float d; asm("s_nop 1\n\tv_max_f32_dpp %0, %1, %1 row_ror:8 row_mask:0xf bank_mask:0xf" : "=&v"(d) : "v"(x)); return d;
}
__device__ __forceinline__ float sum8(float x) {
  float d; asm("s_nop 1\n\tv_add_f32_dpp %0, %1, %1 row_ror:8 row_mask:0xf bank_mask:0xf" : "=&v"(d) : "v"(x)); return d;
}
__device__ __forceinline__ float sum16(float x) {
  auto t = __builtin_amdgcn_permlane16_swap(__float_as_uint(x), __float_as_uint(x), false, false);
  return __uint_as_float(t[0]) + __uint_as_float(t[1]);
}
__device__ __forceinline__ void st64(u32x2 v, rsrc_t R, unsigned vo, unsigned so) { __builtin_amdgcn_raw_buffer_store_b64(v, R, vo, so, SC1); }
template <int N>
__device__ __forceinline__ void mem_issue2(i32x4 D, unsigned m0, unsigned m1, unsigned m2, unsigned so, unsigned l0, unsigned l1, unsigned l2, u64& ew) {
  asm volatile("s_mov_b32 m0, %[l0]\n\ts_barrier\n\t" EVASM
               "buffer_load_dwordx4 %[m0], %[D], %[so] offen sc1 lds\n\t"
               "s_mov_b32 m0, %[l1]\n\ts_nop 0\n\t"
               "buffer_load_dwordx4 %[m1], %[D], %[so] offen sc1 lds\n\t"
               ".if %[n] == 3\n\ts_mov_b32 m0, %[l2]\n\ts_nop 0\n\t"
               "buffer_load_dwordx4 %[m2], %[D], %[so] offen sc1 lds\n\t.endif"
               : [ew] "=&s"(ew)
               : [m0] "v"(m0), [m1] "v"(m1), [m2] "v"(m2), [D] "s"(D), [so] "s"(so), [l0] "s"(l0), [l1] "s"(l1), [l2] "s"(l2), [n] "n"(N)
               : "memory");
}
#define TRYB ".if %[id] >= 0\n\ts_cbranch_vccnz .Ltry%[id]\n.Lend%[id]:\n\t.endif\n\t"
#define TRYC "vcc", "scc", "v236", "v237", "v238", "v239", "v240"
template <int ID>
__device__ __forceinline__ void try_top(unsigned& nt, unsigned& ms, unsigned& tm, unsigned& rcnt, unsigned ia) {
  asm volatile(".Ltry%[id]:\n\ts_waitcnt lgkmcnt(0)\n\ts_cmp_ge_i32 %[nt], " XSTR(NSC) "\n\ts_cbranch_scc1 1f\n"
               "2:\n\ts_nop 4\n\ts_xor_b32 %[ms], %[ms], %[fl]\n\ts_add_u32 %[tm], %[tm], " XSTR(GRID) "+" XSTR(XT) "\n\ts_add_u32 %[nt], %[nt], 1\n\t.subsection 1\n"
               "1:\n\tv_mov_b32 v240, 0\n\t"
               ".irp o, 0, 1024, 2048, 3072, 4096, 5120, 6144, 7168, " XSTR(F8_OFF) "\n\tds_read_b128 v[236:239], %[ia] offset:\\o\n\ts_waitcnt lgkmcnt(0)\n\t"
               "v_max3_u32 v240, v240, v236, v237\n\tv_max3_u32 v240, v240, v238, v239\n\t.endr\n\t"
               "v_cmp_eq_u32 vcc, -1, v240\n\ts_cbranch_vccz .Lend%[id]\n\ts_add_u32 %[rc], %[rc], 1\n\ts_cmp_lt_u32 %[rc], %[cap]\n\t"
               "s_cbranch_scc1 2b\n\ts_branch .Lend%[id]\n\t.subsection 0"
               : [nt] "+s"(nt), [ms] "+s"(ms), [tm] "+s"(tm), [rc] "+s"(rcnt)
               : [fl] "n"(CPY2 + CPY1), [id] "n"(ID), [ia] "v"(ia), [cap] "n"(SPINCAP / 2) : "memory", TRYC);
}
__device__ __forceinline__ void try_end(unsigned ia, unsigned nt, unsigned ms, unsigned tm, unsigned rcnt,
                                        unsigned m0, unsigned m1, unsigned m2, unsigned l0, unsigned l1, unsigned l2, i32x4 D) {
  asm volatile("" :: "s"(rcnt), "s"(nt), "s"(ms), "s"(tm), "v"(ia), "v"(m0), "v"(m1), "v"(m2), "s"(l0), "s"(l1), "s"(l2), "s"(D) : "memory");
}
__device__ __forceinline__ int spin_late(unsigned& t, unsigned d) {
  int r, k; unsigned T;
  asm volatile("s_add_u32 %[T], %[t], %[d]\n\t" SPIN(T, "1", "2", "3") : [r] "=&s"(r), [k] "=&s"(k), [T] "=&s"(T), [t] "+s"(t) : [d] "n"(d) : "memory", "vcc", "scc");
  return r;
}
__device__ __forceinline__ void move_on(unsigned& t, int r) {
  asm volatile("1:\n\ts_cmp_lt_i32 %1, -" XSTR(LTOL) "\n\ts_cbranch_scc0 2f\n\ts_add_u32 %0, %0, " XSTR(GRID) "+" XSTR(XT) "\n\ts_add_u32 %1, %1, " XSTR(GRID) "+" XSTR(XT) "\n\ts_branch 1b\n2:"
               : "+s"(t), "+s"(r) :: "scc");
}
__device__ __forceinline__ void gate_wait(rsrc_t R, unsigned vo, int need, int& cache, unsigned& bud) {
#pragma nounroll
  for (; bud != 0; --bud) {
    if (__all(cache >= need)) break;
    CBAR();
    cache = (int)ld32(R, vo, 0);
    __builtin_amdgcn_s_sleep(4);
  }
}
__device__ __forceinline__ unsigned um3v(unsigned a, unsigned b, unsigned c) {
  unsigned d; asm volatile("v_max3_u32 %0, %1, %2, %3" : "=v"(d) : "v"(a), "v"(b), "v"(c)); return d;
}
template <int OFF>
__device__ __forceinline__ void k_put(f32x4 k, unsigned ad) {
  unsigned t0, t1;
  asm volatile("v_cvt_pk_bf16_f32 %0, %2, %3\n\tv_cvt_pk_bf16_f32 %1, %4, %5\n\tds_write2_b32 %6, %0, %1 offset0:%7 offset1:%8"
               : "=&v"(t0), "=&v"(t1) : "v"(k[0]), "v"(k[1]), "v"(k[2]), "v"(k[3]), "v"(ad), "n"(OFF), "n"(OFF + 2) : "memory");
}
template <int OFF>
__device__ __forceinline__ void q_put(f32x4 k, unsigned ad) {
  unsigned t0, t1;
  asm volatile("v_cvt_pk_bf16_f32 %0, %2, %3\n\tv_cvt_pk_bf16_f32 %1, %4, %5\n\tds_write2_b32 %6, %0, %1 offset0:%7 offset1:%8"
               : "=&v"(t0), "=&v"(t1) : "v"(k[0]), "v"(k[1]), "v"(k[2]), "v"(k[3]), "v"(ad), "n"(OFF), "n"(OFF + 1) : "memory");
}
__device__ __forceinline__ f32x4 mf0(u32x4 a, u32x4 b) {
  f32x4 d; asm volatile("v_mfma_f32_16x16x32_bf16 %0, %1, %2, 0" : "=&v"(d) : "v"(a), "v"(b)); return d;
}
__device__ __forceinline__ void mfa(f32x4& d, u32x4 a, u32x4 b) {
  asm volatile("v_mfma_f32_16x16x32_bf16 %0, %1, %2, %0" : "+v"(d) : "v"(a), "v"(b));
}
__device__ __forceinline__ f32x4 mfA0(u32x4 a, u32x4 b) {
  f32x4 d; asm volatile("v_mfma_f32_16x16x32_bf16 %0, %1, %2, 0" : "=&v"(d) : "a"(a), "v"(b)); return d;
}
__device__ __forceinline__ void mfA(f32x4& d, u32x4 a, u32x4 b) {
  asm volatile("v_mfma_f32_16x16x32_bf16 %0, %1, %2, %0" : "+v"(d) : "a"(a), "v"(b));
}
#define DPPLO " row_ror:8 row_mask:0xf bank_mask:0x3\n\t"
#define DPPHI " row_ror:8 row_mask:0xf bank_mask:0xc\n\t"
#define DPPAL " row_ror:8 row_mask:0xf bank_mask:0xf\n\t"
#define SCK "0x3e0293ee"
#define SCN "0xbe0293ee"
__device__ __forceinline__ void px_mask(f32x4& T0, f32x4& T1, int qt, int lane) {
  const float ninf = -__builtin_inff();
#pragma unroll
  for (int r = 0; r < 4; ++r) {
    const bool diag = (4 * (lane >> 4) + r) <= (lane & 15);
    T0[r] = (qt == 1 || diag) ? T0[r] : ninf;
    T1[r] = (qt == 1 && diag) ? T1[r] : ninf;
  }
}
__device__ __forceinline__ void px_seg(f32x4& T0, f32x4& T1, float& mx, float& sx) {
  const float sc = 0.08838834764831845f * 1.4426950408889634f;
  mx = xmax(fmaxf(fmaxf(fmaxf(T0[0], T0[1]), fmaxf(T0[2], T0[3])), fmaxf(fmaxf(T1[0], T1[1]), fmaxf(T1[2], T1[3]))));
  const float ms = -mx * sc;
  sx = 0.f;
#pragma unroll
  for (int r = 0; r < 4; ++r) {
    T0[r] = __builtin_amdgcn_exp2f(fmaf(T0[r], sc, ms)); T1[r] = __builtin_amdgcn_exp2f(fmaf(T1[r], sc, ms));
    sx += T0[r] + T1[r];
  }
}
__device__ __forceinline__ void px_fin(f32x4 G, f32x4 T0, f32x4 T1, float mx, float sx, u32x4& px, unsigned& pw, int lane) {
  const float sc = 0.08838834764831845f * 1.4426950408889634f;
  float g0 = (lane & 16) ? G[2] : G[0], g1 = (lane & 16) ? G[3] : G[1];
  const float ms = -xmax(fmaxf(fmaxf(g0, g1), mx)) * sc;
  g0 = __builtin_amdgcn_exp2f(fmaf(g0, sc, ms)); g1 = __builtin_amdgcn_exp2f(fmaf(g1, sc, ms));
  const float f = __builtin_amdgcn_exp2f(fmaf(mx, sc, ms));
  asm volatile("" : "+v"(g0), "+v"(g1)); PAD();
  const float inv = __builtin_amdgcn_rcpf(xsum(fmaf(f, sx, g0 + g1))), fi = f * inv;
  px = (u32x4){pk2(T0[0] * fi, T0[1] * fi), pk2(T0[2] * fi, T0[3] * fi), pk2(T1[0] * fi, T1[1] * fi), pk2(T1[2] * fi, T1[3] * fi)};
  pw = pk2(g0 * inv, g1 * inv);
}
#define WPOS 4
#define QPOS 1
#define LPOS 1
#define QIM_OFF 8192
#define KIM_OFF 10240
#define FLG_OFF 17408
#define F8W 3
#define KX_OFF 36864
typedef __attribute__((ext_vector_type(16))) float f32x16;
typedef __attribute__((ext_vector_type(16))) unsigned u32x16;
__device__ __forceinline__ void ds_flag(unsigned ad, unsigned v) { asm volatile("ds_write_b32 %0, %1" :: "v"(ad), "v"(v) : "memory"); }
#define SMX(d, j, w) "v_smfmac_f32_16x16x64_bf16 v[" d "], %[a" #j "], %[" #w "], %[ix]\n\t"
#define CVX(d, x, y) "v_cvt_pk_bf16_f32 v" #d ", v" #x ", v" #y "\n\t"
#define PKA(d, b) "v_pk_add_f32 v[" #d ":" #d "+1], v[" #b ":" #b "+1], v[" #b "+2:" #b "+3]\n\t"
#define IMW(a, o) "ds_write_b32 " a ", v232 offset:" o "\n\t"
template <int T, int AG>
__device__ __forceinline__ void smo(f32x4& c, u32x4 a, u32x8 b, int ix) {
  if (T == 0 && AG) asm volatile(SMX("140:143", 0, w) : "+{v[140:143]}"(c) : [a0] "v"(a), [w] "a"(b), [ix] "v"(ix));
  if (T == 0 && !AG) asm volatile(SMX("140:143", 0, w) : "+{v[140:143]}"(c) : [a0] "v"(a), [w] "v"(b), [ix] "v"(ix));
  if (T == 1 && AG) asm volatile(SMX("144:147", 0, w) : "+{v[144:147]}"(c) : [a0] "v"(a), [w] "a"(b), [ix] "v"(ix));
  if (T == 1 && !AG) asm volatile(SMX("144:147", 0, w) : "+{v[144:147]}"(c) : [a0] "v"(a), [w] "v"(b), [ix] "v"(ix));
}
__device__ __forceinline__ void v_zero(f32x4& v0, f32x4& v1) {
  asm volatile("v_mov_b64 v[140:141], 0\n\tv_mov_b64 v[142:143], 0\n\tv_mov_b64 v[144:145], 0\n\tv_mov_b64 v[146:147], 0\n\ts_nop 1"
               : "={v[140:143]}"(v0), "={v[144:147]}"(v1));
}
__device__ __forceinline__ void k_zero(f32x4& k0, f32x4& k1, f32x4& k2) {
  asm volatile(".rept 6\n\tv_mov_b64 v[148+2*\\+:149+2*\\+], 0\n\t.endr" : "={v[148:151]}"(k0), "={v[152:155]}"(k1), "={v[156:159]}"(k2));
}
#define TZERO ".rept 6\n\tv_mov_b64 v[200+2*\\+:201+2*\\+], 0\n\t.endr\n\t"
__device__ __forceinline__ void t_zero(f32x4& k0, f32x16& T) {
  asm volatile("v_mov_b64 v[148:149], 0\n\tv_mov_b64 v[150:151], 0\n\t" TZERO : "={v[148:151]}"(k0), "={v[200:215]}"(T));
}
#define AD0(b) "v_add_f32 v230, v[" #b "], v[" #b "+2]\n\t"
#define AD1(b) "v_add_f32 v231, v[" #b "+1], v[" #b "+3]\n\t"
#define CVP CVX(232, 230, 231)
#define Q1(j) SMX("144:147", j, w##j)
template <int ID>
__device__ __forceinline__ void q_mid(const u32x4 (&af)[9], const u32x8 (&w)[9], int ix, unsigned qw, f32x4 q0, f32x4& q1) {
  asm volatile(Q1(3) Q1(4) AD0(140) Q1(5) AD1(140) "v_cmp_u_f32 vcc, v230, v231\n\t" Q1(6) CVP Q1(7) TRYB IMW("%[qw]", "0") Q1(8)
               : "+{v[144:147]}"(q1)
               : "{v[140:143]}"(q0), [a3] "v"(af[3]), [a4] "v"(af[4]), [a5] "v"(af[5]), [a6] "v"(af[6]), [a7] "v"(af[7]), [a8] "v"(af[8]),
                 [w3] "a"(w[3]), [w4] "a"(w[4]), [w5] "a"(w[5]), [w6] "a"(w[6]), [w7] "a"(w[7]), [w8] "v"(w[8]), [ix] "v"(ix), [qw] "v"(qw), [id] "n"(ID)
               : "memory", "v230", "v231", "v232", TRYC);
}
__device__ __forceinline__ void k_last(unsigned kw, f32x4 q1, unsigned fw, unsigned fv) {
  asm volatile("s_nop 7\n\t" PKA(230, 144) CVP IMW("%[kw]", "1024") "ds_write_b32 %[fw], %[fv]"
               :: "{v[144:147]}"(q1), [kw] "v"(kw), [fw] "v"(fw), [fv] "v"(fv) : "memory", "v230", "v231", "v232");
}
#define K0(j) SMX("148:151", j, u##j)
#define K1(j) SMX("152:155", j, u##j)
#define AF9 [a0] "v"(af[0]), [a1] "v"(af[1]), [a2] "v"(af[2]), [a3] "v"(af[3]), [a4] "v"(af[4]), [a5] "v"(af[5]), [a6] "v"(af[6]), [a7] "v"(af[7]), [a8] "v"(af[8])
#define WK9 [u0] "a"(wk[0]), [u1] "a"(wk[1]), [u2] "a"(wk[2]), [u3] "a"(wk[3]), [u4] "a"(wk[4]), [u5] "a"(wk[5]), [u6] "a"(wk[6]), [u7] "a"(wk[7]), [u8] "v"(wk[8])
__device__ __forceinline__ void k_one(const u32x4 (&af)[9], const u32x8 (&wk)[9], const u32x8 (&wp)[3], int ix, unsigned qw, f32x4 q1, f32x4& k0, f32x4& k2, float& t) {
  asm volatile(K0(8) K0(0) AD0(144) AD1(144) CVP K0(4) IMW("%[qw]", "1024") K0(1) K0(2) K0(3) K0(5) PADA K0(6) K0(7)
               SMX("156:159", 0, p0) SMX("156:159", 4, p1) AD0(148)
               : "+{v[148:151]}"(k0), "+{v[156:159]}"(k2), "=&{v230}"(t)
               : "{v[144:147]}"(q1), AF9, WK9, [p0] "v"(wp[0]), [p1] "v"(wp[1]), [ix] "v"(ix), [qw] "v"(qw) : "memory", "v231", "v232");
}
#define KB
template <int PO, int JC>
__device__ __forceinline__ void k_end(const u32x4 (&af)[9], const u32x8 (&wk)[9], u32x8 wp, int ix, f32x4 k0, f32x4 k1, f32x4 k2, float t, unsigned kw, unsigned fw, unsigned fv) {
  asm volatile(".if %[jc] == 3\n\t" SMX("156:159", 3, p2) ".endif\n\t.if %[jc] == 6\n\t" SMX("156:159", 6, p2) ".endif\n\t.if %[jc] == 8\n\t" SMX("156:159", 8, p2) ".endif\n\t"
               AD1(148) K1(8) CVP K1(0) IMW("%[kw]", "0") K1(4) AD0(156) K1(1) AD1(156) K1(2) CVP K1(3) IMW("%[kw]", "%[po]") K1(5) PADA K1(6) K1(7)
               "s_nop 7\n\t" PKA(230, 152) CVP IMW("%[kw]", "1024") KB "ds_write_b32 %[fw], %[fv]\n\t"
               :: "{v[148:151]}"(k0), "{v[152:155]}"(k1), "{v[156:159]}"(k2), "{v230}"(t), AF9, WK9, [p2] "v"(wp), [ix] "v"(ix), [kw] "v"(kw), [fw] "v"(fw),
                  [fv] "v"(fv), [po] "n"(PO), [jc] "n"(JC)
               : "memory", "v231", "v232");
}
__device__ __forceinline__ unsigned add3bf(unsigned a, unsigned b, unsigned c) {
  return pk2(__uint_as_float(a << 16) + __uint_as_float(b << 16) + __uint_as_float(c << 16),
             __uint_as_float(a & 0xffff0000u) + __uint_as_float(b & 0xffff0000u) + __uint_as_float(c & 0xffff0000u));
}
#define IS(n)
#define cKW "v112"
#define cKA "v113"
#define cQ0 "v114"
#define cQ1 "v115"
#define cQ3 "v116"
#define cFA "v117"
#define cEO "v118"
#define cLO "v119"
#define cC0 "v120"
#define cC1 "v121"
#define cC2 "v122"
#define cFW "v123"
#define cQW "v124"
#define cKX "v125"
#define TRD(r, a, o) "ds_read_b64_tr_b16 v[" #r ":" #r "+1], " a " offset:" #o "\n\t"
#define LGK(n) "s_waitcnt lgkmcnt(" #n ")\n\t"
#define SXM(t, c) "v_smfmac_f32_16x16x64_bf16 v[200+4*" #t ":203+4*" #t "], v[160+4*" #c ":163+4*" #c "], v[64+16*" #t "+8*" #c ":71+16*" #t "+8*" #c "], %[ix]\n\t"
#define SMM(c) "v_smfmac_f32_16x16x64_bf16 v[208:211], v[160+4*" #c ":163+4*" #c "], v[176+8*" #c ":183+8*" #c "], %[ix]\n\t"
#define PQ ".if pq\n97:\n\ts_cbranch_execz 97f\n\t.endif\n\t"
#define PQ0 ".set pq, 0\n\t"
#define PQA ".set pq, %[wid] >= 0\n\t.if pq\n.Lwa%[wid]:\n\t.endif\n\t" PQ
#define PQB ".if pq\n97:\n\ts_cbranch_execz .Lwb%[wid]\n\t.endif\n\t" PQ0
template <int A> __device__ __forceinline__ void pq_walk() {
  asm volatile(".if %1\n\ts_mov_b64 exec, 0\n\ts_branch .Lwa%0\n\t.endif\n.Lwb%0:\n\ts_mov_b64 exec, -1" :: "n"(A), "n"(WRM >> 1 & 1) : "memory");
}
#define QRE TRD(160, cQ0, 0) TRD(162, cQ1, 0)
#define QRL TRD(164, cQ0, 1024) TRD(166, cQ1, 1024)
#define CN(k)
#define QE 0
#define QL 1
#define KL -1
#define QP(n) ".if " XSTR(QE) " == " #n "\n\t" QRE ".endif\n\t"
#define QQ(n) ".if " XSTR(QL) " == " #n "\n\t" QRL ".endif\n\t"
template <int PH>
__device__ __forceinline__ void kx_get(u32x16 cst, u32x16& x0, u32x16& x1) {
#define KXR(k) "ds_read_b128 v[64+4*" #k ":67+4*" #k "], " cKX " offset:%[o]+1024*" #k "\n\tds_read_b128 v[80+4*" #k ":83+4*" #k "], " cKX " offset:%[o]+1024*" #k "+16\n\t"
  asm volatile(KXR(0) KXR(1) KXR(2) KXR(3)
               : "={v[64:79]}"(x0), "={v[80:95]}"(x1) : "{v[112:127]}"(cst), [o] "n"(KX_OFF + PH * 8192) : "memory");
}
#define NK 2
#define KIF(c, t) ".if " XSTR(NK) c "\n\t" t ".endif\n\t"
__device__ __forceinline__ void s_first(const u32x4 (&af)[9], const u32x8 (&wk)[9], int ix, u32x16 cst, u32x16 x0, u32x16 x1, f32x4 q1, f32x4& k0, f32x16& T, u32x8& Q) {
  asm volatile(K0(8) QP(0) K0(0) AD0(144) AD1(144) CVP K0(4) IMW(cQW, "1024") K0(1) QP(1) QQ(0) K0(2) QQ(1) K0(3) QQ(2) KIF(" == 0", K0(5))
               LGK(2) SXM(0, 0) SXM(1, 0) LGK(0) SXM(0, 1) SXM(1, 1)
               : "+{v[148:151]}"(k0), "+{v[200:215]}"(T), "=&{v[160:167]}"(Q)
               : "{v[144:147]}"(q1), [a0] "v"(af[0]), [a1] "v"(af[1]), [a2] "v"(af[2]), [a3] "v"(af[3]), [a4] "v"(af[4]), [a5] "v"(af[5]), [a8] "v"(af[8]),
                 [u0] "a"(wk[0]), [u1] "a"(wk[1]), [u2] "a"(wk[2]), [u3] "a"(wk[3]), [u4] "a"(wk[4]), [u5] "a"(wk[5]), [u8] "v"(wk[8]),
                 [ix] "v"(ix), "{v[112:127]}"(cst), "{v[64:79]}"(x0), "{v[80:95]}"(x1)
               : "memory", "v230", "v231", "v232");
}
#define DPPF " row_mask:0xf bank_mask:0xf\n\t"
#define DX1 " quad_perm:[1,0,3,2]" DPPF
#define DX2 " quad_perm:[2,3,0,1]" DPPF
#define DX4 " row_half_mirror" DPPF
template <int WID>
__device__ __forceinline__ void el_phase(i32x4 D, const u32x4 (&af)[9], const u32x8 (&wk)[9], int ix, u32x16 cst, u32x16 x0, u32x16 x1, u32x8& Q, f32x4& k0, f32x16& T,
                                         unsigned& bud, u64& e1, u64& e4, unsigned& nr, u32x8 sc, unsigned fv) {
#define EST(s) "buffer_store_dwordx2 v[222:223], " cEO ", %[D], " s " offen sc1\n\t"
#define LST(s) "buffer_store_dword v227, " cLO ", %[D], " s " offen sc1\n\t"
#define EPART(KA, KB_)                                                                                                             \
               IS(4) KA "v_add_f32 v212, v200, v202\n\t" KB_ "v_add_f32 v213, v201, v203\n\tv_add_f32 v214, v204, v206\n\tv_add_f32 v215, v205, v207\n\t" \
               "v_max_f32 v216, v212, v214\n\tv_max_f32 v217, v213, v215\n\tv_mov_b32 v219, v212\n\t"                              \
               "v_max_f32_dpp v218, v216, v216" DPPLO "v_max_f32_dpp v218, v217, v217" DPPHI PQ                                    \
               "v_mov_b32 v220, v214\n\ts_nop 0\n\t"                                                                             \
               "v_max_f32_dpp v218, v218, v218" DX1 "v_mov_b32_dpp v212, v213" DPPHI "v_mov_b32_dpp v214, v215" DPPHI              \
               "v_max_f32_dpp v218, v218, v218" DX2 "v_mov_b32_dpp v213, v219" DPPLO "v_mov_b32_dpp v215, v220" DPPLO              \
               PQ "v_max_f32_dpp v218, v218, v218" DX4 IS(5)                                                                       \
               "v_mul_f32 v216, " SCN ", v218\n\t"                                                                                 \
               "v_pk_fma_f32 v[212:213], v[212:213], s[44:45], v[216:217] op_sel_hi:[1,0,0]\n\t"                                   \
               "v_pk_fma_f32 v[214:215], v[214:215], s[44:45], v[216:217] op_sel_hi:[1,0,0]\n\t"                                   \
               "v_exp_f32 v212, v212\n\tv_exp_f32 v214, v214\n\tv_exp_f32 v213, v213\n\tv_exp_f32 v215, v215\n\t"                  \
               PQ CVX(222, 212, 214) CVX(223, 213, 215)
#define MPART(KA, KB_, KBW)                                                                                                        \
               KA EST("s40") TRD(168, cQ3, 0) TRD(192, cKA, 2048) EST("s41") EVA(4) IS(6) PQ TRD(194, cKA, 2304) TRD(176, cKA, 0)   \
               KB_ TRD(178, cKA, 256) TRD(180, cKA, 512) TRD(182, cKA, 768)                                                        \
               "v_add_f32 v212, v212, v214\n\tv_add_f32 v213, v213, v215\n\tv_mov_b32 v225, " cC2 "\n\tv_add_f32 v212, v212, v213\n\t"  \
               PQ LGK(4) PKA(230, 148) CVP "v_smfmac_f32_16x16x32_bf16 v[208:211], v[168:169], v[192:195], %[ix]\n\t"                 \
               IMW(cKW, "1024") TRD(190, cKA, 1792) KBW TRD(184, cKA, 1024) PQ TRD(186, cKA, 1280) TRD(188, cKA, 1536) LGK(5) SMM(0)  \
               EST("s42") "ds_write_b32 " cFW ", %[fv]\n\t" LGK(1) SMM(1) IS(7)
#define GADD   "v_add_f32_dpp v220, v208, v210 quad_perm:[0,1,2,3] row_mask:0xf bank_mask:0x3\n\t"                                 \
               "v_add_f32_dpp v220, v209, v211 quad_perm:[0,1,2,3] row_mask:0xf bank_mask:0xc\n\t"
#define LEND   "v_add_f32_dpp v217, v217, v217" DX4 "v_rcp_f32 v218, v217\n\t" PADN                                                \
               "v_pk_mul_f32 v[224:225], v[224:225], v[218:219] op_sel_hi:[1,0]\n\t"                                               \
               CVX(227, 224, 225) IS(8) LST("s40") LST("s41") EVA(1) IS(9) LST("s42") EST("s43") LST("s43")
#define KBF
#define EVO
  asm volatile(PQ0 EPART(K0(6) KIF(" == 2", "s_nop 1\n\t"), KIF(" < 2", K0(7)))
               MPART(KIF(" > 0", K0(5)), KIF(" == 2", K0(7)), KBF)
               "s_nop 1\n\tv_add_f32_dpp v212, v212, v212" DX1 "s_nop 4\n\t" GADD
               "v_fmamk_f32 v221, v220, " SCK ", v216\n14:\n\tv_exp_f32 v220, v221\n\tv_cmp_nge_f32 vcc, s45, v221\n\tv_add_f32 v217, v220, v212\n\t"
               "v_add_f32_dpp v217, v220, v217" DX1 "v_fma_f32 v224, v220, " cC0 ", " cC1 "\n\tv_fmac_f32_dpp v225, v220, " cC0 DX1
               "v_add_f32_dpp v217, v217, v217" DX2 "s_cmp_eq_u64 vcc, 0\n\ts_cselect_b64 exec, -1, 0\n\t" LEND "s_cbranch_scc0 19f\n"
               "13:\n\t.subsection 1\n19:\n\ts_mov_b64 exec, -1\n\t" PQA CN(10000)
               "20:\n\tds_read_b32 v224, " cFA "\n\t" LGK(0) "v_cmp_gt_i32 vcc, %[fv], v224\n\ts_cbranch_vccz 21f\n\t" BUD("20b")
               "21:\n\t" QRE PQ QRL PQ TZERO PQ LGK(0) SXM(0, 0) SXM(1, 0) SXM(0, 1) SXM(1, 1) PQ
               EPART("s_nop 7\n\t", "") MPART("", "", "") PQ
               "v_mov_b32 v213, v212\n\tv_add_f32_dpp v212, v212, v212" DX1 "s_nop 5\n\t" GADD
               "v_fmamk_f32 v221, v220, " SCK ", v216\n\tv_cmp_nge_f32 vcc, s45, v221\n\ts_cbranch_vccz 14b\n\t" PQB
               "v_max_f32 v216, v220, v218\n\tv_mul_f32 v221, " SCK ", v218\n\ts_nop 0\n\t"
               "v_max_f32_dpp v216, v216, v216" DX1 "v_mul_f32 v220, " SCK ", v220\n\ts_nop 0\n\t"
               "v_max_f32_dpp v216, v216, v216" DX2 "s_nop 1\n\t"
               "v_max_f32_dpp v216, v216, v216" DX4
               "v_pk_fma_f32 v[220:221], v[216:217], s[44:45], v[220:221] op_sel_hi:[0,0,1] neg_lo:[1,0,0] neg_hi:[1,0,0]\n\t"
               "v_exp_f32 v221, v221\n\tv_exp_f32 v220, v220\n\t"
               "v_mul_f32 v224, v221, " cC1 "\n\tv_fma_f32 v217, v221, v213, v220\n\t"
               "v_mul_f32 v225, v221, " cC2 "\n\tv_fmac_f32 v224, v220, " cC0 "\n\t"
               "v_add_f32_dpp v217, v217, v217" DX1 "v_fmac_f32_dpp v225, v220, " cC0 DX1 "s_nop 0\n\t"
               "v_add_f32_dpp v217, v217, v217" DX2 "s_nop 1\n\t" LEND "s_branch 13b\n\t.subsection 0"
               : [bud] "+s"(bud), "+{v[160:167]}"(Q), "+{v[148:151]}"(k0), "+{v[200:215]}"(T) EVO
               : [a5] "v"(af[5]), [a6] "v"(af[6]), [a7] "v"(af[7]), [u5] "a"(wk[5]), [u6] "a"(wk[6]), [u7] "a"(wk[7]), [ix] "v"(ix),
                 "{v[112:127]}"(cst), "{v[64:79]}"(x0), "{v[80:95]}"(x1), [D] "s"(D), "{s[40:47]}"(sc), [fv] "v"(fv), [wid] "n"(WID)
               : "memory", "vcc", "scc", "v168", "v169", "v176", "v177", "v178", "v179", "v180", "v181", "v182", "v183", "v184",
                 "v185", "v186", "v187", "v188", "v189", "v190", "v191", "v192", "v193", "v194", "v195",
                 "v216", "v217", "v218", "v219", "v220", "v221", "v222", "v223", "v224", "v225", "v227", "v230", "v231", "v232");
}
#define YMAX 0x42700000u
#define TWO 2
#define ARD(j, p) do { af[j] = *(lds_u32x4*)(p); SB(); } while (0)
#define SECOND(n, NXV) do { if (TWO == n) { asm volatile("s_waitcnt vmcnt(%0)" :: "n"(NXV) : "memory"); __builtin_amdgcn_s_barrier(); CBAR(); SB(); \
                         ARD(4, ard0 + 4096); ARD(5, ard1 + 4096); } if (TWO + 1 == n) { ARD(6, ard2 + 4096); ARD(7, ard3 + 4096); }               \
                         if (TWO + 2 == n) ARD(8, lds + F8_OFF + ar); } while (0)
#define HEAD(M, NXV, H7) do {                                                                                                                     \
    ARD(0, ard0); __builtin_amdgcn_s_barrier(); CBAR(); SB(); ARD(1, ard1); ARD(2, ard2); ARD(3, ard3);                                       \
    if (!TWO) { ARD(4, ard0 + 4096); ARD(5, ard1 + 4096); }                                                                                   \
    M(0, 0); SECOND(1, NXV); M(1, 0); SECOND(2, NXV); M(0, 1); SECOND(3, NXV); M(1, 1); SECOND(4, NXV); M(0, 2); SECOND(5, NXV);              \
    if (!(H7)) M(1, 2);                                                                                                                       \
    SECOND(6, NXV); M(0, 3); M(0, 4); M(0, 5); M(0, 6); M(0, 7); M(0, 8); } while (0)
__device__ __forceinline__ void role_qkc(const Args& a, rsrc_t R, const Go& go, lds_char* lds, int h, int tid, int hw, int lane) {
  const int wave = 3 - hw;
  const int l15 = lane & 15, g = lane >> 4;
  u32x8 wq[2][9], wk[2][9], wp[3];
  int cls = -1, kv0 = -1;
  unsigned g1;
  u32x4 af0[3];
  const int wz = h >> 3, tq = (h >> 1) & 3, hp = h & 1, qi = 2 * g + (l15 >> 3), kq = l15 & 7;
  unsigned pxo = (unsigned)nearoff(NEAR_E + (h >> 1) * 1024 + ((kq >> 1) * 16 + 8 * (qi >> 2) + 4 * hp + (qi & 3)) * 16 + (kq & 1) * 8);
  unsigned plo = !(kq & 1) ? (unsigned)nearoff(NEAR_PM + wz * 1024 + ((2 * hp + (tq >> 1)) * 16 + 4 * (qi >> 1) + (kq & 2) + (qi & 1)) * 16 + (tq & 1) * 8 + (kq >> 2) * 4)
                           : (kq == 1 ? (unsigned)nearoff(NEAR_AL + wz * 256 + ((tq >> 1) * 8 + qi) * 16 + (2 * hp + (tq & 1)) * 4) : OOB);
  {
    u32x4 sg[QDEP][8];
#pragma unroll
    for (int c = 0; c < QDEP; ++c) qk_gload<NTM & 1>(sg[c], a, h, c, tid);
#define WK3(b) do { WALK(b + 2); WALK(b + 1); WALK(b); } while (0)
    if (wave == 0) WK3(200); else if (wave == 1) WK3(208); else if (wave == 2) WK3(216); else WK3(224);
    if (wave == 0) pq_walk<24>(); else if (wave == 1) pq_walk<25>();
    const int q = l15 >> 2;
    const int lrow = 16 * (g >> 1) + 4 * (g & 1) + 8 * (q >> 1) + (q & 1);
    auto wrd = [&](lds_char* p) {
      u32x2 v0 = trd(p), v1 = trd(p + 2 * 544), v2 = trd(p + 32 * 544), v3 = trd(p + 34 * 544);
      return (u32x8){v0[0], v0[1], v1[0], v1[1], v2[0], v2[1], v3[0], v3[1]};
    };
#pragma unroll
    for (int ph = 0; ph < 3; ++ph) {
      if (ph == 2) {
        cls = get_cls(R, lane, cls);
        pxo += (unsigned)(cls & 1) * 8192u; plo += (unsigned)(cls & 1) * 8192u;
        CBAR();
        kv0 = (int)ld32(R, FX(FL_PROG, cls, 5) + h * 128 + (lane & 3) * 4, 0);
#pragma unroll
        for (int f = 0; f < 3; ++f) {
          const int u = (f == 2 ? 8 : f * 4 + wave) * 64 + lane;
          af0[f] = *(const u32x4*)(a.mem0 + (u & 7) * DM + (u >> 3) * 8);
        }
        CBAR();
      }
#pragma unroll
      for (int i = 0; i < 4; ++i) {
        const int c = ph * 4 + i;
        if (c < 9) {
          qk_swrite(sg[c % QDEP], lds + i * QSTG, tid);
          if (c + QDEP < 9) qk_gload<NTM & 1>(sg[c % QDEP], a, h, c + QDEP, tid);
          CBAR();
        }
      }
      RT(8 + ph);
      if (ph == 0) { g1 = ld32(R, go.vo, 0); CBAR(); }
      if (ph == 1) {
        if (__all(g1 != go.v0)) go_ack(R, go, lane); else go_wait(R, go, lane);
        cls = cls_look(R, lane);
        CBAR();
      }
      if (ph == 2) {
        if (wave < 3) {
#pragma unroll
          for (int k = 0; k < 4; ++k) {
            const unsigned so = wave * SLOT_REAL + (k & 1) * CPY1 + (k >> 1) * CPY2;
            st64((u32x2){~0u, ~0u}, R, pxo, so); st32(~0u, R, plo, so);
          }
        }
        CBAR();
        RT(11);
      }
      BAR();
#pragma unroll
      for (int i = 0; i < 4; ++i) {
        const int j = ph * 4 + i;
        if (j < 9) {
          lds_char* buf = lds + (ph < 2 ? ((i + wave) & 3) : 0) * QSTG + lrow * 544 + (l15 & 3) * 8;
#pragma unroll
          for (int t = 0; t < 4; ++t) {
            u32x8& w = t < 2 ? wq[t & 1][j] : wk[t & 1][j];
            w = wrd(buf + t * 128 + wave * 32);
          }
          if (j == 0 || j == 4) wp[j >> 2] = wrd(buf + 256 + 96);
        }
      }
      SB();
#pragma unroll
      for (int k = 0; k < 16; ++k) {
        const int j = ph * 4 + (k >> 2);
        if (j < 9) { u32x8& w = (k & 2) ? wk[k & 1][j] : wq[k & 1][j]; if (j < 8) pin_a(w); else pin_v(w); }
      }
      if (ph < 2) pin_v(wp[ph]);
      if (ph == 0 || wave == ph) wp[2] = wrd(lds + (ph < 2 ? 3 : 0) * QSTG + lrow * 544 + (l15 & 3) * 8 + 256 + 96);
      pin_v(wp[2]);
      BAR();
    }
  }
  RT(1);
  if (cls < 0) return;
  RT(2);
  auto run = [&](auto ROLEC) {
  constexpr int ROLE = decltype(ROLEC)::value;
  lds_char* img = lds;
  const unsigned fo = (unsigned)(cls & 1) * 8192u;
  const int r0 = ROLE * 1024 + lane * 16, r1 = r0 + 4096, r2 = 8192 + lane * 16;
  const unsigned m0 = nearoff(r0) + fo, m1 = nearoff(r1) + fo, m2 = nearoff(r2) + fo;
  const unsigned lb = (unsigned)(uintptr_t)lds, lw0 = lb + ROLE * 1024;
  const i32x4 D = mkdesc(a.wb);
  const int ar = g * 256 + ((l15 >> 1) & 1) * 128 + (2 * (l15 >> 2) + (l15 & 1)) * 16;
  lds_char* ard0 = img + ROLE * 1024 + ar;
  lds_char* ard1 = img + ((ROLE + 1) & 3) * 1024 + ar;
  lds_char* ard2 = img + ((ROLE + 2) & 3) * 1024 + ar;
  lds_char* ard3 = img + ((ROLE + 3) & 3) * 1024 + ar;
  const int gy = ((tq & 1) << 1) | (tq >> 1);
  const unsigned pcl = lane < 32 ? (unsigned)nearoff(NEAR_E + (h >> 1) * 1024 + ((lane >> 2) * 8 + 4 * hp + (lane & 3)) * 16) + fo
                     : (lane < 40 ? (unsigned)nearoff(NEAR_PM + wz * 1024 + ((2 * hp + (tq >> 1)) * 16 + (tq & 1) * 8 + (lane & 7)) * 16) + fo
                                  : (lane < 42 ? (unsigned)nearoff(NEAR_AL + wz * 256 + ((tq >> 1) * 8 + (2 * hp + (tq & 1)) * 2 + (lane & 1)) * 16) + fo : OOB));
  const unsigned kfo = OFF_KF + (unsigned)h * 8192u + lane * 16;
  constexpr int QT = ROLE >> 1;
  const unsigned kmo = OFF_KMF + (unsigned)h * 4096u + lane * 16;
  const unsigned pxs = OFF_PXF + (unsigned)((QT * 40 + h) * 1024 + lane * 16);
  const unsigned pws = OFF_PXF + (unsigned)((QT * 40 + 32 + wz * 2 + hp) * 1024 + (gy * 16 + l15) * 16 + g * 4);
  const unsigned kpf = FX(FL_PROG, cls, 5) + h * 128 + (lane & 3) * 4;
  const unsigned kmf = (lane == 0) ? FX(FL_KMPROG, cls, 3) + h * 128 : OOB;
  const unsigned wpo = lane == 0 ? FX(FL_WP, cls, 0) + h * 32 : OOB;
  const int idx = (lane & 2) ? 0xEEEE : 0x4444;
  const int jr = l15 >> 2;
  const unsigned qw = lb + QIM_OFF + (ROLE * 8 + ((2 * jr + (l15 & 1)) ^ ((ROLE & 1) << 1))) * 32 + g * 8 + (l15 & 2) * 2;
  const unsigned kw = lb + KIM_OFF + ROLE * 256 + l15 * 16 + g * 4;
  lds_char* krd = lds + KIM_OFF + (4 * g + jr) * 16 + (l15 & 2) * 4;
  unsigned fw = lb + FLG_OFF + ROLE * 4;
  pin_v(fw);
  lds_char* frd = lds + FLG_OFF + (lane & 3) * 4;
  const unsigned qa = lb + QIM_OFF + (16 * (g >> 1) + 4 * (g & 1) + (jr & 1) + 10 * (jr >> 1)) * 32 + (l15 & 3) * 8;
  u32x16 cst = {kw, lb + KIM_OFF + (4 * g + jr) * 16 + (l15 & 1) * 8, qa, qa ^ 64, lb + QIM_OFF + ((24 + 2 * g + (jr & 1)) ^ 2) * 32 + (l15 & 3) * 8,
                lb + FLG_OFF + min(lane & 3, 2) * 4, pxo, plo, (l15 & 1) ? 0u : 0x3f800000u, l15 == 1 ? 0x3f800000u : 0u, l15 == 9 ? 0x3f800000u : 0u, fw, qw,
                lb + (l15 >> 3) * 4096 + (g * 16 + 4 * (l15 & 3) + 2 * (jr & 1)) * 16, 0u, 0u};
  const unsigned own = own_cpy(cls, h >> 3);
  unsigned ima = lb + lane * 16;
  pin_v(ima);
  ds_flag(fw, 0u);
  ds_flag(fw + 16, 0u);
  auto poison = [&]() {
#pragma unroll
    for (int f = 0; f < 5; ++f) *(lds_u32x4*)(lds + QIM_OFF + f * 1024 + lane * 16) = (u32x4){~0u, ~0u, ~0u, ~0u};
    asm volatile("s_waitcnt lgkmcnt(0)" ::: "memory");
  };
  if (ROLE == 0) poison();

  int fl = kv0;
  auto k_dma = [&](int sg, int half) {
#pragma unroll
    for (int f = 0; f < 4; ++f)
      asm volatile("s_mov_b32 m0, %2\n\ts_nop 0\n\tbuffer_load_dwordx4 %0, %1, %3 offen offset:%4 sc1 lds"
                   :: "v"(kfo), "s"(D), "s"(lb + KX_OFF + (sg & 1) * 8192 + half * 4096), "s"((unsigned)(sg & RMASK) * 262144u + half * 4096), "n"(f * 1024) : "memory");
  };
  u32x4 qq[4], F[4];
#pragma unroll
  for (int f = 0; f < 4; ++f) { qq[f] = af0[0]; F[f] = af0[0]; }
  f32x4 T0 = {0.f, 0.f, 0.f, 0.f}, T1 = T0;
  float tmx = 0.f, tsx = 0.f;
  auto q_ld = [&](u32x4 (&qx)[4], int sg) {
#pragma unroll
    for (int f = 0; f < 4; ++f)
      asm volatile("buffer_load_dwordx4 %0, %1, %2, %3 offen offset:%4 sc1"
                   : "+v"(qx[f]) : "v"(kfo), "s"(D), "s"((unsigned)sg * 262144u + (OFF_QF - OFF_KF) + QT * 4096), "n"(f * 1024) : "memory");
  };
  auto t_part = [&](u32x4 (&qx)[4], int par, auto NC) {
    asm volatile("s_waitcnt vmcnt(%4)" : "+v"(qx[0]), "+v"(qx[1]), "+v"(qx[2]), "+v"(qx[3]) : "n"(decltype(NC)::value) : "memory");
    T0 = (f32x4){0.f, 0.f, 0.f, 0.f}; T1 = T0;
#pragma unroll
    for (int ks = 0; ks < 4; ++ks) {
      T0 = mfma(*(lds_u32x4*)(lds + KX_OFF + par * 8192 + ks * 1024 + lane * 16), qx[ks], T0);
      if (QT) T1 = mfma(*(lds_u32x4*)(lds + KX_OFF + par * 8192 + 4096 + ks * 1024 + lane * 16), qx[ks], T1);
    }
    pin_v(T0); pin_v(T1); PAD();
    px_mask(T0, T1, QT, lane);
    pin_v(T0); pin_v(T1); PAD();
    px_seg(T0, T1, tmx, tsx);
    pin_v(T0); pin_v(T1); pin_v(tmx); pin_v(tsx); PAD();
  };
  if (ROLE == 3) {
    const bool ok = wait_flag_c<8>(R, kpf, 0, fl);
    k_dma(0, 0); k_dma(0, 1);
    if (!ok) return;
  }
  VMW(0);

  unsigned iso_acc = 0, bud = SPINCAP, tm = 0, rcnt = 0;
  unsigned cv = 0;
  const unsigned cwo = FX(FL_AN, cls, 0) + (h & 15) * 128;
  EVDECL; CKDECL;
  auto k_get = [&](u32x4 (&F)[4], int s, bool parts) {
#pragma nounroll
    for (; bud != 0; --bud) {
      CBAR();
      if (__all((int)*(lds_u32*)frd > s)) break;
      __builtin_amdgcn_s_sleep(1);
    }
    CBAR();
#pragma unroll
    for (int f = 0; f < 4; ++f) { u32x2 lo = trd(krd + f * 512), hi = trd(krd + f * 512 + 256); F[f] = (u32x4){lo[0], lo[1], hi[0], hi[1]}; }
    if (!parts) return;
    const u32x2 p0 = trd(krd + 2048), p1 = trd(krd + 2304);
    F[1][2] = add3bf(F[1][2], p0[0], p1[0]); F[1][3] = add3bf(F[1][3], p0[1], p1[1]);
  };
  auto step = [&](auto PHC, int s) {
    constexpr int PH = decltype(PHC)::value & 1, FIRST = (decltype(PHC)::value >> 1) & 1, LAST = decltype(PHC)::value >> 2;
    constexpr int JC = ROLE == 0 ? 3 : (ROLE == 1 ? 6 : 8);
    u64 e0 = 0, e1 = 0, e3 = 0, e4 = 0; unsigned nr0 = 0;
    u32x4 af[9];
    f32x4 q0, q1, k0, k1, k2;
    PADS(200 + ROLE * 8 + decltype(PHC)::value);
    unsigned f3 = s + 1;
    pin_v(f3);
    constexpr int RD = ROLE == 3 && !LAST;
    f32x16 T;
    u32x16 x0, x1;
    if (RD) t_zero(k0, T); else if (!LAST) k_zero(k0, k1, k2);
    const u32x8 (&wf)[2][9] = LAST ? wk : wq;
#define MQ(t, j) do { smo<t, (j) < 8>(q##t, af[j], wf[t][j], idx); SB(); } while (0)
    lds_char* f8 = lds + F8_OFF + lane * 16;
    bud = __builtin_amdgcn_readfirstlane(bud);
    asm volatile("" : "+s"(bud));
    unsigned nt = 0; int rq = 0;
    tm = __builtin_amdgcn_readfirstlane(tm); rcnt = __builtin_amdgcn_readfirstlane(rcnt);
    asm volatile("" : "+s"(tm), "+s"(rcnt));
    if (RD) asm volatile("" : "+{v[112:127]}"(cst));
    unsigned p0 = (s & 7) * SLOT_REAL, p1 = p0 + CPY1, p2 = p0 ^ CPY2, p3 = p2 + CPY1;
    unsigned ms = (p2 + own) ^ ((s & 4) ? 0 : CPY1);
    u32x8 sc = {p0, p1, p2, p3, 0x3e0293eeu, YMAX, 0u, 0u};
    if (RD) asm volatile("" : "+{s[40:47]}"(sc));
    constexpr int NX = ROLE == 3 || LAST ? 0 : (ROLE == 0 ? 8 : 4);
    constexpr int ID = FIRST ? -1 : ROLE * 8 + decltype(PHC)::value;
    auto head = [&]() {
    v_zero(q0, q1);
    if (!FIRST) {
      int r = 0;
      if (ROLE == 1) r = spin_late(tm, 0);
      rq = r;
      mem_issue2<ROLE == F8W ? 3 : 2>(D, m0, m1, m2, ms, lw0, lw0 + 4096, lb + F8_OFF, e3);
      if (ROLE == 1) { move_on(tm, r); SB(); }
      PAD();
      if (RD) { kx_get<PH>(cst, x0, x1); asm volatile("s_waitcnt lgkmcnt(0)" ::: "memory"); }
      if (ROLE == 3) asm volatile("s_waitcnt vmcnt(%0)" :: "n"(TWO ? 1 + (F8W == 3) : 0) : "memory");
      else if (nt != 0) VMW(0);
      else {
      __builtin_amdgcn_s_sleep(XSLP);
      if (!LAST && (ROLE == 1 || ROLE == 2)) k_dma(s + 1, ROLE - 1);
      if (ROLE == 0 && !LAST) {
#pragma unroll
        for (int ks = 0; ks < 4; ++ks) st128(F[ks], R, kmo + ks * 1024, (unsigned)(s - 1) * 131072u);
      }
      if (ROLE == 0 && !LAST) {
        st128((u32x4){~0u, ~0u, ~0u, ~0u}, R, pcl, (unsigned)((s + 2) & 3) * SLOT_REAL);
        st128((u32x4){~0u, ~0u, ~0u, ~0u}, R, pcl, (unsigned)((s + 2) & 3) * SLOT_REAL + CPY1);
        st128((u32x4){~0u, ~0u, ~0u, ~0u}, R, pcl, (unsigned)((s + 2) & 3) * SLOT_REAL + CPY2);
        st128((u32x4){~0u, ~0u, ~0u, ~0u}, R, pcl, (unsigned)((s + 2) & 3) * SLOT_REAL + CPY2 + CPY1);
      }
      PAD();
      CBAR();
      asm volatile("s_waitcnt vmcnt(%0)" :: "n"(NX + (TWO ? 1 + (ROLE == F8W) : 0)) : "memory");
      }
    } else {
      *(lds_u32x4*)(img + r0) = af0[0]; *(lds_u32x4*)(img + r1) = af0[1]; *(lds_u32x4*)f8 = af0[2];
      asm volatile("s_waitcnt lgkmcnt(0)" ::: "memory");
    }
    SB();
    HEAD(MQ, NX, 0);
    if (s == 0) RT(3);
    if (s == 1) RT(4);
    if (s == 8) RT(5);
    if (s == 60) RT(6);
    ISOQ(1);
    q_mid<ID>(af, wf[1], idx, LAST ? kw : qw, q0, q1); SB();
    };
    if (!FIRST) {
      tm -= GRID + XT; nt = ~0u;
      try_top<ID>(nt, ms, tm, rcnt, ima); SB();
      head();
      try_end(ima, nt, ms, tm, rcnt, m0, m1, m2, lw0, lw0 + 4096, lb + F8_OFF, D); SB();
    } else
    {
      if (FIRST && RD) { kx_get<PH>(cst, x0, x1); asm volatile("s_waitcnt lgkmcnt(0)" ::: "memory"); }
      head();
    }
    ISOQ(2);
    EV(e0);
    if (LAST) k_last(kw, q1, fw, f3);
    if (ROLE < 3) {
      if (!LAST) {
        float t;
        k_one(af, wk[0], wp, idx, qw, q1, k0, k2, t); SB();
        ISOQ(4);
        if (ROLE == 1 && FIRST) { st32(f3, R, wpo, 0); SB(); }
        k_end<ROLE == 2 ? 256 : 2048, JC>(af, wk[1], wp[2], idx, k0, k1, k2, t, kw, fw, f3); SB();
      }
      ISOQ(6);
      PAD();
      if (ROLE == 1 && !LAST) {
        nr0 = nt != 0;
        if (!FIRST) next_time(tm, s);
      }
      if (ROLE == 0 && !LAST) {
        __builtin_amdgcn_s_sleep(TSLP);
        k_get(F, s, true);
        ds_flag(lb + FLG_OFF + 20, f3);
        pin_v(F[0]); pin_v(F[1]); pin_v(F[2]); pin_v(F[3]);
        VMW(0);
        st32((unsigned)(s - 1), R, kmf, 0);
        if (!PH && !FIRST && UNLIKELY(s == NSEG - 2)) {
#pragma unroll
          for (int ks = 0; ks < 4; ++ks) st128(F[ks], R, kmo + ks * 1024, (unsigned)s * 131072u);
          VMW(0);
          st32((unsigned)s, R, kmf, 0);
        }
      }
      if (ROLE == 2 && !LAST) {
        if (FIRST) { k_dma(1, 0); k_dma(1, 1); }
        gate_wait(R, kpf, min(2 * s + 4 + (s >= NSEG - 4), 2 * NSEG - 1), fl, bud);
        fl = (int)ld32(R, kpf, 0);
        VMW(0);
        pin_v(fl);
      }
      if ((ROLE == 0 || ROLE == 2) && !PH && !FIRST && UNLIKELY(s == NSEG - 2)) {
        k_dma(s + 1, 0);
        q_ld(qq, s + 1);
        t_part(qq, 1, std::integral_constant<int, 0>());
      }
      if ((ROLE == 0 || ROLE == 2) && LAST) {
        f32x4 G = {0.f, 0.f, 0.f, 0.f};
        u32x4 px; unsigned pw;
        k_get(F, s, false);
#pragma unroll
        for (int ks = 0; ks < 4; ++ks) G = mfma(F[ks], qq[ks], G);
        px_fin(G, T0, T1, tmx, tsx, px, pw, lane);
        st128(px, R, pxs, (unsigned)s * 81920u); st32(pw, R, pws, (unsigned)s * 81920u);
        VMW(0);
        st32((unsigned)s, R, lane == 0 ? FX(FL_PXLAST, cls, 1) + h * 256 + QT * 128 : OOB, 0);
      }
      CBAR();
      if (ROLE == 0) CKS();
    } else if (!LAST) {
      u32x8 Q;
      s_first(af, wk[1], idx, cst, x0, x1, q1, k0, T, Q); SB();
      el_phase<ID>(D, af, wk[1], idx, cst, x0, x1, Q, k0, T, bud, e1, e4, nr0, sc, f3);
      ds_flag(fw + 4, f3);
      EVPUT(2, e1); EVPUT(4, e4);
    }
    EVPUT(0, e0);
    EVPUTI(1, nr0); if (!FIRST) EVPUT(3, e3);
    if (ROLE == 1 && !LAST) {
#pragma nounroll
      for (; bud != 0; --bud) {
        CBAR();
        if (__all((int)*(lds_u32*)(lds + FLG_OFF + 16 + (lane & 1) * 4) > s)) break;
        __builtin_amdgcn_s_sleep(1);
      }
      poison();
      VMW(0);
      if (FIRST) { tm = get_t1(R, cwo, bud); asm volatile("" : "+s"(tm)); }
    }
    if (PH && !LAST && UNLIKELY(s == WLAST)) WALK(200 + ROLE * 8 + 5);
    PADE();
#undef MQ
  };
  step(std::integral_constant<int, 2>(), 0);
#pragma nounroll
  for (int s = 1; s < NSEG - 1; s += 2) {
    step(std::integral_constant<int, 1>(), s);
    step(std::integral_constant<int, 0>(), s + 1);
  }
  step(std::integral_constant<int, 5>(), NSEG - 1);
  EVDUMP();
  if (ROLE == 0) CKDUMP();
  };
  if (hw == 0) run(std::integral_constant<int, 3>());
  else if (hw == 1) run(std::integral_constant<int, 2>());
  else if (hw == 2) run(std::integral_constant<int, 1>());
  else run(std::integral_constant<int, 0>());
  RT(7);
  SV(15, cls);
}

#define VIM_OFF 8192
#define NOP(n) do { asm volatile("s_nop " #n); SB(); } while (0)
#define cVW "v112"
#define cRA "v113"
#define cRB "v114"
#define cHA "v115"
#define cHB "v116"
#define cVF "v117"
#define cVR "v118"
#define cIY "v119"
#define YP(p, c) "v_smfmac_f32_16x16x64_bf16 v[148+4*" #p ":151+4*" #p "], v[168+4*" #c "+4*(" #c ">>1):171+4*" #c "+4*(" #c ">>1)], %[w" #p #c "], " cIY "\n\t"
#define Y8(k) "v_smfmac_f32_16x16x64_bf16 v[164:167], v[176+12*" #k ":179+12*" #k "], %[w8" #k "], " cIY "\n\t"
#define HA(d, b) "v_add_f32 v" #d ", v[" b "], v[" b "+2]\n\t"
#define HB(d, b) "v_add_f32 v" #d ", v[" b "+1], v[" b "+3]\n\t"
#define YC(p, e) "v_cvt_pk_bf16_f32 v[192+2*" #p "+" #e "], v[148+4*" #p "+2*" #e "], v[149+4*" #p "+2*" #e "]\n\t"
#define YS(p) ".if %[st]\n\tbuffer_store_dwordx2 v[192+2*" #p ":193+2*" #p "], v[120+" #p "], %[D], %[ys] offen sc1\n\t.endif\n\t"
#define S16(a, b) "v_permlane16_swap_b32 v" #a ", v" #b "\n\t"
#define Z2(r) "v_mov_b64 v[" #r ":" #r "+1], 0\n\tv_mov_b64 v[" #r "+2:" #r "+3], 0\n\t"
#define YZERO Z2(148) Z2(152) Z2(156) Z2(160) Z2(164)
#define VR0 TRD(168, cRA, 0) TRD(170, cRB, 0)
#define VR1 TRD(172, cRA, 512) TRD(174, cRB, 512) TRD(176, cHA, 0) TRD(178, cHB, 0)
__device__ __forceinline__ void y_zero(f32x16& ya, f32x4& yc) {
  asm volatile(YZERO : "={v[148:163]}"(ya), "={v[164:167]}"(yc));
}
#define HV 0
#define VO 1
#define VR 1
#define YEO
#define YEN
template <int ID>
__device__ __forceinline__ void v_end(const u32x4 (&af)[9], const u32x8 (&w)[9], int ix, u32x16 cst, f32x4 v0, f32x4& v1, u32x16& A,
                                      const u32x8 (&wo)[4][4], f32x16& ya, u64& e1) {
#define VH0 HA(201, "140") HB(202, "140") CVX(203, 201, 202)
#define VH1 HA(201, "144") HB(202, "144") CVX(203, 201, 202)
#define VW0 "ds_write_b32 " cVW ", v203\n\t"
#define VW1 "ds_write_b32 " cVW ", v203 offset:1024\n\t"
#define VCM "v_cmp_u_f32 vcc, v201, v202\n\t"
#define TRYL TRYB
  asm volatile(
               Q1(3) Q1(4) VH0 Q1(5) VW0 Q1(6) "s_barrier\n\t" VR0 Q1(7) TRD(172, cRA, 512) TRD(174, cRB, 512) Q1(8) TRD(176, cHA, 0) TRD(178, cHB, 0) VCM
               LGK(4) YP(0, 0) IS(16) VH1 YP(1, 0) VW1 TRYL
               : "+{v[144:147]}"(v1), "=&{v[168:183]}"(A), "+{v[148:163]}"(ya) YEO
               : "{v[140:143]}"(v0), [a2] "v"(af[2]), [a3] "v"(af[3]), [a4] "v"(af[4]), [a5] "v"(af[5]), [a6] "v"(af[6]), [a7] "v"(af[7]), [a8] "v"(af[8]),
                 [w2] "a"(w[2]), [w3] "a"(w[3]), [w4] "a"(w[4]), [w5] "a"(w[5]), [w6] "a"(w[6]), [w7] "a"(w[7]), [w8] "v"(w[8]), [ix] "v"(ix), "{v[112:127]}"(cst), [id] "n"(ID),
                 [w00] "a"(wo[0][0]), [w10] "a"(wo[1][0])
               : "memory", "v201", "v202", "v203", TRYC);
}
template <int ST, int WID>
__device__ __forceinline__ void y_all(const u32x8 (&wo)[4][4], const u32x8 (&w8)[2], u32x16 cst, f32x16 ya, f32x4 yc, u32x16 A, i32x4 D,
                                      unsigned ys, unsigned fv, u32x8& o, unsigned& o8, u64& e1, unsigned& nr) {
#define RH1 TRD(188, cHA, 1024) TRD(190, cHB, 1024)
#define RQ2 TRD(180, cRA, 1024) TRD(182, cRB, 1024)
#define RQ3 TRD(184, cRA, 1536) TRD(186, cRB, 1536)
#define VFL "ds_write_b32 " cVF ", %[fv]\n\t"
#define YK0 YP(2, 0) YP(3, 0) RH1 LGK(3) YP(0, 1) RQ2 Y8(0) RQ3 YP(1, 1) VFL IS(17)
#define YK1    LGK(5) Y8(1) IS(18) YP(2, 1) YP(3, 1) S16(164, 166) LGK(3) YP(0, 2) PQ S16(165, 167) LGK(1) YP(0, 3) HA(201, "164") YP(1, 2) HB(202, "164") CVX(200, 201, 202) PQ \
               YP(1, 3) ".if %[st]\n\tbuffer_store_dword v200, v124, %[D], %[ys] offen sc1\n\t.endif\n\t" YC(0, 0) YP(2, 2) YC(0, 1) YP(2, 3) PQ YS(0) YC(1, 0)\
               YP(3, 2) YC(1, 1) YP(3, 3) IS(19) YS(1) PQ YC(2, 0) YC(2, 1) YS(2) "s_nop 5-2*%[st]\n\t" YC(3, 0) YC(3, 1) YS(3) IS(20) EVA(1)
  asm volatile(PQ0 YK0 YK1
               "v_cmp_u_f32 vcc, v148, v150\n\tv_cmp_u_f32 s[46:47], v201, v202\n\ts_or_b64 vcc, vcc, s[46:47]\n\ts_cbranch_scc1 19f\n"
               "13:\n\t.subsection 1\n"
               PQA "19:\n\tds_read_b32 v204, " cVR "\n\t" LGK(0) "v_cmp_gt_i32 vcc, %[fv], v204\n\ts_cbranch_vccnz 19b\n\t"
               CN(100) VR0 PQ VR1 RH1 PQ RQ2 RQ3 Z2(148) PQ Z2(152) Z2(156) Z2(160) PQ Z2(164) LGK(0) YP(0, 0) YP(1, 0) YP(2, 0) YP(3, 0) PQ YP(0, 1) Y8(0) YP(1, 1) PQ YK1
               "s_branch 13b\n\t" PQB ".subsection 0"
               : "=&{v[192:199]}"(o), "=&{v200}"(o8) YEO YEN
               : [w00] "a"(wo[0][0]), [w01] "a"(wo[0][1]), [w02] "a"(wo[0][2]), [w03] "a"(wo[0][3]), [w10] "a"(wo[1][0]), [w11] "a"(wo[1][1]),
                 [w12] "a"(wo[1][2]), [w13] "a"(wo[1][3]), [w20] "a"(wo[2][0]), [w21] "a"(wo[2][1]), [w22] "a"(wo[2][2]), [w23] "a"(wo[2][3]),
                 [w30] "a"(wo[3][0]), [w31] "a"(wo[3][1]), [w32] "a"(wo[3][2]), [w33] "a"(wo[3][3]), [w80] "v"(w8[0]), [w81] "v"(w8[1]),
                 "{v[112:127]}"(cst), "{v[148:163]}"(ya), "{v[164:167]}"(yc), "{v[168:183]}"(A),
                 [D] "s"(D), [ys] "s"(ys), [fv] "v"(fv), [st] "n"(ST), [wid] "n"(WID)
               : "memory", "vcc", "scc", "s46", "s47", "v184", "v185", "v186", "v187", "v188", "v189", "v190", "v191", "v201", "v202", "v204");
}
__device__ __forceinline__ void role_vcu(const Args& a, rsrc_t R, const Go& go, lds_char* lds, int h, int tid, int wave, int lane) {
  const int l15 = lane & 15, g = lane >> 4;
  u32x8 wv[2][9], wo[4][4], w8[2];
  int cv0 = -1;
  unsigned g1;
  u32x4 af0[3];
  {
    u32x4 st[36], so[36];
#pragma unroll
    for (int k = 0; k < 36; ++k) so[k] = gld<NTM & 2>(a.Wo + (((size_t)h * 128 * DM + (tid + 256 * k) * 8) & ((NOCW & 2) ? 2047 : ~0ull)));
    if (wave == 0) WK3(232); else if (wave == 1) WK3(240); else if (wave == 2) WK3(248); else WK3(256);
    if (wave == 0) { pq_walk<32>(); pq_walk<33>(); } else if (wave == 1) { pq_walk<40>(); pq_walk<41>(); }
    else if (wave == 2) { pq_walk<48>(); pq_walk<49>(); } else { pq_walk<56>(); pq_walk<57>(); }
    const int q = l15 >> 2;
    const int lrow = 16 * (g >> 1) + 4 * (g & 1) + 8 * (q >> 1) + (q & 1), lrow2 = 8 * (g >> 1) + 4 * (g & 1) + (q & 1), up2 = (g & 1) ? 12 : 20;
    auto wrd = [&](lds_char* p, int rs, int up) {
      u32x2 v0 = trd(p), v1 = trd(p + 2 * rs), v2 = trd(p + up * rs), v3 = trd(p + (up + 2) * rs);
      return (u32x8){v0[0], v0[1], v1[0], v1[1], v2[0], v2[1], v3[0], v3[1]};
    };
#pragma unroll
    for (int ph = 0; ph < 4; ++ph) {
      if (ph < 2) {
#pragma unroll
        for (int k = 0; k < 18; ++k) { const int u = tid + 256 * k; *(lds_u32x4*)(lds + (u / 72) * 1184 + (u % 72) * 16) = so[ph * 18 + k]; }
#pragma unroll
        for (int k = ph * 18; k < ph * 18 + 18; ++k) { const int u = tid + 256 * k; st[k] = gld<NTM & 2>(a.Wv + (((size_t)(u >> 4) * HD + h * 128 + (u & 15) * 8) & ((NOCW & 2) ? 2047 : ~0ull))); }
      } else {
#pragma unroll
        for (int k = (ph - 2) * 16; k < (ph == 2 ? 16 : 36); ++k) { const int u = tid + 256 * k; *(lds_u32x4*)(lds + ((u >> 4) - (ph - 2) * 256) * 288 + (u & 15) * 16) = st[k]; }
      }
      if (ph == 0) { CBAR(); g1 = ld32(R, go.vo, 0); CBAR(); }
      if (ph == 2) {
        if (__all(g1 != go.v0)) go_ack(R, go, lane); else go_wait(R, go, lane);
        cv0 = cls_look(R, lane);
#pragma unroll
        for (int f = 0; f < 3; ++f) {
          const int u = (f == 2 ? 8 : f * 4 + wave) * 64 + lane;
          af0[f] = *(const u32x4*)(a.mem0 + (u & 7) * DM + (u >> 3) * 8);
        }
        CBAR();
      }
      BAR();
      if (ph < 2) {
#pragma unroll
        for (int k = 0; k < 8; ++k) wo[k >> 1][2 * ph + (k & 1)] = wrd(lds + (lrow2 + 32 * (k & 1)) * 1184 + (wave * 9 + (k & 6) + (q >> 1)) * 32 + (l15 & 3) * 8, 1184, up2);
        w8[ph] = wrd(lds + (lrow2 + 32 * (q >> 1)) * 1184 + (wave * 9 + 8) * 32 + (l15 & 3) * 8, 1184, up2);
        SB();
#pragma unroll
        for (int k = 0; k < 8; ++k) pin_a(wo[k >> 1][2 * ph + (k & 1)]);
        pin_v(w8[ph]);
      } else {
#pragma unroll
        for (int k = 0; k < (ph == 2 ? 8 : 10); ++k) {
          const int i = k >> 1, t = k & 1;
          wv[t][(ph - 2) * 4 + i] = wrd(lds + ((i < 4 ? (i + wave) & 3 : 4) * 64 + lrow) * 288 + (64 * t + 16 * wave) * 2 + (l15 & 3) * 8, 288, 32);
        }
        SB();
#pragma unroll
        for (int k = 0; k < (ph == 2 ? 8 : 10); ++k) { if (k < 8) pin_a(wv[k & 1][(ph - 2) * 4 + (k >> 1)]); else pin_v(wv[k & 1][8]); }
      }
      BAR();
    }
  }
  RT(1);
  const int cls = get_cls(R, lane, cv0);
  if (cls < 0) return;
  RT(2);
  auto run = [&](auto ROLEC) {
  constexpr int ROLE = decltype(ROLEC)::value;
  lds_char* img = lds;
  const unsigned fo = (unsigned)(cls & 1) * 8192u;
  const int r0 = ROLE * 1024 + lane * 16, r1 = r0 + 4096, r2 = 8192 + lane * 16;
  const unsigned m0 = nearoff(r0) + fo, m1 = nearoff(r1) + fo, m2 = nearoff(r2) + fo;
  const unsigned lb = (unsigned)(uintptr_t)lds, lw0 = lb + ROLE * 1024;
  const i32x4 D = mkdesc(a.wb);
  const int ar = g * 256 + ((l15 >> 1) & 1) * 128 + (2 * (l15 >> 2) + (l15 & 1)) * 16;
  lds_char* ard0 = img + ROLE * 1024 + ar;
  lds_char* ard1 = img + ((ROLE + 1) & 3) * 1024 + ar;
  lds_char* ard2 = img + ((ROLE + 2) & 3) * 1024 + ar;
  lds_char* ard3 = img + ((ROLE + 3) & 3) * 1024 + ar;
  const int wz = h >> 3, tq = (h >> 1) & 3, hp = h & 1;
  const int gh = ((tq & 1) << 1) | (tq >> 1);
  unsigned yr[5], yz[5];
#pragma unroll
  for (int q = 0; q < 5; ++q) {
    const int m = ROLE * 9 + q * 2 + (q < 4 ? g & 1 : 0), o = l15 * 16 + (q < 4 ? (g >> 1) * 8 : g * 4);
    yr[q] = ywin(cls, m) + (unsigned)((wz * 2 + hp) * 1024 + tq * 256 + o);
    yz[q] = NOYC ? OOB : OFF_ZF + (unsigned)(m * 40960 + (32 + wz * 2 + hp) * 1024 + gh * 256 + o);
  }
  lds_char* vw = lds + VIM_OFF + (ROLE * 16 + l15) * 16 + g * 4;
  const unsigned ra = lb + VIM_OFF + (8 * g + 4 * (g >> 1) + (l15 >> 2)) * 16 + (l15 & 2) * 4, rh = ra + (l15 & 1) * 512;
  u32x16 cst = {(unsigned)(uintptr_t)vw, ra, ra ^ 64, rh, rh ^ 64, lb + FLG_OFF + ROLE * 4, lb + FLG_OFF + (lane & 3) * 4, (lane & 4) ? 0xEEEEu : 0x4444u,
                yr[0], yr[1], yr[2], yr[3], yr[4], 0u, 0u, 0u};
  ds_flag(lb + FLG_OFF + ROLE * 4, 0u);
  const unsigned own = own_cpy(cls, h >> 3);
  unsigned ima = lb + lane * 16;
  pin_v(ima);
  const int idx = (lane & 2) ? 0xEEEE : 0x4444;
  unsigned tm = 0, rcnt = 0, cv = 0, iso_acc = 0, o8 = 0, bud = SPINCAP;
  u32x8 o = {0u, 0u, 0u, 0u, 0u, 0u, 0u, 0u};
  auto y_copy = [&](int s) {
#pragma unroll
    for (int q = 0; q < 4; ++q) st64((u32x2){o[2 * q], o[2 * q + 1]}, R, yz[q], (unsigned)s * ZS);
    st32(o8, R, yz[4], (unsigned)s * ZS);
  };
  auto poison = [&]() { asm volatile("v_mov_b32 v203, -1\n\tds_write2st64_b32 " cVW ", v203, v203 offset1:4" :: "{v[112:127]}"(cst) : "memory", "v203"); };
  const unsigned cwo = FX(FL_AN, cls, 0) + ((h + 8) & 15) * 128;
  EVDECL;
  auto step = [&](auto PHC, int s) {
    constexpr int PH = decltype(PHC)::value & 1, FIRST = (decltype(PHC)::value >> 1) & 1, LAST = decltype(PHC)::value >> 2;
    u64 e0 = 0, e1 = 0, e3 = 0, e4 = 0, e5 = 0, e6 = 0;
    u32x4 af[9];
    f32x4 v0, v1, yc;
    f32x16 ya;
    u32x16 A;
    unsigned nsl = 0;
    PADS(232 + ROLE * 8 + decltype(PHC)::value);
    unsigned f3 = s + 1;
    pin_v(f3);
    asm volatile("" : "+{v[112:127]}"(cst));
    PAD();
    unsigned nt = 0;
    tm = __builtin_amdgcn_readfirstlane(tm); rcnt = __builtin_amdgcn_readfirstlane(rcnt);
    asm volatile("" : "+s"(tm), "+s"(rcnt));
#define MV(t, j) do { smo<t, (j) < 8>(v##t, af[j], wv[t][j], idx); SB(); } while (0)
    unsigned ms = ((((s & 7) * SLOT_REAL) ^ CPY2) + own) ^ ((s & 4) ? 0 : CPY1);
    constexpr int ID = FIRST ? -1 : 32 + ROLE * 8 + decltype(PHC)::value;
    auto head = [&]() {
      y_zero(ya, yc);
      v_zero(v0, v1);
      if (!FIRST) {
        int r = 0;
        if (ROLE == 1) r = spin_late(tm, DVQ);
        mem_issue2<ROLE == F8W ? 3 : 2>(D, m0, m1, m2, ms, lw0, lw0 + 4096, lb + F8_OFF, e3);
        if (ROLE == 1) { move_on(tm, r); SB(); }
        poison();
        PAD();
        if (LAST || nt != 0) VMW(0);
        else {
          __builtin_amdgcn_s_sleep(XSLP);
          y_copy(s - 1);
          CBAR();
          asm volatile("s_waitcnt vmcnt(%0)" :: "n"(5 + (TWO ? 1 + (ROLE == F8W) : 0)) : "memory");
        }
      } else {
        *(lds_u32x4*)(img + r0) = af0[0]; *(lds_u32x4*)(img + r1) = af0[1]; *(lds_u32x4*)(lds + F8_OFF + lane * 16) = af0[2];
        poison();
        asm volatile("s_waitcnt lgkmcnt(0)" ::: "memory");
      }
      SB();
      EV(e6);
      HEAD(MV, 5, HV);
      ISOQ(11);
      v_end<ID>(af, wv[1], idx, cst, v0, v1, A, wo, ya, e0); SB();
    };
    if (!FIRST) {
      tm -= GRID + XT; nt = ~0u;
      try_top<ID>(nt, ms, tm, rcnt, ima); SB();
      head();
      try_end(ima, nt, ms, tm, rcnt, m0, m1, m2, lw0, lw0 + 4096, lb + F8_OFF, D); SB();
    } else
    head();
    if (LAST) { st32((unsigned)(s - 1), R, lane == 0 ? FX(FL_YPROG, cls, 2) + h * 16 + ROLE * 4 : OOB, 0); SB(); }
    EV(e4); ISOQ(13);
    y_all<!LAST, LAST ? -1 : ID>(wo, w8, cst, ya, yc, A, D, (unsigned)(s & 1) * YSLOT_REAL, f3, o, o8, e5, nsl); SB();
    ISOQ(15);
    PAD();
#undef MV
    if (FIRST) {
      VMW(0); BAR();
      st32(0u, R, tid == 0 ? FX(FL_WP, cls, 0) + (32 + h) * 32 : OOB, 0);
    }
    if (LAST || (!PH && !FIRST && UNLIKELY(s == NSEG - 2))) y_copy(s);
    CBAR();
    if (ROLE == 1 && !LAST) {
      if (FIRST) { tm = get_t1(R, cwo, bud); asm volatile("" : "+s"(tm)); }
      else
      next_time(tm, s);
    }
    if (PH && !LAST && UNLIKELY(s == WLAST)) WALK(232 + ROLE * 8 + 5);
    PADE();
    EVPUT(0, e0); EVPUT(6, e6); EVPUT(4, e4);
    EVPUTI(1, nt); EVPUT(2, e1); if (!FIRST) EVPUT(3, e3);
  };
  step(std::integral_constant<int, 2>(), 0);
#pragma nounroll
  for (int s = 1; s < NSEG - 1; s += 2) {
    step(std::integral_constant<int, 1>(), s);
    step(std::integral_constant<int, 0>(), s + 1);
  }
  step(std::integral_constant<int, 5>(), NSEG - 1);
  VMW(0);
  st32((unsigned)(NSEG - 1), R, lane == 0 ? FX(FL_YPROG, cls, 2) + h * 16 + ROLE * 4 : OOB, 0);
  EVDUMP();
  };
  if (wave == 0) run(std::integral_constant<int, 0>());
  else if (wave == 1) run(std::integral_constant<int, 1>());
  else if (wave == 2) run(std::integral_constant<int, 2>());
  else run(std::integral_constant<int, 3>());
  RT(7);
}

#define NG 4
#define UL(k, o) "buffer_load_dwordx4 v[128+4*" #k ":128+4*" #k "+3], %[u" #o ", %[D], %[su] offen offset:(" #k "&3)*1024 sc1\n\t"
#define UL4(c, k0, k1, k2, k3, o) ".if " #k0 c "4*" XSTR(NG) "\n\t" UL(k0, o) UL(k1, o) UL(k2, o) UL(k3, o) ".endif\n\t"
#define ULD(c) UL4(c, 0, 1, 2, 3, 0]) UL4(c, 4, 5, 6, 7, 1]) UL4(c, 8, 9, 10, 11, 2]) UL4(c, 12, 13, 14, 15, 3])   \
               UL4(c, 16, 17, 18, 19, 4]) UL4(c, 20, 21, 22, 23, 5]) UL4(c, 24, 25, 26, 27, 6]) UL4(c, 28, 29, 30, 31, 7])
#define UG(G, k0, k1, k2, k3, o) "s_bfe_u32 %[k], %[r], 0x40000+4*" #G "\n\ts_cmp_lg_u32 %[k], 15\n\ts_cbranch_scc1 23f\n\t" \
                                 UL(k0, o) UL(k1, o) UL(k2, o) UL(k3, o) "s_or_b32 %[sa], %[sa], 15<<(4*" #G ")\n23:\n\t"
#define USL 2
#define ULD0 ULD(" < ")
#define ULD1 ULD(" >= ")
#define TB 12
#define UM(T, lo, hi, n) ".if " #T " >= " lo " && " #T " < " hi "\n\t.if !(" #T " & 1)\n\ts_waitcnt vmcnt(" n "+28-2*" #T ")\n\t.endif\n\t"   \
                         "v_swap_b32 v[129+8*" #T "], v[132+8*" #T "]\n\tv_swap_b32 v[131+8*" #T "], v[134+8*" #T "]\n\t.endif\n\t"
#define UMS(...) UM(0, __VA_ARGS__) UM(1, __VA_ARGS__) UM(2, __VA_ARGS__) UM(3, __VA_ARGS__) UM(4, __VA_ARGS__) UM(5, __VA_ARGS__) UM(6, __VA_ARGS__) UM(7, __VA_ARGS__) \
                 UM(8, __VA_ARGS__) UM(9, __VA_ARGS__) UM(10, __VA_ARGS__) UM(11, __VA_ARGS__) UM(12, __VA_ARGS__) UM(13, __VA_ARGS__) UM(14, __VA_ARGS__) UM(15, __VA_ARGS__)
#define TIMES "s_and_b32 %[tt], %[lev], 255\n\ts_add_u32 %[tn], %[tq], %[tt]\n\ts_add_u32 %[tn], %[tn], %[t0]\n\t" \
              "s_lshr_b32 %[tt], %[lev], 8\n\ts_add_u32 %[tt], %[tt], %[tq]\n\t"
#define DZERO ".rept 32\n\tv_mov_b64 v[64+2*\\+:65+2*\\+], 0\n\t.endr\n\tv_mov_b64 v[60:61], 0\n\tv_mov_b64 v[62:63], 0\n\t"
#define WP(a, b) #a ":\n\ts_cbranch_execz " #b "\n\t"
#define DZ6(k) ".rept 6\n\tv_mov_b64 v[60+12*" #k "+2*\\+:61+12*" #k "+2*\\+], 0\n\t.endr\n\t"
#define YLD(k, o) "buffer_load_dwordx4 a[64+4*" #k ":64+4*" #k "+3], %[y" #o ", %[D], %[sy] offen offset:(" #k "&3)*1024 sc1\n\t"
#define YPO(k, o) "buffer_store_dwordx4 %[one], %[y" #o ", %[D], %[sy] offen offset:(" #k "&3)*1024 sc1\n\t"
#define ELD(T, o) "buffer_load_dwordx4 a[4*" #T ":4*" #T "+3], %[e" #o ", %[D], %[sq] offen offset:(" #T "&3)*1024 sc1\n\t"
#define PLD(G) "buffer_load_dwordx4 a[96+4*" #G ":96+4*" #G "+3], %[pm], %[D], %[sq] offen offset:" #G "*1024 sc1\n\t"
#define ALD(G) "buffer_load_dwordx4 a[112+4*" #G ":112+4*" #G "+3], %[al], %[D], %[sq] offen offset:" #G "*256 sc1\n\t"
#define VW(X, n) "s_waitcnt vmcnt(1+" n ")\n\t"
#define EM(T) "v_smfmac_f32_16x16x64_bf16 v[64+4*" T ":64+4*" T "+3], a[4*" T ":4*" T "+3], v[128+8*" T ":128+8*" T "+7], %[ie]\n\t"
#define WC0(T) "v_cvt_pk_bf16_f32 v[64+2*" #T "], v[64+4*" #T "], v[64+4*" #T "+1]\n\t"
#define WC1(T) "v_cvt_pk_bf16_f32 v[65+2*" #T "], v[64+4*" #T "+2], v[64+4*" #T "+3]\n\t"
#define WCV(T) WC0(T) WC1(T)
#define EM2_0(T, C, o) VW("0", "30-" #T) EM(#T) WC0(C) EM("(" #T "+1)") WC1(C)

#define PMM(X, G) VW(X, "7-" #G) "v_smfmac_f32_16x16x64_bf16 v[60:63], a[96+4*" #G ":96+4*" #G "+3], a[64+8*" #G ":64+8*" #G "+7], %[ip]\n\t"
#define AMM(X, G) VW(X, "3-" #G) "v_smfmac_f32_16x16x64_bf16 v[60:63], a[112+4*" #G ":112+4*" #G "+3], v[64+8*" #G ":64+8*" #G "+7], %[ia]\n\t"
#define TRY(X, EM2, a, b, c, d)                                                                                                       \
      SPIN(tt, a, b, c) MEV(1) MCY(0)                                                                                                \
      "buffer_load_dword v59, %[es], %[D], %[sq] offen sc1\n\t" ELD(0, 0]) ELD(1, 0]) ELD(2, 0]) ELD(3, 0]) ELD(4, 1]) ELD(5, 1]) ELD(6, 1]) ELD(7, 1])                                        \
      ELD(8, 2]) ELD(9, 2]) ELD(10, 2]) ELD(11, 2]) ELD(12, 3]) ELD(13, 3]) ELD(14, 3]) ELD(15, 3])                                  \
      YLD(0, 0]) YLD(1, 0]) YLD(2, 0]) YLD(3, 0]) YLD(4, 1]) YLD(5, 1]) YLD(6, 1]) YLD(7, 1])                                        \
      PLD(0) PLD(1) PLD(2) PLD(3) ALD(0) ALD(1) ALD(2) ALD(3) MCY(1) "buffer_load_dword %[ub], %[uq], %[D], 0 offen sc1\n\ts_cmp_lg_u32 %[sx], 0\n\ts_cbranch_scc1 77f\n\t" UMS(XSTR(TB), "16", "43") "s_mov_b32 %[sx], 1\n77:\n\t"                                                                 \
      VW("0", "32") "v_cmp_eq_u32 vcc, -1, v59\n\t" VW("0", "30") "s_cbranch_vccnz 61f\n62:\n\t" EM("0") MCY(2) EM("1")                                                                                           \
      EM2(2, 0, 0]) EM2(4, 1, 0]) EM2(6, 2, 0]) EM2(8, 3, 0]) EM2(10, 4, 1]) EM2(12, 5, 1]) EM2(14, 6, 1]) MCY(3)                           \
      "s_cmp_lt_i32 %[r], -" XSTR(LTOL) "\n\ts_cbranch_scc0 " d "f\n\t"                              \
      "63:\n\ts_add_u32 %[tq], %[tq], " XSTR(GRID) "\n\ts_add_u32 %[t0], %[t0], " XSTR(XT) "\n\ts_add_u32 %[tn], %[tn], " XSTR(GRID) "+" XSTR(XT) "\n\ts_add_u32 %[r], %[r], " XSTR(GRID) "\n\t"   \
      "s_cmp_lt_i32 %[r], -" XSTR(LTOL) "\n\ts_cbranch_scc1 63b\n\t" MCA(1000)                                                        \
      d ":\n\t" WCV(7) WCV(8) WCV(9) WCV(10) WCV(11) WCV(12) WCV(13) WCV(14) WCV(15)                                                 \
      PMM(X, 0) MCY(4) PMM(X, 1) PMM(X, 2) PMM(X, 3) AMM(X, 0) AMM(X, 1) AMM(X, 2) AMM(X, 3) MCY(5)                                  \
      PADN "s_nop 6\n\t"                                                                                                             \
      "v_pk_add_f32 v[60:61], v[60:61], v[62:63]\n\t"                                                                                \
      "v_cvt_pk_bf16_f32 v62, v60, v61\n\tv_cmp_u_f32 vcc, v60, v61\n\ts_nop 0\n\t"                                                  \
      "v_mov_b32_dpp v63, v62 quad_perm:[1,0,3,2] row_mask:0xf bank_mask:0xf\n\t"                                                    \
      "v_perm_b32 v61, v63, v62, %[sel]\n\ts_cbranch_vccnz 50f\n\t"
#define MEV(n)
#define MEVPUT
#define MCY(n)
#define MCYPUT
#define MCYO
#define MCN
#define MCA(k)
#define FME
#define FML
#define FMZ
#define VR8(p, b) p #b "0", p #b "1", p #b "2", p #b "3", p #b "4", p #b "5", p #b "6", p #b "7", p #b "8", p #b "9"
__device__ __forceinline__ void role_mix(const Args& a, rsrc_t R, const Go& go, int m, int wave, int lane) {
  if (wave >= 2) {
    unsigned t[4];
#pragma unroll
    for (int k = 0; k < 4; ++k) {
      const int u = ((m * 2 + wave - 2) * 4 + k) * 64 + lane;
      t[k] = ld32(R, u < 8192 ? (unsigned)u * 128u : (u < 12800 ? OFF_YR + (unsigned)(u - 8192) * 128u : (u < 17408 ? OFF_YR + YSLOT_REAL + (unsigned)(u - 12800) * 128u : OOB)), 0);
    }
    asm volatile("" :: "v"(t[0]), "v"(t[1]), "v"(t[2]), "v"(t[3]));
  }
  if (wave != 0 && (wave != 2 || m != 0)) return;
  const int l15 = lane & 15, g = lane >> 4;
  if (wave == 0) {
#pragma unroll
    for (int k = 0; k < 32; ++k) st128((u32x4){~0u, ~0u, ~0u, ~0u}, R, OFF_ZF + (unsigned)(m * 40960 + lane * 16), k * 1024);
  }
  if (!go_wait(R, go, lane)) return;
  VMW(0);
  st32(0u, R, lane == 0 && wave == 0 ? (unsigned)(FL_MPOI + m * 4) : OOB, 0);
  RT(3);
  if (wave == 2) {
    for (int k = 0; k < CSL; ++k) __builtin_amdgcn_s_sleep(32);
    const int cls = get_cls(R, lane);
    for (int k = CSL; k < TSL; ++k) __builtin_amdgcn_s_sleep(32);
    for (unsigned spins = 0; spins < SPINCAP; ++spins) {
      CBAR();
      if (__all((int)(ld32(R, FX(FL_MRDY, cls, 0) + min(lane, 35) * 4, 0) | ld32(R, FX(FL_WP, cls, 0) + lane * 32, 0)) >= 0)) break;
      __builtin_amdgcn_s_sleep(1);
    }
    st32((rt_now() + T1D) & ~1u, R, lane < 16 ? FX(FL_AN, cls, 0) + lane * 128 : OOB, 0);
    return;
  }
  for (int k = 0; k < CSL; ++k) __builtin_amdgcn_s_sleep(32);
  const int cls = get_cls(R, lane);
  if (cls < 0) return;
  RT(4);
  const unsigned fo = (unsigned)(cls & 1) * 8192u, y0 = ywin(cls, m) + lane * 16;
  const u32x4 one = {~0u, ~0u, ~0u, ~0u};
  const unsigned mc = (unsigned)nearoff(m * 256 + l15 * 16) + fo + (g & 1) * CPY1 + (g >> 1) * CPY2;
#pragma unroll
  for (int k = 0; k < 8; ++k) st128(one, R, y0 + k * 1024, YSLOT_REAL);
  st128(one, R, mc, SLOT_REAL);
  RT(1);
  unsigned uo[8], eo[4];
#pragma unroll
  for (int k = 0; k < 8; ++k) uo[k] = OFF_ZF + (unsigned)(m * 40960 + k * 4096 + lane * 16);
#pragma unroll
  for (int k = 0; k < 4; ++k) {
    eo[k] = (unsigned)nearoff(NEAR_E + k * 4096 + lane * 16) + fo;
  }
  const unsigned pm = (unsigned)nearoff(NEAR_PM + lane * 16) + fo;
  const int qi = 2 * (l15 >> 2) + (l15 & 1);
  const unsigned al = !(l15 & 2) && (g & 1) == (qi >> 2) ? (unsigned)nearoff(NEAR_AL + ((g >> 1) * 8 + qi) * 16) + fo : OOB;
  const unsigned mo = (unsigned)nearoff((2 * m + (l15 >> 3)) * 128 + (2 * g + (l15 & 1)) * 16 + ((l15 & 7) >> 1) * 4) + fo;
  const unsigned sm = lane < 36 ? (unsigned)nearoff(lane * 256) + fo : OOB, own = own_cpy(cls, m / 9);
  const unsigned an = FX(FL_AN, cls, 0) + (m & 15) * 128, wp = FX(FL_WP, cls, 0) + lane * 32;
  const unsigned ie = (lane & 4) ? 0xEEEE : 0x4444, ip = (lane & 2) ? 0xEEEE : 0x4444, ia = ie;
  const unsigned sel = (lane & 1) ? 0x03020706u : 0x05040100u;
  const i32x4 D = mkdesc(a.wb);
  const unsigned uf = lane < 32 ? FX(FL_UPROG, cls, 4) + lane * UH + (m / 9) * UW : OOB;
  for (int k = 0; k < MSL; ++k) __builtin_amdgcn_s_sleep(16);
  RT(2);
  unsigned tq = 0, lev = PACE0, bud = SPINCAP, s = 0, su = 0, sy = 0, sp = 0, tt, sa, sb, sc, sn, tn, ln, ue, t0, sx, sq, cw, sv, ua = ~0u, ub, uq, nr = 0, cv = 0;
  int ufl = lane < 32 ? -1 : 0x7fffffff;
  unsigned ox = lane < 32 ? OOB : an;
  unsigned ev0 = 0, ev1 = 0, ev2 = 0, ev3 = 0, ec[7] = {0, 0, 0, 0, 0, 0, 0};
  int r, k;
  asm volatile(
      "s_mov_b64 exec, 0\n\ts_branch 200f\n299:\n\ts_mov_b64 exec, -1\n\t"
      "s_mov_b32 %[sa], 0\n\ts_mov_b32 %[sb], " XSTR(D0) "\n"
      "27:\n\tbuffer_load_dword v59, %[up], %[D], 0 offen sc1\n\tbuffer_load_dword %[cw], %[wp], %[D], 0 offen sc1\n\t"
      "s_mov_b32 %[tq], %[sb]\n\ts_waitcnt vmcnt(0)\n\tv_cmp_ne_u32 vcc, -1, v59\n\ts_andn2_b32 %[r], vcc_lo, %[sa]\n\ts_cbranch_scc0 26f\n\t"
      UG(0, 0, 1, 2, 3, 0]) UG(1, 4, 5, 6, 7, 1]) UG(2, 8, 9, 10, 11, 2]) UG(3, 12, 13, 14, 15, 3])
      UG(4, 16, 17, 18, 19, 4]) UG(5, 20, 21, 22, 23, 5]) UG(6, 24, 25, 26, 27, 6]) UG(7, 28, 29, 30, 31, 7])
      "s_cmp_lg_u32 %[sa], -1\n\ts_cbranch_scc1 26f\n\tv_mov_b32 v59, 0\n\tbuffer_store_dword v59, %[rdy], %[D], 0 offen sc1\n"
      "26:\n\tv_cmp_gt_i32 vcc, 0, %[cw]\n\ts_cbranch_vccnz 25f\n\ts_mov_b32 %[sb], 0\n\ts_cmp_lg_u32 %[sa], -1\n\ts_cbranch_scc0 24f\n"
      "25:\n\ts_sleep " XSTR(USL) "\n\t" BUD("27b")
      "24:\n\ts_memrealtime vcc\n\ts_waitcnt lgkmcnt(0)\n\ts_add_u32 %[tq], %[tq], vcc_lo\n\ts_sub_u32 %[tq], %[tq], " XSTR(DE0) "\n\ts_branch 28f\n"
      "29:\n\t" ULD0 ULD1 "28:\n\ts_mov_b32 %[t0], 0\n\ts_mov_b32 %[sb], 0\n\ts_mov_b32 %[ue], 2*" XSTR(SLOT_REAL) "\n\ts_waitcnt vmcnt(0)\n\t"
      "30:\n\t" FMZ DZERO
      "s_add_u32 %[su], %[s], 1\n\ts_and_b32 %[su], %[su], " XSTR(RMASK) "\n\ts_mul_i32 %[su], %[su], 36*40960\n\t"
      "s_add_u32 %[sn], %[sp], " XSTR(SLOT_REAL) "\n\ts_and_b32 %[sn], %[sn], 7*" XSTR(SLOT_REAL) "\n\t"
      "s_lshr_b32 %[sq], %[sp], 5\n\ts_and_b32 %[sq], %[sq], " XSTR(CPY1) "\n\ts_xor_b32 %[sq], %[sq], %[own]\n\ts_add_u32 %[sq], %[sq], %[sp]\n\t"
      "s_movk_i32 %[ln], " XSTR(PACEC) "\n\ts_cmp_lt_u32 %[s], " XSTR(S2) "-1\n\ts_cmovk_i32 %[ln], " XSTR(PACEB) "\n\t"
      "s_cmp_lt_u32 %[s], " XSTR(S1) "-1\n\ts_cmovk_i32 %[ln], " XSTR(PACEA) "\n\ts_cmp_lt_u32 %[s], " XSTR(S0) "-1\n\ts_cmovk_i32 %[ln], " XSTR(PACE0) "\n\t"
      "s_add_u32 %[sc], %[sn], " XSTR(CPY1) "\n\ts_xor_b32 %[sb], %[sn], " XSTR(CPY2) "\n\ts_mov_b32 %[sx], 0\n\t" TIMES
      UMS("0", "2*" XSTR(NG), "8") "s_cmp_lg_u32 %[s], 0\n\t"
      "buffer_store_dwordx4 %[one], %[mc], %[D], %[ue] offen sc1\n\tv_max_i32 %[ufl], %[ufl], %[ua]\n\ts_cbranch_scc0 33f\n\t"
      "v_cmp_eq_u32 vcc, -1, %[sv]\n\ts_cbranch_vccnz 35f\n"
      "33:\n\tv_cmp_ge_i32 vcc, %[s], %[ufl]\n\ts_nop 0\n\tv_cndmask_b32 %[uq], %[ox], %[uf], vcc\n\t"
      UMS("2*" XSTR(NG), XSTR(TB), "9") MEV(3)
      "75:\n\t" TRY("0", EM2_0, "40", "41", "42", "43")
      "51:\n\tbuffer_store_dword v61, %[mo], %[D], %[sn] offen sc1\n\tbuffer_store_dword v61, %[mo], %[D], %[sc] offen sc1\n\ts_memrealtime vcc\n\t" MEV(2) MCY(6)
      "buffer_store_dword v61, %[mo], %[D], %[sb] offen sc1\n\ts_add_u32 %[sb], %[sb], " XSTR(CPY1) "\n\tbuffer_store_dword v61, %[mo], %[D], %[sb] offen sc1\n\t"
      "s_waitcnt vmcnt(4) lgkmcnt(0)\n\ts_add_u32 %[k], vcc_lo, " XSTR(MGAP) "\n"
      "97:\n\ts_sub_u32 %[r], %[tn], %[k]\n\ts_cmp_lt_i32 %[r], 0\n\ts_cbranch_scc0 98f\n\ts_add_u32 %[tn], %[tn], " XSTR(GRID) "+" XSTR(XT) "\n\ts_branch 97b\n"
      "98:\n\t" MEVPUT MCYPUT MCN
      "s_add_u32 %[s], %[s], 1\n\ts_cmp_ge_u32 %[s], 63\n\ts_cbranch_scc1 60f\n\t"
      "v_max_i32 %[ufl], %[ufl], %[ub]\n\tv_cmp_gt_i32 vcc, %[s], %[ufl]\n\ts_mov_b32 %[sx], 1\n\ts_cbranch_vccnz 100f\n\t"
      "s_mov_b32 %[sx], 0\n\t" ULD0 "100:\n\t"
      "s_cmp_lg_u32 %[s], 1\n\ts_cbranch_scc1 67f\n\t"
      "v_readlane_b32 %[tn], %[ub], 32\n\tv_mov_b32 %[ox], " XSTR(OOB) "\n\ts_cmp_lg_u32 %[tn], -1\n\ts_cbranch_scc1 67f\n"
      "66:\n\tbuffer_load_dword %[cw], %[an], %[D], 0 offen sc1\n\ts_waitcnt vmcnt(0)\n\tv_readfirstlane_b32 %[tn], %[cw]\n\t"
      "s_cmp_lg_u32 %[tn], -1\n\ts_cbranch_scc1 67f\n\t" BUD("66b")
      "67:\n\t"
      "s_xor_b32 %[ue], %[sn], 2*" XSTR(SLOT_REAL) "\n\ts_and_b32 %[ue], %[ue], 3*" XSTR(SLOT_REAL) "\n\ts_mov_b32 %[sp], %[sn]\n\t"
      "s_mov_b32 %[tq], %[tn]\n\ts_mov_b32 %[t0], 0\n\ts_mov_b32 %[lev], %[ln]\n\t"
      "v_cmp_ge_i32 vcc, %[s], %[ufl]\n\tv_mov_b32 %[ua], " XSTR(OOB) "\n\tv_cndmask_b32 %[ua], %[ua], %[uf], vcc\n\t"
      "s_lshr_b32 %[sa], %[sn], 5\n\ts_and_b32 %[sa], %[sa], " XSTR(CPY1) "\n\ts_xor_b32 %[sa], %[sa], %[own]\n\ts_add_u32 %[sa], %[sa], %[sn]\n\t"
      SPIN(tq, "52", "53", "54") "buffer_load_dword %[sv], %[sm], %[D], %[sa] offen sc1\n\t"
      "buffer_load_dword %[ua], %[ua], %[D], 0 offen sc1\n\t"
      "s_cmp_eq_u32 %[sx], 0\n\ts_cbranch_scc1 101f\n\t"
      "55:\n\ts_waitcnt vmcnt(0)\n\tv_max_i32 %[ufl], %[ufl], %[ua]\n\tv_cmp_gt_i32 vcc, %[s], %[ufl]\n\ts_cbranch_vccz 57f\n\t" MCA(100000)
      "buffer_load_dword %[ua], %[uf], %[D], 0 offen sc1\n\t" BUD("55b")
      "57:\n\t" ULD0 "101:\n\t" ULD1 YPO(0, 0]) YPO(1, 0]) YPO(2, 0]) YPO(3, 0]) YPO(4, 1]) YPO(5, 1]) YPO(6, 1]) YPO(7, 1])
      "s_xor_b32 %[sy], %[sy], " XSTR(YSLOT_REAL) "\n\ts_branch 30b\n\t"
      WP(200, 201f) "35:\n\t" MCA(100) BUD("36f") "s_branch 32f\n\t"
      "36:\n\ts_add_u32 %[tq], %[tq], " XSTR(GRID) "+" XSTR(XT) "\n\ts_xor_b32 %[sa], %[sa], %[flp]\n\t" WP(201, 202f)
      SPIN(tq, "37", "38", "39") "buffer_load_dword %[sv], %[sm], %[D], %[sa] offen sc1\n\t" WP(202, 203f) "s_waitcnt vmcnt(0)\n\t"
      "v_cmp_eq_u32 vcc, -1, %[sv]\n\ts_cbranch_vccnz 35b\n"
      "32:\n\t" TIMES "s_branch 33b\n" WP(203, 204f)
      "59:\n\t" WP(204, 205f) "s_waitcnt lgkmcnt(0)\n\ts_lshr_b32 %[tt], %[lev], 8\n\ts_sub_u32 %[k], vcc_lo, %[tt]\n\ts_sub_u32 %[k], %[k], " XSTR(RTOL) "\n\t"
      "34:\n\ts_add_u32 %[tq], %[tq], " XSTR(GRID) "\n\ts_add_u32 %[t0], %[t0], " XSTR(XT) "\n\ts_sub_u32 %[r], %[tq], %[k]\n\ts_cmp_lt_i32 %[r], 0\n\ts_cbranch_scc1 34b\n\t"
      WP(205, 206f) TIMES "s_branch 75b\n\t" WP(206, 207f)
      "58:\n\ts_cmp_lg_u32 %[s], 0\n\ts_cbranch_scc1 102f\n\ts_mov_b32 %[su], 0\n\ts_branch 29b\n102:\n\ts_memrealtime vcc\n\ts_xor_b32 %[sq], %[sq], %[flp]\n\t"
      WP(207, 208f) DZ6(0) WP(208, 209f) DZ6(1) WP(209, 210f) DZ6(2) WP(210, 211f) DZ6(3) WP(211, 212f) DZ6(4) WP(212, 213f)
      ".rept 4\n\tv_mov_b64 v[120+2*\\+:121+2*\\+], 0\n\t.endr\n\ts_branch 59b\n\t" WP(213, 214f)
      "50:\n\t" FML MCA(1) BUD("58b") "v_min_u32 v61, -2, v61\n\ts_branch 51b\n\t" WP(214, 215f)
      "99:\n\ts_memrealtime vcc\n\ts_xor_b32 %[sq], %[sq], %[flp]\n\ts_branch 59b\n\t"
      "61:\n\t" FME MCA(1) BUD("99b") "s_branch 62b\n\t" WP(215, 299b)
      "60:\n\ts_waitcnt vmcnt(0)"
      : [tq] "+&s"(tq), [lev] "+&s"(lev), [bud] "+&s"(bud), [s] "+&s"(s), [su] "+&s"(su), [sy] "+&s"(sy), [sp] "+&s"(sp), [tt] "=&s"(tt), [sa] "=&s"(sa),
        [sn] "=&s"(sn), [sb] "=&s"(sb), [sc] "=&s"(sc), [tn] "=&s"(tn), [ln] "=&s"(ln), [ue] "=&s"(ue), [t0] "=&s"(t0), [sx] "=&s"(sx), [sq] "=&s"(sq), [cw] "=&v"(cw), [sv] "=&v"(sv), [ua] "+&v"(ua), [ub] "=&v"(ub), [uq] "=&v"(uq), [ufl] "+&v"(ufl), [ox] "+&v"(ox), [r] "=&s"(r), [k] "=&s"(k), [nr] "+&s"(nr), [cv] "+&v"(cv),
        [ev0] "+&v"(ev0), [ev1] "+&v"(ev1), [ev2] "+&v"(ev2), [ev3] "+&v"(ev3) MCYO
      : [D] "s"(D), [u0] "v"(uo[0]), [u1] "v"(uo[1]), [u2] "v"(uo[2]), [u3] "v"(uo[3]), [u4] "v"(uo[4]), [u5] "v"(uo[5]), [u6] "v"(uo[6]), [u7] "v"(uo[7]), [e0] "v"(eo[0]), [e1] "v"(eo[1]), [e2] "v"(eo[2]),
        [e3] "v"(eo[3]), [y0] "v"(y0), [y1] "v"(y0 + 4096), [pm] "v"(pm), [al] "v"(al), [mo] "v"(mo), [mc] "v"(mc), [an] "v"(an), [wp] "v"(wp),
        [ie] "v"(ie), [ip] "v"(ip), [ia] "v"(ia), [sel] "v"(sel), [one] "v"(one), [sm] "v"(sm), [uf] "v"(uf), [es] "v"(lane < 32 ? (unsigned)nearoff(NEAR_E + lane * 512 + (lane & 1) * (64 - 512)) + fo : OOB), [rdy] "v"(lane == 0 ? FX(FL_MRDY, cls, 0) + m * 4 : OOB), [own] "s"(own), [flp] "s"(CPY2 + CPY1),
        [up] "v"(OFF_ZF + (unsigned)(m * 40960 + (lane & 31) * 1024 + 1020))
      : "memory", "vcc", "scc", "s68", "s69", "s70", "s71", "s72", "s73", "s74", "s75", "s76", "s77", "s78", "s79", "s80", "s81", "s84", "s85", "s86", "s87", "s88", "s89", "s90", "s91",
        "v59", VR8("v", 6), VR8("v", 7), VR8("v", 8), VR8("v", 9), VR8("v", 10), VR8("v", 11), VR8("v", 12), VR8("v", 13), VR8("v", 14), VR8("v", 15),
        VR8("v", 16), VR8("v", 17), VR8("v", 18), VR8("v", 19), VR8("v", 20), VR8("v", 21), VR8("v", 22), VR8("v", 23), VR8("v", 24),
        "v250", "v251", "v252", "v253", "v254", "v255",
        "a0", "a1", "a2", "a3", "a4", "a5", "a6", "a7", "a8", "a9", VR8("a", 1), VR8("a", 2), VR8("a", 3), VR8("a", 4), VR8("a", 5), VR8("a", 6),
        VR8("a", 7), VR8("a", 8), VR8("a", 9), VR8("a", 10), VR8("a", 11), VR8("a", 12), VR8("a", 13), VR8("a", 14), VR8("a", 15),
        VR8("a", 16), VR8("a", 17), VR8("a", 18), VR8("a", 19), VR8("a", 20), VR8("a", 21), VR8("a", 22), VR8("a", 23), VR8("a", 24),
        "a250", "a251", "a252", "a253", "a254", "a255");
  RT(7);
}

struct XL { rsrc_t X; i32x4 D; unsigned vo; int wr, rd; };
struct XP { lds_char* p[2]; };
__device__ __forceinline__ void x_init(XL& xl, const u16* x, int wave, int lane) {
  xl.X = __builtin_amdgcn_make_buffer_rsrc((void*)x, 0, S_LEN * DM * 2, 0x00020000);
  xl.D = (i32x4){(int)(unsigned)(u64)x, (int)(unsigned)((u64)x >> 32), S_LEN * DM * 2, 0x00020000};
  xl.vo = (unsigned)((wave * 8 + (lane >> 3)) * 1152 + ((lane ^ (lane >> 3)) & 7) * 16);
  xl.wr = wave * 9216 + lane * 16;
  xl.rd = ((lane >> 3) & 1) * 9216 + (lane & 7) * 128 + ((lane >> 4) ^ (lane & 7)) * 16;
}
__device__ __forceinline__ XP x_at(lds_char* buf, const XL& xl) {
  XP r = {{buf + xl.rd, buf + (xl.rd ^ 64)}};
  pin_v(r.p[0]); pin_v(r.p[1]);
  return r;
}
__device__ __forceinline__ void x_m0(unsigned b, int k) { asm volatile("s_add_u32 m0, %0, %1" :: "s"(b), "n"(k * 896) : "scc"); }
__device__ __forceinline__ void x_dma(const XL& xl, unsigned so, int k) {
  asm volatile("buffer_load_dwordx4 %0, %1, %2 offen offset:%3 lds" :: "v"(xl.vo), "s"(xl.D), "s"(so), "n"(k * 128) : "memory");
}
__device__ __forceinline__ void x_dma0(const XL& xl, unsigned so, int k) {
  asm volatile("s_nop 0\n\tbuffer_load_dwordx4 %0, %1, %2 offen offset:%3 " XNT " lds" :: "v"(xl.vo), "s"(xl.D), "s"(so), "n"(k * 128) : "memory");
}
__device__ __forceinline__ i32x4 wdesc(const u16* p) { return (i32x4){(int)(unsigned)(u64)p, (int)(unsigned)((u64)p >> 32), DM * HD * 2, 0x00020000}; }
__device__ __forceinline__ void w_dma(i32x4 D, unsigned vo, unsigned so, unsigned b, int o) {
  asm volatile("s_add_u32 m0, %3, %4\n\ts_nop 0\n\tbuffer_load_dwordx4 %0, %1, %2 offen nt lds" :: "v"(vo), "s"(D), "s"(so), "s"(b), "n"(o) : "memory", "scc");
}
#define VB_OFF 73728
#define VX_OFF (VB_OFF + 73728)
constexpr int vbuf(int j) { return j % 5 == 3 ? VX_OFF : VB_OFF + ((j + 1) % 5) * 18432; }
__device__ __forceinline__ void x_move(lds_char*& p, unsigned d) { asm volatile("v_add_u32 %0, %1, %0" : "+v"(p) : "s"(d)); }
__device__ __forceinline__ u32x4 x_ld1(const XL& xl, int s, int k) { return __builtin_amdgcn_raw_buffer_load_b128(xl.X, xl.vo + k * 128, (unsigned)s * 36864u, 0); }
__device__ __forceinline__ void x_gload(u32x4 (&xr)[9], const XL& xl, int s) {
#pragma unroll
  for (int k = 0; k < 9; ++k) xr[k] = x_ld1(xl, s, k);
}
__device__ __forceinline__ void x_swrite(const u32x4 (&xr)[9], lds_char* buf, const XL& xl) {
#pragma unroll
  for (int k = 0; k < 9; ++k) *(lds_u32x4*)(buf + xl.wr + k * 1024) = xr[k];
}
#define XF(buf, tt, ks) (*(lds_u32x4*)((buf).p[(ks) & 1] + (tt) * 18432 + ((ks) >> 1) * 1024))
__device__ __forceinline__ void proj4(lds_char* cbuf, lds_char* nbuf, u32x4 (&xr)[9], const XL& xl, int snext, int wave, int lane,
                                      const u32x4 (&w0)[18], const u32x4 (&w1)[18], const u32x4 (&w2)[18], const u32x4 (&w3)[18],
                                      f32x4 (&acc)[4][2]) {
  u32x4 bb[3][2];
  const XP buf = x_at(cbuf, xl);
  nbuf += xl.wr;
#pragma unroll
  for (int ks = 0; ks < 2; ++ks) { bb[ks][0] = XF(buf, 0, ks); bb[ks][1] = XF(buf, 1, ks); }
#pragma unroll
  for (int t = 0; t < 4; ++t) { acc[t][0] = (f32x4){0.f, 0.f, 0.f, 0.f}; acc[t][1] = acc[t][0]; }
#pragma unroll
  for (int ks = 0; ks < 18; ++ks) {
    const u32x4 b0 = bb[ks % 3][0], b1 = bb[ks % 3][1];
    SB();
    acc[0][0] = mfma(w0[ks], b0, acc[0][0]); SB();
    if (ks + 2 < 18) bb[(ks + 2) % 3][0] = XF(buf, 0, ks + 2);
    SB();
    acc[0][1] = mfma(w0[ks], b1, acc[0][1]); SB();
    if (ks + 2 < 18) bb[(ks + 2) % 3][1] = XF(buf, 1, ks + 2);
    SB();
    acc[1][0] = mfma(w1[ks], b0, acc[1][0]); SB();
    if ((ks & 1) == 0) *(lds_u32x4*)(nbuf + (ks >> 1) * 1024) = xr[ks >> 1];
    SB();
    acc[1][1] = mfma(w1[ks], b1, acc[1][1]);
    acc[2][0] = mfma(w2[ks], b0, acc[2][0]); SB();
    if ((ks & 1) == 0) xr[ks >> 1] = x_ld1(xl, snext, ks >> 1);
    SB();
    acc[2][1] = mfma(w2[ks], b1, acc[2][1]);
    acc[3][0] = mfma(w3[ks], b0, acc[3][0]); acc[3][1] = mfma(w3[ks], b1, acc[3][1]);
    SB();
  }
}
#define PFL 13
#define STG0 6
#define STGD 0
#define NOST 0
#define NOFL 0
#define FG 7
#define UFL 5
__device__ __forceinline__ int clampseg(int s) { return s < NSEG ? s : NSEG - 1; }

__device__ __forceinline__ void role_prj(const Args& a, rsrc_t R, const Go& go, lds_char* lds, const u16* W0, const u16* W1, unsigned o0, unsigned o1, unsigned flag,
                                         int tid, int wave, int lane) {
  const int l15 = lane & 15, g = lane >> 4;
  u32x4 w[2][2][18];
  u32x4 xr[9], xq[9];
  XL xl; x_init(xl, a.x, wave, lane);
  x_gload(xq, xl, 0);
  x_gload(xr, xl, 1);
  const unsigned fo = wave * 1024 + lane * 16;
  const unsigned pf = lane < 2 ? flag + lane * 8192 + wave * 4 : OOB;
  f32x4 acc[2][2][2];
  {
    u32x4 sg[PDEP][8];
    unsigned g1;
    const rsrc_t B0 = __builtin_amdgcn_make_buffer_rsrc((void*)W0, 0, DM * HD * 2, 0x00020000), B1 = __builtin_amdgcn_make_buffer_rsrc((void*)W1, 0, DM * HD * 2, 0x00020000);
    const unsigned wof = (unsigned)((tid >> 4) * (HD * 2) + (tid & 15) * 16);
    auto gl = [&](int i, int k) {
      if (i < 10 && (i % 5 < 4 || k < 4)) { sg[i % PDEP][k] = __builtin_amdgcn_raw_buffer_load_b128(i < 5 ? B0 : B1, wof, (NOCW & 4) ? 0u : (unsigned)(128 * (i % 5) + 16 * k) * (HD * 2), (NTM & 4) ? 2 : 0); SB(); }
    };
#pragma unroll
    for (int k = 0; k < 8 * PDEP; ++k) gl(k >> 3, k & 7);
    if (wave == 0) WALK(100); else if (wave == 1) WALK(101); else if (wave == 2) WALK(102); else WALK(103);
    RT(2);
    PADS(100);
    lds_char *x0p = lds + X0_OFF, *x1p = lds + X1_OFF;
    pin_v(x0p); pin_v(x1p);
    x_swrite(xq, x0p, xl);
    PAD();
    x_swrite(xr, x1p, xl);
    PAD();
    const XP x0r = x_at(x0p, xl), x1r = x_at(x1p, xl);
    lds_char *wp = lds + (tid >> 4) * 288 + (tid & 15) * 16, *rp = lds + (g * 8 + (l15 >> 2)) * 288 + wave * 64 + (l15 & 3) * 8;
    pin_v(wp); pin_v(rp);
    auto put = [&](int i, int k) {
      if (i < 10 && (i % 5 < 4 || k < 4)) { *(lds_u32x4*)(wp + (i & 1) * STG_BYTES + k * (16 * 288)) = sg[i % PDEP][k]; SB(); }
    };
#pragma unroll
    for (int k = 0; k < 8; ++k) { put(0, k); gl(PDEP, k); }
    auto chunk = [&](auto IC) {
      constexpr int i = decltype(IC)::value, m = i / 5, c = i % 5, nk = c < 4 ? 4 : 2;
      if (c == 0) {
#pragma unroll
        for (int k = 0; k < 8; ++k) { acc[k >> 2][(k >> 1) & 1][k & 1] = (f32x4){0.f, 0.f, 0.f, 0.f}; pin_v(acc[k >> 2][(k >> 1) & 1][k & 1]); }
      }
      if (i == 4) { PADE(); PADS(101); } else if (i == 8) { PADE(); PADS(102); } else PAD();
      BAR();
      if (i == 0) RT(3);
      if (i == 2) RT(4);
      if (i == 4) RT(5);
      if (i == 6) RT(6);
      if (i == 7) RT(8);
      if (i == 8) RT(9);
      u32x4 xb[2][4];
      u32x2 wh[2][4];
      auto rd = [&](int kk, int j) {
        if (kk >= nk) return;
        if (j & 1) xb[kk & 1][j >> 1] = XF((j & 4) ? x1r : x0r, (j >> 1) & 1, 4 * c + kk);
        else wh[kk & 1][j >> 1] = trd(rp + (i & 1) * STG_BYTES + (kk * 32 + (j & 2) * 2) * 288 + (j >> 2) * 32);
        SB();
      };
#pragma unroll
      for (int j = 0; j < 8; ++j) rd(0, j);
#pragma unroll
      for (int k = 0; k < 8 * nk; ++k) {
        const int kk = k >> 3;
        f32x4& d = acc[(k >> 2) & 1][k & 1][(k >> 1) & 1];
        u32x4& f = w[m][k & 1][4 * c + kk];
        if ((k & 7) < 2) {
          f = (u32x4){wh[kk & 1][(k & 1) * 2][0], wh[kk & 1][(k & 1) * 2][1], wh[kk & 1][(k & 1) * 2 + 1][0], wh[kk & 1][(k & 1) * 2 + 1][1]};
          if (c < 4) pin_a(f); else pin_v(f);
        }
        if (c < 4) mfA(d, f, xb[kk & 1][(k >> 1) & 3]); else mfa(d, f, xb[kk & 1][(k >> 1) & 3]);
        SB();
        rd(kk + 1, k & 7);
        if (k % nk == 0) put(i + 1, k / nk);
        if (k % nk == 1) gl(i + 1 + PDEP, k / nk);
        if (k % 12 == 11 || k == 8 * nk - 1) PAD();
      }
      if (i == 2) g1 = ld32(R, go.vo, 0);
      if (i == 5) x_gload(xq, xl, 2);
      if (i == 6) x_gload(xr, xl, 3);
      if (i == KFI) {
        SB();
        asm volatile("s_waitcnt vmcnt(%0)" :: "n"(KFI == 5 ? 13 : 22) : "memory");
        if (__all(g1 != go.v0)) go_ack(R, go, lane); else go_wait(R, go, lane);
        st32(0u, R, pf, 0);
      }
      SB();
      if (c == 4) {
        asm volatile("s_nop 7"); SB();
#pragma unroll
        for (int k = 0; k < 4; ++k) {
          PAD();
          const f32x4 (&e)[2][2] = acc[k >> 1];
          const int tt = k & 1;
          st128((u32x4){pk2(e[0][tt][0], e[0][tt][1]), pk2(e[0][tt][2], e[0][tt][3]), pk2(e[1][tt][0], e[1][tt][1]), pk2(e[1][tt][2], e[1][tt][3])}, R,
                (m ? o1 : o0) + fo + tt * 4096, (unsigned)(k >> 1) * 262144u);
        }
        SB();
      }
    };
#define CH(n) chunk(std::integral_constant<int, n>())
    CH(0); CH(1); CH(2); CH(3); CH(4); CH(5); CH(6); CH(7); CH(8); CH(9);
#undef CH
    BAR();
  }
  RT(12);
  PAD();
  x_swrite(xq, lds, xl);
  x_swrite(xr, lds + STG_BYTES, xl);
  PAD();
  u32x4 bb[3][2];
  XP rd0 = x_at(lds, xl), rd1 = x_at(lds + STG_BYTES, xl);
  unsigned b0 = 0, b1 = STG_BYTES, b2 = X1_OFF;
  const unsigned xw = (unsigned)(uintptr_t)lds + __builtin_amdgcn_readfirstlane(wave) * 9216;
  BAR();
#pragma unroll
  for (int ks = 0; ks < 2; ++ks) { bb[ks][0] = XF(rd0, 0, ks); bb[ks][1] = XF(rd0, 1, ks); }
  unsigned oa = o0 + fo, ob = o1 + fo, tc0 = 0, tr0 = 0;
  pin_v(oa); pin_v(ob);
  auto half = [&](auto HC, int s) {
    constexpr int H = decltype(HC)::value & 1, P = decltype(HC)::value >> 1;
    XP& buf = P ? rd1 : rd0;
    XP& nrd = H ? (P ? rd0 : rd1) : buf;
    const unsigned oo = H ? oa : ob;
    unsigned so, sx, xm, xd;
    f32x4 (&c)[2][2] = acc[H];
    f32x4 (&e)[2][2] = acc[H ^ 1];
    u32x4 o;
#define PM(t, tt) do { if (ks == 0) c[t][tt] = mfA0(w[H][t][0], bb[0][tt]); else if (ks < 16) mfA(c[t][tt], w[H][t][ks], bb[ks % 3][tt]); \
                       else mfa(c[t][tt], w[H][t][ks], bb[ks % 3][tt]); SB(); } while (0)
#define PR(tt) do { bb[(ks + 2) % 3][tt] = ks < 16 ? XF(buf, tt, ks + 2) : XF(nrd, tt, ks - 16); SB(); } while (0)
#pragma unroll
    for (int ks = 0; ks < 18; ++ks) {
      const int xi = H * 5 + ((ks - 7) >> 1);
      const bool xs = ks >= 7 && ks < 17 && xi < 9 && (H == 0 || ks < 15);
      if (H == 1 && ks == 16) { __builtin_amdgcn_s_waitcnt(0xC27F); __builtin_amdgcn_s_barrier(); SB(); }
      else { __builtin_amdgcn_s_waitcnt(0xC27F); SB(); }
      PM(0, 0); PR(0);
      if (ks == 0) {
        so = (unsigned)(s + H - 1) * 262144u;
        sx = (unsigned)clampseg(s + 2) * 36864u;
        asm volatile("" : "+s"(so), "+s"(sx)); SB();
      }
      if (ks == 1) {
        xm = xw + b2; xd = b2 - b0;
        if (H) { const unsigned t = b0; b0 = b1; b1 = b2; b2 = t; }
        asm volatile("" : "+s"(xm), "+s"(xd), "+s"(b0), "+s"(b1), "+s"(b2)); SB();
      }
      PM(0, 1); PR(1);
      PM(1, 0);
      if (ks == 1 || ks == 2) { o[(ks - 1) * 2] = pkv(e[ks - 1][0][0], e[ks - 1][0][1]); SB(); }
      if (ks == 4 || ks == 5) { o[(ks - 4) * 2] = pkv(e[ks - 4][1][0], e[ks - 4][1][1]); SB(); }
      if (ks == 3 && NOST < 2) { st128(o, R, oo, so); SB(); }
      if (ks == 6 && NOST < 2) { st128(o, R, oo + 4096, so); SB(); }
      if (xs && (ks & 1)) { x_m0(xm, xi); SB(); }
      if (xs && !(ks & 1)) { x_dma(xl, sx, xi); SB(); }
      if (H && ks >= 16) { x_move(buf.p[ks & 1], xd); SB(); }
      PM(1, 1);
      if (ks % 3 == 0) { if (H == 1 && P == 0 && ks == 0) { PADE(); PADS(103); } else PAD(); SB(); }
      if (ks == 1 || ks == 2) { o[(ks - 1) * 2 + 1] = pkv(e[ks - 1][0][2], e[ks - 1][0][3]); SB(); }
      if (ks == 4 || ks == 5) { o[(ks - 4) * 2 + 1] = pkv(e[ks - 4][1][2], e[ks - 4][1][3]); SB(); }
      if (ks == PFL) {
        asm volatile("s_waitcnt vmcnt(%0)" :: "n"((H ? 8 : 7) + (PFL - 6) / 2) : "memory");
        st32((unsigned)(H || P || s > 2 ? 2 * s + H - 2 : 0), R, pf, 0); SB();
      }
      if (ks == 17 && H == 1 && UNLIKELY(s < KEF)) { RT(10); VMW(5); st32((unsigned)(2 * s), R, pf, 0); RT(11); SB(); }
    }
#undef PM
#undef PR
  };
#pragma nounroll
  for (int s = 2; s < NSEG; ++s) {
    half(std::integral_constant<int, 0>(), s);
    half(std::integral_constant<int, 1>(), s);
    STEPRT(0, 0);
    ++s;
    half(std::integral_constant<int, 2>(), s);
    half(std::integral_constant<int, 3>(), s);
    STEPRT(0, 0);
  }
  PADE();
  mf_wait2(acc[1][0][0], acc[1][0][1]); mf_wait2(acc[1][1][0], acc[1][1][1]);
#pragma unroll
  for (int tt = 0; tt < 2; ++tt) {
    const u32x4 o = {pk2(acc[1][0][tt][0], acc[1][0][tt][1]), pk2(acc[1][0][tt][2], acc[1][0][tt][3]), pk2(acc[1][1][tt][0], acc[1][1][tt][1]), pk2(acc[1][1][tt][2], acc[1][1][tt][3])};
    st128(o, R, o1 + fo + tt * 4096, (unsigned)((NSEG - 1) & RMASK) * 262144u);
  }
  VMW(0);
  st32((unsigned)(2 * NSEG - 1), R, pf, 0);
  RT(7);
}

__device__ __forceinline__ void role_px(const Args& a, rsrc_t R, const Go& go, int pi, int wave, int lane) {
  const int l15 = lane & 15, g = lane >> 4;
  const int h = pi * 2 + (wave >> 1), par = wave & 1;
  const int wz = h >> 3, tq = (h >> 1) & 3, hp = h & 1;
  const int gy = ((tq & 1) << 1) | (tq >> 1);
  const unsigned kmo = OFF_KMF + (unsigned)h * 4096u + lane * 16;
  const unsigned pmo = OFF_PXF + (unsigned)((32 + wz * 2 + hp) * 1024 + (gy * 16 + l15) * 16 + g * 4), pxo = OFF_PXF + (unsigned)(h * 1024 + lane * 16);
  const unsigned qo = OFF_QF + (unsigned)h * 8192u + lane * 16, ko = OFF_KF + (unsigned)h * 8192u + lane * 16;
  int fm = -1;
  RT(1);
  u32x4 qf[2][4], kf[2][4];
  auto qk = [&](int s) {
#pragma unroll
    for (int k = 0; k < 8; ++k) { kf[k >> 2][k & 3] = ld128(R, ko + k * 1024, (unsigned)s * 262144u); qf[k >> 2][k & 3] = ld128(R, qo + k * 1024, (unsigned)s * 262144u); }
  };
  if (!go_wait(R, go, lane)) return;
  for (int k = 0; k < PXW; ++k) __builtin_amdgcn_s_sleep(32);
  const int cls = get_cls(R, lane);
  const unsigned ppf = (lane == 0) ? FX(FL_PXPROG, cls, 0) + h * 128 + par * 4 : OOB;
  const unsigned kmf = lane == 4 ? FX(FL_KMPROG, cls, 3) + h * 128 : (lane < 4 ? FX(FL_PROG + 8192, cls, 6) + h * 128 + lane * 4 : OOB);
  if (!wait_flag_c<16>(R, lane == 4 ? kmf : OOB, lane == 4 ? 0 : -1, fm)) return;
  qk(par);
  for (int s = par; s < NSEG - 1; s += 2) {
    u32x4 km[4], qc[2][4];
    f32x4 T[2][2];
    float mx[2], sx[2];
#pragma unroll
    for (int qt = 0; qt < 2; ++qt) {
      T[qt][0] = T[qt][1] = (f32x4){0.f, 0.f, 0.f, 0.f};
#pragma unroll
      for (int ks = 0; ks < 4; ++ks) {
        T[qt][0] = mfma(kf[0][ks], qf[qt][ks], T[qt][0]);
        if (qt) T[1][1] = mfma(kf[1][ks], qf[1][ks], T[1][1]);
        qc[qt][ks] = qf[qt][ks];
      }
      px_mask(T[qt][0], T[qt][1], qt, lane);
      px_seg(T[qt][0], T[qt][1], mx[qt], sx[qt]);
      pin_v(T[qt][0]); pin_v(T[qt][1]); pin_v(mx[qt]); pin_v(sx[qt]);
    }
    VMW(0);
    st32((unsigned)(s - 2), R, ppf, 0);
    STEPRT(2, 0);
    const int need = lane < 4 ? min(2 * s + 5, 2 * NSEG - 1) : (lane == 4 ? s : -1);
    for (unsigned spins = 0; !__all(fm >= need); ++spins) {
      if (spins > SPINCAP) return;
      if (spins) __builtin_amdgcn_s_sleep(PXS);
      CBAR();
      fm = max(fm, (int)ld32(R, fm < need && (lane == 4 || __builtin_amdgcn_readlane(fm, 4) >= s) ? kmf : OOB, 0));
    }
    CBAR();
#pragma unroll
    for (int ks = 0; ks < 4; ++ks) km[ks] = ld128(R, kmo + ks * 1024, (unsigned)s * 131072u);
    SB();
    qk(clampseg(s + 2));
    CBAR();
#pragma unroll
    for (int qt = 0; qt < 2; ++qt) {
      f32x4 G = {0.f, 0.f, 0.f, 0.f};
#pragma unroll
      for (int ks = 0; ks < 4; ++ks) G = mfma(km[ks], qc[qt][ks], G);
      u32x4 px; unsigned pw;
      px_fin(G, T[qt][0], T[qt][1], mx[qt], sx[qt], px, pw, lane);
      st128(px, R, pxo, (unsigned)s * 81920u + qt * 40960);
      st32(pw, R, pmo, (unsigned)s * 81920u + qt * 40960);
    }
    CBAR();
    fm = (int)ld32(R, kmf, 0);
    CBAR();
  }
  VMW(0);
  st32((unsigned)(NSEG - 2 - par), R, ppf, 0);
  RT(7);
}

__device__ __forceinline__ void role_vuw(const Args& a, rsrc_t R, const Go& go, lds_char* lds, int h, int tid, int wave, int lane) {
  const int l15 = lane & 15, g = lane >> 4;
  u32x4 wv[2][18], wo[9][4];
  XL xl; x_init(xl, a.x, wave, lane);
  unsigned zo = OFF_ZF + (unsigned)(wave * 9 * 40960 + lane * 16) + h * 1024;
  const unsigned vpf = lane < 2 ? (unsigned)(FL_UPROG + lane * 8192 + h * UH + wave * UW) : OOB;
  const unsigned lb = (unsigned)(uintptr_t)lds, xw = lb + wave * 9216;
  lds_char *vr = lds + VX_OFF + lane * 16, *vw = vr + wave * 1024;
  f32x4 cc[2][2], av[2][2][2];
  u32x4 u1[9];
  TKD;
  {
    Go g0 = go;
    const i32x4 DV = wdesc(a.Wv + h * 128), DO = wdesc(a.Wo + (size_t)h * 128 * DM), D = mkdesc(a.wb);
    const unsigned wa = (unsigned)((16 * wave + g) * (HD * 2) + (l15 ^ (2 * g)) * 16), wb = wa ^ 128, ww = lb + wave * 4096, w1 = lb + wave * 1024;
    auto stage = [&](int j) {
      x_m0(xw, j); x_dma0(xl, 0, j);
      x_m0(xw + 36864, j); x_dma0(xl, 36864, j);
#pragma unroll
      for (int k = 0; k < 4; ++k) w_dma(DV, k < 2 ? wa : wb, (64 * j + 4 * k) * (HD * 2), ww, vbuf(j) + k * 1024);
      SB();
    };
#pragma unroll
    for (int j = 0; j < 3; ++j) stage(j);
    TK(0);
    unsigned oo[9], g1, g2;
#pragma unroll
    for (int i = 0; i < 9; ++i) { const int u = (4 * i + wave) * 64 + lane, r = u / 72; oo[i] = r * 1152 + ((u % 72) ^ (r & 6)) * 16; pin_v(oo[i]); }
    auto o_dma = [&](int k, int i0, int i1) {
#pragma unroll
      for (int i = i0; i < i1; ++i) w_dma(DO, oo[i], k * 36864, w1, ((k + 2) & 3) * 36864 + i * 4096);
      SB();
    };
    const XP x0r = x_at(lds, xl), x1r = x_at(lds + 36864, xl);
    lds_char* rp[2][2];
#pragma unroll
    for (int k = 0; k < 4; ++k) {
      rp[k >> 1][k & 1] = lds + ((k >> 1) ? VX_OFF : VB_OFF) + (g * 8 + (l15 >> 2)) * 256 + ((4 * wave + 2 * (k & 1)) ^ (2 * (l15 >> 2) + 8 * (g & 1))) * 16 + (l15 & 3) * 8;
      pin_v(rp[k >> 1][k & 1]);
    }
    asm volatile("s_load_dword %0, %1, %2\n\ts_waitcnt lgkmcnt(0)" : "=s"(g0.v0) : "s"(a.wb), "s"(__builtin_amdgcn_readfirstlane(go.vo)) : "memory");
    const unsigned pf = lane < 9 ? (unsigned)(FL_MPOI + (wave * 9 + lane) * 4) : OOB;
    TK(1);
    stage(3); stage(4);
    TK(2);
    auto vst = [&](auto JC) {
      constexpr int j = decltype(JC)::value, n = j == 0 ? 24 : (j < 4 ? 18 : (j < 6 ? 19 : (j < 7 ? 17 : 15))), hi = vbuf(j) == VX_OFF, bo = hi ? 0 : vbuf(j) - VB_OFF;
      asm volatile("s_waitcnt vmcnt(%0)" :: "n"(n) : "memory");
      __builtin_amdgcn_s_barrier(); CBAR(); SB();
      if (!(j & 1)) TK(3 + j / 2);
      u32x4 xb[2][4];
      u32x2 wh[2][4];
#pragma unroll
      for (int k = 0; k < 16; ++k) {
        const int kk = k >> 3, q = (k >> 1) & 3;
        if (k & 1) xb[kk][q] = XF((q & 2) ? x1r : x0r, q & 1, 2 * j + kk);
        else wh[kk][q] = trd(rp[hi][q >> 1] + bo + (kk * 32 + (q & 1) * 4) * 256);
        SB();
      }
      if (j == 3) { asm volatile("buffer_load_dword %0, %1, %2, 0 offen sc1" : "=v"(g1) : "v"(go.vo), "s"(D) : "memory"); SB(); }
      if (j == 7) {
        asm volatile("" : "+v"(g1));
        if (__all(g1 != g0.v0)) go_ack(R, g0, lane); else go_wait(R, g0, lane);
        asm volatile("buffer_load_dword %0, %1, %2, 0 offen sc1" : "=v"(g2) : "v"(pf), "s"(D) : "memory"); SB();
      }
      if (j == 0) {} else if (j < 5) stage(j + 4); else o_dma((j - 5) >> 1, (j & 1) ? 0 : 4, (j & 1) ? 4 : 9);
#pragma unroll
      for (int k = 0; k < 16; ++k) {
        const int kk = k >> 3;
        u32x4& f = wv[k & 1][2 * j + kk];
        if ((k & 7) < 2) { f = (u32x4){wh[kk][(k & 1) * 2][0], wh[kk][(k & 1) * 2][1], wh[kk][(k & 1) * 2 + 1][0], wh[kk][(k & 1) * 2 + 1][1]}; pin_a(f); }
        f32x4& d = av[(k >> 2) & 1][k & 1][(k >> 1) & 1];
        if (j == 0 && k < 8) d = mfA0(f, xb[kk][(k >> 1) & 3]); else mfA(d, f, xb[kk][(k >> 1) & 3]);
        SB();
      }
    };
#define CH(n) vst(std::integral_constant<int, n>())
    CH(0); CH(1); CH(2); CH(3); CH(4); CH(5); CH(6); CH(7); CH(8);
#undef CH
    BAR();
    TK(8);
    o_dma(2, 0, 9); o_dma(3, 0, 9);
    TK(9);
#pragma unroll
    for (int k = 0; k < 4; ++k) {
      const f32x4 (&e)[2][2] = av[k >> 1];
      const int tt = k & 1;
      *(lds_u32x4*)(vw + k * 4096) = (u32x4){pkv(e[0][tt][0], e[0][tt][1]), pkv(e[0][tt][2], e[0][tt][3]), pkv(e[1][tt][0], e[1][tt][1]), pkv(e[1][tt][2], e[1][tt][3])};
    }
    SB();
    lds_char* op[4];
#pragma unroll
    for (int k = 0; k < 4; ++k) {
      const int T = 9 * wave + k;
      op[k] = lds + VB_OFF + (4 * g + (l15 >> 2)) * 1152 + (T ^ ((2 * g + (l15 >> 3)) & 3)) * 32 + (l15 & 3) * 8;
      pin_v(op[k]);
    }
    f32x4 ua[18][2];
    auto ust = [&](auto KC) {
      constexpr int k = decltype(KC)::value;
      asm volatile("s_waitcnt vmcnt(%0) lgkmcnt(0)" :: "n"(k == 0 ? 29 : (k < 3 ? 18 : 9)) : "memory");
      __builtin_amdgcn_s_barrier(); CBAR(); SB();
      TK(10 + k);
      if (k == 2) {
#pragma unroll
        for (int i = 0; i < 4; ++i) x_move(op[i], -VB_OFF);
      }
      u32x4 f[4];
      u32x2 wh[9][2];
#pragma unroll
      for (int i = 0; i < 2; ++i) { f[i] = *(lds_u32x4*)(vr + i * 4096 + k * 1024); SB(); }
#pragma unroll
      for (int i = 0; i < 18; ++i) { wh[i >> 1][i & 1] = trd(op[(i >> 1) & 3] + (k & 1) * 36864 + (i & 1) * 18432 + (i >> 3) * 128); SB(); }
#pragma unroll
      for (int i = 2; i < 4; ++i) { f[i] = *(lds_u32x4*)(vr + i * 4096 + k * 1024); SB(); }
      if (k == 1) {
#pragma unroll
        for (int i = 0; i < 9; ++i) { x_m0(xw + VB_OFF, i); x_dma0(xl, 2 * 36864, i); }
        SB();
      }
      if (k == 2) {
        asm volatile("" : "+v"(g2));
        int c = (int)g2;
        wait_flag_c<4>(R, pf, 0, c); SB();
      }
#pragma unroll
      for (int t = 0; t < 19; ++t) {
        const int ct = t % 9;
        if (t < 9) { wo[ct][k] = (u32x4){wh[ct][0][0], wh[ct][0][1], wh[ct][1][0], wh[ct][1][1]}; if (ct < 7) pin_a(wo[ct][k]); else pin_v(wo[ct][k]); }
#pragma unroll
        for (int tt = 0; tt < 2; ++tt) {
          if (t == 18) break;
          f32x4& d = ua[t][tt];
          const u32x4& v = f[(t / 9) * 2 + tt];
          if (ct < 7) { if (k == 0) d = mfB0(v, wo[ct][k]); else mfB(d, v, wo[ct][k]); }
          else { if (k == 0) d = mf0(v, wo[ct][k]); else mfa(d, v, wo[ct][k]); }
          SB();
        }
        if (k == 3 && t > 0) {
          f32x4 (&e)[2] = ua[t - 1];
          if (t == 18) mf_wait2(e[0], e[1]);
          const u32x4 uo = {pkv(e[0][0], e[0][1]), pkv(e[0][2], e[0][3]), pkv(e[1][0], e[1][1]), pkv(e[1][2], e[1][3])};
          if (t < 10) st128(uo, R, zo, (t - 1) * 40960); else { u1[t < 10 ? 0 : t - 10] = uo; pin_v(u1[t < 10 ? 0 : t - 10]); }
          SB();
        }
      }
    };
    ust(std::integral_constant<int, 0>()); ust(std::integral_constant<int, 1>()); ust(std::integral_constant<int, 2>()); ust(std::integral_constant<int, 3>());
    asm volatile("" :: "v"(go.v0));
#pragma unroll
    for (int i = 0; i < 9; ++i) { x_m0(xw + VB_OFF + 36864, i); x_dma0(xl, 3 * 36864, i); }
    SB();
  }
  TK(14);
  XP rd0 = x_at(lds + VB_OFF, xl), rd1 = x_at(lds + VB_OFF + 36864, xl);
  unsigned b0 = VB_OFF, b1 = VB_OFF + 36864, b2 = 0;
  pin_v(vr); pin_v(vw); pin_v(zo);
  u32x4 bb[3][2], vf[2][2][4];
#pragma unroll
  for (int k = 0; k < 4; ++k) cc[k >> 1][k & 1] = (f32x4){0.f, 0.f, 0.f, 0.f};
#pragma unroll
  for (int k = 0; k < 16; ++k) asm volatile("" : "=v"(vf[k >> 3][(k >> 2) & 1][k & 3]));
  VMW(18);
  BAR();
#pragma unroll
  for (int ks = 0; ks < 2; ++ks) { bb[ks][0] = XF(rd0, 0, ks); bb[ks][1] = XF(rd0, 1, ks); }
  auto iter = [&](auto PC, int s) {
    constexpr int P = decltype(PC)::value & 1, W = 0, NOV = 0, NOU = decltype(PC)::value >> 1;
    XP& buf = P ? rd1 : rd0;
    XP& nrd = P ? rd0 : rd1;
    f32x4 (&c)[2][2] = av[P];
    f32x4 (&e)[2][2] = av[P ^ 1];
    u32x4 o, q;
    unsigned s1, s2, sx, xm, xd, sf;
#define VM(ks, t, tt) do { if (NOV) break; if ((ks) == 0) c[t][tt] = mfA0(wv[t][ks], bb[(ks) % 3][tt]); else mfA(c[t][tt], wv[t][ks], bb[(ks) % 3][tt]); SB(); } while (0)
#define UM(k, tt) do { if (NOU) break; f32x4& d = cc[(gq + P) & 1][tt]; const u32x4& f = vf[gq < 2 ? P : P ^ 1][tt][k];                                      \
                       if (ct < 7) { if ((k) == 0) d = mfB0(f, wo[ct][k]); else mfB(d, f, wo[ct][k]); }                                       \
                       else { if ((k) == 0) d = mf0(f, wo[ct][k]); else mfa(d, f, wo[ct][k]); } SB(); } while (0)
#define BRD(ks, tt) do { if (NOV) break; bb[(ks) % 3][tt] = (ks) < 18 ? XF(buf, tt, (ks) % 18) : XF(nrd, tt, (ks) % 18); SB(); } while (0)
#define CV(j) do { if (NOU) break; o[j] = pkv(cc[(gq + P + 1) & 1][(j) >> 1][((j) & 1) * 2], cc[(gq + P + 1) & 1][(j) >> 1][((j) & 1) * 2 + 1]); SB(); } while (0)
#define CE(j, tt) do { if (gq == 0 && !NOU) { q[j] = pkv(e[(j) >> 1][tt][((j) & 1) * 2], e[(j) >> 1][tt][((j) & 1) * 2 + 1]); SB(); } } while (0)
#define VR(j) do { if (gq == 1 && !NOU) { vf[P ^ 1][(j) & 1][(j) >> 1] = *(lds_u32x4*)(vr + P * 8192 + ((j) & 1) * 4096 + ((j) >> 1) * 1024); SB(); } } while (0)
#define LW(n) do { __builtin_amdgcn_s_waitcnt(0xC07F | ((n) << 8)); SB(); } while (0)
#pragma unroll
    for (int gq = NOV ? 2 : 0; gq < 9; ++gq) {
      const int ct = (gq + 7) % 9, k0 = 2 * gq, k1 = 2 * gq + 1, pt = (gq + 6) % 9;
      if (NOU || gq == 0 || gq > 2) LW(2); else if (gq == 1) LW(6); else LW(8);
      if (gq == 8 && !NOV) { __builtin_amdgcn_s_barrier(); SB(); }
      VM(k0, 0, 0); BRD(k0 + 2, 0);
      if (gq == (NOV ? 2 : 0)) {
        s1 = (unsigned)(s - 1) < NSEG ? (unsigned)(s - 1) * ZS : OOB; s2 = s >= 4 ? (unsigned)(s - 2) * ZS : OOB; sx = (unsigned)clampseg(s + 2) * 36864u;
        s1 = __builtin_amdgcn_readfirstlane(s1); s2 = __builtin_amdgcn_readfirstlane(s2); sx = __builtin_amdgcn_readfirstlane(sx);
        asm volatile("" : "+s"(s1), "+s"(s2), "+s"(sx)); SB();
      }
      UM(0, 0); CV(0);
      unsigned su = (gq < 3 ? s2 : s1) + pt * 40960;
      asm volatile("" : "+s"(su)); SB();
      VM(k0, 0, 1); BRD(k0 + 2, 1);
      if (!NOV && gq == 0) { sf = FG >= 7 ? (unsigned)(s - 2) : (s == 2 ? ~1u : (unsigned)(s - 3)); pin_v(sf); SB(); }
      if (!NOV && gq == 2) {
        xm = xw + b2; xd = b2 - b0;
        const unsigned t = b0; b0 = b1; b1 = b2; b2 = t;
        asm volatile("" : "+s"(xm), "+s"(xd), "+s"(b0), "+s"(b1), "+s"(b2)); SB();
      }
      UM(0, 1); CV(1);
      if (gq == 1 && !NOU) { LW(2); __builtin_amdgcn_s_barrier(); SB(); }
      VM(k0, 1, 0); VR(0); CV(2);
      UM(1, 0); VR(1); CV(3);
#define ST(n) do { if (!NOU && !NOST && STG0 + W * STGD == n) { st128(o, R, zo, su); SB(); } } while (0)
#define VW2(j, tt) do { if (gq == 0 && !NOU) { *(lds_u32x2*)(vw + P * 8192 + (tt) * 4096 + (j) * 4) = (u32x2){q[j], q[(j) + 1]}; SB(); } } while (0)
      ST(5);
      if (NOU && gq < 5) { st128(u1[gq < 5 ? 2 * gq : 0], R, zo, ZS + 2 * gq * 40960); SB(); }
      VM(k0, 1, 1); ST(6);
      UM(1, 1); CE(0, 0); CE(1, 0); VR(2); ST(7);
      if (gq == 1 && !NOU) LW(5); else LW(2);
      VM(k1, 0, 0); BRD(k1 + 2, 0); CE(2, 0); ST(8);
      UM(2, 0); VW2(0, 0); VR(3); ST(9);
      if (!NOV && gq >= 2 && gq < 7) { x_m0(xm, gq - 2); SB(); }
      if (!NOV && gq == 8) { x_move(buf.p[0], xd); x_move(buf.p[1], xd); SB(); }
      VM(k1, 0, 1); BRD(k1 + 2, 1); CE(3, 0); ST(10);
      UM(2, 1); VW2(2, 0); VR(4); ST(11);
      if (!NOV && gq >= 2 && gq < 7) { x_dma(xl, sx, gq - 2); SB(); }
      VM(k1, 1, 0); VR(5); ST(12);
      CE(0, 1); CE(1, 1);
      if (!NOV && gq >= 3 && gq < 7) { x_m0(xm, gq + 2); SB(); }
      UM(3, 0); VW2(0, 1); VR(6); ST(13);
      if (!NOV && gq == 0) { PADE(); if (P) PADS(113); else PADS(112); } else PAD();
      SB();
      VM(k1, 1, 1); CE(2, 1); CE(3, 1); VR(7); ST(14);
      if (!NOV && gq >= 3 && gq < 7) { x_dma(xl, sx, gq + 2); SB(); }
      UM(3, 1); VW2(2, 1); ST(15);
      if (NOU && gq < 4) { st128(u1[gq < 4 ? 2 * gq + 1 : 0], R, zo, ZS + (2 * gq + 1) * 40960); SB(); }
#undef ST
#undef VW2
      if (NOU && gq == 7) { VMW(18); SB(); }
      if (!NOU && P && gq == EG1 && UNLIKELY(s == 3)) { asm volatile("s_waitcnt vmcnt(%0)" :: "n"(EG1 + 7) : "memory"); st32(sf, R, vpf, 0); SB(); }
      if (!NOU && gq == FG && !NOFL) {
        asm volatile("s_waitcnt vmcnt(%0)" :: "n"(FG >= 7 ? FG + 7 : (FG < 2 ? FG + 16 : 20)) : "memory");
        st32(sf, R, vpf, 0); SB();
      }
      if (!NOU && gq == 7 && FG != 7) { VMW(20); SB(); }
      if (!NOU && gq == EG && UNLIKELY((unsigned)(s - 4) < UEF || s >= NSEG)) { asm volatile("s_waitcnt vmcnt(%0)" :: "n"(3 * EG - 5) : "memory"); st32(sf, R, vpf, 0); SB(); }
    }
#undef VM
#undef UM
#undef BRD
#undef CV
#undef CE
#undef VR
#undef LW
  };
  iter(std::integral_constant<int, 2>(), 2);
  RT(8);
  TKS;
#pragma nounroll
  for (int s = 3;; s += 2) {
    iter(std::integral_constant<int, 1>(), s);
    if (s == NSEG + 1) break;
    iter(std::integral_constant<int, 0>(), s + 1);
  }
  PADE();
  VMW(0);
  RT(7);
}

#define RING_OFF (2 * STG_BYTES)
#define RING_STR 456
#define OST_OFF (RING_OFF + 64 * RING_STR)
__device__ __forceinline__ float dot2(unsigned a, unsigned b, float c) {
  return __builtin_amdgcn_fdot2_f32_bf16(__builtin_bit_cast(bf16x2, a), __builtin_bit_cast(bf16x2, b), c, false);
}
__device__ __forceinline__ float swp1(float x) { return __uint_as_float(__builtin_amdgcn_update_dpp(0u, __float_as_uint(x), 0xB1, 0xf, 0xf, false)); }
__device__ __forceinline__ void role_aw(const Args& a, rsrc_t R, const Go& go, lds_char* lds, int ai, int tid, int wave, int lane) {
  const int l15 = lane & 15, g = lane >> 4;
  const int ga = ai & 7, rr = ai >> 3;
  go_wait(R, go, lane);
  const int cls = get_cls(R, lane);
  u32x4 w[4][18];
  {
    u32x4 sg[3][7];
    unsigned gv[9], wo[7], lo[7];
    const rsrc_t W = __builtin_amdgcn_make_buffer_rsrc((void*)a.wqkv, 0, DM * 1728 * 2, 0x00020000);
    const rsrc_t G = __builtin_amdgcn_make_buffer_rsrc((void*)a.gw, 0, DM * 2, 0x00020000);
#pragma unroll
    for (int k = 0; k < 7; ++k) {
      const int u = tid + 256 * k, row = u / 27, cu = u % 27;
      wo[k] = row < 64 ? (unsigned)(row * 3456 + (cu / 9) * 1152 + ga * 144 + (cu % 9) * 16) : OOB;
      lo[k] = row < 64 ? (unsigned)(row * 544 + cu * 16) : (unsigned)((tid & 63) * 544 + 448);
    }
    auto gl = [&](u32x4 (&st)[7], int c) {
#pragma unroll
      for (int k = 0; k < 7; ++k) st[k] = __builtin_amdgcn_raw_buffer_load_b128(W, wo[k], (unsigned)c * (64u * 3456u), (NTM & 16) ? 2 : 0);
    };
#pragma unroll
    for (int c = 0; c < 9; ++c) gv[c] = __builtin_amdgcn_raw_buffer_load_b16(G, (unsigned)(tid & 63) * 2, (unsigned)c * 128u, 0);
    gl(sg[0], 0); if (ASLP > 0) __builtin_amdgcn_s_sleep(ASLP);
    gl(sg[1], 1); if (ASLP > 0) __builtin_amdgcn_s_sleep(ASLP);
    gl(sg[2], 2);
#pragma unroll
    for (int c = 0; c < 9; ++c) {
      lds_char* buf = lds + (c & 1) * STG_BYTES;
#pragma unroll
      for (int k = 0; k < 7; ++k) *(lds_u32x4*)(buf + lo[k]) = sg[c % 3][k];
      *(lds_u32x4*)(buf + (tid & 63) * 544 + 432) = (u32x4){gv[c], 0u, 0u, 0u};
      if (ASLP > 0) __builtin_amdgcn_s_sleep(ASLP);
      if (c + 3 < 9) gl(sg[c % 3], c + 3);
      BAR();
#pragma unroll
      for (int kk = 0; kk < 2; ++kk)
#pragma unroll
        for (int t = 0; t < 4; ++t) {
          lds_char* p = buf + (kk * 32 + g * 8 + (l15 >> 2)) * 544 + (wave * 64 + t * 16) * 2 + (l15 & 3) * 8;
          u32x2 v0 = trd(p), v1 = trd(p + 4 * 544);
          w[t][2 * c + kk] = (u32x4){v0[0], v0[1], v1[0], v1[1]};
        }
      SB();
#pragma unroll
      for (int k = 0; k < 8; ++k) { if (c < 7) pin_a(w[k & 3][2 * c + (k >> 2)]); else pin_v(w[k & 3][2 * c + (k >> 2)]); }
    }
    BAR();
  }
  RT(1);
  auto sgm = [&](int it) { const int j = it / 5; return min(16 * j + 4 * rr + it - 5 * j - 1, NSEG - 1); };
  const int i0 = rr > 0 ? 0 : 1;
  u32x4 xr[9];
  XL xl; x_init(xl, a.x, wave, lane);
  x_gload(xr, xl, sgm(i0));
  x_swrite(xr, lds + i0 * STG_BYTES, xl);
  x_gload(xr, xl, sgm(i0 + 1));
  lds_char* ring = lds + RING_OFF;
  lds_char* ost = lds + OST_OFF;
  const int t = tid >> 3, hh = (tid >> 1) & 3, half = tid & 1;
  const float sc = 0.23570226039551584f * 1.4426950408889634f;
  auto done = [&](int s) { __builtin_amdgcn_raw_ptr_buffer_atomic_add_i32(1, R, tid == 0 && s >= 0 ? FX(FL_APROG, cls, 1) + s * 128 : OOB, 0, SC1); };
  const unsigned gto = (ga == 0 && wave == 3 && g == 2) ? OFF_GATE + l15 * 4 : OOB;
  int sp = -1;
  for (int it = i0; it < 20; ++it) {
    const int s = sgm(it);
    BAR();
    {
      f32x4 acc[4][2];
      proj4(lds + (it & 1) * STG_BYTES, lds + ((it + 1) & 1) * STG_BYTES, xr, xl, sgm(it + 2), wave, lane, w[0], w[1], w[2], w[3], acc);
      VMW(9);
#pragma unroll
      for (int tl = 0; tl < 4; ++tl)
#pragma unroll
        for (int tt = 0; tt < 2; ++tt) {
          const int col = wave * 64 + tl * 16 + g * 4;
          u32x2 o = {pk2(acc[tl][tt][0], acc[tl][tt][1]), pk2(acc[tl][tt][2], acc[tl][tt][3])};
          if (col < 216) *(__attribute__((address_space(3))) u32x2*)(ring + (((s & 1) * 32 + tt * 16 + l15) * RING_STR) + col * 2) = o;
        }
#pragma unroll
      for (int tt = 0; tt < 2; ++tt) {
        float z = acc[1][tt][0];
        float gv = __builtin_amdgcn_rcpf(1.f + __builtin_amdgcn_exp2f(-1.4426950408889634f * z));
        st32(__float_as_uint(gv), R, gto + tt * 64, (unsigned)s * 128u);
      }
    }
    BAR();
    done(sp);
    sp = it % 5 != 0 ? s : -1;
    { const int s = it; STEPRT(2, 0); }
    if (it % 5 != 0) {
      const int row = s * 32 + t;
      unsigned q[9];
      lds_char* qp = ring + (row & 63) * RING_STR + hh * 36;
#pragma unroll
      for (int j = 0; j < 9; ++j) q[j] = *(lds_u32*)(qp + j * 4);
      float lg[4];
      lds_char* vp[4];
#pragma unroll
      for (int wi = 0; wi < 4; ++wi) {
        const int r2 = row - 2 * (half * 4 + wi);
        const bool ok = r2 >= 0;
        lds_char* kp = ring + ((ok ? r2 : row) & 63) * RING_STR + hh * 36 + 144;
        vp[wi] = kp + 144;
        float d = 0.f;
#pragma unroll
        for (int j = 0; j < 9; ++j) d = dot2(q[j], *(lds_u32*)(kp + j * 4), d);
        lg[wi] = ok ? d : -__builtin_inff();
      }
      float m = fmaxf(fmaxf(lg[0], lg[1]), fmaxf(lg[2], lg[3]));
      m = fmaxf(m, swp1(m));
      const float ms = -m * sc;
      float sum = 0.f;
#pragma unroll
      for (int wi = 0; wi < 4; ++wi) { lg[wi] = __builtin_amdgcn_exp2f(fmaf(lg[wi], sc, ms)); sum += lg[wi]; }
      sum += swp1(sum);
      const float inv = __builtin_amdgcn_rcpf(sum);
      const unsigned p01 = pk2(lg[0] * inv, lg[1] * inv), p23 = pk2(lg[2] * inv, lg[3] * inv);
#pragma unroll
      for (int j = 0; j < 9; ++j) {
        const unsigned v0 = *(lds_u32*)(vp[0] + j * 4), v1 = *(lds_u32*)(vp[1] + j * 4), v2 = *(lds_u32*)(vp[2] + j * 4), v3 = *(lds_u32*)(vp[3] + j * 4);
        float lo = dot2(__builtin_amdgcn_perm(v1, v0, 0x05040100u), p01, 0.f);
        lo = dot2(__builtin_amdgcn_perm(v3, v2, 0x05040100u), p23, lo);
        float hi = dot2(__builtin_amdgcn_perm(v1, v0, 0x07060302u), p01, 0.f);
        hi = dot2(__builtin_amdgcn_perm(v3, v2, 0x07060302u), p23, hi);
        lo += swp1(lo); hi += swp1(hi);
        if ((j & 1) == half) *(lds_u32*)(ost + t * 144 + hh * 36 + j * 4) = pk2(lo, hi);
      }
      BAR();
#pragma unroll
      for (int k = 0; k < 2; ++k) {
        const int u = tid + 256 * k;
        const bool ok = u < 288;
        const int ur = u & 31, uc = ok ? u >> 5 : 0, cc = ga * 9 + uc;
        u32x4 o = *(lds_u32x4*)(ost + ur * 144 + uc * 16);
        st128(o, R, ok ? OFF_OA + (unsigned)(((ur >> 4) * 18 + (cc >> 2)) * 1024 + ((cc & 3) * 16 + (ur & 15)) * 16) : OOB, (unsigned)s * 36864u);
      }
      CBAR();
    }
  }
  VMW(0);
  BAR();
  done(sp);
  RT(7);
}

__device__ __forceinline__ void role_out(const Args& a, rsrc_t R, const Go& go, lds_char* lds, int o, int tid, int wave, int lane) {
  const int l15 = lane & 15, g = lane >> 4;
  const int cb = o % 9, oj = o / 9, sw = cb == 8 ? OSW : 0, grp = oj < 4 ? oj : (oj == 4 ? sw + 4 : NSEG - 16 + oj);
  auto nxt = [&](int s) { return s < sw ? s + 4 : (s < NSEG - 16 ? s + 5 : s + 8); };
  u32x4 wf[4][5];
  {
    u32x4 st[18];
#pragma unroll
    for (int k = 0; k < 18; ++k) { int u = tid + 256 * k; st[k] = *(const u32x4*)(a.wo + (size_t)(u >> 3) * DM + cb * 64 + (u & 7) * 8); }
#pragma unroll
    for (int k = 0; k < 18; ++k) { int u = tid + 256 * k; *(lds_u32x4*)(lds + (u >> 3) * 144 + (u & 7) * 16) = st[k]; }
    BAR();
#pragma unroll
    for (int i = 0; i < 5; ++i)
#pragma unroll
      for (int ct = 0; ct < 4; ++ct) {
        const int ks = wave + 4 * i;
        lds_char* p = lds + ((ks < 18 ? ks : 17) * 32 + g * 8 + (l15 >> 2)) * 144 + ct * 32 + (l15 & 3) * 8;
        u32x2 v0 = trd(p), v1 = trd(p + 4 * 144);
        wf[ct][i] = (ks < 18) ? (u32x4){v0[0], v0[1], v1[0], v1[1]} : (u32x4){0u, 0u, 0u, 0u};
        pin_a(wf[ct][i]);
      }
    BAR();
  }
  const unsigned zb = OFF_ZF + (unsigned)(4 * cb) * 40960u + lane * 16;
  if (o < 44 && o != 40 && !go_wait(R, go, lane)) return;
  const int cls = get_cls(R, lane);
  const unsigned flk = lane == 32 ? FX(FL_KMPROG, cls, 3) + ((o * 4 + wave) & 31) * 128 : OOB;
  const bool isu = (lane & 24) == 8 && lane < 48;
  auto faddr = [&](int s) -> unsigned {
    return lane < 8 ? (lane == 0 ? FX(FL_APROG, cls, 1) + s * 128 : OOB)
                    : (isu ? FX(FL_UPROG + 8192, cls, 6) + (wave * 8 + (lane & 7)) * UH + ((cb * 64 + (lane < 16 ? 0 : 63)) / 144) * UW
                           : (lane < 32 ? (s == NSEG - 1 ? FX(FL_PXLAST, cls, 1) + (wave * 16 + lane - 16) * 128 : FX(FL_PXPROG, cls, 0) + (wave * 8 + (lane & 7)) * 128 + (s & 1) * 4)
                                        : (s + 3 < NSEG ? flk : FX(FL_YPROG, cls, 2) + (wave * 8 + (lane & 7)) * 16 + ((cb * 64 + (lane < 48 ? 0 : 63)) / 144) * 4 + (lane < 56 ? 0 : OOB))));
  };
  const int t = tid >> 3, n8 = tid & 7;
  const rsrc_t O = __builtin_amdgcn_make_buffer_rsrc(a.out, 0, S_LEN * DM * 2, 0x00020000);
  const i32x4 D = mkdesc(a.wb);
  int fv = -1;
  RT(1);
  const unsigned ofo = OFF_OA + (unsigned)(wave * 1024 + lane * 16), ofl = wave < 2 ? ofo : OOB, gto = OFF_GATE + l15 * 4;
  const unsigned pfo = OFF_PXF + (unsigned)(wave * 8192 + lane * 16), pfy = OFF_PXF + (unsigned)((24 + wave * 2) * 1024 + lane * 16);
  u32x4 zu[4][8];
  int fu = -1;
  const unsigned fua = isu ? faddr(0) : OOB;
  const unsigned zw = zb + wave * 8192;
#pragma unroll
  for (int k = 0; k < 32; ++k) asm volatile("" : "=a"(zu[k >> 3][k & 7]));
  auto zu4 = [&](unsigned vo, int s, int hh) {
#pragma unroll
    for (int ct = 0; ct < 4; ++ct)
      asm volatile("buffer_load_dwordx4 %0, %1, %2, %3 offen offset:%4 sc1" : "+a"(zu[ct][hh])
                   : "v"(vo), "s"(D), "s"((unsigned)(s & RMASK) * ZS + ct * 40960 + (hh >> 2) * 4096), "n"((hh & 3) * 1024) : "memory");
  };
  auto zuld = [&](int s) -> bool {
    unsigned spins = 0;
    while (ballot(isu && fu < s) != 0) {
      if (++spins > SPINCAP) return false;
      if (spins > 1) __builtin_amdgcn_s_sleep(16);
      if (s == NSEG - 1 && spins > 1) __builtin_amdgcn_s_sleep(100);
      CBAR();
      fu = (int)ld32(R, fua, 0);
    }
#pragma unroll
    for (int hh = 0; hh < 8; ++hh) zu4(zw, s, hh);
    SB();
    return true;
  };
  if (!zuld(grp)) return;
  unsigned acc_t[8] = {0, 0, 0, 0, 0, 0, 0, 0}, t0 = 0, t1 = 0, t2 = 0, t3 = 0, t4 = 0, t5 = 0, t6 = 0;
  for (int s = grp; s < NSEG; s = nxt(s)) {
    TS(t0);
    const unsigned fa = faddr(s);
    const int need = isu || fa >= OOB ? -1 : (lane == 0 ? 7 : ((lane >= 32 && s + 3 < NSEG) ? s + 2 : s));
    const bool far = lane >= 8 && lane < 48 && (isu || (lane < 32 && s < NSEG - 1));
    unsigned spins = 0;
    while ((ballot(fv < need) & 0xFFull) != 0) {
      if (++spins > SPINCAP) return;
      if (spins > 1) __builtin_amdgcn_s_sleep(32);
      CBAR();
      const int nv = (int)lda(R, fv < need && lane < 8 ? fa : OOB);
      fv = lane < 8 ? max(fv, nv) : fv;
    }
    TS(t1);
#define OLD(d, vo, so, o) asm volatile("buffer_load_dwordx4 %0, %1, %2, %3 offen offset:%4 sc1" : "=v"(d) : "v"(vo), "s"(D), "s"(so), "n"(o) : "memory")
    u32x4 zy[4][2], pf[2][10], of[2][5];
    float gt[2];
#pragma unroll
    for (int qt = 0; qt < 2; ++qt) {
#pragma unroll
      for (int i = 0; i < 5; ++i) OLD(of[qt][i], i < 4 ? ofo : ofl, (unsigned)s * 36864u + qt * 18432 + i * 4096, 0);
      asm volatile("buffer_load_dword %0, %1, %2, %3 offen offset:%4 sc1" : "=v"(gt[qt]) : "v"(gto), "s"(D), "s"((unsigned)s * 128u), "n"(qt * 64) : "memory");
    }
    SB();
#define FWAIT(m) do { spins = 0;                                                                                          \
      while ((ballot(fv < need) & (m)) != 0) {                                                                            \
        if (++spins > SPINCAP) return;                                                                                    \
        if (spins > 1) { if (s < OFAST) __builtin_amdgcn_s_sleep(OSLP); else __builtin_amdgcn_s_sleep(OSL2); }            \
        CBAR();                                                                                                           \
        const int n0 = (int)lda(R, fv < need && !far ? fa : OOB), n1 = (int)ld32(R, fv < need && far ? fa : OOB, 0), nv = far ? n1 : n0;                                                             \
        fv = max(fv, nv);                                                                                                 \
      } } while (0)
#define ZYLD(zv) do { _Pragma("unroll") for (int hp = 0; hp < 2; ++hp) _Pragma("unroll") for (int ct = 0; ct < 4; ++ct)                        \
                        asm volatile("buffer_load_dwordx4 %0, %1, %2, %3 offen offset:%4 sc1" : "+v"(zy[ct][hp])                                   \
                                     : "v"(zv), "s"(D), "s"((unsigned)(s & RMASK) * ZS + ct * 40960 + 32768 + wave * 2048), "n"(hp * 1024) : "memory"); } while (0)
#define MFS(hh) do { _Pragma("unroll") for (int qt = 0; qt < 2; ++qt) _Pragma("unroll") for (int ct = 0; ct < 4; ++ct) {                           \
                        ab[ct][qt] = mfma(hh < 8 ? zu[ct][hh & 7] : zy[ct][hh & 1], pf[qt][hh], hh ? ab[ct][qt] : (f32x4){0.f, 0.f, 0.f, 0.f}); SB(); } } while (0)
    TS(t2);
#pragma unroll
    for (int k = 0; k < 8; ++k) asm volatile("" : "=v"(zy[k >> 1][k & 1]));
    bool yr = false;
#define ZYW() asm volatile("s_waitcnt vmcnt(0)" : "+v"(zy[0][0]), "+v"(zy[0][1]), "+v"(zy[1][0]), "+v"(zy[1][1]), "+v"(zy[2][0]), "+v"(zy[2][1]), "+v"(zy[3][0]), "+v"(zy[3][1]) :: "memory")
#define ZYTRY(w) do { if (!yr && (ballot(fv < need) >> 32) == 0) { ZYLD(zb); if (w) ZYW(); yr = true; } } while (0)
    spins = 0;
#pragma nounroll
    while ((unsigned)ballot(fv < need) != 0) {
      if (++spins > SPINCAP) return;
      if (spins > 1) { if (s < OFAST) __builtin_amdgcn_s_sleep(OSLP); else __builtin_amdgcn_s_sleep(OSL2); }
      CBAR();
      const int nv = (int)ld32(R, fv < need ? fa : OOB, 0);
      CBAR();
      ZYTRY(1);
      fv = max(fv, nv);
    }
    TS(t3);
#pragma unroll
    for (int hh = 0; hh < 10; ++hh)
#pragma unroll
      for (int qt = 0; qt < 2; ++qt) OLD(pf[qt][hh], hh < 8 ? pfo : pfy, (unsigned)(s & RMASK) * 81920u + qt * 40960 + (hh >> 2) * 4096, (hh & 3) * 1024);
    ZYTRY(0);
    const int sn = nxt(s) < NSEG ? nxt(s) : s;
    int n1;
    asm volatile("buffer_load_dword %0, %1, %2, 0 offen sc1" : "=v"(n1) : "v"(faddr(sn)), "s"(D) : "memory");
    const bool uok = sn != s && ballot(isu && fu < sn) == 0;
    unsigned zn = uok ? zw : OOB;
    pin_v(zn);
    asm volatile("s_waitcnt vmcnt(21)" : "+v"(of[0][0]), "+v"(of[0][1]), "+v"(of[0][2]), "+v"(of[0][3]), "+v"(of[0][4]), "+v"(of[1][0]), "+v"(of[1][1]),
                 "+v"(of[1][2]), "+v"(of[1][3]), "+v"(of[1][4]), "+v"(gt[0]), "+v"(gt[1]) :: "memory");
    SB();
    f32x4 aa[4][2];
#pragma unroll
    for (int ct = 0; ct < 4; ++ct)
#pragma unroll
      for (int qt = 0; qt < 2; ++qt) {
        f32x4 acc = {0.f, 0.f, 0.f, 0.f};
#pragma unroll
        for (int i = 0; i < 5; ++i) acc = mfma(wf[ct][i], of[qt][i], acc);
        aa[ct][qt] = acc * gt[qt];
        pin_v(aa[ct][qt]);
      }
    SB();
    f32x4 ab[4][2];
#pragma unroll
    for (int hh = 0; hh < 8; ++hh) {
      asm volatile("s_waitcnt vmcnt(%2)" : "+v"(pf[0][hh]), "+v"(pf[1][hh]) : "n"(19 + 2 * hh) : "memory");
      SB();
      MFS(hh);
      zu4(zn, sn, hh);
      SB();
    }
    TS(t4);
    asm volatile("s_waitcnt vmcnt(32)" : "+v"(n1), "+v"(pf[0][8]), "+v"(pf[1][8]), "+v"(pf[0][9]), "+v"(pf[1][9]) :: "memory");
    fu = isu ? n1 : fu;
    if (sn != s && !uok && !zuld(sn)) return;
    if (!yr) { FWAIT(~0ull); ZYLD(zb); ZYW(); }
#pragma unroll
    for (int k = 0; k < 8; ++k) pin_v(zy[k >> 1][k & 1]);
    fv = n1;
    SB();
    MFS(8); MFS(9);
    TS(t5);
#undef OLD
#undef ZYTRY
#undef ZYW
#undef FWAIT
#undef ZYLD
#undef MFS
#pragma unroll
    for (int qt = 0; qt < 2; ++qt)
#pragma unroll
      for (int ct = 0; ct < 4; ++ct)
        *(lds_f32x4*)(lds + wave * 8704 + (qt * 16 + l15) * 272 + (ct * 16 + g * 4) * 4) = aa[ct][qt] + ab[ct][qt] * (1.f - gt[qt]);
    BAR();
    f32x4 b0 = *(lds_f32x4*)(lds + t * 272 + n8 * 32), b1 = *(lds_f32x4*)(lds + t * 272 + n8 * 32 + 16);
#pragma unroll
    for (int w = 1; w < 4; ++w) { b0 += *(lds_f32x4*)(lds + w * 8704 + t * 272 + n8 * 32); b1 += *(lds_f32x4*)(lds + w * 8704 + t * 272 + n8 * 32 + 16); }
    u32x4 ov = {pk2(b0[0], b0[1]), pk2(b0[2], b0[3]), pk2(b1[0], b1[1]), pk2(b1[2], b1[3])};
    __builtin_amdgcn_raw_buffer_store_b128(ov, O, (unsigned)((s * 32 + t) * DM + cb * 64 + n8 * 8) * 2, 0, OAUX);
    BAR();
    if (s < 4) RT(2);
    TS(t6); ACC(0, 4 * (t1 - t0)); ACC(1, 4 * (t2 - t1)); ACC(2, 4 * (t3 - t2)); ACC(3, 4 * (t4 - t3)); ACC(4, 4 * (t5 - t4)); ACC(5, 4 * (t6 - t5));
    STEPRT(0, 0);
  }
  for (int k = 0; k < 8; ++k) SV(16 + k, acc_t[k]);
  RT(7);
}

#define LDS_BYTES 163840
__device__ __forceinline__ void code_touch(u64 pc, unsigned len, unsigned off, unsigned ld) {
  const i32x4 P = {(int)(unsigned)pc, (int)(unsigned)(pc >> 32), (int)len, 0x00020000};
  asm volatile("s_mov_b32 m0, %2\n\ts_nop 0\n\tbuffer_load_dword %0, %1, 0 offen lds" :: "v"(off), "s"(P), "s"(ld) : "memory");
}
__global__ void __launch_bounds__(256, 1) mega(Args a) {
  __shared__ __attribute__((aligned(16))) char smem[LDS_BYTES];
  lds_char* lds = (lds_char*)smem;
  const int b = blockIdx.x, tid = threadIdx.x, lane = tid & 63, wave = __builtin_amdgcn_readfirstlane(tid >> 6);
  const int x = b & 7, sl = b >> 3;
  const rsrc_t R = __builtin_amdgcn_make_buffer_rsrc(a.wb, 0, (int)OFF_END, 0x00020000);
  const Go go = go_look(R, b + wave * 4);
  if (b == 244) go_lead(R, tid, wave, lane);
  u64 pc; unsigned len;
  const unsigned tst = rt_now();
  {
    asm volatile("s_getpc_b64 %0\n.Lcode_here:\n\ts_mov_b32 %1, zz_code_end-.Lcode_here" : "=s"(pc), "=s"(len));
    if (wave == CPF) code_touch(pc, len, (unsigned)(sl * 8192 + lane * 128), (unsigned)(uintptr_t)lds + (x >= 4 && sl >= 8 && sl < 16 ? VB_OFF + 16384 : 153600));
  }
  RT(0);
  if (wave == 3 && (sl == 24 || sl == 29)) {
    unsigned d[4];
#pragma unroll
    for (int k = 0; k < 3; ++k) d[k] = ld32(R, (unsigned)(k * 64 + lane) << 20, 0);
    d[3] = *(const unsigned*)(a.out + min(lane, 2) * (1 << 19));
    asm volatile("" :: "v"(d[0]), "v"(d[1]), "v"(d[2]), "v"(d[3]));
  }
  if (x < 4 ? sl >= 25 : sl >= 16) {
    const unsigned t0 = rt_now();
    const int nw = x < 4 ? 28 : 56, id = __builtin_amdgcn_readfirstlane((sl - (x < 4 ? 25 : 16)) * 4 + wave);
    const int fa = x < 4 ? HF1 : HF3, fb = x < 4 ? HF0 : HF2, na = 6 * (18 - fa);
    unsigned d = 0;
    for (int e = id < nw ? id : 999; e < na + 3 * (18 - fb); e += nw) {
      const int j = e % 3, q = e / 3, blk = e < na ? fa + (q >> 1) : q - 2 * (18 - fa) + fb;
      const u16* W = e < na && (q & 1) ? a.Wk : ((e < na) == (x < 4) ? a.Wq : a.Wv);
      const int c = (int)((u64)W >> 8), hh = j < 2 ? (x & 3) * 8 + ((1 - c) & 3) + 4 * j : (31 - c) & 31;
      if ((hh >> 3) != (x & 3) || !HLP) continue;
      const u16* p = W + (size_t)(blk * 32 + (lane >> 1)) * HD + hh * 128 + (lane & 1) * 64;
      asm volatile("global_load_dword %0, %1, off nt" : "+v"(d) : "v"(p) : "memory");
    }
    for (int k = 0; k < GSL; ++k) __builtin_amdgcn_s_sleep(16);
    for (int e = id < nw ? id + 16 * (x < 4 ? G0 : G1) : 999; e < 576; e += nw) {
      const int t = e >> 4, k = e & 7, rb = e & 8, late = t >= 18, blk = x < 4 && !rb ? t >> 1 : t - 18 * late;
      const u16* W = rb ? a.Wv : ((x < 4 ? t & 1 : !late) ? a.Wk : a.Wq);
      const u16* p = rb && late == (x >= 4) ? a.Wo + (size_t)((x & 3) * 8 + k) * (128 * DM) + (blk * 64 + lane) * 64
                                            : W + (size_t)(blk * 32 + k * 4 + (lane >> 4)) * HD + (x & 3) * 1024 + (lane & 15) * 64;
      if (GM >> (rb * 2 / 8 + (x >> 2)) & 1) asm volatile("global_load_dword %0, %1, off nt" : "+v"(d) : "v"(p) : "memory");
    }
    asm volatile("s_waitcnt vmcnt(0)" : "+v"(d) :: "memory");
    if (x >= 4 && sl >= 20 && sl < 30) {
#pragma unroll
      for (int m = 0; m < 2; ++m) {
        const bool xm = (m == 0) == (XPF < APF);
        if (!(xm ? XPF : APF)) continue;
        for (int k = 0; k < 64 && rt_now() - t0 < (xm ? XPF : APF); ++k) __builtin_amdgcn_s_sleep(8);
        for (int e = (id - 16) * 4 + (x & 3); e < (xm ? 288 - XP0 : 243); e += 160)
          asm volatile("global_load_dword %0, %1, off" : "+v"(d) : "v"((xm ? a.x + XP0 * 4096 : a.wqkv) + e * 4096 + lane * 64) : "memory");
      }
      asm volatile("s_waitcnt vmcnt(0)" : "+v"(d) :: "memory");
      RT(13);
    }
    if (sl >= 20) for (int k = 0; k < 64 && rt_now() - t0 < (x < 4 || sl == 31 ? AWT : XPT); ++k) __builtin_amdgcn_s_sleep(8);
    if (AL2 && x < 4) {
      for (int e = id; e < 243; e += 28) asm volatile("global_load_dword %0, %1, off" : "+v"(d) : "v"(a.wqkv + (e * 64 + lane) * 64) : "memory");
      asm volatile("s_waitcnt vmcnt(0)" : "+v"(d) :: "memory");
    }
  }
  if (x < 4 && wave == 1 && sl >= 16 && sl < 24) calibrate(R, go, x + (sl & 4), sl & 3, lane);
  if (CT0 && sl >= 16 && (x < 4 ? wave == 1 && sl < 24 : sl < 18)) {
    for (int k = 0; k < 200 && rt_now() - tst < (x < 4 ? CT0 : CT1); ++k) __builtin_amdgcn_s_sleep(8);
#pragma unroll
    for (int k = 0; k < 4; ++k) code_touch(pc, len, (unsigned)((k * 8 + (x < 4 ? sl - 16 : (sl - 16) * 4 + wave)) * 8192 + lane * 128), (unsigned)(uintptr_t)lds + 153600);
    VMW(0);
  }
  if (x < 4) {
    if (sl < 16) for (int k = 0; k < QDL; ++k) __builtin_amdgcn_s_sleep(32);
    if (sl < 8) role_qkc(a, R, go, lds, x * 8 + sl, tid, wave, lane);
    else if (sl < 16) role_vcu(a, R, go, lds, x * 8 + sl - 8, tid, wave, lane);
    else if (sl < 25) role_mix(a, R, go, x * 9 + sl - 16, wave, lane);
    else role_aw(a, R, go, lds, x * 8 + sl - 25, tid, wave, lane);
  } else {
    const int y = x - 4;
    if (sl < 8) {
      const int h = y * 8 + ((sl + HSH) & 7);
      role_prj(a, R, go, lds, a.Wk + h * 128, a.Wq + h * 128, OFF_KF + h * 8192u, OFF_QF + h * 8192u, FL_PROG + h * 128, tid, wave, lane);
    } else if (sl < 16) role_vuw(a, R, go, lds, y * 8 + sl - 8, tid, wave, lane);
    else if (sl < 20) { role_px(a, R, go, y * 4 + sl - 16, wave, lane); __builtin_amdgcn_s_waitcnt(0x0F70); }
    else {
      if (sl == 31) { role_aw(a, R, go, lds, y * 8 + 7, tid, wave, lane); __builtin_amdgcn_s_waitcnt(0x0F70); }
      if (sl == 29 && !XPF) {
        const rsrc_t X = __builtin_amdgcn_make_buffer_rsrc((void*)a.x, 0, S_LEN * DM * 2, 0x00020000);
#pragma unroll 1
        for (int k = 0; k < 3; ++k) {
          unsigned t[12];
#pragma unroll
          for (int j = 0; j < 12; ++j) t[j] = __builtin_amdgcn_raw_buffer_load_b32(X, (unsigned)(((k * 12 + j) * 16 + y * 4 + wave) * 64 + lane) * 64, 0, 0);
#pragma unroll
          for (int j = 0; j < 12; ++j) asm volatile("" :: "v"(t[j]));
        }
      }
    }
    const int o = sl < 8 ? 45 + y * 8 + sl : (sl < 20 ? 99 : (sl < 29 ? y * 9 + sl - 20 : (sl < 31 ? 36 + (sl - 29) * 4 + y : 44 + y * 99)));
    if (o < 72) { BAR(); role_out(a, R, go, lds, o, tid, wave, lane); }
  }
}

extern "C" __device__ __attribute__((noinline, used, visibility("hidden"))) void zz_code_end() {}
extern "C" void kernel_launch(void* const* d_in, const int* in_sizes, int n_in, void* d_out, int out_size, void* d_ws,
                              size_t ws_size, hipStream_t stream) {
  char* wb = (char*)(((uintptr_t)d_ws + 0x1FFFFF) & ~(uintptr_t)0x1FFFFF);
  Args a;
  a.x = (const u16*)d_in[0]; a.mem0 = (const u16*)d_in[4]; a.Wq = (const u16*)d_in[5]; a.Wk = (const u16*)d_in[6]; a.Wv = (const u16*)d_in[7];
  a.Wo = (const u16*)d_in[8]; a.wqkv = (const u16*)d_in[1]; a.wo = (const u16*)d_in[2]; a.gw = (const u16*)d_in[10]; a.out = (u16*)d_out; a.wb = wb;
  mega<<<256, 256, 0, stream>>>(a);
}
```
